```python
import torch

def _softmax(x, axis=-1):
    return torch.softmax(x, dim=axis)

def _gelu(x):
    return torch.nn.functional.gelu(x, approximate='tanh')

def _silu(x):
    return torch.nn.functional.silu(x)

def _sigmoid(x):
    return torch.sigmoid(x)

def _layernorm(x, axis=-1, eps=1e-05):
    m = x.mean(dim=axis, keepdim=True)
    v = x.var(dim=axis, keepdim=True, unbiased=False)
    return (x - m) / torch.sqrt(v + eps)

def _scatter_add(tgt, idx, src, axis=0):
    return tgt.clone().index_add_(axis, idx, src)

def _segment_sum(src, seg, n):
    out = torch.zeros((n,) + src.shape[1:], dtype=src.dtype)
    return out.index_add_(0, seg, src)

def _pad(x, pw):
    flat = tuple((v for pair in reversed(pw) for v in pair))
    return torch.nn.functional.pad(x, flat)
kernel_name = 'adaLN_cond__sliding_block_cached__glu_gated_bilinear__prompt'

def setup_inputs():

    _w = lambda a: (lambda t: t.to(torch.bfloat16) if t.is_floating_point() else t)(torch.as_tensor(a))

    def _A_setup():
        B, Sq, D = (1, 2048, 640)
        Hq, Hkv, Sk, Dh = (32, 32, 2048, 128)
        assert Hq % Hkv == 0, (Hq, Hkv)
        return {'x': _w((lambda *s: torch.randn(*s, dtype=torch.bfloat16) * 0.05)(B, Sq, D)), 'k_cache': _w((lambda *s: torch.randn(*s, dtype=torch.bfloat16) * 1.6)(B, Hkv, Sk, Dh)), 'v_cache': _w((lambda *s: torch.randn(*s, dtype=torch.bfloat16) * 0.8)(B, Hkv, Sk, Dh)), 'wq': _w((lambda *s: torch.randn(*s, dtype=torch.bfloat16) * 1.6)(D, Hq * Dh) * D ** (-0.5)), 'wo': _w((lambda *s: torch.randn(*s, dtype=torch.bfloat16) * 0.8)(Hq * Dh, D) * (Hq * Dh) ** (-0.5))}
    _A_in = _A_setup()

    def _B_setup():
        B, S, D, F = (1, 2048, 640, 1728)
        return {'x': _w((lambda *s: torch.randn(*s, dtype=torch.bfloat16) * 0.05)(B, S, D)), 'w_gate': _w((lambda *s: torch.randn(*s, dtype=torch.bfloat16) * 0.4)(D, F) * D ** (-0.5)), 'w_up': _w((lambda *s: torch.randn(*s, dtype=torch.bfloat16) * 0.4)(D, F) * D ** (-0.5)), 'w_down': _w((lambda *s: torch.randn(*s, dtype=torch.bfloat16) * 0.4)(F, D) * F ** (-0.5))}
    _B_in = _B_setup()
    return {'A_x': _A_in['x'], 'A_k_cache': _A_in['k_cache'], 'A_v_cache': _A_in['v_cache'], 'A_wq': _A_in['wq'], 'A_wo': _A_in['wo'], 'B_x': _B_in['x'], 'B_w_gate': _B_in['w_gate'], 'B_w_up': _B_in['w_up'], 'B_w_down': _B_in['w_down'], 'c': _w((lambda *s: torch.randn(*s, dtype=torch.bfloat16) * 0.2)(1, 640) * 8.0), 'w_mod': _w((lambda *s: torch.randn(*s, dtype=torch.bfloat16) * 0.2)(640, 6 * 640) * 640 ** (-0.5) * 8.0)}

def reference(A_x, A_k_cache, A_v_cache, A_wq, A_wo, B_x, B_w_gate, B_w_up, B_w_down, c, w_mod):

    def _A_ref(x, k_cache, v_cache, wq, wo):
        B, Sq, D = x.shape
        Hq, Dh = (32, 128)
        Hkv, Sk = (k_cache.shape[1], k_cache.shape[2])
        W = min(4096, max(256, Sk // 4))
        rep = Hq // Hkv
        q = (lambda x, *d: x.permute(*d))((x @ wq).reshape(B, Sq, Hq, Dh), 0, 2, 1, 3)
        k = (lambda x, n, axis: torch.repeat_interleave(x, n, dim=axis))(k_cache[:, :, Sk - W:, :], rep, axis=1)
        v = (lambda x, n, axis: torch.repeat_interleave(x, n, dim=axis))(v_cache[:, :, Sk - W:, :], rep, axis=1)
        scores = torch.einsum('bhqd,bhkd->bhqk', q, k) * Dh ** (-0.5)
        p = _softmax(scores, axis=-1)
        o = torch.einsum('bhqk,bhkd->bhqd', p, v)
        return (lambda x, *d: x.permute(*d))(o, 0, 2, 1, 3).reshape(B, Sq, Hq * Dh) @ wo

    def _B_ref(x, w_gate, w_up, w_down):
        g = torch.einsum('bsd,df->bsf', x, w_gate)
        u = torch.einsum('bsd,df->bsf', x, w_up)
        h = g * u
        return torch.einsum('bsf,fd->bsd', h, w_down)
    x = A_x
    mod = (_silu(c) @ w_mod).reshape(c.shape[0], 6, 1, -1)
    sa, ca, ga, sb, cb, gb = (mod[:, 0].clone(), mod[:, 1].clone(), mod[:, 2].clone(), mod[:, 3].clone(), mod[:, 4].clone(), mod[:, 5].clone())
    h = _layernorm(x, axis=-1) * (1 + ca) + sa
    x = x + ga * _A_ref(h, A_k_cache, A_v_cache, A_wq, A_wo)
    h = _layernorm(x, axis=-1) * (1 + cb) + sb
    x = x + gb * _B_ref(h, B_w_gate, B_w_up, B_w_down)
    return x
```

```cpp
#include <hip/hip_runtime.h>
#include <hip/hip_bf16.h>
#include <stdint.h>

typedef unsigned short u16;
typedef short bf16x8 __attribute__((ext_vector_type(8)));
typedef short s16x4 __attribute__((ext_vector_type(4)));
typedef float f32x4 __attribute__((ext_vector_type(4)));
typedef float f32x16 __attribute__((ext_vector_type(16)));
typedef float f32x2 __attribute__((ext_vector_type(2)));
typedef __bf16 bf16x2_t __attribute__((ext_vector_type(2)));
typedef unsigned u32x4 __attribute__((ext_vector_type(4)));
typedef unsigned u32x2 __attribute__((ext_vector_type(2)));
#define DI __device__ __forceinline__
#define LDSP __attribute__((address_space(3)))

constexpr int T = 2048, D = 640, H = 32, DH = 128, SK = 2048, W = 512, F = 1728, NMOD = 3840;
constexpr float QSCALE = 0.12751743082f;
constexpr float LN_EPS = 1e-5f;

constexpr size_t WS_MOD  = 0;
constexpr size_t WS_H1T  = 1 << 16;
constexpr size_t WS_OT   = WS_H1T + (size_t)T * D * 2;
constexpr size_t WS_X1   = WS_OT + (size_t)T * H * DH * 2;
constexpr size_t WS_H2T  = WS_X1 + (size_t)T * D * 2;
constexpr size_t WS_HHT  = WS_H2T + (size_t)T * D * 2;

DI float bf2f(u16 v) { return __uint_as_float((unsigned)v << 16); }
DI unsigned cvtpk(float lo, float hi) { f32x2 v = {lo, hi}; bf16x2_t b = __builtin_convertvector(v, bf16x2_t); return __builtin_bit_cast(unsigned, b); }
DI float bflo(unsigned w) { return __uint_as_float(w << 16); }
DI float bfhi(unsigned w) { return __uint_as_float(w & 0xffff0000u); }
DI float rnd_bf(float x) { return bflo(cvtpk(x, 0.f)); }
DI void dma16(unsigned m0v, unsigned voff, __amdgpu_buffer_rsrc_t rs, unsigned soff) {
  asm volatile("s_mov_b32 m0, %0\n\ts_nop 0\n\tbuffer_load_dwordx4 %1, %2, %3 offen lds" :: "s"(m0v), "v"(voff), "s"(rs), "s"(soff) : "memory");
}
DI unsigned rfl(unsigned x) { return __builtin_amdgcn_readfirstlane(x); }
DI s16x4 trread(unsigned addr) { return __builtin_amdgcn_ds_read_tr16_b64_v4i16((LDSP s16x4*)addr); }
DI bf16x8 cat(s16x4 a, s16x4 b) { return __builtin_shufflevector(a, b, 0, 1, 2, 3, 4, 5, 6, 7); }
DI bf16x8 ldsread128(unsigned addr) { return *(LDSP bf16x8*)addr; }
#define MFMA32(a, b, c) __builtin_amdgcn_mfma_f32_32x32x16_bf16(__builtin_bit_cast(bf16x8_t, a), __builtin_bit_cast(bf16x8_t, b), c, 0, 0, 0)
#define MFMA16(a, b, c) __builtin_amdgcn_mfma_f32_16x16x32_bf16(__builtin_bit_cast(bf16x8_t, a), __builtin_bit_cast(bf16x8_t, b), c, 0, 0, 0)
typedef __bf16 bf16x8_t __attribute__((ext_vector_type(8)));
#define WAITALL() asm volatile("s_waitcnt vmcnt(0) lgkmcnt(0)" ::: "memory")

__global__ __launch_bounds__(256) void k0_mod(const u16* __restrict__ c, const u16* __restrict__ wmod, float* __restrict__ mod) {
  __shared__ float red[64][33];
  const int t = threadIdx.x, cg = t & 3, rg = t >> 2;
  const int n0 = blockIdx.x * 32 + cg * 8;
  float acc[8] = {};
  #pragma unroll
  for (int i = 0; i < 10; ++i) {
    const int k = rg + 64 * i;
    float cv = bf2f(c[k]);
    float s = rnd_bf(cv / (1.f + __expf(-cv)));
    u32x4 w = *(const u32x4*)(wmod + (size_t)k * NMOD + n0);
    #pragma unroll
    for (int e = 0; e < 4; ++e) { acc[2 * e] += s * bflo(w[e]); acc[2 * e + 1] += s * bfhi(w[e]); }
  }
  #pragma unroll
  for (int e = 0; e < 8; ++e) red[rg][cg * 8 + e] = acc[e];
  __syncthreads();
  if (t < 32) { float s = 0; for (int i = 0; i < 64; ++i) s += red[i][t]; mod[blockIdx.x * 32 + t] = rnd_bf(s); }
}

template <int MODE>
__global__ __launch_bounds__(256) void k1_ln(const u16* __restrict__ x, const float* __restrict__ mod, u16* __restrict__ out) {
  const int t = threadIdx.x, sub = t & 15, row = blockIdx.x * 16 + (t >> 4);
  const float* sh = mod + (MODE == 0 ? 0 : 3 * D);
  const float* sc = mod + (MODE == 0 ? D : 4 * D);
  float v[5][8]; float s = 0;
  #pragma unroll
  for (int i = 0; i < 5; ++i) {
    u32x4 w = *(const u32x4*)(x + (size_t)row * D + (sub + 16 * i) * 8);
    #pragma unroll
    for (int e = 0; e < 4; ++e) { v[i][2 * e] = bflo(w[e]); v[i][2 * e + 1] = bfhi(w[e]); s += v[i][2 * e] + v[i][2 * e + 1]; }
  }
  #pragma unroll
  for (int m = 1; m < 16; m <<= 1) s += __shfl_xor(s, m);
  const float mean = s * (1.f / D); float q = 0;
  #pragma unroll
  for (int i = 0; i < 5; ++i)
    #pragma unroll
    for (int e = 0; e < 8; ++e) { float d = v[i][e] - mean; q += d * d; }
  #pragma unroll
  for (int m = 1; m < 16; m <<= 1) q += __shfl_xor(q, m);
  const float rstd = rsqrtf(q * (1.f / D) + LN_EPS);
  #pragma unroll
  for (int i = 0; i < 5; ++i) {
    const int ch = sub + 16 * i;
    u32x4 o;
    #pragma unroll
    for (int e = 0; e < 4; ++e) {
      const int c0 = ch * 8 + 2 * e;
      float a0 = rnd_bf(1.f + sc[c0]), a1 = rnd_bf(1.f + sc[c0 + 1]);
      float h0 = (v[i][2 * e] - mean) * rstd * a0 + sh[c0], h1 = (v[i][2 * e + 1] - mean) * rstd * a1 + sh[c0 + 1];
      if (MODE == 0) { h0 *= QSCALE; h1 *= QSCALE; }
      o[e] = cvtpk(h0, h1);
    }
    size_t off;
    if (MODE == 0) off = (((size_t)(row >> 5) * 40 + (ch >> 1)) * 64 + (ch & 1) * 32 + (row & 31)) * 8;
    else           off = ((((size_t)(row >> 5) * 20 + (ch >> 2)) * 2 + ((row >> 4) & 1)) * 64 + (ch & 3) * 16 + (row & 15)) * 8;
    *(u32x4*)(out + off) = o;
  }
}

__global__ __launch_bounds__(256, 1) void k2_attn(const u16* __restrict__ h1t, const u16* __restrict__ wq, const u16* __restrict__ kc, const u16* __restrict__ vc, u16* __restrict__ ot) {
  extern __shared__ __attribute__((aligned(16))) char lds[];
  const int lane = threadIdx.x & 63, w = rfl(threadIdx.x >> 6);
  const int bid = blockIdx.x, xcd = bid & 7, idx = bid >> 3;
  const int head = xcd * 4 + (idx & 3), rt = idx >> 2;
  const int l31 = lane & 31, hi = lane >> 5;
  const unsigned ldsb = (unsigned)(size_t)lds;
  __amdgpu_buffer_rsrc_t rsW = __builtin_amdgcn_make_buffer_rsrc((void*)(wq + head * DH), (short)0, (D * H * DH - head * DH) * 2, 0x00020000);
  __amdgpu_buffer_rsrc_t rsK = __builtin_amdgcn_make_buffer_rsrc((void*)(kc + ((size_t)head * SK + (SK - W)) * DH), (short)0, W * DH * 2, 0x00020000);
  __amdgpu_buffer_rsrc_t rsV = __builtin_amdgcn_make_buffer_rsrc((void*)(vc + ((size_t)head * SK + (SK - W)) * DH), (short)0, W * DH * 2, 0x00020000);
  const unsigned tsw = 16u * ((lane & 15) ^ ((lane >> 4) << 2));
  const unsigned voW = (lane >> 4) * 8192u + tsw;
  const unsigned voV = (lane >> 4) * 256u + tsw;
  const unsigned voK = ((lane >> 2) & 7) * 256u + (lane >> 5) * 64u + 16u * ((lane & 3) ^ ((2 * w + ((lane >> 4) & 1)) & 3));
  const int qq = (lane & 15) >> 2, pp = lane & 3, gi = (lane >> 4) & 1, sp = ((pp & 1) << 1) | (pp >> 1);
  unsigned tb[4];
  #pragma unroll
  for (int db = 0; db < 4; ++db) tb[db] = 256u * (8 * hi + qq) + 16u * ((4 * db + 2 * gi + (sp >> 1)) ^ (qq << 2)) + 8u * (sp & 1);
  const int krow = (l31 & 0x13) | ((l31 & 4) << 1) | ((l31 & 8) >> 1);
  unsigned kb_[2];
  #pragma unroll
  for (int par = 0; par < 2; ++par) kb_[par] = 2048u * (krow >> 3) + 64u * (krow & 7) + 16u * ((2 * par + hi) ^ ((krow >> 2) & 3));

  f32x16 qacc[4][2] = {};
  const int qblk0 = rt * 8 + w * 2;
  for (int ch = 0; ch < 10; ++ch) {
    __syncthreads();
    #pragma unroll
    for (int m = 0; m < 4; ++m) dma16(rfl(ldsb + 1024 * (4 * w + m)), voW, rsW, rfl((64u * ch + 4 * (4 * w + m)) * 8192u));
    bf16x8 hb[2][4];
    #pragma unroll
    for (int qb = 0; qb < 2; ++qb)
      #pragma unroll
      for (int s = 0; s < 4; ++s) hb[qb][s] = *(const bf16x8*)(h1t + (((size_t)(qblk0 + qb) * 40 + 4 * ch + s) * 64 + lane) * 8);
    WAITALL(); __syncthreads();
    #pragma unroll
    for (int s = 0; s < 4; ++s)
      #pragma unroll
      for (int db = 0; db < 4; ++db) {
        bf16x8 a = cat(trread(ldsb + tb[db] + 4096 * s), trread(ldsb + tb[db] + 4096 * s + 1024));
        #pragma unroll
        for (int qb = 0; qb < 2; ++qb) qacc[db][qb] = MFMA32(a, hb[qb][s], qacc[db][qb]);
      }
  }
  bf16x8 qf[2][8];
  #pragma unroll
  for (int qb = 0; qb < 2; ++qb)
    #pragma unroll
    for (int s = 0; s < 8; ++s) {
      u32x4 u;
      #pragma unroll
      for (int e = 0; e < 4; ++e) u[e] = cvtpk(qacc[s >> 1][qb][8 * (s & 1) + 2 * e], qacc[s >> 1][qb][8 * (s & 1) + 2 * e + 1]);
      qf[qb][s] = __builtin_bit_cast(bf16x8, u);
    }
  f32x16 o[4][2] = {};
  float m[2] = {-1e30f, -1e30f}, l[2] = {0.f, 0.f};
  for (int t = 0; t < 8; ++t) {
    __syncthreads();
    #pragma unroll
    for (int i = 0; i < 4; ++i) { const int R = w + 4 * (i >> 1), ap = i & 1;
      dma16(rfl(ldsb + 2048 * R + 1024 * ap), voK, rsK, rfl((64u * t + 8 * R) * 256u + 128u * ap)); }
    #pragma unroll
    for (int i = 0; i < 4; ++i) dma16(rfl(ldsb + 16384 + 1024 * (4 * w + i)), voV, rsV, rfl((64u * t + 4 * (4 * w + i)) * 256u));
    WAITALL(); __syncthreads();
    f32x16 s[2][2] = {};
    #pragma unroll
    for (int d0 = 0; d0 < 8; ++d0)
      #pragma unroll
      for (int kb = 0; kb < 2; ++kb) {
        bf16x8 a = ldsread128(ldsb + kb_[d0 & 1] + 8192 * kb + 512 * (d0 >> 1));
        #pragma unroll
        for (int qb = 0; qb < 2; ++qb) s[kb][qb] = MFMA32(a, qf[qb][d0], s[kb][qb]);
      }
    #pragma unroll
    for (int qb = 0; qb < 2; ++qb) {
      float mx = m[qb];
      #pragma unroll
      for (int r = 0; r < 16; ++r) mx = fmaxf(mx, fmaxf(s[0][qb][r], s[1][qb][r]));
      mx = fmaxf(mx, __shfl_xor(mx, 32));
      const float f = exp2f(m[qb] - mx); m[qb] = mx; l[qb] *= f;
      #pragma unroll
      for (int db = 0; db < 4; ++db) o[db][qb] *= f;
      float rs = 0;
      #pragma unroll
      for (int kb = 0; kb < 2; ++kb)
        #pragma unroll
        for (int r = 0; r < 16; ++r) { float p = exp2f(s[kb][qb][r] - mx); s[kb][qb][r] = p; rs += p; }
      l[qb] += rs;
    }
    #pragma unroll
    for (int ks = 0; ks < 4; ++ks) {
      bf16x8 p[2];
      #pragma unroll
      for (int qb = 0; qb < 2; ++qb) { u32x4 u;
        #pragma unroll
        for (int e = 0; e < 4; ++e) u[e] = cvtpk(s[ks >> 1][qb][8 * (ks & 1) + 2 * e], s[ks >> 1][qb][8 * (ks & 1) + 2 * e + 1]);
        p[qb] = __builtin_bit_cast(bf16x8, u); }
      #pragma unroll
      for (int db = 0; db < 4; ++db) {
        bf16x8 a = cat(trread(ldsb + 16384 + tb[db] + 4096 * ks), trread(ldsb + 16384 + tb[db] + 4096 * ks + 1024));
        #pragma unroll
        for (int qb = 0; qb < 2; ++qb) o[db][qb] = MFMA32(a, p[qb], o[db][qb]);
      }
    }
  }
  #pragma unroll
  for (int qb = 0; qb < 2; ++qb) {
    const float lt = l[qb] + __shfl_xor(l[qb], 32);
    const float il = 1.f / lt;
    #pragma unroll
    for (int db = 0; db < 4; ++db)
      #pragma unroll
      for (int c = 0; c < 2; ++c) {
        u32x4 u;
        #pragma unroll
        for (int e = 0; e < 4; ++e) u[e] = cvtpk(o[db][qb][8 * c + 2 * e] * il, o[db][qb][8 * c + 2 * e + 1] * il);
        const size_t off = ((size_t)(rt * 4 + w) * 128 + 4 * head + db) * 2048 + (2 * qb + (l31 >> 4)) * 512 + (2 * c + hi) * 128 + (l31 & 15) * 8;
        *(u32x4*)(ot + off) = u;
      }
  }
}

template <int NKB>
__global__ __launch_bounds__(256, 1) void k3_gemm(const u16* __restrict__ at, const u16* __restrict__ wt, const u16* __restrict__ res, const float* __restrict__ gate, u16* __restrict__ out) {
  extern __shared__ __attribute__((aligned(16))) char lds[];
  const int lane = threadIdx.x & 63, w = rfl(threadIdx.x >> 6);
  const int bid = blockIdx.x, xcd = bid & 7, idx = bid >> 3;
  const int tt = (xcd >> 1) * 8 + (idx >> 2), nt0 = (xcd & 1) * 4 + (idx & 3);
  const unsigned ldsb = (unsigned)(size_t)lds + w * 5120;
  __amdgpu_buffer_rsrc_t rsW = __builtin_amdgcn_make_buffer_rsrc((void*)(wt + nt0 * 80), (short)0, (NKB * 32 * D - nt0 * 80) * 2, 0x00020000);
  unsigned vo[5];
  #pragma unroll
  for (int p = 0; p < 5; ++p) { const int f = 64 * p + lane, pos = f / 10, chn = f % 10; const int k = (pos & 0x13) | ((pos & 4) << 1) | ((pos & 8) >> 1); vo[p] = k * (D * 2) + chn * 16; }
  const int g = lane >> 4, qq = (lane & 15) >> 2, pp = lane & 3;
  const unsigned rb = ldsb + 160u * (16 * (g >> 1) + 4 * (g & 1) + qq) + 8u * pp;
  f32x4 acc[5][4] = {};
  for (int kb = w; kb < NKB; kb += 4) {
    #pragma unroll
    for (int p = 0; p < 5; ++p) dma16(rfl(ldsb + 1024 * p), vo[p], rsW, rfl((unsigned)kb * 32u * (D * 2)));
    bf16x8 b[4];
    #pragma unroll
    for (int tb = 0; tb < 4; ++tb) b[tb] = *(const bf16x8*)(at + (((size_t)tt * NKB + kb) * 4 + tb) * 512 + lane * 8);
    WAITALL();
    #pragma unroll
    for (int nt = 0; nt < 5; ++nt) {
      bf16x8 a = cat(trread(rb + 32 * nt), trread(rb + 32 * nt + 8 * 160));
      #pragma unroll
      for (int tb = 0; tb < 4; ++tb) acc[nt][tb] = MFMA16(a, b[tb], acc[nt][tb]);
    }
    asm volatile("s_waitcnt lgkmcnt(0)" ::: "memory");
  }
  __syncthreads();
  f32x4* red = (f32x4*)lds;
  #pragma unroll
  for (int nt = 0; nt < 5; ++nt)
    #pragma unroll
    for (int tb = 0; tb < 4; ++tb) red[((w * 4 + tb) * 5 + nt) * 64 + lane] = acc[nt][tb];
  __syncthreads();
  const int tok = tt * 64 + w * 16 + (lane & 15);
  #pragma unroll
  for (int nt = 0; nt < 5; ++nt) {
    f32x4 s = red[((0 * 4 + w) * 5 + nt) * 64 + lane];
    #pragma unroll
    for (int ww = 1; ww < 4; ++ww) s += red[((ww * 4 + w) * 5 + nt) * 64 + lane];
    const int n = nt0 * 80 + 16 * nt + 4 * g;
    u32x2 r = *(const u32x2*)(res + (size_t)tok * D + n);
    f32x4 gt = *(const f32x4*)(gate + n);
    u32x2 o;
    o[0] = cvtpk(bflo(r[0]) + gt[0] * s[0], bfhi(r[0]) + gt[1] * s[1]);
    o[1] = cvtpk(bflo(r[1]) + gt[2] * s[2], bfhi(r[1]) + gt[3] * s[3]);
    *(u32x2*)(out + (size_t)tok * D + n) = o;
  }
}

__global__ __launch_bounds__(256, 1) void k4_glu(const u16* __restrict__ h2t, const u16* __restrict__ wg, const u16* __restrict__ wu, u16* __restrict__ hht) {
  extern __shared__ __attribute__((aligned(16))) char lds[];
  const int lane = threadIdx.x & 63, w = rfl(threadIdx.x >> 6);
  const int bid = blockIdx.x, xcd = bid & 7, idx = bid >> 3;
  const int tt = (xcd >> 2) * 8 + (idx >> 2), cg = (xcd & 3) * 4 + (idx & 3);
  const int NT0 = min(7 * cg, 101);
  const unsigned ldsb = (unsigned)(size_t)lds;
  __amdgpu_buffer_rsrc_t rsG = __builtin_amdgcn_make_buffer_rsrc((void*)(wg + NT0 * 16), (short)0, (D * F - NT0 * 16) * 2, 0x00020000);
  __amdgpu_buffer_rsrc_t rsU = __builtin_amdgcn_make_buffer_rsrc((void*)(wu + NT0 * 16), (short)0, (D * F - NT0 * 16) * 2, 0x00020000);
  const int g = lane >> 4, qq = (lane & 15) >> 2, pp = lane & 3;
  const unsigned rb = ldsb + 224u * (16 * (g >> 1) + 4 * (g & 1) + qq) + 8u * pp;
  f32x4 ag[7][2] = {}, au[7][2] = {};
  for (int kb = 0; kb < 20; ++kb) {
    __syncthreads();
    for (int pc = w; pc < 14; pc += 4) {
      const int p = pc % 7, f = 64 * p + lane, pos = f / 14, chn = f % 14; const int k = (pos & 0x13) | ((pos & 4) << 1) | ((pos & 8) >> 1);
      dma16(rfl(ldsb + 1024 * pc), k * (F * 2) + chn * 16, pc < 7 ? rsG : rsU, rfl((unsigned)kb * 32u * (F * 2)));
    }
    bf16x8 b[2];
    #pragma unroll
    for (int tb = 0; tb < 2; ++tb) b[tb] = *(const bf16x8*)(h2t + (((size_t)(tt * 4 + w) * 20 + kb) * 2 + tb) * 512 + lane * 8);
    WAITALL(); __syncthreads();
    #pragma unroll
    for (int nt = 0; nt < 7; ++nt) {
      bf16x8 a = cat(trread(rb + 32 * nt), trread(rb + 32 * nt + 8 * 224));
      bf16x8 u = cat(trread(rb + 7168 + 32 * nt), trread(rb + 7168 + 32 * nt + 8 * 224));
      #pragma unroll
      for (int tb = 0; tb < 2; ++tb) { ag[nt][tb] = MFMA16(a, b[tb], ag[nt][tb]); au[nt][tb] = MFMA16(u, b[tb], au[nt][tb]); }
    }
  }
  #pragma unroll
  for (int tb = 0; tb < 2; ++tb) {
    const int tok = tt * 128 + w * 32 + tb * 16 + (lane & 15);
    #pragma unroll
    for (int nt = 0; nt < 7; ++nt) {
      const int NT = NT0 + nt;
      u32x2 o; o[0] = cvtpk(ag[nt][tb][0] * au[nt][tb][0], ag[nt][tb][1] * au[nt][tb][1]); o[1] = cvtpk(ag[nt][tb][2] * au[nt][tb][2], ag[nt][tb][3] * au[nt][tb][3]);
      const size_t off = (((size_t)(tok >> 6) * 54 + (NT >> 1)) * 4 + ((tok >> 4) & 3)) * 512 + ((2 * (NT & 1) + (g >> 1)) * 16 + (tok & 15)) * 8 + (g & 1) * 4;
      *(u32x2*)(hht + off) = o;
    }
  }
}

extern "C" void kernel_launch(void* const* d_in, const int* in_sizes, int n_in, void* d_out, int out_size, void* d_ws, size_t ws_size, hipStream_t stream) {
  const u16* x = (const u16*)d_in[0]; const u16* kc = (const u16*)d_in[1]; const u16* vc = (const u16*)d_in[2];
  const u16* wq = (const u16*)d_in[3]; const u16* wo = (const u16*)d_in[4];
  const u16* wg = (const u16*)d_in[6]; const u16* wu = (const u16*)d_in[7]; const u16* wd = (const u16*)d_in[8];
  const u16* c = (const u16*)d_in[9]; const u16* wmod = (const u16*)d_in[10];
  char* ws = (char*)d_ws;
  float* mod = (float*)(ws + WS_MOD); u16* h1t = (u16*)(ws + WS_H1T); u16* ot = (u16*)(ws + WS_OT); u16* x1 = (u16*)(ws + WS_X1);
  u16* h2t = (u16*)(ws + WS_H2T); u16* hht = (u16*)(ws + WS_HHT);
  (void)hipFuncSetAttribute((const void*)k2_attn, hipFuncAttributeMaxDynamicSharedMemorySize, 96 * 1024);
  (void)hipFuncSetAttribute((const void*)k3_gemm<128>, hipFuncAttributeMaxDynamicSharedMemorySize, 96 * 1024);
  (void)hipFuncSetAttribute((const void*)k3_gemm<54>, hipFuncAttributeMaxDynamicSharedMemorySize, 96 * 1024);
  k0_mod<<<NMOD / 32, 256, 0, stream>>>(c, wmod, mod);
  k1_ln<0><<<T / 16, 256, 0, stream>>>(x, mod, h1t);
  k2_attn<<<256, 256, 96 * 1024, stream>>>(h1t, wq, kc, vc, ot);
  k3_gemm<128><<<256, 256, 96 * 1024, stream>>>(ot, wo, x, mod + 2 * D, x1);
  k1_ln<1><<<T / 16, 256, 0, stream>>>(x1, mod, h2t);
  k4_glu<<<256, 256, 16 * 1024, stream>>>(h2t, wg, wu, hht);
  k3_gemm<54><<<256, 256, 96 * 1024, stream>>>(hht, wd, x1, mod + 5 * D, (u16*)d_out);
}
```

```cpp
#include <hip/hip_runtime.h>
#include <hip/hip_bf16.h>
#include <stdint.h>

typedef unsigned short u16;

__global__ __launch_bounds__(256, 1) void k2_mega(const u16* x, const u16* kc, const u16* vc, const u16* wq, const u16* wo, const u16* wd, const u16* wg, const u16* wu,
                                                  u16* out, char* ws, const u16* c, const u16* wmod) {
  __shared__ __attribute__((aligned(16))) char lds[163840];
  asm volatile(
    "s_getpc_b64 s[42:43]\n"
    "Lpc%=:\n"
    "v_lshrrev_b32 v29, 2, v0\n"
    "v_lshlrev_b32 v29, 7, v29\n"
    "global_load_dword v30, v29, s[42:43]\n"
    "s_load_dwordx16 s[8:23], %0, 0x0\n"
    "s_load_dwordx8 s[24:31], %0, 0x40\n"
    "v_lshrrev_b32 v29, 6, v0\n"
    "v_and_b32 v31, 63, v0\n"
    "v_readfirstlane_b32 s34, v29\n"
    "v_lshlrev_b32 v1, 4, v31\n"
    "s_and_b32 s46, %1, 7\n"
    "s_lshr_b32 s45, %1, 3\n"
    "s_lshr_b32 s35, s46, 1\n"
    "s_and_b32 s39, s46, 1\n"
    "s_lshl_b32 s36, s39, 3\n"
    "s_lshr_b32 s47, s45, 2\n"
    "s_add_u32 s36, s36, s47\n"
    "s_and_b32 s37, s45, 3\n"
    "s_lshl_b32 s38, s35, 2\n"
    "s_add_u32 s38, s38, s37\n"
    "s_mul_i32 s46, s45, 24\n"
    "s_mul_i32 s47, s34, 6\n"
    "s_add_u32 s46, s46, s47\n"
    "v_lshrrev_b32 v190, 4, v1\n"
    "v_min_u32 v190, 5, v190\n"
    "v_add_u32 v190, s46, v190\n"
    "v_lshlrev_b32 v190, 7, v190\n"
    "v_min_u32 v190, Lend%=-Lpc%=-4, v190\n"
    "global_load_dword v191, v190, s[42:43]\n"
    "s_waitcnt lgkmcnt(0)\n"
    "s_load_dwordx2 s[40:41], s[26:27], 0x0\n"
    "; ==== operand requests of the two front phases\n"
    "s_lshl_b32 s87, s35, 3\n"
    "s_and_b32 s48, s45, 7\n"
    "s_add_u32 s87, s87, s48\n"
    "s_lshl_b32 s88, s39, 2\n"
    "s_lshr_b32 s48, s45, 3\n"
    "s_add_u32 s88, s88, s48\n"
    "s_lshr_b32 s90, %1, 2\n"
    "s_and_b32 s91, %1, 3\n"
    "s_lshl_b32 s46, s90, 7\n"
    "s_mul_i32 s47, s91, 0x12c000\n"
    "s_add_u32 s46, s46, s47\n"
    "s_sub_u32 s47, 0x4b0000, s46\n"
    "s_cmp_lt_u32 %1, 0xf0\n"
    "s_cselect_b32 s47, s47, 0\n"
    "s_cselect_b32 s48, 0x100, 0\n"
    "s_add_u32 s56, s30, s46\n"
    "s_addc_u32 s57, s31, 0\n"
    "s_mov_b32 s58, s47\n"
    "s_mov_b32 s59, 0x20000\n"
    "s_mul_i32 s46, s91, 0x3c00\n"
    "s_lshl_b32 s47, s90, 8\n"
    "s_add_u32 s46, s46, s47\n"
    "s_add_u32 s46, s46, 0x12000\n"
    "s_add_u32 s64, s26, s46\n"
    "s_addc_u32 s65, s27, 0\n"
    "s_mov_b32 s66, s48\n"
    "s_mov_b32 s67, 0x20000\n"
    "s_mul_i32 s46, s91, 0x140\n"
    "s_add_u32 s60, s28, s46\n"
    "s_addc_u32 s61, s29, 0\n"
    "s_mov_b32 s62, 0x140\n"
    "s_mov_b32 s63, 0x20000\n"
    "v_lshrrev_b32 v124, 3, v0\n"
    "v_mul_u32_u24 v144, 0x1e00, v124\n"
    "v_and_b32 v125, 7, v0\n"
    "v_lshl_add_u32 v144, v125, 4, v144\n"
    "v_lshlrev_b32 v145, 1, v124\n"
    "s_mov_b32 s89, 0\n"
    "buffer_load_dwordx4 v[40:43], v144, s[56:59], s89 offen offset:0\n"
    "buffer_load_ushort v60, v145, s[60:63], 0 offen offset:0\n"
    "s_mov_b32 s89, 0x3c000\n"
    "buffer_load_dwordx4 v[44:47], v144, s[56:59], s89 offen offset:0\n"
    "buffer_load_ushort v61, v145, s[60:63], 0 offen offset:64\n"
    "s_mov_b32 s89, 0x78000\n"
    "buffer_load_dwordx4 v[48:51], v144, s[56:59], s89 offen offset:0\n"
    "buffer_load_ushort v62, v145, s[60:63], 0 offen offset:128\n"
    "s_mov_b32 s89, 0xb4000\n"
    "buffer_load_dwordx4 v[52:55], v144, s[56:59], s89 offen offset:0\n"
    "buffer_load_ushort v63, v145, s[60:63], 0 offen offset:192\n"
    "s_mov_b32 s89, 0xf0000\n"
    "buffer_load_dwordx4 v[56:59], v144, s[56:59], s89 offen offset:0\n"
    "buffer_load_ushort v64, v145, s[60:63], 0 offen offset:256\n"
    "s_lshl_b32 s46, s88, 5\n"
    "s_add_u32 s46, s46, s87\n"
    "s_mul_i32 s46, s46, 0x2800\n"
    "s_mul_i32 s47, s34, 0x140\n"
    "s_add_u32 s46, s46, s47\n"
    "s_sub_u32 s48, 0x2800, s47\n"
    "s_add_u32 s68, s8, s46\n"
    "s_addc_u32 s69, s9, 0\n"
    "s_mov_b32 s70, s48\n"
    "s_mov_b32 s71, 0x20000\n"
    "s_lshl_b32 s46, s47, 1\n"
    "s_add_u32 s46, s46, 0x12000\n"
    "s_add_u32 s72, s26, s46\n"
    "s_addc_u32 s73, s27, 0\n"
    "s_mov_b32 s74, 0xe880\n"
    "s_mov_b32 s75, 0x20000\n"
    "s_lshl_b32 s46, s88, 3\n"
    "s_lshr_b32 s47, s87, 2\n"
    "s_add_u32 s46, s46, s47\n"
    "s_mul_i32 s46, s46, 0xa000\n"
    "s_mul_i32 s47, s34, 0x2800\n"
    "s_add_u32 s46, s46, s47\n"
    "s_lshr_b32 s47, s87, 1\n"
    "s_and_b32 s47, s47, 1\n"
    "s_lshl_b32 s47, s47, 10\n"
    "s_add_u32 s46, s46, s47\n"
    "s_and_b32 s47, s87, 1\n"
    "s_lshl_b32 s47, s47, 7\n"
    "s_add_u32 s46, s46, s47\n"
    "s_add_u32 s46, s46, 0x30000\n"
    "s_add_u32 s76, s26, s46\n"
    "s_addc_u32 s77, s27, 0\n"
    "s_mov_b32 s78, 0x2800\n"
    "s_mov_b32 s79, 0x20000\n"
    "v_lshrrev_b32 v124, 4, v1\n"
    "v_and_b32 v125, 7, v124\n"
    "v_bfe_u32 v126, v124, 3, 2\n"
    "v_lshrrev_b32 v127, 5, v124\n"
    "v_lshlrev_b32 v147, 4, v126\n"
    "v_lshl_add_u32 v147, v127, 3, v147\n"
    "v_mul_u32_u24 v146, 0x500, v125\n"
    "v_add_u32 v146, v146, v147\n"
    "v_lshlrev_b32 v147, 1, v147\n"
    "v_lshlrev_b32 v148, 8, v126\n"
    "v_lshl_add_u32 v148, v125, 4, v148\n"
    "v_lshl_add_u32 v148, v127, 3, v148\n"
    "v_lshlrev_b32 v151, 8, v126\n"
    "v_lshl_add_u32 v151, v125, 4, v151\n"
    "v_lshl_add_u32 v151, v127, 11, v151\n"
    "v_lshlrev_b32 v149, 3, v125\n"
    "v_add_u32 v149, 0x20800, v149\n"
    "buffer_load_dwordx2 v[74:75], v146, s[68:71], 0 offen offset:0\n"
    "buffer_load_dwordx2 v[76:77], v146, s[68:71], 0 offen offset:64\n"
    "buffer_load_dwordx2 v[78:79], v146, s[68:71], 0 offen offset:128\n"
    "buffer_load_dwordx2 v[80:81], v146, s[68:71], 0 offen offset:192\n"
    "buffer_load_dwordx2 v[82:83], v146, s[68:71], 0 offen offset:256\n"
    "s_lshl_b32 s87, s35, 3\n"
    "s_and_b32 s48, s45, 7\n"
    "s_add_u32 s87, s87, s48\n"
    "s_lshl_b32 s88, s39, 2\n"
    "s_lshr_b32 s48, s45, 3\n"
    "s_add_u32 s88, s88, s48\n"
    "s_mov_b32 s86, 0x3e0293ee\n"
    "s_mov_b32 s98, 0\n"
    "s_lshl_b32 s46, s87, 8\n"
    "s_lshl_b32 s47, s34, 17\n"
    "s_add_u32 s46, s46, s47\n"
    "s_sub_u32 s47, 0x500000, s46\n"
    "s_add_u32 s52, s14, s46\n"
    "s_addc_u32 s53, s15, 0\n"
    "s_mov_b32 s54, s47\n"
    "s_mov_b32 s55, 0x20000\n"
    "s_lshl_b32 s80, s34, 12\n"
    "s_lshl_b32 s81, s34, 11\n"
    "s_lshl_b32 s96, %1, 2\n"
    "s_add_u32 s96, s96, s34\n"
    "s_lshl_b32 s82, s34, 13\n"
    "s_add_u32 s82, s82, 0x20000\n"
    "v_lshrrev_b32 v29, 4, v1\n"
    "v_lshrrev_b32 v32, 4, v29\n"
    "v_lshlrev_b32 v33, 2, v32\n"
    "v_and_b32 v34, 15, v29\n"
    "v_xor_b32 v33, v33, v34\n"
    "v_lshlrev_b32 v33, 4, v33\n"
    "v_lshl_add_u32 v10, v32, 8, v33\n"
    "v_add_u32 v11, 0x400, v10\n"
    "v_add_u32 v12, 0x800, v10\n"
    "v_add_u32 v13, 0xc00, v10\n"
    "v_xor_b32 v38, 0, v33\n"
    "v_lshl_add_u32 v2, v32, 13, v38\n"
    "v_xor_b32 v38, 0, v33\n"
    "v_lshl_add_u32 v3, v32, 13, v38\n"
    "v_add_u32 v3, 0x8000, v3\n"
    "v_xor_b32 v38, 32, v33\n"
    "v_lshl_add_u32 v4, v32, 13, v38\n"
    "v_add_u32 v4, 0x10000, v4\n"
    "v_xor_b32 v38, 32, v33\n"
    "v_lshl_add_u32 v5, v32, 13, v38\n"
    "v_add_u32 v5, 0x18000, v5\n"
    "v_bfe_u32 v38, v29, 2, 2\n"
    "v_lshl_add_u32 v39, v32, 3, v38\n"
    "v_lshlrev_b32 v39, 8, v39\n"
    "v_and_b32 v37, 1, v29\n"
    "v_lshl_add_u32 v39, v37, 3, v39\n"
    "v_bfe_u32 v37, v29, 1, 1\n"
    "v_lshl_add_u32 v39, v37, 4, v39\n"
    "v_lshlrev_b32 v38, 6, v38\n"
    "v_and_b32 v37, 1, v32\n"
    "v_lshl_add_u32 v38, v37, 5, v38\n"
    "v_xor_b32 v37, 0, v38\n"
    "v_add_u32 v236, v37, v39\n"
    "v_add_u32 v244, 0x10000, v236\n"
    "v_xor_b32 v37, 32, v38\n"
    "v_add_u32 v237, v37, v39\n"
    "v_add_u32 v245, 0x10000, v237\n"
    "v_xor_b32 v37, 64, v38\n"
    "v_add_u32 v238, v37, v39\n"
    "v_add_u32 v246, 0x10000, v238\n"
    "v_xor_b32 v37, 0x60, v38\n"
    "v_add_u32 v239, v37, v39\n"
    "v_add_u32 v247, 0x10000, v239\n"
    "v_xor_b32 v37, 0x80, v38\n"
    "v_add_u32 v240, v37, v39\n"
    "v_add_u32 v248, 0x10000, v240\n"
    "v_xor_b32 v37, 0xa0, v38\n"
    "v_add_u32 v241, v37, v39\n"
    "v_add_u32 v249, 0x10000, v241\n"
    "v_xor_b32 v37, 0xc0, v38\n"
    "v_add_u32 v242, v37, v39\n"
    "v_add_u32 v250, 0x10000, v242\n"
    "v_xor_b32 v37, 0xe0, v38\n"
    "v_add_u32 v243, v37, v39\n"
    "v_add_u32 v251, 0x10000, v243\n"
    "s_lshl_b32 s47, s34, 12\n"
    "v_add_u32 v10, s47, v10\n"
    "v_add_u32 v11, s47, v11\n"
    "v_add_u32 v12, s47, v12\n"
    "v_add_u32 v13, s47, v13\n"
    "v_and_b32 v33, 1, v32\n"
    "s_lshl_b32 s47, s34, 1\n"
    "v_add_u32 v33, s47, v33\n"
    "v_xor_b32 v33, v33, v29\n"
    "v_and_b32 v33, 3, v33\n"
    "v_lshlrev_b32 v33, 4, v33\n"
    "v_bfe_u32 v34, v29, 2, 3\n"
    "v_lshl_add_u32 v33, v34, 8, v33\n"
    "v_lshrrev_b32 v34, 5, v29\n"
    "v_lshl_add_u32 v33, v34, 6, v33\n"
    "s_lshl_b32 s47, s34, 11\n"
    "v_add_u32 v6, s47, v33\n"
    "v_add_u32 v7, 0x80, v6\n"
    "v_add_u32 v8, 0x2000, v6\n"
    "v_add_u32 v9, 0x2080, v6\n"
    "v_lshrrev_b32 v34, 5, v29\n"
    "v_bfe_u32 v35, v29, 2, 2\n"
    "v_and_b32 v36, 1, v32\n"
    "v_lshl_add_u32 v33, v34, 3, v35\n"
    "v_lshlrev_b32 v33, 8, v33\n"
    "v_bfe_u32 v37, v29, 1, 1\n"
    "v_lshl_add_u32 v33, v37, 3, v33\n"
    "v_and_b32 v37, 1, v29\n"
    "v_lshl_add_u32 v37, v36, 1, v37\n"
    "v_lshlrev_b32 v38, 2, v35\n"
    "v_xor_b32 v38, 0, v38\n"
    "v_add_u32 v38, v38, v37\n"
    "v_lshl_add_u32 v14, v38, 4, v33\n"
    "v_add_u32 v18, 0x10000, v14\n"
    "v_lshlrev_b32 v38, 2, v35\n"
    "v_xor_b32 v38, 4, v38\n"
    "v_add_u32 v38, v38, v37\n"
    "v_lshl_add_u32 v15, v38, 4, v33\n"
    "v_add_u32 v19, 0x10000, v15\n"
    "v_lshlrev_b32 v38, 2, v35\n"
    "v_xor_b32 v38, 8, v38\n"
    "v_add_u32 v38, v38, v37\n"
    "v_lshl_add_u32 v16, v38, 4, v33\n"
    "v_add_u32 v20, 0x10000, v16\n"
    "v_lshlrev_b32 v38, 2, v35\n"
    "v_xor_b32 v38, 12, v38\n"
    "v_add_u32 v38, v38, v37\n"
    "v_lshl_add_u32 v17, v38, 4, v33\n"
    "v_add_u32 v21, 0x10000, v17\n"
    "v_and_b32 v35, 31, v29\n"
    "v_bfe_u32 v36, v35, 2, 1\n"
    "v_bfe_u32 v37, v35, 3, 1\n"
    "v_and_b32 v33, 19, v35\n"
    "v_lshl_add_u32 v33, v36, 3, v33\n"
    "v_lshl_add_u32 v33, v37, 2, v33\n"
    "v_lshrrev_b32 v38, 3, v33\n"
    "v_lshlrev_b32 v38, 11, v38\n"
    "v_and_b32 v39, 7, v33\n"
    "v_lshl_add_u32 v38, v39, 6, v38\n"
    "v_bfe_u32 v39, v33, 2, 2\n"
    "v_add_u32 v33, 0, v34\n"
    "v_xor_b32 v33, v33, v39\n"
    "v_lshl_add_u32 v22, v33, 4, v38\n"
    "v_add_u32 v33, 2, v34\n"
    "v_xor_b32 v33, v33, v39\n"
    "v_lshl_add_u32 v23, v33, 4, v38\n"
    "v_lshrrev_b32 v33, 4, v29\n"
    "v_bfe_u32 v38, v29, 2, 2\n"
    "v_lshl_add_u32 v33, v33, 3, v38\n"
    "v_lshlrev_b32 v33, 7, v33\n"
    "v_and_b32 v38, 3, v29\n"
    "v_lshl_add_u32 v33, v38, 4, v33\n"
    "v_add_u32 v233, s82, v33\n"
    "v_lshrrev_b32 v33, 4, v35\n"
    "v_lshlrev_b32 v33, 10, v33\n"
    "v_lshl_add_u32 v33, v34, 8, v33\n"
    "v_and_b32 v38, 15, v29\n"
    "v_lshl_add_u32 v24, v38, 4, v33\n"
    "s_waitcnt lgkmcnt(0)\n"
    "s_add_u32 s40, s40, 1\n"
    "s_addc_u32 s41, s41, 0\n"
    "; ==== P0: 64 columns x 160 rows of silu(c) @ w_mod (f32 partial sums), published with flag F0\n"
    "s_waitcnt vmcnt(6)\n"
    "v_mov_b32 v143, v56\n"
    "v_lshlrev_b32 v60, 16, v60\n"
    "v_mul_f32 v124, 0xbfb8aa3b, v60\n"
    "v_exp_f32 v124, v124\n"
    "v_lshlrev_b32 v61, 16, v61\n"
    "v_mul_f32 v125, 0xbfb8aa3b, v61\n"
    "v_exp_f32 v125, v125\n"
    "v_lshlrev_b32 v62, 16, v62\n"
    "v_mul_f32 v126, 0xbfb8aa3b, v62\n"
    "v_exp_f32 v126, v126\n"
    "v_lshlrev_b32 v63, 16, v63\n"
    "v_mul_f32 v127, 0xbfb8aa3b, v63\n"
    "v_exp_f32 v127, v127\n"
    "s_waitcnt vmcnt(5)\n"
    "v_lshlrev_b32 v64, 16, v64\n"
    "v_mul_f32 v128, 0xbfb8aa3b, v64\n"
    "v_exp_f32 v128, v128\n"
    "v_add_f32 v124, 1.0, v124\n"
    "v_rcp_f32 v124, v124\n"
    "v_add_f32 v125, 1.0, v125\n"
    "v_rcp_f32 v125, v125\n"
    "v_add_f32 v126, 1.0, v126\n"
    "v_rcp_f32 v126, v126\n"
    "v_add_f32 v127, 1.0, v127\n"
    "v_rcp_f32 v127, v127\n"
    "v_add_f32 v128, 1.0, v128\n"
    "v_rcp_f32 v128, v128\n"
    "v_mul_f32 v60, v60, v124\n"
    "v_mul_f32 v61, v61, v125\n"
    "v_mul_f32 v62, v62, v126\n"
    "v_mul_f32 v63, v63, v127\n"
    "v_mul_f32 v64, v64, v128\n"
    "v_cvt_pk_bf16_f32 v129, v60, v61\n"
    "v_lshlrev_b32 v60, 16, v129\n"
    "v_and_b32 v61, 0xffff0000, v129\n"
    "v_cvt_pk_bf16_f32 v130, v62, v63\n"
    "v_lshlrev_b32 v62, 16, v130\n"
    "v_and_b32 v63, 0xffff0000, v130\n"
    "v_cvt_pk_bf16_f32 v131, v64, v64\n"
    "v_lshlrev_b32 v64, 16, v131\n"
    "v_lshlrev_b32 v132, 16, v40\n"
    "v_and_b32 v133, 0xffff0000, v40\n"
    "v_mul_f32 v66, v60, v132\n"
    "v_mul_f32 v67, v60, v133\n"
    "v_lshlrev_b32 v132, 16, v41\n"
    "v_and_b32 v133, 0xffff0000, v41\n"
    "v_mul_f32 v68, v60, v132\n"
    "v_mul_f32 v69, v60, v133\n"
    "v_lshlrev_b32 v132, 16, v42\n"
    "v_and_b32 v133, 0xffff0000, v42\n"
    "v_mul_f32 v70, v60, v132\n"
    "v_mul_f32 v71, v60, v133\n"
    "v_lshlrev_b32 v132, 16, v43\n"
    "v_and_b32 v133, 0xffff0000, v43\n"
    "v_mul_f32 v72, v60, v132\n"
    "v_mul_f32 v73, v60, v133\n"
    "v_lshlrev_b32 v132, 16, v44\n"
    "v_and_b32 v133, 0xffff0000, v44\n"
    "v_fmac_f32 v66, v61, v132\n"
    "v_fmac_f32 v67, v61, v133\n"
    "v_lshlrev_b32 v132, 16, v45\n"
    "v_and_b32 v133, 0xffff0000, v45\n"
    "v_fmac_f32 v68, v61, v132\n"
    "v_fmac_f32 v69, v61, v133\n"
    "v_lshlrev_b32 v132, 16, v46\n"
    "v_and_b32 v133, 0xffff0000, v46\n"
    "v_fmac_f32 v70, v61, v132\n"
    "v_fmac_f32 v71, v61, v133\n"
    "v_lshlrev_b32 v132, 16, v47\n"
    "v_and_b32 v133, 0xffff0000, v47\n"
    "v_fmac_f32 v72, v61, v132\n"
    "v_fmac_f32 v73, v61, v133\n"
    "v_lshlrev_b32 v132, 16, v48\n"
    "v_and_b32 v133, 0xffff0000, v48\n"
    "v_fmac_f32 v66, v62, v132\n"
    "v_fmac_f32 v67, v62, v133\n"
    "v_lshlrev_b32 v132, 16, v49\n"
    "v_and_b32 v133, 0xffff0000, v49\n"
    "v_fmac_f32 v68, v62, v132\n"
    "v_fmac_f32 v69, v62, v133\n"
    "v_lshlrev_b32 v132, 16, v50\n"
    "v_and_b32 v133, 0xffff0000, v50\n"
    "v_fmac_f32 v70, v62, v132\n"
    "v_fmac_f32 v71, v62, v133\n"
    "v_lshlrev_b32 v132, 16, v51\n"
    "v_and_b32 v133, 0xffff0000, v51\n"
    "v_fmac_f32 v72, v62, v132\n"
    "v_fmac_f32 v73, v62, v133\n"
    "v_lshlrev_b32 v132, 16, v52\n"
    "v_and_b32 v133, 0xffff0000, v52\n"
    "v_fmac_f32 v66, v63, v132\n"
    "v_fmac_f32 v67, v63, v133\n"
    "v_lshlrev_b32 v132, 16, v53\n"
    "v_and_b32 v133, 0xffff0000, v53\n"
    "v_fmac_f32 v68, v63, v132\n"
    "v_fmac_f32 v69, v63, v133\n"
    "v_lshlrev_b32 v132, 16, v54\n"
    "v_and_b32 v133, 0xffff0000, v54\n"
    "v_fmac_f32 v70, v63, v132\n"
    "v_fmac_f32 v71, v63, v133\n"
    "v_lshlrev_b32 v132, 16, v55\n"
    "v_and_b32 v133, 0xffff0000, v55\n"
    "v_fmac_f32 v72, v63, v132\n"
    "v_fmac_f32 v73, v63, v133\n"
    "v_lshlrev_b32 v132, 16, v56\n"
    "v_and_b32 v133, 0xffff0000, v56\n"
    "v_fmac_f32 v66, v64, v132\n"
    "v_fmac_f32 v67, v64, v133\n"
    "v_lshlrev_b32 v132, 16, v57\n"
    "v_and_b32 v133, 0xffff0000, v57\n"
    "v_fmac_f32 v68, v64, v132\n"
    "v_fmac_f32 v69, v64, v133\n"
    "v_lshlrev_b32 v132, 16, v58\n"
    "v_and_b32 v133, 0xffff0000, v58\n"
    "v_fmac_f32 v70, v64, v132\n"
    "v_fmac_f32 v71, v64, v133\n"
    "v_lshlrev_b32 v132, 16, v59\n"
    "v_and_b32 v133, 0xffff0000, v59\n"
    "v_fmac_f32 v72, v64, v132\n"
    "v_fmac_f32 v73, v64, v133\n"
    "v_add_f32_dpp v66, v66, v66 row_ror:8 row_mask:0xf bank_mask:0xf\n"
    "v_add_f32_dpp v67, v67, v67 row_ror:8 row_mask:0xf bank_mask:0xf\n"
    "v_add_f32_dpp v68, v68, v68 row_ror:8 row_mask:0xf bank_mask:0xf\n"
    "v_add_f32_dpp v69, v69, v69 row_ror:8 row_mask:0xf bank_mask:0xf\n"
    "v_add_f32_dpp v70, v70, v70 row_ror:8 row_mask:0xf bank_mask:0xf\n"
    "v_add_f32_dpp v71, v71, v71 row_ror:8 row_mask:0xf bank_mask:0xf\n"
    "v_add_f32_dpp v72, v72, v72 row_ror:8 row_mask:0xf bank_mask:0xf\n"
    "v_add_f32_dpp v73, v73, v73 row_ror:8 row_mask:0xf bank_mask:0xf\n"
    "v_mov_b32 v124, v66\n"
    "v_mov_b32 v125, v67\n"
    "v_mov_b32 v126, v68\n"
    "v_mov_b32 v127, v69\n"
    "v_mov_b32 v128, v70\n"
    "v_mov_b32 v129, v71\n"
    "v_mov_b32 v130, v72\n"
    "v_mov_b32 v131, v73\n"
    "v_permlane16_swap_b32 v124, v66\n"
    "v_permlane16_swap_b32 v125, v67\n"
    "v_permlane16_swap_b32 v126, v68\n"
    "v_permlane16_swap_b32 v127, v69\n"
    "v_permlane16_swap_b32 v128, v70\n"
    "v_permlane16_swap_b32 v129, v71\n"
    "v_permlane16_swap_b32 v130, v72\n"
    "v_permlane16_swap_b32 v131, v73\n"
    "v_add_f32 v66, v66, v124\n"
    "v_add_f32 v67, v67, v125\n"
    "v_add_f32 v68, v68, v126\n"
    "v_add_f32 v69, v69, v127\n"
    "v_add_f32 v70, v70, v128\n"
    "v_add_f32 v71, v71, v129\n"
    "v_add_f32 v72, v72, v130\n"
    "v_add_f32 v73, v73, v131\n"
    "v_mov_b32 v124, v66\n"
    "v_mov_b32 v125, v67\n"
    "v_mov_b32 v126, v68\n"
    "v_mov_b32 v127, v69\n"
    "v_mov_b32 v128, v70\n"
    "v_mov_b32 v129, v71\n"
    "v_mov_b32 v130, v72\n"
    "v_mov_b32 v131, v73\n"
    "v_permlane32_swap_b32 v124, v66\n"
    "v_permlane32_swap_b32 v125, v67\n"
    "v_permlane32_swap_b32 v126, v68\n"
    "v_permlane32_swap_b32 v127, v69\n"
    "v_permlane32_swap_b32 v128, v70\n"
    "v_permlane32_swap_b32 v129, v71\n"
    "v_permlane32_swap_b32 v130, v72\n"
    "v_permlane32_swap_b32 v131, v73\n"
    "v_add_f32 v66, v66, v124\n"
    "v_add_f32 v67, v67, v125\n"
    "v_add_f32 v68, v68, v126\n"
    "v_add_f32 v69, v69, v127\n"
    "v_add_f32 v70, v70, v128\n"
    "v_add_f32 v71, v71, v129\n"
    "v_add_f32 v72, v72, v130\n"
    "v_add_f32 v73, v73, v131\n"
    "v_and_b32 v132, 7, v0\n"
    "v_lshlrev_b32 v132, 5, v132\n"
    "v_add_u32 v132, 0x20400, v132\n"
    "s_lshl_b32 s89, s34, 8\n"
    "v_add_u32 v133, s89, v132\n"
    "v_mov_b32 v164, v66\n"
    "v_mov_b32 v168, v70\n"
    "v_mov_b32 v165, v67\n"
    "v_mov_b32 v169, v71\n"
    "v_mov_b32 v166, v68\n"
    "v_mov_b32 v170, v72\n"
    "v_mov_b32 v167, v69\n"
    "v_mov_b32 v171, v73\n"
    "ds_write_b128 v133, v[164:167] offset:0\n"
    "ds_write_b128 v133, v[168:171] offset:16\n"
    "s_waitcnt lgkmcnt(0)\n"
    "s_barrier\n"
    "v_and_b32 v132, 0xf0, v1\n"
    "v_add_u32 v150, 0x20400, v132\n"
    "ds_read_b128 v[40:43], v150 offset:0\n"
    "ds_read_b128 v[44:47], v150 offset:256\n"
    "ds_read_b128 v[48:51], v150 offset:512\n"
    "ds_read_b128 v[52:55], v150 offset:768\n"
    "s_waitcnt lgkmcnt(2)\n"
    "v_add_f32 v40, v40, v44\n"
    "s_waitcnt lgkmcnt(0)\n"
    "v_add_f32 v48, v48, v52\n"
    "v_add_f32 v40, v40, v48\n"
    "v_add_f32 v41, v41, v45\n"
    "v_add_f32 v49, v49, v53\n"
    "v_add_f32 v41, v41, v49\n"
    "v_add_f32 v42, v42, v46\n"
    "v_add_f32 v50, v50, v54\n"
    "v_add_f32 v42, v42, v50\n"
    "v_add_f32 v43, v43, v47\n"
    "v_add_f32 v51, v51, v55\n"
    "v_add_f32 v43, v43, v51\n"
    "s_mov_b64 exec, 0xffff\n"
    "buffer_store_dwordx4 v[40:43], v132, s[64:67], 0 offen offset:0 sc1\n"
    "s_mov_b64 exec, -1\n"
    "s_lshl_b32 s46, %1, 3\n"
    "s_add_u32 s46, s46, 0xc000\n"
    "v_mov_b32 v152, s40\n"
    "v_mov_b32 v153, s41\n"
    "v_lshlrev_b32 v154, 7, v1\n"
    "v_add_u32 v154, s46, v154\n"
    "s_waitcnt vmcnt(0)\n"
    "s_barrier\n"
    "s_mov_b64 exec, 0xff\n"
    "global_store_dwordx2 v154, v[152:153], s[26:27] offset:0 sc1\n"
    "s_mov_b64 exec, -1\n"
    "s_and_b32 s46, s88, 3\n"
    "v_bfe_u32 v38, v1, 5, 4\n"
    "s_lshl_b32 s47, s46, 4\n"
    "v_add_u32 v38, s47, v38\n"
    "v_lshlrev_b32 v234, 13, v38\n"
    "v_bfe_u32 v38, v1, 4, 1\n"
    "v_lshl_add_u32 v234, v38, 7, v234\n"
    "s_lshl_b32 s47, s46, 5\n"
    "s_lshr_b32 s48, s34, 1\n"
    "s_lshl_b32 s48, s48, 4\n"
    "s_add_u32 s47, s47, s48\n"
    "v_bfe_u32 v38, v1, 4, 4\n"
    "v_add_u32 v38, s47, v38\n"
    "v_lshlrev_b32 v235, 7, v38\n"
    "s_and_b32 s47, s34, 1\n"
    "s_cmp_eq_u32 s47, 0\n"
    "s_cselect_b64 s[42:43], s[10:11], s[12:13]\n"
    "s_lshl_b32 s47, s87, 19\n"
    "s_add_u32 s47, s47, 0x60000\n"
    "s_add_u32 s42, s42, s47\n"
    "s_addc_u32 s43, s43, 0\n"
    "s_mov_b32 s47, s34\n"
    "s_mul_i32 s47, s47, 0x80000\n"
    "v_add_u32 v39, s47, v234\n"
    "s_lshl_b32 s47, s87, 8\n"
    "s_add_u32 s50, s14, s47\n"
    "s_addc_u32 s51, s15, 0\n"
    "global_load_dword v36, v39, s[50:51]\n"
    "v_lshlrev_b32 v84, 16, v74\n"
    "v_and_b32 v85, 0xffff0000, v74\n"
    "v_lshlrev_b32 v86, 16, v75\n"
    "v_and_b32 v87, 0xffff0000, v75\n"
    "v_add_f32 v124, v84, v85\n"
    "v_add_f32 v124, v124, v86\n"
    "v_add_f32 v124, v124, v87\n"
    "v_lshlrev_b32 v88, 16, v76\n"
    "v_and_b32 v89, 0xffff0000, v76\n"
    "v_lshlrev_b32 v90, 16, v77\n"
    "v_and_b32 v91, 0xffff0000, v77\n"
    "v_add_f32 v124, v124, v88\n"
    "v_add_f32 v124, v124, v89\n"
    "v_add_f32 v124, v124, v90\n"
    "v_add_f32 v124, v124, v91\n"
    "v_lshlrev_b32 v92, 16, v78\n"
    "v_and_b32 v93, 0xffff0000, v78\n"
    "v_lshlrev_b32 v94, 16, v79\n"
    "v_and_b32 v95, 0xffff0000, v79\n"
    "v_add_f32 v124, v124, v92\n"
    "v_add_f32 v124, v124, v93\n"
    "v_add_f32 v124, v124, v94\n"
    "v_add_f32 v124, v124, v95\n"
    "v_lshlrev_b32 v96, 16, v80\n"
    "v_and_b32 v97, 0xffff0000, v80\n"
    "v_lshlrev_b32 v98, 16, v81\n"
    "v_and_b32 v99, 0xffff0000, v81\n"
    "v_add_f32 v124, v124, v96\n"
    "v_add_f32 v124, v124, v97\n"
    "v_add_f32 v124, v124, v98\n"
    "v_add_f32 v124, v124, v99\n"
    "v_lshlrev_b32 v100, 16, v82\n"
    "v_and_b32 v101, 0xffff0000, v82\n"
    "v_lshlrev_b32 v102, 16, v83\n"
    "v_and_b32 v103, 0xffff0000, v83\n"
    "v_add_f32 v124, v124, v100\n"
    "v_add_f32 v124, v124, v101\n"
    "v_add_f32 v124, v124, v102\n"
    "v_add_f32 v124, v124, v103\n"
    "s_nop 1\n"
    "v_add_f32_dpp v124, v124, v124 row_ror:8 row_mask:0xf bank_mask:0xf\n"
    "v_mov_b32 v126, v124\n"
    "s_nop 1\n"
    "v_permlane16_swap_b32 v126, v124\n"
    "v_add_f32 v124, v124, v126\n"
    "v_mov_b32 v126, v124\n"
    "s_nop 1\n"
    "v_permlane32_swap_b32 v126, v124\n"
    "v_add_f32 v124, v124, v126\n"
    "v_mul_f32 v124, 0x3bcccccd, v124\n"
    "v_sub_f32 v126, v84, v124\n"
    "v_mul_f32 v125, v126, v126\n"
    "v_sub_f32 v126, v85, v124\n"
    "v_fmac_f32 v125, v126, v126\n"
    "v_sub_f32 v126, v86, v124\n"
    "v_fmac_f32 v125, v126, v126\n"
    "v_sub_f32 v126, v87, v124\n"
    "v_fmac_f32 v125, v126, v126\n"
    "v_sub_f32 v126, v88, v124\n"
    "v_fmac_f32 v125, v126, v126\n"
    "v_sub_f32 v126, v89, v124\n"
    "v_fmac_f32 v125, v126, v126\n"
    "v_sub_f32 v126, v90, v124\n"
    "v_fmac_f32 v125, v126, v126\n"
    "v_sub_f32 v126, v91, v124\n"
    "v_fmac_f32 v125, v126, v126\n"
    "v_sub_f32 v126, v92, v124\n"
    "v_fmac_f32 v125, v126, v126\n"
    "v_sub_f32 v126, v93, v124\n"
    "v_fmac_f32 v125, v126, v126\n"
    "v_sub_f32 v126, v94, v124\n"
    "v_fmac_f32 v125, v126, v126\n"
    "v_sub_f32 v126, v95, v124\n"
    "v_fmac_f32 v125, v126, v126\n"
    "v_sub_f32 v126, v96, v124\n"
    "v_fmac_f32 v125, v126, v126\n"
    "v_sub_f32 v126, v97, v124\n"
    "v_fmac_f32 v125, v126, v126\n"
    "v_sub_f32 v126, v98, v124\n"
    "v_fmac_f32 v125, v126, v126\n"
    "v_sub_f32 v126, v99, v124\n"
    "v_fmac_f32 v125, v126, v126\n"
    "v_sub_f32 v126, v100, v124\n"
    "v_fmac_f32 v125, v126, v126\n"
    "v_sub_f32 v126, v101, v124\n"
    "v_fmac_f32 v125, v126, v126\n"
    "v_sub_f32 v126, v102, v124\n"
    "v_fmac_f32 v125, v126, v126\n"
    "v_sub_f32 v126, v103, v124\n"
    "v_fmac_f32 v125, v126, v126\n"
    "s_nop 1\n"
    "v_add_f32_dpp v125, v125, v125 row_ror:8 row_mask:0xf bank_mask:0xf\n"
    "v_mov_b32 v126, v125\n"
    "s_nop 1\n"
    "v_permlane16_swap_b32 v126, v125\n"
    "v_add_f32 v125, v125, v126\n"
    "v_mov_b32 v126, v125\n"
    "s_nop 1\n"
    "v_permlane32_swap_b32 v126, v125\n"
    "v_add_f32 v125, v125, v126\n"
    "s_lshl_b32 s89, s34, 6\n"
    "v_add_u32 v127, s89, v149\n"
    "v_mov_b32 v128, v124\n"
    "v_mov_b32 v129, v125\n"
    "ds_write_b64 v127, v[128:129] offset:0\n"
    "s_waitcnt lgkmcnt(0)\n"
    "s_barrier\n"
    "ds_read_b64 v[130:131], v149 offset:0\n"
    "ds_read_b64 v[132:133], v149 offset:64\n"
    "ds_read_b64 v[134:135], v149 offset:128\n"
    "ds_read_b64 v[136:137], v149 offset:192\n"
    "s_waitcnt lgkmcnt(2)\n"
    "v_add_f32 v124, v130, v132\n"
    "s_waitcnt lgkmcnt(0)\n"
    "v_add_f32 v126, v134, v136\n"
    "v_add_f32 v124, v124, v126\n"
    "v_mul_f32 v124, 0x3e800000, v124\n"
    "v_add_f32 v125, v131, v133\n"
    "v_add_f32 v126, v135, v137\n"
    "v_add_f32 v125, v125, v126\n"
    "v_sub_f32 v126, v130, v124\n"
    "v_mul_f32 v126, v126, v126\n"
    "v_fmac_f32 v125, 0x43200000, v126\n"
    "v_sub_f32 v126, v132, v124\n"
    "v_mul_f32 v126, v126, v126\n"
    "v_fmac_f32 v125, 0x43200000, v126\n"
    "v_sub_f32 v126, v134, v124\n"
    "v_mul_f32 v126, v126, v126\n"
    "v_fmac_f32 v125, 0x43200000, v126\n"
    "v_sub_f32 v126, v136, v124\n"
    "v_mul_f32 v126, v126, v126\n"
    "v_fmac_f32 v125, 0x43200000, v126\n"
    "v_mul_f32 v125, 0x3acccccd, v125\n"
    "v_cvt_pk_bf16_f32 v126, v124, v125\n"
    "v_lshlrev_b32 v124, 16, v126\n"
    "v_and_b32 v125, 0xffff0000, v126\n"
    "v_add_f32 v125, 0x3727c5ac, v125\n"
    "v_cvt_pk_bf16_f32 v126, v125, v125\n"
    "v_lshlrev_b32 v125, 16, v126\n"
    "v_sqrt_f32 v125, v125\n"
    "s_nop 0\n"
    "v_cvt_pk_bf16_f32 v126, v125, v125\n"
    "v_lshlrev_b32 v125, 16, v126\n"
    "v_rcp_f32 v127, v125\n"
    "s_nop 0\n"
    "v_fma_f32 v126, -v125, v127, 1.0\n"
    "v_fma_f32 v127, v127, v126, v127\n"
    "v_sub_f32 v84, v84, v124\n"
    "v_sub_f32 v85, v85, v124\n"
    "v_sub_f32 v86, v86, v124\n"
    "v_sub_f32 v87, v87, v124\n"
    "v_cvt_pk_bf16_f32 v128, v84, v85\n"
    "v_lshlrev_b32 v84, 16, v128\n"
    "v_and_b32 v85, 0xffff0000, v128\n"
    "v_cvt_pk_bf16_f32 v129, v86, v87\n"
    "v_lshlrev_b32 v86, 16, v129\n"
    "v_and_b32 v87, 0xffff0000, v129\n"
    "v_mul_f32 v84, v84, v127\n"
    "v_mul_f32 v85, v85, v127\n"
    "v_mul_f32 v86, v86, v127\n"
    "v_mul_f32 v87, v87, v127\n"
    "v_cvt_pk_bf16_f32 v128, v84, v85\n"
    "v_lshlrev_b32 v84, 16, v128\n"
    "v_and_b32 v85, 0xffff0000, v128\n"
    "v_cvt_pk_bf16_f32 v129, v86, v87\n"
    "v_lshlrev_b32 v86, 16, v129\n"
    "v_and_b32 v87, 0xffff0000, v129\n"
    "v_sub_f32 v88, v88, v124\n"
    "v_sub_f32 v89, v89, v124\n"
    "v_sub_f32 v90, v90, v124\n"
    "v_sub_f32 v91, v91, v124\n"
    "v_cvt_pk_bf16_f32 v128, v88, v89\n"
    "v_lshlrev_b32 v88, 16, v128\n"
    "v_and_b32 v89, 0xffff0000, v128\n"
    "v_cvt_pk_bf16_f32 v129, v90, v91\n"
    "v_lshlrev_b32 v90, 16, v129\n"
    "v_and_b32 v91, 0xffff0000, v129\n"
    "v_mul_f32 v88, v88, v127\n"
    "v_mul_f32 v89, v89, v127\n"
    "v_mul_f32 v90, v90, v127\n"
    "v_mul_f32 v91, v91, v127\n"
    "v_cvt_pk_bf16_f32 v128, v88, v89\n"
    "v_lshlrev_b32 v88, 16, v128\n"
    "v_and_b32 v89, 0xffff0000, v128\n"
    "v_cvt_pk_bf16_f32 v129, v90, v91\n"
    "v_lshlrev_b32 v90, 16, v129\n"
    "v_and_b32 v91, 0xffff0000, v129\n"
    "v_sub_f32 v92, v92, v124\n"
    "v_sub_f32 v93, v93, v124\n"
    "v_sub_f32 v94, v94, v124\n"
    "v_sub_f32 v95, v95, v124\n"
    "v_cvt_pk_bf16_f32 v128, v92, v93\n"
    "v_lshlrev_b32 v92, 16, v128\n"
    "v_and_b32 v93, 0xffff0000, v128\n"
    "v_cvt_pk_bf16_f32 v129, v94, v95\n"
    "v_lshlrev_b32 v94, 16, v129\n"
    "v_and_b32 v95, 0xffff0000, v129\n"
    "v_mul_f32 v92, v92, v127\n"
    "v_mul_f32 v93, v93, v127\n"
    "v_mul_f32 v94, v94, v127\n"
    "v_mul_f32 v95, v95, v127\n"
    "v_cvt_pk_bf16_f32 v128, v92, v93\n"
    "v_lshlrev_b32 v92, 16, v128\n"
    "v_and_b32 v93, 0xffff0000, v128\n"
    "v_cvt_pk_bf16_f32 v129, v94, v95\n"
    "v_lshlrev_b32 v94, 16, v129\n"
    "v_and_b32 v95, 0xffff0000, v129\n"
    "v_sub_f32 v96, v96, v124\n"
    "v_sub_f32 v97, v97, v124\n"
    "v_sub_f32 v98, v98, v124\n"
    "v_sub_f32 v99, v99, v124\n"
    "v_cvt_pk_bf16_f32 v128, v96, v97\n"
    "v_lshlrev_b32 v96, 16, v128\n"
    "v_and_b32 v97, 0xffff0000, v128\n"
    "v_cvt_pk_bf16_f32 v129, v98, v99\n"
    "v_lshlrev_b32 v98, 16, v129\n"
    "v_and_b32 v99, 0xffff0000, v129\n"
    "v_mul_f32 v96, v96, v127\n"
    "v_mul_f32 v97, v97, v127\n"
    "v_mul_f32 v98, v98, v127\n"
    "v_mul_f32 v99, v99, v127\n"
    "v_cvt_pk_bf16_f32 v128, v96, v97\n"
    "v_lshlrev_b32 v96, 16, v128\n"
    "v_and_b32 v97, 0xffff0000, v128\n"
    "v_cvt_pk_bf16_f32 v129, v98, v99\n"
    "v_lshlrev_b32 v98, 16, v129\n"
    "v_and_b32 v99, 0xffff0000, v129\n"
    "v_sub_f32 v100, v100, v124\n"
    "v_sub_f32 v101, v101, v124\n"
    "v_sub_f32 v102, v102, v124\n"
    "v_sub_f32 v103, v103, v124\n"
    "v_cvt_pk_bf16_f32 v128, v100, v101\n"
    "v_lshlrev_b32 v100, 16, v128\n"
    "v_and_b32 v101, 0xffff0000, v128\n"
    "v_cvt_pk_bf16_f32 v129, v102, v103\n"
    "v_lshlrev_b32 v102, 16, v129\n"
    "v_and_b32 v103, 0xffff0000, v129\n"
    "v_mul_f32 v100, v100, v127\n"
    "v_mul_f32 v101, v101, v127\n"
    "v_mul_f32 v102, v102, v127\n"
    "v_mul_f32 v103, v103, v127\n"
    "v_cvt_pk_bf16_f32 v128, v100, v101\n"
    "v_lshlrev_b32 v100, 16, v128\n"
    "v_and_b32 v101, 0xffff0000, v128\n"
    "v_cvt_pk_bf16_f32 v129, v102, v103\n"
    "v_lshlrev_b32 v102, 16, v129\n"
    "v_and_b32 v103, 0xffff0000, v129\n"
    "v_bfe_u32 v134, v1, 4, 4\n"
    "v_lshrrev_b32 v135, 8, v1\n"
    "v_mul_u32_u24 v184, 0x3c00, v135\n"
    "v_lshl_add_u32 v184, v134, 4, v184\n"
    "v_cmp_lt_u32 vcc, 7, v134\n"
    "v_mov_b32 v136, 0\n"
    "v_mov_b32 v137, 0x780\n"
    "v_cndmask_b32 v136, v136, v137, vcc\n"
    "v_add_u32 v185, v184, v136\n"
    "v_mov_b32 v136, 0\n"
    "v_mov_b32 v137, 1.0\n"
    "v_cndmask_b32 v188, v136, v137, vcc\n"
    "s_lshl_b32 s89, s34, 11\n"
    "s_add_u32 s89, s89, 0x21000\n"
    "v_lshlrev_b32 v186, 4, v134\n"
    "v_add_u32 v186, s89, v186\n"
    "v_add_u32 v187, s89, v147\n"
    "s_and_b32 s49, %1, 7\n"
    "s_lshl_b32 s49, s49, 11\n"
    "s_add_u32 s92, s49, 0xc000\n"
    "s_add_u32 s93, s49, 0xc200\n"
    "s_add_u32 s94, s49, 0xc400\n"
    "s_add_u32 s95, s49, 0xc600\n"
    "s_cmp_eq_u32 s34, 0\n"
    "s_cbranch_scc0 Lpollskip3_%=\n"
    "v_lshrrev_b32 v154, 4, v1\n"
    "v_lshlrev_b32 v154, 3, v154\n"
    "v_add_u32 v154, s92, v154\n"
    "v_lshrrev_b32 v158, 4, v1\n"
    "v_lshlrev_b32 v158, 3, v158\n"
    "v_add_u32 v158, s93, v158\n"
    "v_lshrrev_b32 v162, 4, v1\n"
    "v_lshlrev_b32 v162, 3, v162\n"
    "v_add_u32 v162, s94, v162\n"
    "v_lshrrev_b32 v166, 4, v1\n"
    "v_min_u32 v166, 47, v166\n"
    "v_lshlrev_b32 v166, 3, v166\n"
    "v_add_u32 v166, s95, v166\n"
    "s_mov_b32 s44, 0\n"
    "Lpoll1_%=:\n"
    "global_load_dwordx2 v[152:153], v154, s[26:27] sc1\n"
    "global_load_dwordx2 v[156:157], v158, s[26:27] sc1\n"
    "global_load_dwordx2 v[160:161], v162, s[26:27] sc1\n"
    "global_load_dwordx2 v[164:165], v166, s[26:27] sc1\n"
    "s_waitcnt vmcnt(0)\n"
    "v_cmp_ne_u64 vcc, s[40:41], v[152:153]\n"
    "s_nop 4\n"
    "s_mov_b64 s[50:51], vcc\n"
    "v_cmp_ne_u64 vcc, s[40:41], v[156:157]\n"
    "s_nop 4\n"
    "s_or_b64 s[50:51], s[50:51], vcc\n"
    "v_cmp_ne_u64 vcc, s[40:41], v[160:161]\n"
    "s_nop 4\n"
    "s_or_b64 s[50:51], s[50:51], vcc\n"
    "v_cmp_ne_u64 vcc, s[40:41], v[164:165]\n"
    "s_nop 4\n"
    "s_or_b64 s[50:51], s[50:51], vcc\n"
    "s_cmp_eq_u64 s[50:51], 0\n"
    "s_cbranch_scc1 Lpollok2_%=\n"
    "s_add_u32 s44, s44, 1\n"
    "s_cmp_ge_u32 s44, 32768\n"
    "s_cbranch_scc1 Lpollok2_%=\n"
    "s_branch Lpoll1_%=\n"
    "Lpollok2_%=:\n"
    "Lpollskip3_%=:\n"
    "s_barrier\n"
    "; ==== P1: h1 = LN(x) * (1 + ca) + sa for 8 rows (B fragments), published with flag FH\n"
    "s_mov_b32 s89, 0\n"
    "buffer_load_dwordx4 v[164:167], v184, s[72:75], s89 offen offset:0 sc1\n"
    "s_mov_b32 s89, 0x100\n"
    "buffer_load_dwordx4 v[168:171], v184, s[72:75], s89 offen offset:0 sc1\n"
    "s_mov_b32 s89, 0x200\n"
    "buffer_load_dwordx4 v[172:175], v185, s[72:75], s89 offen offset:0 sc1\n"
    "s_mov_b32 s89, 0xa80\n"
    "buffer_load_dwordx4 v[176:179], v184, s[72:75], s89 offen offset:0 sc1\n"
    "s_mov_b32 s89, 0xb80\n"
    "buffer_load_dwordx4 v[180:183], v184, s[72:75], s89 offen offset:0 sc1\n"
    "s_waitcnt vmcnt(4)\n"
    "v_mov_b32 v192, v164\n"
    "v_mov_b32 v193, v165\n"
    "v_mov_b32 v194, v166\n"
    "v_mov_b32 v195, v167\n"
    "v_permlane16_swap_b32 v192, v164\n"
    "v_permlane16_swap_b32 v193, v165\n"
    "v_permlane16_swap_b32 v194, v166\n"
    "v_permlane16_swap_b32 v195, v167\n"
    "v_add_f32 v164, v164, v192\n"
    "v_add_f32 v165, v165, v193\n"
    "v_add_f32 v166, v166, v194\n"
    "v_add_f32 v167, v167, v195\n"
    "v_mov_b32 v192, v164\n"
    "v_mov_b32 v193, v165\n"
    "v_mov_b32 v194, v166\n"
    "v_mov_b32 v195, v167\n"
    "v_permlane32_swap_b32 v192, v164\n"
    "v_permlane32_swap_b32 v193, v165\n"
    "v_permlane32_swap_b32 v194, v166\n"
    "v_permlane32_swap_b32 v195, v167\n"
    "v_add_f32 v164, v164, v192\n"
    "v_add_f32 v165, v165, v193\n"
    "v_add_f32 v166, v166, v194\n"
    "v_add_f32 v167, v167, v195\n"
    "v_cvt_pk_bf16_f32 v130, v164, v165\n"
    "v_lshlrev_b32 v164, 16, v130\n"
    "v_and_b32 v165, 0xffff0000, v130\n"
    "v_cvt_pk_bf16_f32 v131, v166, v167\n"
    "v_lshlrev_b32 v166, 16, v131\n"
    "v_and_b32 v167, 0xffff0000, v131\n"
    "ds_write_b128 v186, v[164:167] offset:0\n"
    "s_waitcnt vmcnt(3)\n"
    "v_mov_b32 v192, v168\n"
    "v_mov_b32 v193, v169\n"
    "v_mov_b32 v194, v170\n"
    "v_mov_b32 v195, v171\n"
    "v_permlane16_swap_b32 v192, v168\n"
    "v_permlane16_swap_b32 v193, v169\n"
    "v_permlane16_swap_b32 v194, v170\n"
    "v_permlane16_swap_b32 v195, v171\n"
    "v_add_f32 v168, v168, v192\n"
    "v_add_f32 v169, v169, v193\n"
    "v_add_f32 v170, v170, v194\n"
    "v_add_f32 v171, v171, v195\n"
    "v_mov_b32 v192, v168\n"
    "v_mov_b32 v193, v169\n"
    "v_mov_b32 v194, v170\n"
    "v_mov_b32 v195, v171\n"
    "v_permlane32_swap_b32 v192, v168\n"
    "v_permlane32_swap_b32 v193, v169\n"
    "v_permlane32_swap_b32 v194, v170\n"
    "v_permlane32_swap_b32 v195, v171\n"
    "v_add_f32 v168, v168, v192\n"
    "v_add_f32 v169, v169, v193\n"
    "v_add_f32 v170, v170, v194\n"
    "v_add_f32 v171, v171, v195\n"
    "v_cvt_pk_bf16_f32 v130, v168, v169\n"
    "v_lshlrev_b32 v168, 16, v130\n"
    "v_and_b32 v169, 0xffff0000, v130\n"
    "v_cvt_pk_bf16_f32 v131, v170, v171\n"
    "v_lshlrev_b32 v170, 16, v131\n"
    "v_and_b32 v171, 0xffff0000, v131\n"
    "ds_write_b128 v186, v[168:171] offset:256\n"
    "s_waitcnt vmcnt(2)\n"
    "v_mov_b32 v192, v172\n"
    "v_mov_b32 v193, v173\n"
    "v_mov_b32 v194, v174\n"
    "v_mov_b32 v195, v175\n"
    "v_permlane16_swap_b32 v192, v172\n"
    "v_permlane16_swap_b32 v193, v173\n"
    "v_permlane16_swap_b32 v194, v174\n"
    "v_permlane16_swap_b32 v195, v175\n"
    "v_add_f32 v172, v172, v192\n"
    "v_add_f32 v173, v173, v193\n"
    "v_add_f32 v174, v174, v194\n"
    "v_add_f32 v175, v175, v195\n"
    "v_mov_b32 v192, v172\n"
    "v_mov_b32 v193, v173\n"
    "v_mov_b32 v194, v174\n"
    "v_mov_b32 v195, v175\n"
    "v_permlane32_swap_b32 v192, v172\n"
    "v_permlane32_swap_b32 v193, v173\n"
    "v_permlane32_swap_b32 v194, v174\n"
    "v_permlane32_swap_b32 v195, v175\n"
    "v_add_f32 v172, v172, v192\n"
    "v_add_f32 v173, v173, v193\n"
    "v_add_f32 v174, v174, v194\n"
    "v_add_f32 v175, v175, v195\n"
    "v_cvt_pk_bf16_f32 v130, v172, v173\n"
    "v_lshlrev_b32 v172, 16, v130\n"
    "v_and_b32 v173, 0xffff0000, v130\n"
    "v_cvt_pk_bf16_f32 v131, v174, v175\n"
    "v_lshlrev_b32 v174, 16, v131\n"
    "v_and_b32 v175, 0xffff0000, v131\n"
    "v_add_f32 v172, v188, v172\n"
    "v_add_f32 v173, v188, v173\n"
    "v_add_f32 v174, v188, v174\n"
    "v_add_f32 v175, v188, v175\n"
    "v_cvt_pk_bf16_f32 v130, v172, v173\n"
    "v_lshlrev_b32 v172, 16, v130\n"
    "v_and_b32 v173, 0xffff0000, v130\n"
    "v_cvt_pk_bf16_f32 v131, v174, v175\n"
    "v_lshlrev_b32 v174, 16, v131\n"
    "v_and_b32 v175, 0xffff0000, v131\n"
    "ds_write_b128 v186, v[172:175] offset:512\n"
    "s_waitcnt vmcnt(1)\n"
    "v_mov_b32 v192, v176\n"
    "v_mov_b32 v193, v177\n"
    "v_mov_b32 v194, v178\n"
    "v_mov_b32 v195, v179\n"
    "v_permlane16_swap_b32 v192, v176\n"
    "v_permlane16_swap_b32 v193, v177\n"
    "v_permlane16_swap_b32 v194, v178\n"
    "v_permlane16_swap_b32 v195, v179\n"
    "v_add_f32 v176, v176, v192\n"
    "v_add_f32 v177, v177, v193\n"
    "v_add_f32 v178, v178, v194\n"
    "v_add_f32 v179, v179, v195\n"
    "v_mov_b32 v192, v176\n"
    "v_mov_b32 v193, v177\n"
    "v_mov_b32 v194, v178\n"
    "v_mov_b32 v195, v179\n"
    "v_permlane32_swap_b32 v192, v176\n"
    "v_permlane32_swap_b32 v193, v177\n"
    "v_permlane32_swap_b32 v194, v178\n"
    "v_permlane32_swap_b32 v195, v179\n"
    "v_add_f32 v176, v176, v192\n"
    "v_add_f32 v177, v177, v193\n"
    "v_add_f32 v178, v178, v194\n"
    "v_add_f32 v179, v179, v195\n"
    "v_cvt_pk_bf16_f32 v130, v176, v177\n"
    "v_lshlrev_b32 v176, 16, v130\n"
    "v_and_b32 v177, 0xffff0000, v130\n"
    "v_cvt_pk_bf16_f32 v131, v178, v179\n"
    "v_lshlrev_b32 v178, 16, v131\n"
    "v_and_b32 v179, 0xffff0000, v131\n"
    "v_add_f32 v176, 1.0, v176\n"
    "v_add_f32 v177, 1.0, v177\n"
    "v_add_f32 v178, 1.0, v178\n"
    "v_add_f32 v179, 1.0, v179\n"
    "v_cvt_pk_bf16_f32 v130, v176, v177\n"
    "v_lshlrev_b32 v176, 16, v130\n"
    "v_and_b32 v177, 0xffff0000, v130\n"
    "v_cvt_pk_bf16_f32 v131, v178, v179\n"
    "v_lshlrev_b32 v178, 16, v131\n"
    "v_and_b32 v179, 0xffff0000, v131\n"
    "ds_write_b128 v186, v[176:179] offset:768\n"
    "s_waitcnt vmcnt(0)\n"
    "v_mov_b32 v192, v180\n"
    "v_mov_b32 v193, v181\n"
    "v_mov_b32 v194, v182\n"
    "v_mov_b32 v195, v183\n"
    "v_permlane16_swap_b32 v192, v180\n"
    "v_permlane16_swap_b32 v193, v181\n"
    "v_permlane16_swap_b32 v194, v182\n"
    "v_permlane16_swap_b32 v195, v183\n"
    "v_add_f32 v180, v180, v192\n"
    "v_add_f32 v181, v181, v193\n"
    "v_add_f32 v182, v182, v194\n"
    "v_add_f32 v183, v183, v195\n"
    "v_mov_b32 v192, v180\n"
    "v_mov_b32 v193, v181\n"
    "v_mov_b32 v194, v182\n"
    "v_mov_b32 v195, v183\n"
    "v_permlane32_swap_b32 v192, v180\n"
    "v_permlane32_swap_b32 v193, v181\n"
    "v_permlane32_swap_b32 v194, v182\n"
    "v_permlane32_swap_b32 v195, v183\n"
    "v_add_f32 v180, v180, v192\n"
    "v_add_f32 v181, v181, v193\n"
    "v_add_f32 v182, v182, v194\n"
    "v_add_f32 v183, v183, v195\n"
    "v_cvt_pk_bf16_f32 v130, v180, v181\n"
    "v_lshlrev_b32 v180, 16, v130\n"
    "v_and_b32 v181, 0xffff0000, v130\n"
    "v_cvt_pk_bf16_f32 v131, v182, v183\n"
    "v_lshlrev_b32 v182, 16, v131\n"
    "v_and_b32 v183, 0xffff0000, v131\n"
    "v_add_f32 v180, 1.0, v180\n"
    "v_add_f32 v181, 1.0, v181\n"
    "v_add_f32 v182, 1.0, v182\n"
    "v_add_f32 v183, 1.0, v183\n"
    "v_cvt_pk_bf16_f32 v130, v180, v181\n"
    "v_lshlrev_b32 v180, 16, v130\n"
    "v_and_b32 v181, 0xffff0000, v130\n"
    "v_cvt_pk_bf16_f32 v131, v182, v183\n"
    "v_lshlrev_b32 v182, 16, v131\n"
    "v_and_b32 v183, 0xffff0000, v131\n"
    "ds_write_b128 v186, v[180:183] offset:1024\n"
    "s_waitcnt lgkmcnt(0)\n"
    "ds_read_b128 v[192:195], v187 offset:0\n"
    "ds_read_b128 v[196:199], v187 offset:640\n"
    "ds_read_b128 v[200:203], v187 offset:128\n"
    "ds_read_b128 v[204:207], v187 offset:768\n"
    "ds_read_b128 v[208:211], v187 offset:256\n"
    "ds_read_b128 v[212:215], v187 offset:896\n"
    "ds_read_b128 v[216:219], v187 offset:384\n"
    "ds_read_b128 v[220:223], v187 offset:1024\n"
    "ds_read_b128 v[224:227], v187 offset:512\n"
    "ds_read_b128 v[228:231], v187 offset:1152\n"
    "s_waitcnt lgkmcnt(8)\n"
    "v_mul_f32 v84, v84, v196\n"
    "v_mul_f32 v85, v85, v197\n"
    "v_mul_f32 v86, v86, v198\n"
    "v_mul_f32 v87, v87, v199\n"
    "v_cvt_pk_bf16_f32 v128, v84, v85\n"
    "v_lshlrev_b32 v84, 16, v128\n"
    "v_and_b32 v85, 0xffff0000, v128\n"
    "v_cvt_pk_bf16_f32 v129, v86, v87\n"
    "v_lshlrev_b32 v86, 16, v129\n"
    "v_and_b32 v87, 0xffff0000, v129\n"
    "v_add_f32 v84, v84, v192\n"
    "v_add_f32 v85, v85, v193\n"
    "v_add_f32 v86, v86, v194\n"
    "v_add_f32 v87, v87, v195\n"
    "v_cvt_pk_bf16_f32 v74, v84, v85\n"
    "v_cvt_pk_bf16_f32 v75, v86, v87\n"
    "s_waitcnt lgkmcnt(6)\n"
    "v_mul_f32 v88, v88, v204\n"
    "v_mul_f32 v89, v89, v205\n"
    "v_mul_f32 v90, v90, v206\n"
    "v_mul_f32 v91, v91, v207\n"
    "v_cvt_pk_bf16_f32 v128, v88, v89\n"
    "v_lshlrev_b32 v88, 16, v128\n"
    "v_and_b32 v89, 0xffff0000, v128\n"
    "v_cvt_pk_bf16_f32 v129, v90, v91\n"
    "v_lshlrev_b32 v90, 16, v129\n"
    "v_and_b32 v91, 0xffff0000, v129\n"
    "v_add_f32 v88, v88, v200\n"
    "v_add_f32 v89, v89, v201\n"
    "v_add_f32 v90, v90, v202\n"
    "v_add_f32 v91, v91, v203\n"
    "v_cvt_pk_bf16_f32 v76, v88, v89\n"
    "v_cvt_pk_bf16_f32 v77, v90, v91\n"
    "s_mov_b32 s90, 0\n"
    "v_permlane32_swap_b32 v74, v76\n"
    "v_permlane32_swap_b32 v75, v77\n"
    "buffer_store_dwordx4 v[74:77], v151, s[76:79], s90 offen offset:0 sc1\n"
    "s_waitcnt lgkmcnt(4)\n"
    "v_mul_f32 v92, v92, v212\n"
    "v_mul_f32 v93, v93, v213\n"
    "v_mul_f32 v94, v94, v214\n"
    "v_mul_f32 v95, v95, v215\n"
    "v_cvt_pk_bf16_f32 v128, v92, v93\n"
    "v_lshlrev_b32 v92, 16, v128\n"
    "v_and_b32 v93, 0xffff0000, v128\n"
    "v_cvt_pk_bf16_f32 v129, v94, v95\n"
    "v_lshlrev_b32 v94, 16, v129\n"
    "v_and_b32 v95, 0xffff0000, v129\n"
    "v_add_f32 v92, v92, v208\n"
    "v_add_f32 v93, v93, v209\n"
    "v_add_f32 v94, v94, v210\n"
    "v_add_f32 v95, v95, v211\n"
    "v_cvt_pk_bf16_f32 v78, v92, v93\n"
    "v_cvt_pk_bf16_f32 v79, v94, v95\n"
    "s_waitcnt lgkmcnt(2)\n"
    "v_mul_f32 v96, v96, v220\n"
    "v_mul_f32 v97, v97, v221\n"
    "v_mul_f32 v98, v98, v222\n"
    "v_mul_f32 v99, v99, v223\n"
    "v_cvt_pk_bf16_f32 v128, v96, v97\n"
    "v_lshlrev_b32 v96, 16, v128\n"
    "v_and_b32 v97, 0xffff0000, v128\n"
    "v_cvt_pk_bf16_f32 v129, v98, v99\n"
    "v_lshlrev_b32 v98, 16, v129\n"
    "v_and_b32 v99, 0xffff0000, v129\n"
    "v_add_f32 v96, v96, v216\n"
    "v_add_f32 v97, v97, v217\n"
    "v_add_f32 v98, v98, v218\n"
    "v_add_f32 v99, v99, v219\n"
    "v_cvt_pk_bf16_f32 v80, v96, v97\n"
    "v_cvt_pk_bf16_f32 v81, v98, v99\n"
    "s_mov_b32 s90, 0x1000\n"
    "v_permlane32_swap_b32 v78, v80\n"
    "v_permlane32_swap_b32 v79, v81\n"
    "buffer_store_dwordx4 v[78:81], v151, s[76:79], s90 offen offset:0 sc1\n"
    "s_waitcnt lgkmcnt(0)\n"
    "v_mul_f32 v100, v100, v228\n"
    "v_mul_f32 v101, v101, v229\n"
    "v_mul_f32 v102, v102, v230\n"
    "v_mul_f32 v103, v103, v231\n"
    "v_cvt_pk_bf16_f32 v128, v100, v101\n"
    "v_lshlrev_b32 v100, 16, v128\n"
    "v_and_b32 v101, 0xffff0000, v128\n"
    "v_cvt_pk_bf16_f32 v129, v102, v103\n"
    "v_lshlrev_b32 v102, 16, v129\n"
    "v_and_b32 v103, 0xffff0000, v129\n"
    "v_add_f32 v100, v100, v224\n"
    "v_add_f32 v101, v101, v225\n"
    "v_add_f32 v102, v102, v226\n"
    "v_add_f32 v103, v103, v227\n"
    "v_cvt_pk_bf16_f32 v82, v100, v101\n"
    "v_cvt_pk_bf16_f32 v83, v102, v103\n"
    "s_mov_b32 s90, 0x2000\n"
    "buffer_store_dwordx2 v[82:83], v148, s[76:79], s90 offen offset:0 sc1\n"
    "s_lshl_b32 s46, s88, 8\n"
    "s_lshl_b32 s47, s87, 3\n"
    "s_add_u32 s46, s46, s47\n"
    "s_add_u32 s46, s46, 0x7000\n"
    "v_mov_b32 v152, s40\n"
    "v_mov_b32 v153, s41\n"
    "v_mov_b32 v154, s46\n"
    "s_waitcnt vmcnt(0)\n"
    "s_barrier\n"
    "s_mov_b64 exec, 1\n"
    "global_store_dwordx2 v154, v[152:153], s[26:27] offset:0 sc1\n"
    "s_mov_b64 exec, -1\n"
    "s_mov_b32 s83, 0\n"
    "s_add_u32 m0, s80, 0\n"
    "s_nop 0\n"
    "buffer_load_dwordx4 v2, s[52:55], s83 offen lds\n"
    "s_add_u32 m0, s80, 0x400\n"
    "s_nop 0\n"
    "buffer_load_dwordx4 v3, s[52:55], s83 offen lds\n"
    "s_add_u32 m0, s80, 0x800\n"
    "s_nop 0\n"
    "buffer_load_dwordx4 v4, s[52:55], s83 offen lds\n"
    "s_add_u32 m0, s80, 0xc00\n"
    "s_nop 0\n"
    "buffer_load_dwordx4 v5, s[52:55], s83 offen lds\n"
    "s_mov_b32 s83, 0x80000\n"
    "s_add_u32 m0, s80, 0x4000\n"
    "s_nop 0\n"
    "buffer_load_dwordx4 v2, s[52:55], s83 offen lds\n"
    "s_add_u32 m0, s80, 0x4400\n"
    "s_nop 0\n"
    "buffer_load_dwordx4 v3, s[52:55], s83 offen lds\n"
    "s_add_u32 m0, s80, 0x4800\n"
    "s_nop 0\n"
    "buffer_load_dwordx4 v4, s[52:55], s83 offen lds\n"
    "s_add_u32 m0, s80, 0x4c00\n"
    "s_nop 0\n"
    "buffer_load_dwordx4 v5, s[52:55], s83 offen lds\n"
    "s_add_u32 s47, s34, 4\n"
    "s_mul_i32 s47, s47, 0x80000\n"
    "v_add_u32 v39, s47, v234\n"
    "s_lshl_b32 s47, s87, 8\n"
    "s_add_u32 s50, s14, s47\n"
    "s_addc_u32 s51, s15, 0\n"
    "global_load_dword v36, v39, s[50:51]\n"
    "s_and_b32 s47, s34, 1\n"
    "s_add_u32 s47, s47, 8\n"
    "s_mul_i32 s47, s47, 0x80000\n"
    "v_add_u32 v39, s47, v234\n"
    "s_lshl_b32 s47, s87, 8\n"
    "s_add_u32 s50, s14, s47\n"
    "s_addc_u32 s51, s15, 0\n"
    "global_load_dword v36, v39, s[50:51]\n"
    "s_cmp_lt_u32 %1, 40\n"
    "s_cselect_b32 s90, 0x100, 0\n"
    "s_cselect_b32 s91, 0x80, 0\n"
    "s_lshl_b32 s46, %1, 8\n"
    "s_add_u32 s46, s46, 0x13400\n"
    "s_add_u32 s90, s90, 0xb400\n"
    "s_add_u32 s56, s26, s46\n"
    "s_addc_u32 s57, s27, 0\n"
    "s_mov_b32 s58, s90\n"
    "s_mov_b32 s59, 0x20000\n"
    "s_lshl_b32 s46, %1, 7\n"
    "s_add_u32 s46, s46, 0x10a00\n"
    "s_add_u32 s64, s26, s46\n"
    "s_addc_u32 s65, s27, 0\n"
    "s_mov_b32 s66, s91\n"
    "s_mov_b32 s67, 0x20000\n"
    "v_and_b32 v132, 0xf0, v1\n"
    "s_mov_b32 s89, 0\n"
    "buffer_load_dwordx4 v[40:43], v132, s[56:59], s89 offen offset:0 sc1\n"
    "s_mov_b32 s89, 0x3c00\n"
    "buffer_load_dwordx4 v[44:47], v132, s[56:59], s89 offen offset:0 sc1\n"
    "s_mov_b32 s89, 0x7800\n"
    "buffer_load_dwordx4 v[48:51], v132, s[56:59], s89 offen offset:0 sc1\n"
    "s_mov_b32 s89, 0xb400\n"
    "buffer_load_dwordx4 v[52:55], v132, s[56:59], s89 offen offset:0 sc1\n"
    "s_waitcnt vmcnt(2)\n"
    "v_add_f32 v56, v40, v44\n"
    "s_waitcnt vmcnt(0)\n"
    "v_add_f32 v133, v48, v52\n"
    "v_add_f32 v56, v56, v133\n"
    "v_add_f32 v57, v41, v45\n"
    "v_add_f32 v133, v49, v53\n"
    "v_add_f32 v57, v57, v133\n"
    "v_add_f32 v58, v42, v46\n"
    "v_add_f32 v133, v50, v54\n"
    "v_add_f32 v58, v58, v133\n"
    "v_add_f32 v59, v43, v47\n"
    "v_add_f32 v133, v51, v55\n"
    "v_add_f32 v59, v59, v133\n"
    "v_cvt_pk_bf16_f32 v133, v56, v57\n"
    "v_lshlrev_b32 v56, 16, v133\n"
    "v_and_b32 v57, 0xffff0000, v133\n"
    "v_cvt_pk_bf16_f32 v133, v58, v59\n"
    "v_lshlrev_b32 v58, 16, v133\n"
    "v_and_b32 v59, 0xffff0000, v133\n"
    "s_sub_u32 s89, %1, 20\n"
    "s_cmp_lt_u32 s89, 10\n"
    "s_cselect_b32 s90, 1.0, 0\n"
    "v_add_f32 v56, s90, v56\n"
    "v_add_f32 v57, s90, v57\n"
    "v_add_f32 v58, s90, v58\n"
    "v_add_f32 v59, s90, v59\n"
    "v_cvt_pk_bf16_f32 v60, v56, v57\n"
    "v_cvt_pk_bf16_f32 v61, v58, v59\n"
    "v_lshrrev_b32 v132, 1, v132\n"
    "s_mov_b64 exec, 0xffff\n"
    "buffer_store_dwordx2 v[60:61], v132, s[64:67], 0 offen offset:0 sc1\n"
    "s_mov_b64 exec, -1\n"
    "s_lshl_b32 s46, s87, 19\n"
    "s_add_u32 s46, s46, 0x60000\n"
    "s_add_u32 s56, s10, s46\n"
    "s_addc_u32 s57, s11, 0\n"
    "s_mov_b32 s58, 0x20000\n"
    "s_mov_b32 s59, 0x20000\n"
    "s_add_u32 s60, s12, s46\n"
    "s_addc_u32 s61, s13, 0\n"
    "s_mov_b32 s62, 0x20000\n"
    "s_mov_b32 s63, 0x20000\n"
    "s_lshl_b32 s46, s88, 2\n"
    "s_add_u32 s46, s46, s34\n"
    "s_mul_i32 s47, s46, 0x14000\n"
    "s_add_u32 s47, s47, 0x30000\n"
    "s_add_u32 s64, s26, s47\n"
    "s_addc_u32 s65, s27, 0\n"
    "s_mov_b32 s66, 0x14000\n"
    "s_mov_b32 s67, 0x20000\n"
    "s_lshl_b32 s47, s46, 7\n"
    "s_lshl_b32 s48, s87, 2\n"
    "s_add_u32 s47, s47, s48\n"
    "s_lshl_b32 s47, s47, 12\n"
    "s_add_u32 s47, s47, 0x2b0000\n"
    "s_add_u32 s68, s26, s47\n"
    "s_addc_u32 s69, s27, 0\n"
    "s_mov_b32 s70, 0x4000\n"
    "s_mov_b32 s71, 0x20000\n"
    "s_lshl_b32 s48, s88, 8\n"
    "s_add_u32 s48, s48, 0x7000\n"
    "s_cmp_eq_u32 s34, 0\n"
    "s_cbranch_scc0 Lpollskip6_%=\n"
    "v_lshrrev_b32 v154, 4, v1\n"
    "v_min_u32 v154, 31, v154\n"
    "v_lshlrev_b32 v154, 3, v154\n"
    "v_add_u32 v154, s48, v154\n"
    "s_mov_b32 s44, 0\n"
    "Lpoll4_%=:\n"
    "global_load_dwordx2 v[152:153], v154, s[26:27] sc1\n"
    "s_waitcnt vmcnt(0)\n"
    "v_cmp_ne_u64 vcc, s[40:41], v[152:153]\n"
    "s_nop 4\n"
    "s_mov_b64 s[50:51], vcc\n"
    "s_cmp_eq_u64 s[50:51], 0\n"
    "s_cbranch_scc1 Lpollok5_%=\n"
    "s_add_u32 s44, s44, 1\n"
    "s_cmp_ge_u32 s44, 32768\n"
    "s_cbranch_scc1 Lpollok5_%=\n"
    "s_branch Lpoll4_%=\n"
    "Lpollok5_%=:\n"
    "Lpollskip6_%=:\n"
    "s_barrier\n"
    "; ==== P2a: Q projection of 256 rows x one head (16x16x32 MFMA, wq through LDS)\n"
    "s_mov_b32 s84, 0\n"
    "s_mov_b32 s85, 0xa000\n"
    "buffer_load_dwordx4 a[0:3], v1, s[64:67], s84 offen offset:0 sc1\n"
    "buffer_load_dwordx4 a[8:11], v1, s[64:67], s84 offen offset:1024 sc1\n"
    "buffer_load_dwordx4 a[16:19], v1, s[64:67], s85 offen offset:0 sc1\n"
    "buffer_load_dwordx4 a[24:27], v1, s[64:67], s85 offen offset:1024 sc1\n"
    "buffer_load_dwordx4 a[4:7], v1, s[64:67], s84 offen offset:2048 sc1\n"
    "buffer_load_dwordx4 a[12:15], v1, s[64:67], s84 offen offset:3072 sc1\n"
    "buffer_load_dwordx4 a[20:23], v1, s[64:67], s85 offen offset:2048 sc1\n"
    "buffer_load_dwordx4 a[28:31], v1, s[64:67], s85 offen offset:3072 sc1\n"
    "s_mov_b32 s83, 0x100000\n"
    "s_add_u32 m0, s80, 0x8000\n"
    "s_nop 0\n"
    "buffer_load_dwordx4 v2, s[52:55], s83 offen lds\n"
    "s_add_u32 m0, s80, 0x8400\n"
    "s_nop 0\n"
    "buffer_load_dwordx4 v3, s[52:55], s83 offen lds\n"
    "s_add_u32 m0, s80, 0x8800\n"
    "s_nop 0\n"
    "buffer_load_dwordx4 v4, s[52:55], s83 offen lds\n"
    "s_add_u32 m0, s80, 0x8c00\n"
    "s_nop 0\n"
    "buffer_load_dwordx4 v5, s[52:55], s83 offen lds\n"
    "s_mov_b32 s84, 0x1000\n"
    "s_mov_b32 s85, 0xb000\n"
    "buffer_load_dwordx4 a[32:35], v1, s[64:67], s84 offen offset:0 sc1\n"
    "buffer_load_dwordx4 a[40:43], v1, s[64:67], s84 offen offset:1024 sc1\n"
    "buffer_load_dwordx4 a[48:51], v1, s[64:67], s85 offen offset:0 sc1\n"
    "buffer_load_dwordx4 a[56:59], v1, s[64:67], s85 offen offset:1024 sc1\n"
    "buffer_load_dwordx4 a[36:39], v1, s[64:67], s84 offen offset:2048 sc1\n"
    "buffer_load_dwordx4 a[44:47], v1, s[64:67], s84 offen offset:3072 sc1\n"
    "buffer_load_dwordx4 a[52:55], v1, s[64:67], s85 offen offset:2048 sc1\n"
    "buffer_load_dwordx4 a[60:63], v1, s[64:67], s85 offen offset:3072 sc1\n"
    "s_mov_b32 s83, 0x180000\n"
    "s_add_u32 m0, s80, 0xc000\n"
    "s_nop 0\n"
    "buffer_load_dwordx4 v2, s[52:55], s83 offen lds\n"
    "s_add_u32 m0, s80, 0xc400\n"
    "s_nop 0\n"
    "buffer_load_dwordx4 v3, s[52:55], s83 offen lds\n"
    "s_add_u32 m0, s80, 0xc800\n"
    "s_nop 0\n"
    "buffer_load_dwordx4 v4, s[52:55], s83 offen lds\n"
    "s_add_u32 m0, s80, 0xcc00\n"
    "s_nop 0\n"
    "buffer_load_dwordx4 v5, s[52:55], s83 offen lds\n"
    "s_barrier\n"
    "ds_read_b64_tr_b16 a[128:129], v236 offset:0\n"
    "ds_read_b64_tr_b16 a[130:131], v236 offset:1024\n"
    "ds_read_b64_tr_b16 a[132:133], v237 offset:0\n"
    "ds_read_b64_tr_b16 a[134:135], v237 offset:1024\n"
    "ds_read_b64_tr_b16 a[136:137], v238 offset:0\n"
    "ds_read_b64_tr_b16 a[138:139], v238 offset:1024\n"
    "ds_read_b64_tr_b16 a[140:141], v239 offset:0\n"
    "ds_read_b64_tr_b16 a[142:143], v239 offset:1024\n"
    "ds_read_b64_tr_b16 a[144:145], v240 offset:0\n"
    "ds_read_b64_tr_b16 a[146:147], v240 offset:1024\n"
    "ds_read_b64_tr_b16 a[148:149], v241 offset:0\n"
    "ds_read_b64_tr_b16 a[150:151], v241 offset:1024\n"
    "ds_read_b64_tr_b16 a[152:153], v242 offset:0\n"
    "ds_read_b64_tr_b16 a[154:155], v242 offset:1024\n"
    "ds_read_b64_tr_b16 a[156:157], v243 offset:0\n"
    "ds_read_b64_tr_b16 a[158:159], v243 offset:1024\n"
    "s_waitcnt vmcnt(23) lgkmcnt(14)\n"
    "v_mfma_f32_16x16x32_bf16 v[104:107], a[128:131], a[0:3], 0\n"
    "ds_read_b64_tr_b16 a[160:161], v236 offset:8192\n"
    "s_waitcnt vmcnt(22)\n"
    "v_mfma_f32_16x16x32_bf16 v[136:139], a[128:131], a[8:11], 0\n"
    "ds_read_b64_tr_b16 a[162:163], v236 offset:9216\n"
    "s_mov_b32 s84, 0x2000\n"
    "s_mov_b32 s85, 0xc000\n"
    "buffer_load_dwordx4 a[64:67], v1, s[64:67], s84 offen offset:0 sc1\n"
    "s_waitcnt vmcnt(22)\n"
    "v_mfma_f32_16x16x32_bf16 v[168:171], a[128:131], a[16:19], 0\n"
    "s_waitcnt vmcnt(21)\n"
    "v_mfma_f32_16x16x32_bf16 v[200:203], a[128:131], a[24:27], 0\n"
    "ds_read_b64_tr_b16 a[164:165], v237 offset:8192\n"
    "s_waitcnt lgkmcnt(15)\n"
    "v_mfma_f32_16x16x32_bf16 v[108:111], a[132:135], a[0:3], 0\n"
    "v_mfma_f32_16x16x32_bf16 v[140:143], a[132:135], a[8:11], 0\n"
    "ds_read_b64_tr_b16 a[166:167], v237 offset:9216\n"
    "buffer_load_dwordx4 a[72:75], v1, s[64:67], s84 offen offset:1024 sc1\n"
    "v_mfma_f32_16x16x32_bf16 v[172:175], a[132:135], a[16:19], 0\n"
    "v_mfma_f32_16x16x32_bf16 v[204:207], a[132:135], a[24:27], 0\n"
    "ds_read_b64_tr_b16 a[168:169], v238 offset:8192\n"
    "s_waitcnt lgkmcnt(15)\n"
    "v_mfma_f32_16x16x32_bf16 v[112:115], a[136:139], a[0:3], 0\n"
    "v_mfma_f32_16x16x32_bf16 v[144:147], a[136:139], a[8:11], 0\n"
    "ds_read_b64_tr_b16 a[170:171], v238 offset:9216\n"
    "buffer_load_dwordx4 a[80:83], v1, s[64:67], s85 offen offset:0 sc1\n"
    "v_mfma_f32_16x16x32_bf16 v[176:179], a[136:139], a[16:19], 0\n"
    "v_mfma_f32_16x16x32_bf16 v[208:211], a[136:139], a[24:27], 0\n"
    "ds_read_b64_tr_b16 a[172:173], v239 offset:8192\n"
    "s_waitcnt lgkmcnt(15)\n"
    "v_mfma_f32_16x16x32_bf16 v[116:119], a[140:143], a[0:3], 0\n"
    "v_mfma_f32_16x16x32_bf16 v[148:151], a[140:143], a[8:11], 0\n"
    "ds_read_b64_tr_b16 a[174:175], v239 offset:9216\n"
    "buffer_load_dwordx4 a[88:91], v1, s[64:67], s85 offen offset:1024 sc1\n"
    "v_mfma_f32_16x16x32_bf16 v[180:183], a[140:143], a[16:19], 0\n"
    "v_mfma_f32_16x16x32_bf16 v[212:215], a[140:143], a[24:27], 0\n"
    "ds_read_b64_tr_b16 a[176:177], v240 offset:8192\n"
    "s_waitcnt lgkmcnt(15)\n"
    "v_mfma_f32_16x16x32_bf16 v[120:123], a[144:147], a[0:3], 0\n"
    "ds_read_b64_tr_b16 a[178:179], v240 offset:9216\n"
    "v_mfma_f32_16x16x32_bf16 v[152:155], a[144:147], a[8:11], 0\n"
    "buffer_load_dwordx4 a[68:71], v1, s[64:67], s84 offen offset:2048 sc1\n"
    "v_mfma_f32_16x16x32_bf16 v[184:187], a[144:147], a[16:19], 0\n"
    "ds_read_b64_tr_b16 a[180:181], v241 offset:8192\n"
    "v_mfma_f32_16x16x32_bf16 v[216:219], a[144:147], a[24:27], 0\n"
    "s_waitcnt lgkmcnt(15)\n"
    "v_mfma_f32_16x16x32_bf16 v[124:127], a[148:151], a[0:3], 0\n"
    "ds_read_b64_tr_b16 a[182:183], v241 offset:9216\n"
    "v_mfma_f32_16x16x32_bf16 v[156:159], a[148:151], a[8:11], 0\n"
    "buffer_load_dwordx4 a[76:79], v1, s[64:67], s84 offen offset:3072 sc1\n"
    "v_mfma_f32_16x16x32_bf16 v[188:191], a[148:151], a[16:19], 0\n"
    "ds_read_b64_tr_b16 a[184:185], v242 offset:8192\n"
    "v_mfma_f32_16x16x32_bf16 v[220:223], a[148:151], a[24:27], 0\n"
    "s_waitcnt lgkmcnt(15)\n"
    "v_mfma_f32_16x16x32_bf16 v[128:131], a[152:155], a[0:3], 0\n"
    "ds_read_b64_tr_b16 a[186:187], v242 offset:9216\n"
    "v_mfma_f32_16x16x32_bf16 v[160:163], a[152:155], a[8:11], 0\n"
    "buffer_load_dwordx4 a[84:87], v1, s[64:67], s85 offen offset:2048 sc1\n"
    "v_mfma_f32_16x16x32_bf16 v[192:195], a[152:155], a[16:19], 0\n"
    "ds_read_b64_tr_b16 a[188:189], v243 offset:8192\n"
    "v_mfma_f32_16x16x32_bf16 v[224:227], a[152:155], a[24:27], 0\n"
    "s_waitcnt lgkmcnt(15)\n"
    "v_mfma_f32_16x16x32_bf16 v[132:135], a[156:159], a[0:3], 0\n"
    "ds_read_b64_tr_b16 a[190:191], v243 offset:9216\n"
    "v_mfma_f32_16x16x32_bf16 v[164:167], a[156:159], a[8:11], 0\n"
    "buffer_load_dwordx4 a[92:95], v1, s[64:67], s85 offen offset:3072 sc1\n"
    "v_mfma_f32_16x16x32_bf16 v[196:199], a[156:159], a[16:19], 0\n"
    "v_mfma_f32_16x16x32_bf16 v[228:231], a[156:159], a[24:27], 0\n"
    "s_waitcnt vmcnt(27) lgkmcnt(14)\n"
    "v_mfma_f32_16x16x32_bf16 v[104:107], a[160:163], a[4:7], v[104:107]\n"
    "s_waitcnt vmcnt(26)\n"
    "v_mfma_f32_16x16x32_bf16 v[136:139], a[160:163], a[12:15], v[136:139]\n"
    "s_barrier\n"
    "s_waitcnt vmcnt(25)\n"
    "v_mfma_f32_16x16x32_bf16 v[168:171], a[160:163], a[20:23], v[168:171]\n"
    "ds_read_b64_tr_b16 a[128:129], v236 offset:16384\n"
    "s_waitcnt vmcnt(24)\n"
    "v_mfma_f32_16x16x32_bf16 v[200:203], a[160:163], a[28:31], v[200:203]\n"
    "ds_read_b64_tr_b16 a[130:131], v236 offset:17408\n"
    "s_waitcnt lgkmcnt(14)\n"
    "v_mfma_f32_16x16x32_bf16 v[108:111], a[164:167], a[4:7], v[108:111]\n"
    "v_mfma_f32_16x16x32_bf16 v[140:143], a[164:167], a[12:15], v[140:143]\n"
    "s_mov_b32 s83, 0x200000\n"
    "s_add_u32 m0, s80, 0x10000\n"
    "ds_read_b64_tr_b16 a[132:133], v237 offset:16384\n"
    "buffer_load_dwordx4 v2, s[52:55], s83 offen lds\n"
    "v_mfma_f32_16x16x32_bf16 v[172:175], a[164:167], a[20:23], v[172:175]\n"
    "v_mfma_f32_16x16x32_bf16 v[204:207], a[164:167], a[28:31], v[204:207]\n"
    "ds_read_b64_tr_b16 a[134:135], v237 offset:17408\n"
    "s_waitcnt lgkmcnt(14)\n"
    "v_mfma_f32_16x16x32_bf16 v[112:115], a[168:171], a[4:7], v[112:115]\n"
    "v_mfma_f32_16x16x32_bf16 v[144:147], a[168:171], a[12:15], v[144:147]\n"
    "ds_read_b64_tr_b16 a[136:137], v238 offset:16384\n"
    "v_mfma_f32_16x16x32_bf16 v[176:179], a[168:171], a[20:23], v[176:179]\n"
    "ds_read_b64_tr_b16 a[138:139], v238 offset:17408\n"
    "v_mfma_f32_16x16x32_bf16 v[208:211], a[168:171], a[28:31], v[208:211]\n"
    "s_add_u32 m0, s80, 0x10400\n"
    "s_nop 0\n"
    "buffer_load_dwordx4 v3, s[52:55], s83 offen lds\n"
    "s_waitcnt lgkmcnt(14)\n"
    "v_mfma_f32_16x16x32_bf16 v[116:119], a[172:175], a[4:7], v[116:119]\n"
    "ds_read_b64_tr_b16 a[140:141], v239 offset:16384\n"
    "v_mfma_f32_16x16x32_bf16 v[148:151], a[172:175], a[12:15], v[148:151]\n"
    "v_mfma_f32_16x16x32_bf16 v[180:183], a[172:175], a[20:23], v[180:183]\n"
    "ds_read_b64_tr_b16 a[142:143], v239 offset:17408\n"
    "v_mfma_f32_16x16x32_bf16 v[212:215], a[172:175], a[28:31], v[212:215]\n"
    "s_waitcnt lgkmcnt(14)\n"
    "v_mfma_f32_16x16x32_bf16 v[120:123], a[176:179], a[4:7], v[120:123]\n"
    "ds_read_b64_tr_b16 a[144:145], v240 offset:16384\n"
    "v_mfma_f32_16x16x32_bf16 v[152:155], a[176:179], a[12:15], v[152:155]\n"
    "s_add_u32 m0, s80, 0x10800\n"
    "ds_read_b64_tr_b16 a[146:147], v240 offset:17408\n"
    "buffer_load_dwordx4 v4, s[52:55], s83 offen lds\n"
    "v_mfma_f32_16x16x32_bf16 v[184:187], a[176:179], a[20:23], v[184:187]\n"
    "v_mfma_f32_16x16x32_bf16 v[216:219], a[176:179], a[28:31], v[216:219]\n"
    "ds_read_b64_tr_b16 a[148:149], v241 offset:16384\n"
    "s_waitcnt lgkmcnt(15)\n"
    "v_mfma_f32_16x16x32_bf16 v[124:127], a[180:183], a[4:7], v[124:127]\n"
    "v_mfma_f32_16x16x32_bf16 v[156:159], a[180:183], a[12:15], v[156:159]\n"
    "ds_read_b64_tr_b16 a[150:151], v241 offset:17408\n"
    "v_mfma_f32_16x16x32_bf16 v[188:191], a[180:183], a[20:23], v[188:191]\n"
    "v_mfma_f32_16x16x32_bf16 v[220:223], a[180:183], a[28:31], v[220:223]\n"
    "s_add_u32 m0, s80, 0x10c00\n"
    "ds_read_b64_tr_b16 a[152:153], v242 offset:16384\n"
    "buffer_load_dwordx4 v5, s[52:55], s83 offen lds\n"
    "s_waitcnt lgkmcnt(15)\n"
    "v_mfma_f32_16x16x32_bf16 v[128:131], a[184:187], a[4:7], v[128:131]\n"
    "ds_read_b64_tr_b16 a[154:155], v242 offset:17408\n"
    "v_mfma_f32_16x16x32_bf16 v[160:163], a[184:187], a[12:15], v[160:163]\n"
    "v_mfma_f32_16x16x32_bf16 v[192:195], a[184:187], a[20:23], v[192:195]\n"
    "ds_read_b64_tr_b16 a[156:157], v243 offset:16384\n"
    "v_mfma_f32_16x16x32_bf16 v[224:227], a[184:187], a[28:31], v[224:227]\n"
    "s_waitcnt lgkmcnt(15)\n"
    "v_mfma_f32_16x16x32_bf16 v[132:135], a[188:191], a[4:7], v[132:135]\n"
    "ds_read_b64_tr_b16 a[158:159], v243 offset:17408\n"
    "v_mfma_f32_16x16x32_bf16 v[164:167], a[188:191], a[12:15], v[164:167]\n"
    "v_mfma_f32_16x16x32_bf16 v[196:199], a[188:191], a[20:23], v[196:199]\n"
    "v_mfma_f32_16x16x32_bf16 v[228:231], a[188:191], a[28:31], v[228:231]\n"
    "s_waitcnt vmcnt(23) lgkmcnt(14)\n"
    "v_mfma_f32_16x16x32_bf16 v[104:107], a[128:131], a[32:35], v[104:107]\n"
    "ds_read_b64_tr_b16 a[160:161], v236 offset:24576\n"
    "s_waitcnt vmcnt(22)\n"
    "v_mfma_f32_16x16x32_bf16 v[136:139], a[128:131], a[40:43], v[136:139]\n"
    "ds_read_b64_tr_b16 a[162:163], v236 offset:25600\n"
    "s_mov_b32 s84, 0x3000\n"
    "s_mov_b32 s85, 0xd000\n"
    "buffer_load_dwordx4 a[96:99], v1, s[64:67], s84 offen offset:0 sc1\n"
    "s_waitcnt vmcnt(22)\n"
    "v_mfma_f32_16x16x32_bf16 v[168:171], a[128:131], a[48:51], v[168:171]\n"
    "s_waitcnt vmcnt(21)\n"
    "v_mfma_f32_16x16x32_bf16 v[200:203], a[128:131], a[56:59], v[200:203]\n"
    "ds_read_b64_tr_b16 a[164:165], v237 offset:24576\n"
    "s_waitcnt lgkmcnt(15)\n"
    "v_mfma_f32_16x16x32_bf16 v[108:111], a[132:135], a[32:35], v[108:111]\n"
    "v_mfma_f32_16x16x32_bf16 v[140:143], a[132:135], a[40:43], v[140:143]\n"
    "ds_read_b64_tr_b16 a[166:167], v237 offset:25600\n"
    "buffer_load_dwordx4 a[104:107], v1, s[64:67], s84 offen offset:1024 sc1\n"
    "v_mfma_f32_16x16x32_bf16 v[172:175], a[132:135], a[48:51], v[172:175]\n"
    "v_mfma_f32_16x16x32_bf16 v[204:207], a[132:135], a[56:59], v[204:207]\n"
    "ds_read_b64_tr_b16 a[168:169], v238 offset:24576\n"
    "s_waitcnt lgkmcnt(15)\n"
    "v_mfma_f32_16x16x32_bf16 v[112:115], a[136:139], a[32:35], v[112:115]\n"
    "v_mfma_f32_16x16x32_bf16 v[144:147], a[136:139], a[40:43], v[144:147]\n"
    "ds_read_b64_tr_b16 a[170:171], v238 offset:25600\n"
    "buffer_load_dwordx4 a[112:115], v1, s[64:67], s85 offen offset:0 sc1\n"
    "v_mfma_f32_16x16x32_bf16 v[176:179], a[136:139], a[48:51], v[176:179]\n"
    "v_mfma_f32_16x16x32_bf16 v[208:211], a[136:139], a[56:59], v[208:211]\n"
    "ds_read_b64_tr_b16 a[172:173], v239 offset:24576\n"
    "s_waitcnt lgkmcnt(15)\n"
    "v_mfma_f32_16x16x32_bf16 v[116:119], a[140:143], a[32:35], v[116:119]\n"
    "v_mfma_f32_16x16x32_bf16 v[148:151], a[140:143], a[40:43], v[148:151]\n"
    "ds_read_b64_tr_b16 a[174:175], v239 offset:25600\n"
    "buffer_load_dwordx4 a[120:123], v1, s[64:67], s85 offen offset:1024 sc1\n"
    "v_mfma_f32_16x16x32_bf16 v[180:183], a[140:143], a[48:51], v[180:183]\n"
    "v_mfma_f32_16x16x32_bf16 v[212:215], a[140:143], a[56:59], v[212:215]\n"
    "ds_read_b64_tr_b16 a[176:177], v240 offset:24576\n"
    "s_waitcnt lgkmcnt(15)\n"
    "v_mfma_f32_16x16x32_bf16 v[120:123], a[144:147], a[32:35], v[120:123]\n"
    "ds_read_b64_tr_b16 a[178:179], v240 offset:25600\n"
    "v_mfma_f32_16x16x32_bf16 v[152:155], a[144:147], a[40:43], v[152:155]\n"
    "buffer_load_dwordx4 a[100:103], v1, s[64:67], s84 offen offset:2048 sc1\n"
    "v_mfma_f32_16x16x32_bf16 v[184:187], a[144:147], a[48:51], v[184:187]\n"
    "ds_read_b64_tr_b16 a[180:181], v241 offset:24576\n"
    "v_mfma_f32_16x16x32_bf16 v[216:219], a[144:147], a[56:59], v[216:219]\n"
    "s_waitcnt lgkmcnt(15)\n"
    "v_mfma_f32_16x16x32_bf16 v[124:127], a[148:151], a[32:35], v[124:127]\n"
    "ds_read_b64_tr_b16 a[182:183], v241 offset:25600\n"
    "v_mfma_f32_16x16x32_bf16 v[156:159], a[148:151], a[40:43], v[156:159]\n"
    "buffer_load_dwordx4 a[108:111], v1, s[64:67], s84 offen offset:3072 sc1\n"
    "v_mfma_f32_16x16x32_bf16 v[188:191], a[148:151], a[48:51], v[188:191]\n"
    "ds_read_b64_tr_b16 a[184:185], v242 offset:24576\n"
    "v_mfma_f32_16x16x32_bf16 v[220:223], a[148:151], a[56:59], v[220:223]\n"
    "s_waitcnt lgkmcnt(15)\n"
    "v_mfma_f32_16x16x32_bf16 v[128:131], a[152:155], a[32:35], v[128:131]\n"
    "ds_read_b64_tr_b16 a[186:187], v242 offset:25600\n"
    "v_mfma_f32_16x16x32_bf16 v[160:163], a[152:155], a[40:43], v[160:163]\n"
    "buffer_load_dwordx4 a[116:119], v1, s[64:67], s85 offen offset:2048 sc1\n"
    "v_mfma_f32_16x16x32_bf16 v[192:195], a[152:155], a[48:51], v[192:195]\n"
    "ds_read_b64_tr_b16 a[188:189], v243 offset:24576\n"
    "v_mfma_f32_16x16x32_bf16 v[224:227], a[152:155], a[56:59], v[224:227]\n"
    "s_waitcnt lgkmcnt(15)\n"
    "v_mfma_f32_16x16x32_bf16 v[132:135], a[156:159], a[32:35], v[132:135]\n"
    "ds_read_b64_tr_b16 a[190:191], v243 offset:25600\n"
    "v_mfma_f32_16x16x32_bf16 v[164:167], a[156:159], a[40:43], v[164:167]\n"
    "buffer_load_dwordx4 a[124:127], v1, s[64:67], s85 offen offset:3072 sc1\n"
    "v_mfma_f32_16x16x32_bf16 v[196:199], a[156:159], a[48:51], v[196:199]\n"
    "v_add_u32 v39, 0, v235\n"
    "global_load_dword v36, v39, s[42:43]\n"
    "v_mfma_f32_16x16x32_bf16 v[228:231], a[156:159], a[56:59], v[228:231]\n"
    "s_waitcnt vmcnt(28) lgkmcnt(14)\n"
    "v_mfma_f32_16x16x32_bf16 v[104:107], a[160:163], a[36:39], v[104:107]\n"
    "s_waitcnt vmcnt(27)\n"
    "v_mfma_f32_16x16x32_bf16 v[136:139], a[160:163], a[44:47], v[136:139]\n"
    "s_barrier\n"
    "s_waitcnt vmcnt(26)\n"
    "v_mfma_f32_16x16x32_bf16 v[168:171], a[160:163], a[52:55], v[168:171]\n"
    "ds_read_b64_tr_b16 a[128:129], v236 offset:32768\n"
    "s_waitcnt vmcnt(25)\n"
    "v_mfma_f32_16x16x32_bf16 v[200:203], a[160:163], a[60:63], v[200:203]\n"
    "ds_read_b64_tr_b16 a[130:131], v236 offset:33792\n"
    "s_waitcnt lgkmcnt(14)\n"
    "v_mfma_f32_16x16x32_bf16 v[108:111], a[164:167], a[36:39], v[108:111]\n"
    "v_mfma_f32_16x16x32_bf16 v[140:143], a[164:167], a[44:47], v[140:143]\n"
    "s_mov_b32 s83, 0x280000\n"
    "s_add_u32 m0, s80, 0x14000\n"
    "ds_read_b64_tr_b16 a[132:133], v237 offset:32768\n"
    "buffer_load_dwordx4 v2, s[52:55], s83 offen lds\n"
    "v_mfma_f32_16x16x32_bf16 v[172:175], a[164:167], a[52:55], v[172:175]\n"
    "v_mfma_f32_16x16x32_bf16 v[204:207], a[164:167], a[60:63], v[204:207]\n"
    "ds_read_b64_tr_b16 a[134:135], v237 offset:33792\n"
    "s_waitcnt lgkmcnt(14)\n"
    "v_mfma_f32_16x16x32_bf16 v[112:115], a[168:171], a[36:39], v[112:115]\n"
    "v_mfma_f32_16x16x32_bf16 v[144:147], a[168:171], a[44:47], v[144:147]\n"
    "ds_read_b64_tr_b16 a[136:137], v238 offset:32768\n"
    "v_mfma_f32_16x16x32_bf16 v[176:179], a[168:171], a[52:55], v[176:179]\n"
    "ds_read_b64_tr_b16 a[138:139], v238 offset:33792\n"
    "v_mfma_f32_16x16x32_bf16 v[208:211], a[168:171], a[60:63], v[208:211]\n"
    "s_add_u32 m0, s80, 0x14400\n"
    "s_nop 0\n"
    "buffer_load_dwordx4 v3, s[52:55], s83 offen lds\n"
    "s_waitcnt lgkmcnt(14)\n"
    "v_mfma_f32_16x16x32_bf16 v[116:119], a[172:175], a[36:39], v[116:119]\n"
    "ds_read_b64_tr_b16 a[140:141], v239 offset:32768\n"
    "v_mfma_f32_16x16x32_bf16 v[148:151], a[172:175], a[44:47], v[148:151]\n"
    "v_mfma_f32_16x16x32_bf16 v[180:183], a[172:175], a[52:55], v[180:183]\n"
    "ds_read_b64_tr_b16 a[142:143], v239 offset:33792\n"
    "v_mfma_f32_16x16x32_bf16 v[212:215], a[172:175], a[60:63], v[212:215]\n"
    "s_waitcnt lgkmcnt(14)\n"
    "v_mfma_f32_16x16x32_bf16 v[120:123], a[176:179], a[36:39], v[120:123]\n"
    "ds_read_b64_tr_b16 a[144:145], v240 offset:32768\n"
    "v_mfma_f32_16x16x32_bf16 v[152:155], a[176:179], a[44:47], v[152:155]\n"
    "s_add_u32 m0, s80, 0x14800\n"
    "ds_read_b64_tr_b16 a[146:147], v240 offset:33792\n"
    "buffer_load_dwordx4 v4, s[52:55], s83 offen lds\n"
    "v_mfma_f32_16x16x32_bf16 v[184:187], a[176:179], a[52:55], v[184:187]\n"
    "v_mfma_f32_16x16x32_bf16 v[216:219], a[176:179], a[60:63], v[216:219]\n"
    "ds_read_b64_tr_b16 a[148:149], v241 offset:32768\n"
    "s_waitcnt lgkmcnt(15)\n"
    "v_mfma_f32_16x16x32_bf16 v[124:127], a[180:183], a[36:39], v[124:127]\n"
    "v_mfma_f32_16x16x32_bf16 v[156:159], a[180:183], a[44:47], v[156:159]\n"
    "ds_read_b64_tr_b16 a[150:151], v241 offset:33792\n"
    "v_mfma_f32_16x16x32_bf16 v[188:191], a[180:183], a[52:55], v[188:191]\n"
    "v_mfma_f32_16x16x32_bf16 v[220:223], a[180:183], a[60:63], v[220:223]\n"
    "s_add_u32 m0, s80, 0x14c00\n"
    "ds_read_b64_tr_b16 a[152:153], v242 offset:32768\n"
    "buffer_load_dwordx4 v5, s[52:55], s83 offen lds\n"
    "s_waitcnt lgkmcnt(15)\n"
    "v_mfma_f32_16x16x32_bf16 v[128:131], a[184:187], a[36:39], v[128:131]\n"
    "ds_read_b64_tr_b16 a[154:155], v242 offset:33792\n"
    "v_mfma_f32_16x16x32_bf16 v[160:163], a[184:187], a[44:47], v[160:163]\n"
    "v_mfma_f32_16x16x32_bf16 v[192:195], a[184:187], a[52:55], v[192:195]\n"
    "ds_read_b64_tr_b16 a[156:157], v243 offset:32768\n"
    "v_mfma_f32_16x16x32_bf16 v[224:227], a[184:187], a[60:63], v[224:227]\n"
    "s_waitcnt lgkmcnt(15)\n"
    "v_mfma_f32_16x16x32_bf16 v[132:135], a[188:191], a[36:39], v[132:135]\n"
    "ds_read_b64_tr_b16 a[158:159], v243 offset:33792\n"
    "v_mfma_f32_16x16x32_bf16 v[164:167], a[188:191], a[44:47], v[164:167]\n"
    "v_mfma_f32_16x16x32_bf16 v[196:199], a[188:191], a[52:55], v[196:199]\n"
    "v_mfma_f32_16x16x32_bf16 v[228:231], a[188:191], a[60:63], v[228:231]\n"
    "s_waitcnt vmcnt(24) lgkmcnt(14)\n"
    "v_mfma_f32_16x16x32_bf16 v[104:107], a[128:131], a[64:67], v[104:107]\n"
    "ds_read_b64_tr_b16 a[160:161], v236 offset:40960\n"
    "s_waitcnt vmcnt(23)\n"
    "v_mfma_f32_16x16x32_bf16 v[136:139], a[128:131], a[72:75], v[136:139]\n"
    "ds_read_b64_tr_b16 a[162:163], v236 offset:41984\n"
    "s_mov_b32 s84, 0x4000\n"
    "s_mov_b32 s85, 0xe000\n"
    "buffer_load_dwordx4 a[0:3], v1, s[64:67], s84 offen offset:0 sc1\n"
    "s_waitcnt vmcnt(23)\n"
    "v_mfma_f32_16x16x32_bf16 v[168:171], a[128:131], a[80:83], v[168:171]\n"
    "s_waitcnt vmcnt(22)\n"
    "v_mfma_f32_16x16x32_bf16 v[200:203], a[128:131], a[88:91], v[200:203]\n"
    "ds_read_b64_tr_b16 a[164:165], v237 offset:40960\n"
    "s_waitcnt lgkmcnt(15)\n"
    "v_mfma_f32_16x16x32_bf16 v[108:111], a[132:135], a[64:67], v[108:111]\n"
    "v_mfma_f32_16x16x32_bf16 v[140:143], a[132:135], a[72:75], v[140:143]\n"
    "ds_read_b64_tr_b16 a[166:167], v237 offset:41984\n"
    "buffer_load_dwordx4 a[8:11], v1, s[64:67], s84 offen offset:1024 sc1\n"
    "v_mfma_f32_16x16x32_bf16 v[172:175], a[132:135], a[80:83], v[172:175]\n"
    "v_mfma_f32_16x16x32_bf16 v[204:207], a[132:135], a[88:91], v[204:207]\n"
    "ds_read_b64_tr_b16 a[168:169], v238 offset:40960\n"
    "s_waitcnt lgkmcnt(15)\n"
    "v_mfma_f32_16x16x32_bf16 v[112:115], a[136:139], a[64:67], v[112:115]\n"
    "v_mfma_f32_16x16x32_bf16 v[144:147], a[136:139], a[72:75], v[144:147]\n"
    "ds_read_b64_tr_b16 a[170:171], v238 offset:41984\n"
    "buffer_load_dwordx4 a[16:19], v1, s[64:67], s85 offen offset:0 sc1\n"
    "v_mfma_f32_16x16x32_bf16 v[176:179], a[136:139], a[80:83], v[176:179]\n"
    "v_mfma_f32_16x16x32_bf16 v[208:211], a[136:139], a[88:91], v[208:211]\n"
    "ds_read_b64_tr_b16 a[172:173], v239 offset:40960\n"
    "s_waitcnt lgkmcnt(15)\n"
    "v_mfma_f32_16x16x32_bf16 v[116:119], a[140:143], a[64:67], v[116:119]\n"
    "v_mfma_f32_16x16x32_bf16 v[148:151], a[140:143], a[72:75], v[148:151]\n"
    "ds_read_b64_tr_b16 a[174:175], v239 offset:41984\n"
    "buffer_load_dwordx4 a[24:27], v1, s[64:67], s85 offen offset:1024 sc1\n"
    "v_mfma_f32_16x16x32_bf16 v[180:183], a[140:143], a[80:83], v[180:183]\n"
    "v_mfma_f32_16x16x32_bf16 v[212:215], a[140:143], a[88:91], v[212:215]\n"
    "ds_read_b64_tr_b16 a[176:177], v240 offset:40960\n"
    "s_waitcnt lgkmcnt(15)\n"
    "v_mfma_f32_16x16x32_bf16 v[120:123], a[144:147], a[64:67], v[120:123]\n"
    "ds_read_b64_tr_b16 a[178:179], v240 offset:41984\n"
    "v_mfma_f32_16x16x32_bf16 v[152:155], a[144:147], a[72:75], v[152:155]\n"
    "buffer_load_dwordx4 a[4:7], v1, s[64:67], s84 offen offset:2048 sc1\n"
    "v_mfma_f32_16x16x32_bf16 v[184:187], a[144:147], a[80:83], v[184:187]\n"
    "ds_read_b64_tr_b16 a[180:181], v241 offset:40960\n"
    "v_mfma_f32_16x16x32_bf16 v[216:219], a[144:147], a[88:91], v[216:219]\n"
    "s_waitcnt lgkmcnt(15)\n"
    "v_mfma_f32_16x16x32_bf16 v[124:127], a[148:151], a[64:67], v[124:127]\n"
    "ds_read_b64_tr_b16 a[182:183], v241 offset:41984\n"
    "v_mfma_f32_16x16x32_bf16 v[156:159], a[148:151], a[72:75], v[156:159]\n"
    "buffer_load_dwordx4 a[12:15], v1, s[64:67], s84 offen offset:3072 sc1\n"
    "v_mfma_f32_16x16x32_bf16 v[188:191], a[148:151], a[80:83], v[188:191]\n"
    "ds_read_b64_tr_b16 a[184:185], v242 offset:40960\n"
    "v_mfma_f32_16x16x32_bf16 v[220:223], a[148:151], a[88:91], v[220:223]\n"
    "s_waitcnt lgkmcnt(15)\n"
    "v_mfma_f32_16x16x32_bf16 v[128:131], a[152:155], a[64:67], v[128:131]\n"
    "ds_read_b64_tr_b16 a[186:187], v242 offset:41984\n"
    "v_mfma_f32_16x16x32_bf16 v[160:163], a[152:155], a[72:75], v[160:163]\n"
    "buffer_load_dwordx4 a[20:23], v1, s[64:67], s85 offen offset:2048 sc1\n"
    "v_mfma_f32_16x16x32_bf16 v[192:195], a[152:155], a[80:83], v[192:195]\n"
    "ds_read_b64_tr_b16 a[188:189], v243 offset:40960\n"
    "v_mfma_f32_16x16x32_bf16 v[224:227], a[152:155], a[88:91], v[224:227]\n"
    "s_waitcnt lgkmcnt(15)\n"
    "v_mfma_f32_16x16x32_bf16 v[132:135], a[156:159], a[64:67], v[132:135]\n"
    "ds_read_b64_tr_b16 a[190:191], v243 offset:41984\n"
    "v_mfma_f32_16x16x32_bf16 v[164:167], a[156:159], a[72:75], v[164:167]\n"
    "buffer_load_dwordx4 a[28:31], v1, s[64:67], s85 offen offset:3072 sc1\n"
    "v_mfma_f32_16x16x32_bf16 v[196:199], a[156:159], a[80:83], v[196:199]\n"
    "v_add_u32 v39, 0x4000, v235\n"
    "global_load_dword v36, v39, s[42:43]\n"
    "v_mfma_f32_16x16x32_bf16 v[228:231], a[156:159], a[88:91], v[228:231]\n"
    "s_waitcnt vmcnt(29) lgkmcnt(14)\n"
    "v_mfma_f32_16x16x32_bf16 v[104:107], a[160:163], a[68:71], v[104:107]\n"
    "s_waitcnt vmcnt(28)\n"
    "v_mfma_f32_16x16x32_bf16 v[136:139], a[160:163], a[76:79], v[136:139]\n"
    "s_barrier\n"
    "s_waitcnt vmcnt(27)\n"
    "v_mfma_f32_16x16x32_bf16 v[168:171], a[160:163], a[84:87], v[168:171]\n"
    "ds_read_b64_tr_b16 a[128:129], v236 offset:49152\n"
    "s_waitcnt vmcnt(26)\n"
    "v_mfma_f32_16x16x32_bf16 v[200:203], a[160:163], a[92:95], v[200:203]\n"
    "ds_read_b64_tr_b16 a[130:131], v236 offset:50176\n"
    "s_waitcnt lgkmcnt(14)\n"
    "v_mfma_f32_16x16x32_bf16 v[108:111], a[164:167], a[68:71], v[108:111]\n"
    "v_mfma_f32_16x16x32_bf16 v[140:143], a[164:167], a[76:79], v[140:143]\n"
    "s_mov_b32 s83, 0x300000\n"
    "s_add_u32 m0, s80, 0x18000\n"
    "ds_read_b64_tr_b16 a[132:133], v237 offset:49152\n"
    "buffer_load_dwordx4 v2, s[52:55], s83 offen lds\n"
    "v_mfma_f32_16x16x32_bf16 v[172:175], a[164:167], a[84:87], v[172:175]\n"
    "v_mfma_f32_16x16x32_bf16 v[204:207], a[164:167], a[92:95], v[204:207]\n"
    "ds_read_b64_tr_b16 a[134:135], v237 offset:50176\n"
    "s_waitcnt lgkmcnt(14)\n"
    "v_mfma_f32_16x16x32_bf16 v[112:115], a[168:171], a[68:71], v[112:115]\n"
    "v_mfma_f32_16x16x32_bf16 v[144:147], a[168:171], a[76:79], v[144:147]\n"
    "ds_read_b64_tr_b16 a[136:137], v238 offset:49152\n"
    "v_mfma_f32_16x16x32_bf16 v[176:179], a[168:171], a[84:87], v[176:179]\n"
    "ds_read_b64_tr_b16 a[138:139], v238 offset:50176\n"
    "v_mfma_f32_16x16x32_bf16 v[208:211], a[168:171], a[92:95], v[208:211]\n"
    "s_add_u32 m0, s80, 0x18400\n"
    "s_nop 0\n"
    "buffer_load_dwordx4 v3, s[52:55], s83 offen lds\n"
    "s_waitcnt lgkmcnt(14)\n"
    "v_mfma_f32_16x16x32_bf16 v[116:119], a[172:175], a[68:71], v[116:119]\n"
    "ds_read_b64_tr_b16 a[140:141], v239 offset:49152\n"
    "v_mfma_f32_16x16x32_bf16 v[148:151], a[172:175], a[76:79], v[148:151]\n"
    "v_mfma_f32_16x16x32_bf16 v[180:183], a[172:175], a[84:87], v[180:183]\n"
    "ds_read_b64_tr_b16 a[142:143], v239 offset:50176\n"
    "v_mfma_f32_16x16x32_bf16 v[212:215], a[172:175], a[92:95], v[212:215]\n"
    "s_waitcnt lgkmcnt(14)\n"
    "v_mfma_f32_16x16x32_bf16 v[120:123], a[176:179], a[68:71], v[120:123]\n"
    "ds_read_b64_tr_b16 a[144:145], v240 offset:49152\n"
    "v_mfma_f32_16x16x32_bf16 v[152:155], a[176:179], a[76:79], v[152:155]\n"
    "s_add_u32 m0, s80, 0x18800\n"
    "ds_read_b64_tr_b16 a[146:147], v240 offset:50176\n"
    "buffer_load_dwordx4 v4, s[52:55], s83 offen lds\n"
    "v_mfma_f32_16x16x32_bf16 v[184:187], a[176:179], a[84:87], v[184:187]\n"
    "v_mfma_f32_16x16x32_bf16 v[216:219], a[176:179], a[92:95], v[216:219]\n"
    "ds_read_b64_tr_b16 a[148:149], v241 offset:49152\n"
    "s_waitcnt lgkmcnt(15)\n"
    "v_mfma_f32_16x16x32_bf16 v[124:127], a[180:183], a[68:71], v[124:127]\n"
    "v_mfma_f32_16x16x32_bf16 v[156:159], a[180:183], a[76:79], v[156:159]\n"
    "ds_read_b64_tr_b16 a[150:151], v241 offset:50176\n"
    "v_mfma_f32_16x16x32_bf16 v[188:191], a[180:183], a[84:87], v[188:191]\n"
    "v_mfma_f32_16x16x32_bf16 v[220:223], a[180:183], a[92:95], v[220:223]\n"
    "s_add_u32 m0, s80, 0x18c00\n"
    "ds_read_b64_tr_b16 a[152:153], v242 offset:49152\n"
    "buffer_load_dwordx4 v5, s[52:55], s83 offen lds\n"
    "s_waitcnt lgkmcnt(15)\n"
    "v_mfma_f32_16x16x32_bf16 v[128:131], a[184:187], a[68:71], v[128:131]\n"
    "ds_read_b64_tr_b16 a[154:155], v242 offset:50176\n"
    "v_mfma_f32_16x16x32_bf16 v[160:163], a[184:187], a[76:79], v[160:163]\n"
    "v_mfma_f32_16x16x32_bf16 v[192:195], a[184:187], a[84:87], v[192:195]\n"
    "ds_read_b64_tr_b16 a[156:157], v243 offset:49152\n"
    "v_mfma_f32_16x16x32_bf16 v[224:227], a[184:187], a[92:95], v[224:227]\n"
    "s_waitcnt lgkmcnt(15)\n"
    "v_mfma_f32_16x16x32_bf16 v[132:135], a[188:191], a[68:71], v[132:135]\n"
    "ds_read_b64_tr_b16 a[158:159], v243 offset:50176\n"
    "v_mfma_f32_16x16x32_bf16 v[164:167], a[188:191], a[76:79], v[164:167]\n"
    "v_mfma_f32_16x16x32_bf16 v[196:199], a[188:191], a[84:87], v[196:199]\n"
    "v_mfma_f32_16x16x32_bf16 v[228:231], a[188:191], a[92:95], v[228:231]\n"
    "s_waitcnt vmcnt(25) lgkmcnt(14)\n"
    "v_mfma_f32_16x16x32_bf16 v[104:107], a[128:131], a[96:99], v[104:107]\n"
    "ds_read_b64_tr_b16 a[160:161], v236 offset:57344\n"
    "s_waitcnt vmcnt(24)\n"
    "v_mfma_f32_16x16x32_bf16 v[136:139], a[128:131], a[104:107], v[136:139]\n"
    "ds_read_b64_tr_b16 a[162:163], v236 offset:58368\n"
    "s_mov_b32 s84, 0x5000\n"
    "s_mov_b32 s85, 0xf000\n"
    "buffer_load_dwordx4 a[32:35], v1, s[64:67], s84 offen offset:0 sc1\n"
    "s_waitcnt vmcnt(24)\n"
    "v_mfma_f32_16x16x32_bf16 v[168:171], a[128:131], a[112:115], v[168:171]\n"
    "s_waitcnt vmcnt(23)\n"
    "v_mfma_f32_16x16x32_bf16 v[200:203], a[128:131], a[120:123], v[200:203]\n"
    "ds_read_b64_tr_b16 a[164:165], v237 offset:57344\n"
    "s_waitcnt lgkmcnt(15)\n"
    "v_mfma_f32_16x16x32_bf16 v[108:111], a[132:135], a[96:99], v[108:111]\n"
    "v_mfma_f32_16x16x32_bf16 v[140:143], a[132:135], a[104:107], v[140:143]\n"
    "ds_read_b64_tr_b16 a[166:167], v237 offset:58368\n"
    "buffer_load_dwordx4 a[40:43], v1, s[64:67], s84 offen offset:1024 sc1\n"
    "v_mfma_f32_16x16x32_bf16 v[172:175], a[132:135], a[112:115], v[172:175]\n"
    "v_mfma_f32_16x16x32_bf16 v[204:207], a[132:135], a[120:123], v[204:207]\n"
    "ds_read_b64_tr_b16 a[168:169], v238 offset:57344\n"
    "s_waitcnt lgkmcnt(15)\n"
    "v_mfma_f32_16x16x32_bf16 v[112:115], a[136:139], a[96:99], v[112:115]\n"
    "v_mfma_f32_16x16x32_bf16 v[144:147], a[136:139], a[104:107], v[144:147]\n"
    "ds_read_b64_tr_b16 a[170:171], v238 offset:58368\n"
    "buffer_load_dwordx4 a[48:51], v1, s[64:67], s85 offen offset:0 sc1\n"
    "v_mfma_f32_16x16x32_bf16 v[176:179], a[136:139], a[112:115], v[176:179]\n"
    "v_mfma_f32_16x16x32_bf16 v[208:211], a[136:139], a[120:123], v[208:211]\n"
    "ds_read_b64_tr_b16 a[172:173], v239 offset:57344\n"
    "s_waitcnt lgkmcnt(15)\n"
    "v_mfma_f32_16x16x32_bf16 v[116:119], a[140:143], a[96:99], v[116:119]\n"
    "v_mfma_f32_16x16x32_bf16 v[148:151], a[140:143], a[104:107], v[148:151]\n"
    "ds_read_b64_tr_b16 a[174:175], v239 offset:58368\n"
    "buffer_load_dwordx4 a[56:59], v1, s[64:67], s85 offen offset:1024 sc1\n"
    "v_mfma_f32_16x16x32_bf16 v[180:183], a[140:143], a[112:115], v[180:183]\n"
    "v_mfma_f32_16x16x32_bf16 v[212:215], a[140:143], a[120:123], v[212:215]\n"
    "ds_read_b64_tr_b16 a[176:177], v240 offset:57344\n"
    "s_waitcnt lgkmcnt(15)\n"
    "v_mfma_f32_16x16x32_bf16 v[120:123], a[144:147], a[96:99], v[120:123]\n"
    "ds_read_b64_tr_b16 a[178:179], v240 offset:58368\n"
    "v_mfma_f32_16x16x32_bf16 v[152:155], a[144:147], a[104:107], v[152:155]\n"
    "buffer_load_dwordx4 a[36:39], v1, s[64:67], s84 offen offset:2048 sc1\n"
    "v_mfma_f32_16x16x32_bf16 v[184:187], a[144:147], a[112:115], v[184:187]\n"
    "ds_read_b64_tr_b16 a[180:181], v241 offset:57344\n"
    "v_mfma_f32_16x16x32_bf16 v[216:219], a[144:147], a[120:123], v[216:219]\n"
    "s_waitcnt lgkmcnt(15)\n"
    "v_mfma_f32_16x16x32_bf16 v[124:127], a[148:151], a[96:99], v[124:127]\n"
    "ds_read_b64_tr_b16 a[182:183], v241 offset:58368\n"
    "v_mfma_f32_16x16x32_bf16 v[156:159], a[148:151], a[104:107], v[156:159]\n"
    "buffer_load_dwordx4 a[44:47], v1, s[64:67], s84 offen offset:3072 sc1\n"
    "v_mfma_f32_16x16x32_bf16 v[188:191], a[148:151], a[112:115], v[188:191]\n"
    "ds_read_b64_tr_b16 a[184:185], v242 offset:57344\n"
    "v_mfma_f32_16x16x32_bf16 v[220:223], a[148:151], a[120:123], v[220:223]\n"
    "s_waitcnt lgkmcnt(15)\n"
    "v_mfma_f32_16x16x32_bf16 v[128:131], a[152:155], a[96:99], v[128:131]\n"
    "ds_read_b64_tr_b16 a[186:187], v242 offset:58368\n"
    "v_mfma_f32_16x16x32_bf16 v[160:163], a[152:155], a[104:107], v[160:163]\n"
    "buffer_load_dwordx4 a[52:55], v1, s[64:67], s85 offen offset:2048 sc1\n"
    "v_mfma_f32_16x16x32_bf16 v[192:195], a[152:155], a[112:115], v[192:195]\n"
    "ds_read_b64_tr_b16 a[188:189], v243 offset:57344\n"
    "v_mfma_f32_16x16x32_bf16 v[224:227], a[152:155], a[120:123], v[224:227]\n"
    "s_waitcnt lgkmcnt(15)\n"
    "v_mfma_f32_16x16x32_bf16 v[132:135], a[156:159], a[96:99], v[132:135]\n"
    "ds_read_b64_tr_b16 a[190:191], v243 offset:58368\n"
    "v_mfma_f32_16x16x32_bf16 v[164:167], a[156:159], a[104:107], v[164:167]\n"
    "buffer_load_dwordx4 a[60:63], v1, s[64:67], s85 offen offset:3072 sc1\n"
    "v_mfma_f32_16x16x32_bf16 v[196:199], a[156:159], a[112:115], v[196:199]\n"
    "v_mfma_f32_16x16x32_bf16 v[228:231], a[156:159], a[120:123], v[228:231]\n"
    "s_waitcnt vmcnt(29) lgkmcnt(14)\n"
    "v_mfma_f32_16x16x32_bf16 v[104:107], a[160:163], a[100:103], v[104:107]\n"
    "s_waitcnt vmcnt(28)\n"
    "v_mfma_f32_16x16x32_bf16 v[136:139], a[160:163], a[108:111], v[136:139]\n"
    "s_barrier\n"
    "s_waitcnt vmcnt(27)\n"
    "v_mfma_f32_16x16x32_bf16 v[168:171], a[160:163], a[116:119], v[168:171]\n"
    "ds_read_b64_tr_b16 a[128:129], v244 offset:0\n"
    "s_waitcnt vmcnt(26)\n"
    "v_mfma_f32_16x16x32_bf16 v[200:203], a[160:163], a[124:127], v[200:203]\n"
    "ds_read_b64_tr_b16 a[130:131], v244 offset:1024\n"
    "s_waitcnt lgkmcnt(14)\n"
    "v_mfma_f32_16x16x32_bf16 v[108:111], a[164:167], a[100:103], v[108:111]\n"
    "v_mfma_f32_16x16x32_bf16 v[140:143], a[164:167], a[108:111], v[140:143]\n"
    "s_mov_b32 s83, 0x380000\n"
    "s_add_u32 m0, s80, 0x1c000\n"
    "ds_read_b64_tr_b16 a[132:133], v245 offset:0\n"
    "buffer_load_dwordx4 v2, s[52:55], s83 offen lds\n"
    "v_mfma_f32_16x16x32_bf16 v[172:175], a[164:167], a[116:119], v[172:175]\n"
    "v_mfma_f32_16x16x32_bf16 v[204:207], a[164:167], a[124:127], v[204:207]\n"
    "ds_read_b64_tr_b16 a[134:135], v245 offset:1024\n"
    "s_waitcnt lgkmcnt(14)\n"
    "v_mfma_f32_16x16x32_bf16 v[112:115], a[168:171], a[100:103], v[112:115]\n"
    "v_mfma_f32_16x16x32_bf16 v[144:147], a[168:171], a[108:111], v[144:147]\n"
    "ds_read_b64_tr_b16 a[136:137], v246 offset:0\n"
    "v_mfma_f32_16x16x32_bf16 v[176:179], a[168:171], a[116:119], v[176:179]\n"
    "ds_read_b64_tr_b16 a[138:139], v246 offset:1024\n"
    "v_mfma_f32_16x16x32_bf16 v[208:211], a[168:171], a[124:127], v[208:211]\n"
    "s_add_u32 m0, s80, 0x1c400\n"
    "s_nop 0\n"
    "buffer_load_dwordx4 v3, s[52:55], s83 offen lds\n"
    "s_waitcnt lgkmcnt(14)\n"
    "v_mfma_f32_16x16x32_bf16 v[116:119], a[172:175], a[100:103], v[116:119]\n"
    "ds_read_b64_tr_b16 a[140:141], v247 offset:0\n"
    "v_mfma_f32_16x16x32_bf16 v[148:151], a[172:175], a[108:111], v[148:151]\n"
    "v_mfma_f32_16x16x32_bf16 v[180:183], a[172:175], a[116:119], v[180:183]\n"
    "ds_read_b64_tr_b16 a[142:143], v247 offset:1024\n"
    "v_mfma_f32_16x16x32_bf16 v[212:215], a[172:175], a[124:127], v[212:215]\n"
    "s_waitcnt lgkmcnt(14)\n"
    "v_mfma_f32_16x16x32_bf16 v[120:123], a[176:179], a[100:103], v[120:123]\n"
    "ds_read_b64_tr_b16 a[144:145], v248 offset:0\n"
    "v_mfma_f32_16x16x32_bf16 v[152:155], a[176:179], a[108:111], v[152:155]\n"
    "s_add_u32 m0, s80, 0x1c800\n"
    "ds_read_b64_tr_b16 a[146:147], v248 offset:1024\n"
    "buffer_load_dwordx4 v4, s[52:55], s83 offen lds\n"
    "v_mfma_f32_16x16x32_bf16 v[184:187], a[176:179], a[116:119], v[184:187]\n"
    "v_mfma_f32_16x16x32_bf16 v[216:219], a[176:179], a[124:127], v[216:219]\n"
    "ds_read_b64_tr_b16 a[148:149], v249 offset:0\n"
    "s_waitcnt lgkmcnt(15)\n"
    "v_mfma_f32_16x16x32_bf16 v[124:127], a[180:183], a[100:103], v[124:127]\n"
    "v_mfma_f32_16x16x32_bf16 v[156:159], a[180:183], a[108:111], v[156:159]\n"
    "ds_read_b64_tr_b16 a[150:151], v249 offset:1024\n"
    "v_mfma_f32_16x16x32_bf16 v[188:191], a[180:183], a[116:119], v[188:191]\n"
    "v_mfma_f32_16x16x32_bf16 v[220:223], a[180:183], a[124:127], v[220:223]\n"
    "s_add_u32 m0, s80, 0x1cc00\n"
    "ds_read_b64_tr_b16 a[152:153], v250 offset:0\n"
    "buffer_load_dwordx4 v5, s[52:55], s83 offen lds\n"
    "s_waitcnt lgkmcnt(15)\n"
    "v_mfma_f32_16x16x32_bf16 v[128:131], a[184:187], a[100:103], v[128:131]\n"
    "ds_read_b64_tr_b16 a[154:155], v250 offset:1024\n"
    "v_mfma_f32_16x16x32_bf16 v[160:163], a[184:187], a[108:111], v[160:163]\n"
    "v_mfma_f32_16x16x32_bf16 v[192:195], a[184:187], a[116:119], v[192:195]\n"
    "ds_read_b64_tr_b16 a[156:157], v251 offset:0\n"
    "v_mfma_f32_16x16x32_bf16 v[224:227], a[184:187], a[124:127], v[224:227]\n"
    "s_waitcnt lgkmcnt(15)\n"
    "v_mfma_f32_16x16x32_bf16 v[132:135], a[188:191], a[100:103], v[132:135]\n"
    "ds_read_b64_tr_b16 a[158:159], v251 offset:1024\n"
    "v_mfma_f32_16x16x32_bf16 v[164:167], a[188:191], a[108:111], v[164:167]\n"
    "v_mfma_f32_16x16x32_bf16 v[196:199], a[188:191], a[116:119], v[196:199]\n"
    "v_mfma_f32_16x16x32_bf16 v[228:231], a[188:191], a[124:127], v[228:231]\n"
    "s_waitcnt vmcnt(24) lgkmcnt(14)\n"
    "v_mfma_f32_16x16x32_bf16 v[104:107], a[128:131], a[0:3], v[104:107]\n"
    "ds_read_b64_tr_b16 a[160:161], v244 offset:8192\n"
    "s_waitcnt vmcnt(23)\n"
    "v_mfma_f32_16x16x32_bf16 v[136:139], a[128:131], a[8:11], v[136:139]\n"
    "ds_read_b64_tr_b16 a[162:163], v244 offset:9216\n"
    "s_mov_b32 s84, 0x6000\n"
    "s_mov_b32 s85, 0x10000\n"
    "buffer_load_dwordx4 a[64:67], v1, s[64:67], s84 offen offset:0 sc1\n"
    "s_waitcnt vmcnt(23)\n"
    "v_mfma_f32_16x16x32_bf16 v[168:171], a[128:131], a[16:19], v[168:171]\n"
    "s_waitcnt vmcnt(22)\n"
    "v_mfma_f32_16x16x32_bf16 v[200:203], a[128:131], a[24:27], v[200:203]\n"
    "ds_read_b64_tr_b16 a[164:165], v245 offset:8192\n"
    "s_waitcnt lgkmcnt(15)\n"
    "v_mfma_f32_16x16x32_bf16 v[108:111], a[132:135], a[0:3], v[108:111]\n"
    "v_mfma_f32_16x16x32_bf16 v[140:143], a[132:135], a[8:11], v[140:143]\n"
    "ds_read_b64_tr_b16 a[166:167], v245 offset:9216\n"
    "buffer_load_dwordx4 a[72:75], v1, s[64:67], s84 offen offset:1024 sc1\n"
    "v_mfma_f32_16x16x32_bf16 v[172:175], a[132:135], a[16:19], v[172:175]\n"
    "v_mfma_f32_16x16x32_bf16 v[204:207], a[132:135], a[24:27], v[204:207]\n"
    "ds_read_b64_tr_b16 a[168:169], v246 offset:8192\n"
    "s_waitcnt lgkmcnt(15)\n"
    "v_mfma_f32_16x16x32_bf16 v[112:115], a[136:139], a[0:3], v[112:115]\n"
    "v_mfma_f32_16x16x32_bf16 v[144:147], a[136:139], a[8:11], v[144:147]\n"
    "ds_read_b64_tr_b16 a[170:171], v246 offset:9216\n"
    "buffer_load_dwordx4 a[80:83], v1, s[64:67], s85 offen offset:0 sc1\n"
    "v_mfma_f32_16x16x32_bf16 v[176:179], a[136:139], a[16:19], v[176:179]\n"
    "v_mfma_f32_16x16x32_bf16 v[208:211], a[136:139], a[24:27], v[208:211]\n"
    "ds_read_b64_tr_b16 a[172:173], v247 offset:8192\n"
    "s_waitcnt lgkmcnt(15)\n"
    "v_mfma_f32_16x16x32_bf16 v[116:119], a[140:143], a[0:3], v[116:119]\n"
    "v_mfma_f32_16x16x32_bf16 v[148:151], a[140:143], a[8:11], v[148:151]\n"
    "ds_read_b64_tr_b16 a[174:175], v247 offset:9216\n"
    "buffer_load_dwordx4 a[88:91], v1, s[64:67], s85 offen offset:1024 sc1\n"
    "v_mfma_f32_16x16x32_bf16 v[180:183], a[140:143], a[16:19], v[180:183]\n"
    "v_mfma_f32_16x16x32_bf16 v[212:215], a[140:143], a[24:27], v[212:215]\n"
    "ds_read_b64_tr_b16 a[176:177], v248 offset:8192\n"
    "s_waitcnt lgkmcnt(15)\n"
    "v_mfma_f32_16x16x32_bf16 v[120:123], a[144:147], a[0:3], v[120:123]\n"
    "ds_read_b64_tr_b16 a[178:179], v248 offset:9216\n"
    "v_mfma_f32_16x16x32_bf16 v[152:155], a[144:147], a[8:11], v[152:155]\n"
    "buffer_load_dwordx4 a[68:71], v1, s[64:67], s84 offen offset:2048 sc1\n"
    "v_mfma_f32_16x16x32_bf16 v[184:187], a[144:147], a[16:19], v[184:187]\n"
    "ds_read_b64_tr_b16 a[180:181], v249 offset:8192\n"
    "v_mfma_f32_16x16x32_bf16 v[216:219], a[144:147], a[24:27], v[216:219]\n"
    "s_waitcnt lgkmcnt(15)\n"
    "v_mfma_f32_16x16x32_bf16 v[124:127], a[148:151], a[0:3], v[124:127]\n"
    "ds_read_b64_tr_b16 a[182:183], v249 offset:9216\n"
    "v_mfma_f32_16x16x32_bf16 v[156:159], a[148:151], a[8:11], v[156:159]\n"
    "buffer_load_dwordx4 a[76:79], v1, s[64:67], s84 offen offset:3072 sc1\n"
    "v_mfma_f32_16x16x32_bf16 v[188:191], a[148:151], a[16:19], v[188:191]\n"
    "ds_read_b64_tr_b16 a[184:185], v250 offset:8192\n"
    "v_mfma_f32_16x16x32_bf16 v[220:223], a[148:151], a[24:27], v[220:223]\n"
    "s_waitcnt lgkmcnt(15)\n"
    "v_mfma_f32_16x16x32_bf16 v[128:131], a[152:155], a[0:3], v[128:131]\n"
    "ds_read_b64_tr_b16 a[186:187], v250 offset:9216\n"
    "v_mfma_f32_16x16x32_bf16 v[160:163], a[152:155], a[8:11], v[160:163]\n"
    "buffer_load_dwordx4 a[84:87], v1, s[64:67], s85 offen offset:2048 sc1\n"
    "v_mfma_f32_16x16x32_bf16 v[192:195], a[152:155], a[16:19], v[192:195]\n"
    "ds_read_b64_tr_b16 a[188:189], v251 offset:8192\n"
    "v_mfma_f32_16x16x32_bf16 v[224:227], a[152:155], a[24:27], v[224:227]\n"
    "s_waitcnt lgkmcnt(15)\n"
    "v_mfma_f32_16x16x32_bf16 v[132:135], a[156:159], a[0:3], v[132:135]\n"
    "ds_read_b64_tr_b16 a[190:191], v251 offset:9216\n"
    "v_mfma_f32_16x16x32_bf16 v[164:167], a[156:159], a[8:11], v[164:167]\n"
    "buffer_load_dwordx4 a[92:95], v1, s[64:67], s85 offen offset:3072 sc1\n"
    "v_mfma_f32_16x16x32_bf16 v[196:199], a[156:159], a[16:19], v[196:199]\n"
    "v_add_u32 v39, 0x8000, v235\n"
    "global_load_dword v36, v39, s[42:43]\n"
    "v_mfma_f32_16x16x32_bf16 v[228:231], a[156:159], a[24:27], v[228:231]\n"
    "s_waitcnt vmcnt(29) lgkmcnt(14)\n"
    "v_mfma_f32_16x16x32_bf16 v[104:107], a[160:163], a[4:7], v[104:107]\n"
    "s_waitcnt vmcnt(28)\n"
    "v_mfma_f32_16x16x32_bf16 v[136:139], a[160:163], a[12:15], v[136:139]\n"
    "s_barrier\n"
    "s_waitcnt vmcnt(27)\n"
    "v_mfma_f32_16x16x32_bf16 v[168:171], a[160:163], a[20:23], v[168:171]\n"
    "ds_read_b64_tr_b16 a[128:129], v244 offset:16384\n"
    "s_waitcnt vmcnt(26)\n"
    "v_mfma_f32_16x16x32_bf16 v[200:203], a[160:163], a[28:31], v[200:203]\n"
    "ds_read_b64_tr_b16 a[130:131], v244 offset:17408\n"
    "s_waitcnt lgkmcnt(14)\n"
    "v_mfma_f32_16x16x32_bf16 v[108:111], a[164:167], a[4:7], v[108:111]\n"
    "v_mfma_f32_16x16x32_bf16 v[140:143], a[164:167], a[12:15], v[140:143]\n"
    "s_mov_b32 s83, 0x400000\n"
    "s_add_u32 m0, s80, 0\n"
    "ds_read_b64_tr_b16 a[132:133], v245 offset:16384\n"
    "buffer_load_dwordx4 v2, s[52:55], s83 offen lds\n"
    "v_mfma_f32_16x16x32_bf16 v[172:175], a[164:167], a[20:23], v[172:175]\n"
    "v_mfma_f32_16x16x32_bf16 v[204:207], a[164:167], a[28:31], v[204:207]\n"
    "ds_read_b64_tr_b16 a[134:135], v245 offset:17408\n"
    "s_waitcnt lgkmcnt(14)\n"
    "v_mfma_f32_16x16x32_bf16 v[112:115], a[168:171], a[4:7], v[112:115]\n"
    "v_mfma_f32_16x16x32_bf16 v[144:147], a[168:171], a[12:15], v[144:147]\n"
    "ds_read_b64_tr_b16 a[136:137], v246 offset:16384\n"
    "v_mfma_f32_16x16x32_bf16 v[176:179], a[168:171], a[20:23], v[176:179]\n"
    "ds_read_b64_tr_b16 a[138:139], v246 offset:17408\n"
    "v_mfma_f32_16x16x32_bf16 v[208:211], a[168:171], a[28:31], v[208:211]\n"
    "s_add_u32 m0, s80, 0x400\n"
    "s_nop 0\n"
    "buffer_load_dwordx4 v3, s[52:55], s83 offen lds\n"
    "s_waitcnt lgkmcnt(14)\n"
    "v_mfma_f32_16x16x32_bf16 v[116:119], a[172:175], a[4:7], v[116:119]\n"
    "ds_read_b64_tr_b16 a[140:141], v247 offset:16384\n"
    "v_mfma_f32_16x16x32_bf16 v[148:151], a[172:175], a[12:15], v[148:151]\n"
    "v_mfma_f32_16x16x32_bf16 v[180:183], a[172:175], a[20:23], v[180:183]\n"
    "ds_read_b64_tr_b16 a[142:143], v247 offset:17408\n"
    "v_mfma_f32_16x16x32_bf16 v[212:215], a[172:175], a[28:31], v[212:215]\n"
    "s_waitcnt lgkmcnt(14)\n"
    "v_mfma_f32_16x16x32_bf16 v[120:123], a[176:179], a[4:7], v[120:123]\n"
    "ds_read_b64_tr_b16 a[144:145], v248 offset:16384\n"
    "v_mfma_f32_16x16x32_bf16 v[152:155], a[176:179], a[12:15], v[152:155]\n"
    "s_add_u32 m0, s80, 0x800\n"
    "ds_read_b64_tr_b16 a[146:147], v248 offset:17408\n"
    "buffer_load_dwordx4 v4, s[52:55], s83 offen lds\n"
    "v_mfma_f32_16x16x32_bf16 v[184:187], a[176:179], a[20:23], v[184:187]\n"
    "v_mfma_f32_16x16x32_bf16 v[216:219], a[176:179], a[28:31], v[216:219]\n"
    "ds_read_b64_tr_b16 a[148:149], v249 offset:16384\n"
    "s_waitcnt lgkmcnt(15)\n"
    "v_mfma_f32_16x16x32_bf16 v[124:127], a[180:183], a[4:7], v[124:127]\n"
    "v_mfma_f32_16x16x32_bf16 v[156:159], a[180:183], a[12:15], v[156:159]\n"
    "ds_read_b64_tr_b16 a[150:151], v249 offset:17408\n"
    "v_mfma_f32_16x16x32_bf16 v[188:191], a[180:183], a[20:23], v[188:191]\n"
    "v_mfma_f32_16x16x32_bf16 v[220:223], a[180:183], a[28:31], v[220:223]\n"
    "s_add_u32 m0, s80, 0xc00\n"
    "ds_read_b64_tr_b16 a[152:153], v250 offset:16384\n"
    "buffer_load_dwordx4 v5, s[52:55], s83 offen lds\n"
    "s_waitcnt lgkmcnt(15)\n"
    "v_mfma_f32_16x16x32_bf16 v[128:131], a[184:187], a[4:7], v[128:131]\n"
    "ds_read_b64_tr_b16 a[154:155], v250 offset:17408\n"
    "v_mfma_f32_16x16x32_bf16 v[160:163], a[184:187], a[12:15], v[160:163]\n"
    "v_mfma_f32_16x16x32_bf16 v[192:195], a[184:187], a[20:23], v[192:195]\n"
    "ds_read_b64_tr_b16 a[156:157], v251 offset:16384\n"
    "v_mfma_f32_16x16x32_bf16 v[224:227], a[184:187], a[28:31], v[224:227]\n"
    "s_waitcnt lgkmcnt(15)\n"
    "v_mfma_f32_16x16x32_bf16 v[132:135], a[188:191], a[4:7], v[132:135]\n"
    "ds_read_b64_tr_b16 a[158:159], v251 offset:17408\n"
    "v_mfma_f32_16x16x32_bf16 v[164:167], a[188:191], a[12:15], v[164:167]\n"
    "v_mfma_f32_16x16x32_bf16 v[196:199], a[188:191], a[20:23], v[196:199]\n"
    "v_mfma_f32_16x16x32_bf16 v[228:231], a[188:191], a[28:31], v[228:231]\n"
    "s_waitcnt vmcnt(24) lgkmcnt(14)\n"
    "v_mfma_f32_16x16x32_bf16 v[104:107], a[128:131], a[32:35], v[104:107]\n"
    "ds_read_b64_tr_b16 a[160:161], v244 offset:24576\n"
    "s_waitcnt vmcnt(23)\n"
    "v_mfma_f32_16x16x32_bf16 v[136:139], a[128:131], a[40:43], v[136:139]\n"
    "ds_read_b64_tr_b16 a[162:163], v244 offset:25600\n"
    "s_mov_b32 s84, 0x7000\n"
    "s_mov_b32 s85, 0x11000\n"
    "buffer_load_dwordx4 a[96:99], v1, s[64:67], s84 offen offset:0 sc1\n"
    "s_waitcnt vmcnt(23)\n"
    "v_mfma_f32_16x16x32_bf16 v[168:171], a[128:131], a[48:51], v[168:171]\n"
    "s_waitcnt vmcnt(22)\n"
    "v_mfma_f32_16x16x32_bf16 v[200:203], a[128:131], a[56:59], v[200:203]\n"
    "ds_read_b64_tr_b16 a[164:165], v245 offset:24576\n"
    "s_waitcnt lgkmcnt(15)\n"
    "v_mfma_f32_16x16x32_bf16 v[108:111], a[132:135], a[32:35], v[108:111]\n"
    "v_mfma_f32_16x16x32_bf16 v[140:143], a[132:135], a[40:43], v[140:143]\n"
    "ds_read_b64_tr_b16 a[166:167], v245 offset:25600\n"
    "buffer_load_dwordx4 a[104:107], v1, s[64:67], s84 offen offset:1024 sc1\n"
    "v_mfma_f32_16x16x32_bf16 v[172:175], a[132:135], a[48:51], v[172:175]\n"
    "v_mfma_f32_16x16x32_bf16 v[204:207], a[132:135], a[56:59], v[204:207]\n"
    "ds_read_b64_tr_b16 a[168:169], v246 offset:24576\n"
    "s_waitcnt lgkmcnt(15)\n"
    "v_mfma_f32_16x16x32_bf16 v[112:115], a[136:139], a[32:35], v[112:115]\n"
    "v_mfma_f32_16x16x32_bf16 v[144:147], a[136:139], a[40:43], v[144:147]\n"
    "ds_read_b64_tr_b16 a[170:171], v246 offset:25600\n"
    "buffer_load_dwordx4 a[112:115], v1, s[64:67], s85 offen offset:0 sc1\n"
    "v_mfma_f32_16x16x32_bf16 v[176:179], a[136:139], a[48:51], v[176:179]\n"
    "v_mfma_f32_16x16x32_bf16 v[208:211], a[136:139], a[56:59], v[208:211]\n"
    "ds_read_b64_tr_b16 a[172:173], v247 offset:24576\n"
    "s_waitcnt lgkmcnt(15)\n"
    "v_mfma_f32_16x16x32_bf16 v[116:119], a[140:143], a[32:35], v[116:119]\n"
    "v_mfma_f32_16x16x32_bf16 v[148:151], a[140:143], a[40:43], v[148:151]\n"
    "ds_read_b64_tr_b16 a[174:175], v247 offset:25600\n"
    "buffer_load_dwordx4 a[120:123], v1, s[64:67], s85 offen offset:1024 sc1\n"
    "v_mfma_f32_16x16x32_bf16 v[180:183], a[140:143], a[48:51], v[180:183]\n"
    "v_mfma_f32_16x16x32_bf16 v[212:215], a[140:143], a[56:59], v[212:215]\n"
    "ds_read_b64_tr_b16 a[176:177], v248 offset:24576\n"
    "s_waitcnt lgkmcnt(15)\n"
    "v_mfma_f32_16x16x32_bf16 v[120:123], a[144:147], a[32:35], v[120:123]\n"
    "ds_read_b64_tr_b16 a[178:179], v248 offset:25600\n"
    "v_mfma_f32_16x16x32_bf16 v[152:155], a[144:147], a[40:43], v[152:155]\n"
    "buffer_load_dwordx4 a[100:103], v1, s[64:67], s84 offen offset:2048 sc1\n"
    "v_mfma_f32_16x16x32_bf16 v[184:187], a[144:147], a[48:51], v[184:187]\n"
    "ds_read_b64_tr_b16 a[180:181], v249 offset:24576\n"
    "v_mfma_f32_16x16x32_bf16 v[216:219], a[144:147], a[56:59], v[216:219]\n"
    "s_waitcnt lgkmcnt(15)\n"
    "v_mfma_f32_16x16x32_bf16 v[124:127], a[148:151], a[32:35], v[124:127]\n"
    "ds_read_b64_tr_b16 a[182:183], v249 offset:25600\n"
    "v_mfma_f32_16x16x32_bf16 v[156:159], a[148:151], a[40:43], v[156:159]\n"
    "buffer_load_dwordx4 a[108:111], v1, s[64:67], s84 offen offset:3072 sc1\n"
    "v_mfma_f32_16x16x32_bf16 v[188:191], a[148:151], a[48:51], v[188:191]\n"
    "ds_read_b64_tr_b16 a[184:185], v250 offset:24576\n"
    "v_mfma_f32_16x16x32_bf16 v[220:223], a[148:151], a[56:59], v[220:223]\n"
    "s_waitcnt lgkmcnt(15)\n"
    "v_mfma_f32_16x16x32_bf16 v[128:131], a[152:155], a[32:35], v[128:131]\n"
    "ds_read_b64_tr_b16 a[186:187], v250 offset:25600\n"
    "v_mfma_f32_16x16x32_bf16 v[160:163], a[152:155], a[40:43], v[160:163]\n"
    "buffer_load_dwordx4 a[116:119], v1, s[64:67], s85 offen offset:2048 sc1\n"
    "v_mfma_f32_16x16x32_bf16 v[192:195], a[152:155], a[48:51], v[192:195]\n"
    "ds_read_b64_tr_b16 a[188:189], v251 offset:24576\n"
    "v_mfma_f32_16x16x32_bf16 v[224:227], a[152:155], a[56:59], v[224:227]\n"
    "s_waitcnt lgkmcnt(15)\n"
    "v_mfma_f32_16x16x32_bf16 v[132:135], a[156:159], a[32:35], v[132:135]\n"
    "ds_read_b64_tr_b16 a[190:191], v251 offset:25600\n"
    "v_mfma_f32_16x16x32_bf16 v[164:167], a[156:159], a[40:43], v[164:167]\n"
    "buffer_load_dwordx4 a[124:127], v1, s[64:67], s85 offen offset:3072 sc1\n"
    "v_mfma_f32_16x16x32_bf16 v[196:199], a[156:159], a[48:51], v[196:199]\n"
    "v_mfma_f32_16x16x32_bf16 v[228:231], a[156:159], a[56:59], v[228:231]\n"
    "s_waitcnt vmcnt(28) lgkmcnt(14)\n"
    "v_mfma_f32_16x16x32_bf16 v[104:107], a[160:163], a[36:39], v[104:107]\n"
    "s_waitcnt vmcnt(27)\n"
    "v_mfma_f32_16x16x32_bf16 v[136:139], a[160:163], a[44:47], v[136:139]\n"
    "s_barrier\n"
    "s_waitcnt vmcnt(26)\n"
    "v_mfma_f32_16x16x32_bf16 v[168:171], a[160:163], a[52:55], v[168:171]\n"
    "ds_read_b64_tr_b16 a[128:129], v244 offset:32768\n"
    "s_waitcnt vmcnt(25)\n"
    "v_mfma_f32_16x16x32_bf16 v[200:203], a[160:163], a[60:63], v[200:203]\n"
    "ds_read_b64_tr_b16 a[130:131], v244 offset:33792\n"
    "s_waitcnt lgkmcnt(14)\n"
    "v_mfma_f32_16x16x32_bf16 v[108:111], a[164:167], a[36:39], v[108:111]\n"
    "v_mfma_f32_16x16x32_bf16 v[140:143], a[164:167], a[44:47], v[140:143]\n"
    "s_mov_b32 s83, 0x480000\n"
    "s_add_u32 m0, s80, 0x4000\n"
    "ds_read_b64_tr_b16 a[132:133], v245 offset:32768\n"
    "buffer_load_dwordx4 v2, s[52:55], s83 offen lds\n"
    "v_mfma_f32_16x16x32_bf16 v[172:175], a[164:167], a[52:55], v[172:175]\n"
    "v_mfma_f32_16x16x32_bf16 v[204:207], a[164:167], a[60:63], v[204:207]\n"
    "ds_read_b64_tr_b16 a[134:135], v245 offset:33792\n"
    "s_waitcnt lgkmcnt(14)\n"
    "v_mfma_f32_16x16x32_bf16 v[112:115], a[168:171], a[36:39], v[112:115]\n"
    "v_mfma_f32_16x16x32_bf16 v[144:147], a[168:171], a[44:47], v[144:147]\n"
    "ds_read_b64_tr_b16 a[136:137], v246 offset:32768\n"
    "v_mfma_f32_16x16x32_bf16 v[176:179], a[168:171], a[52:55], v[176:179]\n"
    "ds_read_b64_tr_b16 a[138:139], v246 offset:33792\n"
    "v_mfma_f32_16x16x32_bf16 v[208:211], a[168:171], a[60:63], v[208:211]\n"
    "s_add_u32 m0, s80, 0x4400\n"
    "s_nop 0\n"
    "buffer_load_dwordx4 v3, s[52:55], s83 offen lds\n"
    "s_waitcnt lgkmcnt(14)\n"
    "v_mfma_f32_16x16x32_bf16 v[116:119], a[172:175], a[36:39], v[116:119]\n"
    "ds_read_b64_tr_b16 a[140:141], v247 offset:32768\n"
    "v_mfma_f32_16x16x32_bf16 v[148:151], a[172:175], a[44:47], v[148:151]\n"
    "v_mfma_f32_16x16x32_bf16 v[180:183], a[172:175], a[52:55], v[180:183]\n"
    "ds_read_b64_tr_b16 a[142:143], v247 offset:33792\n"
    "v_mfma_f32_16x16x32_bf16 v[212:215], a[172:175], a[60:63], v[212:215]\n"
    "s_waitcnt lgkmcnt(14)\n"
    "v_mfma_f32_16x16x32_bf16 v[120:123], a[176:179], a[36:39], v[120:123]\n"
    "ds_read_b64_tr_b16 a[144:145], v248 offset:32768\n"
    "v_mfma_f32_16x16x32_bf16 v[152:155], a[176:179], a[44:47], v[152:155]\n"
    "s_add_u32 m0, s80, 0x4800\n"
    "ds_read_b64_tr_b16 a[146:147], v248 offset:33792\n"
    "buffer_load_dwordx4 v4, s[52:55], s83 offen lds\n"
    "v_mfma_f32_16x16x32_bf16 v[184:187], a[176:179], a[52:55], v[184:187]\n"
    "v_mfma_f32_16x16x32_bf16 v[216:219], a[176:179], a[60:63], v[216:219]\n"
    "ds_read_b64_tr_b16 a[148:149], v249 offset:32768\n"
    "s_waitcnt lgkmcnt(15)\n"
    "v_mfma_f32_16x16x32_bf16 v[124:127], a[180:183], a[36:39], v[124:127]\n"
    "v_mfma_f32_16x16x32_bf16 v[156:159], a[180:183], a[44:47], v[156:159]\n"
    "ds_read_b64_tr_b16 a[150:151], v249 offset:33792\n"
    "v_mfma_f32_16x16x32_bf16 v[188:191], a[180:183], a[52:55], v[188:191]\n"
    "v_mfma_f32_16x16x32_bf16 v[220:223], a[180:183], a[60:63], v[220:223]\n"
    "s_add_u32 m0, s80, 0x4c00\n"
    "ds_read_b64_tr_b16 a[152:153], v250 offset:32768\n"
    "buffer_load_dwordx4 v5, s[52:55], s83 offen lds\n"
    "s_waitcnt lgkmcnt(15)\n"
    "v_mfma_f32_16x16x32_bf16 v[128:131], a[184:187], a[36:39], v[128:131]\n"
    "ds_read_b64_tr_b16 a[154:155], v250 offset:33792\n"
    "v_mfma_f32_16x16x32_bf16 v[160:163], a[184:187], a[44:47], v[160:163]\n"
    "v_mfma_f32_16x16x32_bf16 v[192:195], a[184:187], a[52:55], v[192:195]\n"
    "ds_read_b64_tr_b16 a[156:157], v251 offset:32768\n"
    "v_mfma_f32_16x16x32_bf16 v[224:227], a[184:187], a[60:63], v[224:227]\n"
    "s_waitcnt lgkmcnt(15)\n"
    "v_mfma_f32_16x16x32_bf16 v[132:135], a[188:191], a[36:39], v[132:135]\n"
    "ds_read_b64_tr_b16 a[158:159], v251 offset:33792\n"
    "v_mfma_f32_16x16x32_bf16 v[164:167], a[188:191], a[44:47], v[164:167]\n"
    "v_mfma_f32_16x16x32_bf16 v[196:199], a[188:191], a[52:55], v[196:199]\n"
    "v_mfma_f32_16x16x32_bf16 v[228:231], a[188:191], a[60:63], v[228:231]\n"
    "s_waitcnt vmcnt(24) lgkmcnt(14)\n"
    "v_mfma_f32_16x16x32_bf16 v[104:107], a[128:131], a[64:67], v[104:107]\n"
    "ds_read_b64_tr_b16 a[160:161], v244 offset:40960\n"
    "s_waitcnt vmcnt(23)\n"
    "v_mfma_f32_16x16x32_bf16 v[136:139], a[128:131], a[72:75], v[136:139]\n"
    "ds_read_b64_tr_b16 a[162:163], v244 offset:41984\n"
    "s_mov_b32 s84, 0x8000\n"
    "s_mov_b32 s85, 0x12000\n"
    "buffer_load_dwordx4 a[0:3], v1, s[64:67], s84 offen offset:0 sc1\n"
    "s_waitcnt vmcnt(23)\n"
    "v_mfma_f32_16x16x32_bf16 v[168:171], a[128:131], a[80:83], v[168:171]\n"
    "s_waitcnt vmcnt(22)\n"
    "v_mfma_f32_16x16x32_bf16 v[200:203], a[128:131], a[88:91], v[200:203]\n"
    "ds_read_b64_tr_b16 a[164:165], v245 offset:40960\n"
    "s_waitcnt lgkmcnt(15)\n"
    "v_mfma_f32_16x16x32_bf16 v[108:111], a[132:135], a[64:67], v[108:111]\n"
    "v_mfma_f32_16x16x32_bf16 v[140:143], a[132:135], a[72:75], v[140:143]\n"
    "ds_read_b64_tr_b16 a[166:167], v245 offset:41984\n"
    "buffer_load_dwordx4 a[8:11], v1, s[64:67], s84 offen offset:1024 sc1\n"
    "v_mfma_f32_16x16x32_bf16 v[172:175], a[132:135], a[80:83], v[172:175]\n"
    "v_mfma_f32_16x16x32_bf16 v[204:207], a[132:135], a[88:91], v[204:207]\n"
    "ds_read_b64_tr_b16 a[168:169], v246 offset:40960\n"
    "s_waitcnt lgkmcnt(15)\n"
    "v_mfma_f32_16x16x32_bf16 v[112:115], a[136:139], a[64:67], v[112:115]\n"
    "v_mfma_f32_16x16x32_bf16 v[144:147], a[136:139], a[72:75], v[144:147]\n"
    "ds_read_b64_tr_b16 a[170:171], v246 offset:41984\n"
    "buffer_load_dwordx4 a[16:19], v1, s[64:67], s85 offen offset:0 sc1\n"
    "v_mfma_f32_16x16x32_bf16 v[176:179], a[136:139], a[80:83], v[176:179]\n"
    "v_mfma_f32_16x16x32_bf16 v[208:211], a[136:139], a[88:91], v[208:211]\n"
    "ds_read_b64_tr_b16 a[172:173], v247 offset:40960\n"
    "s_waitcnt lgkmcnt(15)\n"
    "v_mfma_f32_16x16x32_bf16 v[116:119], a[140:143], a[64:67], v[116:119]\n"
    "v_mfma_f32_16x16x32_bf16 v[148:151], a[140:143], a[72:75], v[148:151]\n"
    "ds_read_b64_tr_b16 a[174:175], v247 offset:41984\n"
    "buffer_load_dwordx4 a[24:27], v1, s[64:67], s85 offen offset:1024 sc1\n"
    "v_mfma_f32_16x16x32_bf16 v[180:183], a[140:143], a[80:83], v[180:183]\n"
    "v_mfma_f32_16x16x32_bf16 v[212:215], a[140:143], a[88:91], v[212:215]\n"
    "ds_read_b64_tr_b16 a[176:177], v248 offset:40960\n"
    "s_waitcnt lgkmcnt(15)\n"
    "v_mfma_f32_16x16x32_bf16 v[120:123], a[144:147], a[64:67], v[120:123]\n"
    "ds_read_b64_tr_b16 a[178:179], v248 offset:41984\n"
    "v_mfma_f32_16x16x32_bf16 v[152:155], a[144:147], a[72:75], v[152:155]\n"
    "buffer_load_dwordx4 a[4:7], v1, s[64:67], s84 offen offset:2048 sc1\n"
    "v_mfma_f32_16x16x32_bf16 v[184:187], a[144:147], a[80:83], v[184:187]\n"
    "ds_read_b64_tr_b16 a[180:181], v249 offset:40960\n"
    "v_mfma_f32_16x16x32_bf16 v[216:219], a[144:147], a[88:91], v[216:219]\n"
    "s_waitcnt lgkmcnt(15)\n"
    "v_mfma_f32_16x16x32_bf16 v[124:127], a[148:151], a[64:67], v[124:127]\n"
    "ds_read_b64_tr_b16 a[182:183], v249 offset:41984\n"
    "v_mfma_f32_16x16x32_bf16 v[156:159], a[148:151], a[72:75], v[156:159]\n"
    "buffer_load_dwordx4 a[12:15], v1, s[64:67], s84 offen offset:3072 sc1\n"
    "v_mfma_f32_16x16x32_bf16 v[188:191], a[148:151], a[80:83], v[188:191]\n"
    "ds_read_b64_tr_b16 a[184:185], v250 offset:40960\n"
    "v_mfma_f32_16x16x32_bf16 v[220:223], a[148:151], a[88:91], v[220:223]\n"
    "s_waitcnt lgkmcnt(15)\n"
    "v_mfma_f32_16x16x32_bf16 v[128:131], a[152:155], a[64:67], v[128:131]\n"
    "ds_read_b64_tr_b16 a[186:187], v250 offset:41984\n"
    "v_mfma_f32_16x16x32_bf16 v[160:163], a[152:155], a[72:75], v[160:163]\n"
    "buffer_load_dwordx4 a[20:23], v1, s[64:67], s85 offen offset:2048 sc1\n"
    "v_mfma_f32_16x16x32_bf16 v[192:195], a[152:155], a[80:83], v[192:195]\n"
    "ds_read_b64_tr_b16 a[188:189], v251 offset:40960\n"
    "v_mfma_f32_16x16x32_bf16 v[224:227], a[152:155], a[88:91], v[224:227]\n"
    "s_waitcnt lgkmcnt(15)\n"
    "v_mfma_f32_16x16x32_bf16 v[132:135], a[156:159], a[64:67], v[132:135]\n"
    "ds_read_b64_tr_b16 a[190:191], v251 offset:41984\n"
    "v_mfma_f32_16x16x32_bf16 v[164:167], a[156:159], a[72:75], v[164:167]\n"
    "buffer_load_dwordx4 a[28:31], v1, s[64:67], s85 offen offset:3072 sc1\n"
    "v_mfma_f32_16x16x32_bf16 v[196:199], a[156:159], a[80:83], v[196:199]\n"
    "v_add_u32 v39, 0xc000, v235\n"
    "global_load_dword v36, v39, s[42:43]\n"
    "v_mfma_f32_16x16x32_bf16 v[228:231], a[156:159], a[88:91], v[228:231]\n"
    "s_waitcnt vmcnt(29) lgkmcnt(14)\n"
    "v_mfma_f32_16x16x32_bf16 v[104:107], a[160:163], a[68:71], v[104:107]\n"
    "s_waitcnt vmcnt(28)\n"
    "v_mfma_f32_16x16x32_bf16 v[136:139], a[160:163], a[76:79], v[136:139]\n"
    "s_barrier\n"
    "s_waitcnt vmcnt(27)\n"
    "v_mfma_f32_16x16x32_bf16 v[168:171], a[160:163], a[84:87], v[168:171]\n"
    "ds_read_b64_tr_b16 a[128:129], v244 offset:49152\n"
    "s_waitcnt vmcnt(26)\n"
    "v_mfma_f32_16x16x32_bf16 v[200:203], a[160:163], a[92:95], v[200:203]\n"
    "ds_read_b64_tr_b16 a[130:131], v244 offset:50176\n"
    "s_waitcnt lgkmcnt(14)\n"
    "v_mfma_f32_16x16x32_bf16 v[108:111], a[164:167], a[68:71], v[108:111]\n"
    "v_mfma_f32_16x16x32_bf16 v[140:143], a[164:167], a[76:79], v[140:143]\n"
    "s_mov_b32 s83, 0\n"
    "s_add_u32 m0, s81, 0x8000\n"
    "ds_read_b64_tr_b16 a[132:133], v245 offset:49152\n"
    "buffer_load_dwordx4 v6, s[56:59], s83 offen lds\n"
    "v_mfma_f32_16x16x32_bf16 v[172:175], a[164:167], a[84:87], v[172:175]\n"
    "v_mfma_f32_16x16x32_bf16 v[204:207], a[164:167], a[92:95], v[204:207]\n"
    "ds_read_b64_tr_b16 a[134:135], v245 offset:50176\n"
    "s_waitcnt lgkmcnt(14)\n"
    "v_mfma_f32_16x16x32_bf16 v[112:115], a[168:171], a[68:71], v[112:115]\n"
    "v_mfma_f32_16x16x32_bf16 v[144:147], a[168:171], a[76:79], v[144:147]\n"
    "ds_read_b64_tr_b16 a[136:137], v246 offset:49152\n"
    "v_mfma_f32_16x16x32_bf16 v[176:179], a[168:171], a[84:87], v[176:179]\n"
    "ds_read_b64_tr_b16 a[138:139], v246 offset:50176\n"
    "v_mfma_f32_16x16x32_bf16 v[208:211], a[168:171], a[92:95], v[208:211]\n"
    "s_add_u32 m0, s81, 0x8400\n"
    "s_nop 0\n"
    "buffer_load_dwordx4 v7, s[56:59], s83 offen lds\n"
    "s_waitcnt lgkmcnt(14)\n"
    "v_mfma_f32_16x16x32_bf16 v[116:119], a[172:175], a[68:71], v[116:119]\n"
    "ds_read_b64_tr_b16 a[140:141], v247 offset:49152\n"
    "v_mfma_f32_16x16x32_bf16 v[148:151], a[172:175], a[76:79], v[148:151]\n"
    "v_mfma_f32_16x16x32_bf16 v[180:183], a[172:175], a[84:87], v[180:183]\n"
    "ds_read_b64_tr_b16 a[142:143], v247 offset:50176\n"
    "v_mfma_f32_16x16x32_bf16 v[212:215], a[172:175], a[92:95], v[212:215]\n"
    "s_waitcnt lgkmcnt(14)\n"
    "v_mfma_f32_16x16x32_bf16 v[120:123], a[176:179], a[68:71], v[120:123]\n"
    "ds_read_b64_tr_b16 a[144:145], v248 offset:49152\n"
    "v_mfma_f32_16x16x32_bf16 v[152:155], a[176:179], a[76:79], v[152:155]\n"
    "s_add_u32 m0, s81, 0xa000\n"
    "ds_read_b64_tr_b16 a[146:147], v248 offset:50176\n"
    "buffer_load_dwordx4 v8, s[56:59], s83 offen lds\n"
    "v_mfma_f32_16x16x32_bf16 v[184:187], a[176:179], a[84:87], v[184:187]\n"
    "v_mfma_f32_16x16x32_bf16 v[216:219], a[176:179], a[92:95], v[216:219]\n"
    "ds_read_b64_tr_b16 a[148:149], v249 offset:49152\n"
    "s_waitcnt lgkmcnt(15)\n"
    "v_mfma_f32_16x16x32_bf16 v[124:127], a[180:183], a[68:71], v[124:127]\n"
    "v_mfma_f32_16x16x32_bf16 v[156:159], a[180:183], a[76:79], v[156:159]\n"
    "ds_read_b64_tr_b16 a[150:151], v249 offset:50176\n"
    "v_mfma_f32_16x16x32_bf16 v[188:191], a[180:183], a[84:87], v[188:191]\n"
    "v_mfma_f32_16x16x32_bf16 v[220:223], a[180:183], a[92:95], v[220:223]\n"
    "s_add_u32 m0, s81, 0xa400\n"
    "ds_read_b64_tr_b16 a[152:153], v250 offset:49152\n"
    "buffer_load_dwordx4 v9, s[56:59], s83 offen lds\n"
    "s_waitcnt lgkmcnt(15)\n"
    "v_mfma_f32_16x16x32_bf16 v[128:131], a[184:187], a[68:71], v[128:131]\n"
    "ds_read_b64_tr_b16 a[154:155], v250 offset:50176\n"
    "v_mfma_f32_16x16x32_bf16 v[160:163], a[184:187], a[76:79], v[160:163]\n"
    "v_mfma_f32_16x16x32_bf16 v[192:195], a[184:187], a[84:87], v[192:195]\n"
    "ds_read_b64_tr_b16 a[156:157], v251 offset:49152\n"
    "v_mfma_f32_16x16x32_bf16 v[224:227], a[184:187], a[92:95], v[224:227]\n"
    "s_waitcnt lgkmcnt(15)\n"
    "v_mfma_f32_16x16x32_bf16 v[132:135], a[188:191], a[68:71], v[132:135]\n"
    "ds_read_b64_tr_b16 a[158:159], v251 offset:50176\n"
    "v_mfma_f32_16x16x32_bf16 v[164:167], a[188:191], a[76:79], v[164:167]\n"
    "v_mfma_f32_16x16x32_bf16 v[196:199], a[188:191], a[84:87], v[196:199]\n"
    "v_mfma_f32_16x16x32_bf16 v[228:231], a[188:191], a[92:95], v[228:231]\n"
    "s_waitcnt vmcnt(24) lgkmcnt(14)\n"
    "v_mfma_f32_16x16x32_bf16 v[104:107], a[128:131], a[96:99], v[104:107]\n"
    "ds_read_b64_tr_b16 a[160:161], v244 offset:57344\n"
    "s_waitcnt vmcnt(23)\n"
    "v_mfma_f32_16x16x32_bf16 v[136:139], a[128:131], a[104:107], v[136:139]\n"
    "ds_read_b64_tr_b16 a[162:163], v244 offset:58368\n"
    "s_mov_b32 s84, 0x9000\n"
    "s_mov_b32 s85, 0x13000\n"
    "buffer_load_dwordx4 a[32:35], v1, s[64:67], s84 offen offset:0 sc1\n"
    "s_waitcnt vmcnt(23)\n"
    "v_mfma_f32_16x16x32_bf16 v[168:171], a[128:131], a[112:115], v[168:171]\n"
    "s_waitcnt vmcnt(22)\n"
    "v_mfma_f32_16x16x32_bf16 v[200:203], a[128:131], a[120:123], v[200:203]\n"
    "ds_read_b64_tr_b16 a[164:165], v245 offset:57344\n"
    "s_waitcnt lgkmcnt(15)\n"
    "v_mfma_f32_16x16x32_bf16 v[108:111], a[132:135], a[96:99], v[108:111]\n"
    "v_mfma_f32_16x16x32_bf16 v[140:143], a[132:135], a[104:107], v[140:143]\n"
    "ds_read_b64_tr_b16 a[166:167], v245 offset:58368\n"
    "buffer_load_dwordx4 a[40:43], v1, s[64:67], s84 offen offset:1024 sc1\n"
    "v_mfma_f32_16x16x32_bf16 v[172:175], a[132:135], a[112:115], v[172:175]\n"
    "v_mfma_f32_16x16x32_bf16 v[204:207], a[132:135], a[120:123], v[204:207]\n"
    "ds_read_b64_tr_b16 a[168:169], v246 offset:57344\n"
    "s_waitcnt lgkmcnt(15)\n"
    "v_mfma_f32_16x16x32_bf16 v[112:115], a[136:139], a[96:99], v[112:115]\n"
    "v_mfma_f32_16x16x32_bf16 v[144:147], a[136:139], a[104:107], v[144:147]\n"
    "ds_read_b64_tr_b16 a[170:171], v246 offset:58368\n"
    "buffer_load_dwordx4 a[48:51], v1, s[64:67], s85 offen offset:0 sc1\n"
    "v_mfma_f32_16x16x32_bf16 v[176:179], a[136:139], a[112:115], v[176:179]\n"
    "v_mfma_f32_16x16x32_bf16 v[208:211], a[136:139], a[120:123], v[208:211]\n"
    "ds_read_b64_tr_b16 a[172:173], v247 offset:57344\n"
    "s_waitcnt lgkmcnt(15)\n"
    "v_mfma_f32_16x16x32_bf16 v[116:119], a[140:143], a[96:99], v[116:119]\n"
    "v_mfma_f32_16x16x32_bf16 v[148:151], a[140:143], a[104:107], v[148:151]\n"
    "ds_read_b64_tr_b16 a[174:175], v247 offset:58368\n"
    "buffer_load_dwordx4 a[56:59], v1, s[64:67], s85 offen offset:1024 sc1\n"
    "v_mfma_f32_16x16x32_bf16 v[180:183], a[140:143], a[112:115], v[180:183]\n"
    "v_mfma_f32_16x16x32_bf16 v[212:215], a[140:143], a[120:123], v[212:215]\n"
    "ds_read_b64_tr_b16 a[176:177], v248 offset:57344\n"
    "s_waitcnt lgkmcnt(15)\n"
    "v_mfma_f32_16x16x32_bf16 v[120:123], a[144:147], a[96:99], v[120:123]\n"
    "ds_read_b64_tr_b16 a[178:179], v248 offset:58368\n"
    "v_mfma_f32_16x16x32_bf16 v[152:155], a[144:147], a[104:107], v[152:155]\n"
    "buffer_load_dwordx4 a[36:39], v1, s[64:67], s84 offen offset:2048 sc1\n"
    "v_mfma_f32_16x16x32_bf16 v[184:187], a[144:147], a[112:115], v[184:187]\n"
    "ds_read_b64_tr_b16 a[180:181], v249 offset:57344\n"
    "v_mfma_f32_16x16x32_bf16 v[216:219], a[144:147], a[120:123], v[216:219]\n"
    "s_waitcnt lgkmcnt(15)\n"
    "v_mfma_f32_16x16x32_bf16 v[124:127], a[148:151], a[96:99], v[124:127]\n"
    "ds_read_b64_tr_b16 a[182:183], v249 offset:58368\n"
    "v_mfma_f32_16x16x32_bf16 v[156:159], a[148:151], a[104:107], v[156:159]\n"
    "buffer_load_dwordx4 a[44:47], v1, s[64:67], s84 offen offset:3072 sc1\n"
    "v_mfma_f32_16x16x32_bf16 v[188:191], a[148:151], a[112:115], v[188:191]\n"
    "ds_read_b64_tr_b16 a[184:185], v250 offset:57344\n"
    "v_mfma_f32_16x16x32_bf16 v[220:223], a[148:151], a[120:123], v[220:223]\n"
    "s_waitcnt lgkmcnt(15)\n"
    "v_mfma_f32_16x16x32_bf16 v[128:131], a[152:155], a[96:99], v[128:131]\n"
    "ds_read_b64_tr_b16 a[186:187], v250 offset:58368\n"
    "v_mfma_f32_16x16x32_bf16 v[160:163], a[152:155], a[104:107], v[160:163]\n"
    "buffer_load_dwordx4 a[52:55], v1, s[64:67], s85 offen offset:2048 sc1\n"
    "v_mfma_f32_16x16x32_bf16 v[192:195], a[152:155], a[112:115], v[192:195]\n"
    "ds_read_b64_tr_b16 a[188:189], v251 offset:57344\n"
    "v_mfma_f32_16x16x32_bf16 v[224:227], a[152:155], a[120:123], v[224:227]\n"
    "s_waitcnt lgkmcnt(15)\n"
    "v_mfma_f32_16x16x32_bf16 v[132:135], a[156:159], a[96:99], v[132:135]\n"
    "ds_read_b64_tr_b16 a[190:191], v251 offset:58368\n"
    "v_mfma_f32_16x16x32_bf16 v[164:167], a[156:159], a[104:107], v[164:167]\n"
    "buffer_load_dwordx4 a[60:63], v1, s[64:67], s85 offen offset:3072 sc1\n"
    "v_mfma_f32_16x16x32_bf16 v[196:199], a[156:159], a[112:115], v[196:199]\n"
    "v_mfma_f32_16x16x32_bf16 v[228:231], a[156:159], a[120:123], v[228:231]\n"
    "s_waitcnt vmcnt(28) lgkmcnt(14)\n"
    "v_mfma_f32_16x16x32_bf16 v[104:107], a[160:163], a[100:103], v[104:107]\n"
    "s_waitcnt vmcnt(27)\n"
    "v_mfma_f32_16x16x32_bf16 v[136:139], a[160:163], a[108:111], v[136:139]\n"
    "s_barrier\n"
    "s_waitcnt vmcnt(26)\n"
    "v_mfma_f32_16x16x32_bf16 v[168:171], a[160:163], a[116:119], v[168:171]\n"
    "ds_read_b64_tr_b16 a[128:129], v236 offset:0\n"
    "s_waitcnt vmcnt(25)\n"
    "v_mfma_f32_16x16x32_bf16 v[200:203], a[160:163], a[124:127], v[200:203]\n"
    "ds_read_b64_tr_b16 a[130:131], v236 offset:1024\n"
    "s_waitcnt lgkmcnt(14)\n"
    "v_mfma_f32_16x16x32_bf16 v[108:111], a[164:167], a[100:103], v[108:111]\n"
    "v_mfma_f32_16x16x32_bf16 v[140:143], a[164:167], a[108:111], v[140:143]\n"
    "s_mov_b32 s83, 0\n"
    "s_add_u32 m0, s80, 0x10000\n"
    "ds_read_b64_tr_b16 a[132:133], v237 offset:0\n"
    "buffer_load_dwordx4 v10, s[60:63], s83 offen lds\n"
    "v_mfma_f32_16x16x32_bf16 v[172:175], a[164:167], a[116:119], v[172:175]\n"
    "v_mfma_f32_16x16x32_bf16 v[204:207], a[164:167], a[124:127], v[204:207]\n"
    "ds_read_b64_tr_b16 a[134:135], v237 offset:1024\n"
    "s_waitcnt lgkmcnt(14)\n"
    "v_mfma_f32_16x16x32_bf16 v[112:115], a[168:171], a[100:103], v[112:115]\n"
    "v_mfma_f32_16x16x32_bf16 v[144:147], a[168:171], a[108:111], v[144:147]\n"
    "ds_read_b64_tr_b16 a[136:137], v238 offset:0\n"
    "v_mfma_f32_16x16x32_bf16 v[176:179], a[168:171], a[116:119], v[176:179]\n"
    "ds_read_b64_tr_b16 a[138:139], v238 offset:1024\n"
    "v_mfma_f32_16x16x32_bf16 v[208:211], a[168:171], a[124:127], v[208:211]\n"
    "s_add_u32 m0, s80, 0x10400\n"
    "s_nop 0\n"
    "buffer_load_dwordx4 v11, s[60:63], s83 offen lds\n"
    "s_waitcnt lgkmcnt(14)\n"
    "v_mfma_f32_16x16x32_bf16 v[116:119], a[172:175], a[100:103], v[116:119]\n"
    "ds_read_b64_tr_b16 a[140:141], v239 offset:0\n"
    "v_mfma_f32_16x16x32_bf16 v[148:151], a[172:175], a[108:111], v[148:151]\n"
    "v_mfma_f32_16x16x32_bf16 v[180:183], a[172:175], a[116:119], v[180:183]\n"
    "ds_read_b64_tr_b16 a[142:143], v239 offset:1024\n"
    "v_mfma_f32_16x16x32_bf16 v[212:215], a[172:175], a[124:127], v[212:215]\n"
    "s_waitcnt lgkmcnt(14)\n"
    "v_mfma_f32_16x16x32_bf16 v[120:123], a[176:179], a[100:103], v[120:123]\n"
    "ds_read_b64_tr_b16 a[144:145], v240 offset:0\n"
    "v_mfma_f32_16x16x32_bf16 v[152:155], a[176:179], a[108:111], v[152:155]\n"
    "s_add_u32 m0, s80, 0x10800\n"
    "ds_read_b64_tr_b16 a[146:147], v240 offset:1024\n"
    "buffer_load_dwordx4 v12, s[60:63], s83 offen lds\n"
    "v_mfma_f32_16x16x32_bf16 v[184:187], a[176:179], a[116:119], v[184:187]\n"
    "v_mfma_f32_16x16x32_bf16 v[216:219], a[176:179], a[124:127], v[216:219]\n"
    "ds_read_b64_tr_b16 a[148:149], v241 offset:0\n"
    "s_waitcnt lgkmcnt(15)\n"
    "v_mfma_f32_16x16x32_bf16 v[124:127], a[180:183], a[100:103], v[124:127]\n"
    "v_mfma_f32_16x16x32_bf16 v[156:159], a[180:183], a[108:111], v[156:159]\n"
    "ds_read_b64_tr_b16 a[150:151], v241 offset:1024\n"
    "v_mfma_f32_16x16x32_bf16 v[188:191], a[180:183], a[116:119], v[188:191]\n"
    "v_mfma_f32_16x16x32_bf16 v[220:223], a[180:183], a[124:127], v[220:223]\n"
    "s_add_u32 m0, s80, 0x10c00\n"
    "ds_read_b64_tr_b16 a[152:153], v242 offset:0\n"
    "buffer_load_dwordx4 v13, s[60:63], s83 offen lds\n"
    "s_waitcnt lgkmcnt(15)\n"
    "v_mfma_f32_16x16x32_bf16 v[128:131], a[184:187], a[100:103], v[128:131]\n"
    "ds_read_b64_tr_b16 a[154:155], v242 offset:1024\n"
    "v_mfma_f32_16x16x32_bf16 v[160:163], a[184:187], a[108:111], v[160:163]\n"
    "v_mfma_f32_16x16x32_bf16 v[192:195], a[184:187], a[116:119], v[192:195]\n"
    "ds_read_b64_tr_b16 a[156:157], v243 offset:0\n"
    "v_mfma_f32_16x16x32_bf16 v[224:227], a[184:187], a[124:127], v[224:227]\n"
    "s_waitcnt lgkmcnt(15)\n"
    "v_mfma_f32_16x16x32_bf16 v[132:135], a[188:191], a[100:103], v[132:135]\n"
    "ds_read_b64_tr_b16 a[158:159], v243 offset:1024\n"
    "v_mfma_f32_16x16x32_bf16 v[164:167], a[188:191], a[108:111], v[164:167]\n"
    "v_mfma_f32_16x16x32_bf16 v[196:199], a[188:191], a[116:119], v[196:199]\n"
    "v_mfma_f32_16x16x32_bf16 v[228:231], a[188:191], a[124:127], v[228:231]\n"
    "s_waitcnt vmcnt(24) lgkmcnt(14)\n"
    "v_mfma_f32_16x16x32_bf16 v[104:107], a[128:131], a[0:3], v[104:107]\n"
    "ds_read_b64_tr_b16 a[160:161], v236 offset:8192\n"
    "s_waitcnt vmcnt(23)\n"
    "v_mfma_f32_16x16x32_bf16 v[136:139], a[128:131], a[8:11], v[136:139]\n"
    "ds_read_b64_tr_b16 a[162:163], v236 offset:9216\n"
    "s_waitcnt vmcnt(22)\n"
    "v_mfma_f32_16x16x32_bf16 v[168:171], a[128:131], a[16:19], v[168:171]\n"
    "s_waitcnt vmcnt(21)\n"
    "v_mfma_f32_16x16x32_bf16 v[200:203], a[128:131], a[24:27], v[200:203]\n"
    "ds_read_b64_tr_b16 a[164:165], v237 offset:8192\n"
    "s_waitcnt lgkmcnt(15)\n"
    "v_mfma_f32_16x16x32_bf16 v[108:111], a[132:135], a[0:3], v[108:111]\n"
    "v_mfma_f32_16x16x32_bf16 v[140:143], a[132:135], a[8:11], v[140:143]\n"
    "ds_read_b64_tr_b16 a[166:167], v237 offset:9216\n"
    "v_mfma_f32_16x16x32_bf16 v[172:175], a[132:135], a[16:19], v[172:175]\n"
    "v_mfma_f32_16x16x32_bf16 v[204:207], a[132:135], a[24:27], v[204:207]\n"
    "ds_read_b64_tr_b16 a[168:169], v238 offset:8192\n"
    "s_waitcnt lgkmcnt(15)\n"
    "v_mfma_f32_16x16x32_bf16 v[112:115], a[136:139], a[0:3], v[112:115]\n"
    "v_mfma_f32_16x16x32_bf16 v[144:147], a[136:139], a[8:11], v[144:147]\n"
    "ds_read_b64_tr_b16 a[170:171], v238 offset:9216\n"
    "v_mfma_f32_16x16x32_bf16 v[176:179], a[136:139], a[16:19], v[176:179]\n"
    "v_mfma_f32_16x16x32_bf16 v[208:211], a[136:139], a[24:27], v[208:211]\n"
    "ds_read_b64_tr_b16 a[172:173], v239 offset:8192\n"
    "s_waitcnt lgkmcnt(15)\n"
    "v_mfma_f32_16x16x32_bf16 v[116:119], a[140:143], a[0:3], v[116:119]\n"
    "v_mfma_f32_16x16x32_bf16 v[148:151], a[140:143], a[8:11], v[148:151]\n"
    "ds_read_b64_tr_b16 a[174:175], v239 offset:9216\n"
    "v_mfma_f32_16x16x32_bf16 v[180:183], a[140:143], a[16:19], v[180:183]\n"
    "v_mfma_f32_16x16x32_bf16 v[212:215], a[140:143], a[24:27], v[212:215]\n"
    "ds_read_b64_tr_b16 a[176:177], v240 offset:8192\n"
    "s_waitcnt lgkmcnt(15)\n"
    "v_mfma_f32_16x16x32_bf16 v[120:123], a[144:147], a[0:3], v[120:123]\n"
    "ds_read_b64_tr_b16 a[178:179], v240 offset:9216\n"
    "v_mfma_f32_16x16x32_bf16 v[152:155], a[144:147], a[8:11], v[152:155]\n"
    "v_mfma_f32_16x16x32_bf16 v[184:187], a[144:147], a[16:19], v[184:187]\n"
    "ds_read_b64_tr_b16 a[180:181], v241 offset:8192\n"
    "v_mfma_f32_16x16x32_bf16 v[216:219], a[144:147], a[24:27], v[216:219]\n"
    "s_waitcnt lgkmcnt(15)\n"
    "v_mfma_f32_16x16x32_bf16 v[124:127], a[148:151], a[0:3], v[124:127]\n"
    "ds_read_b64_tr_b16 a[182:183], v241 offset:9216\n"
    "v_mfma_f32_16x16x32_bf16 v[156:159], a[148:151], a[8:11], v[156:159]\n"
    "v_mfma_f32_16x16x32_bf16 v[188:191], a[148:151], a[16:19], v[188:191]\n"
    "ds_read_b64_tr_b16 a[184:185], v242 offset:8192\n"
    "v_mfma_f32_16x16x32_bf16 v[220:223], a[148:151], a[24:27], v[220:223]\n"
    "s_waitcnt lgkmcnt(15)\n"
    "v_mfma_f32_16x16x32_bf16 v[128:131], a[152:155], a[0:3], v[128:131]\n"
    "ds_read_b64_tr_b16 a[186:187], v242 offset:9216\n"
    "v_mfma_f32_16x16x32_bf16 v[160:163], a[152:155], a[8:11], v[160:163]\n"
    "v_mfma_f32_16x16x32_bf16 v[192:195], a[152:155], a[16:19], v[192:195]\n"
    "ds_read_b64_tr_b16 a[188:189], v243 offset:8192\n"
    "v_mfma_f32_16x16x32_bf16 v[224:227], a[152:155], a[24:27], v[224:227]\n"
    "s_waitcnt lgkmcnt(15)\n"
    "v_mfma_f32_16x16x32_bf16 v[132:135], a[156:159], a[0:3], v[132:135]\n"
    "ds_read_b64_tr_b16 a[190:191], v243 offset:9216\n"
    "v_mfma_f32_16x16x32_bf16 v[164:167], a[156:159], a[8:11], v[164:167]\n"
    "v_mfma_f32_16x16x32_bf16 v[196:199], a[156:159], a[16:19], v[196:199]\n"
    "v_add_u32 v39, 0x10000, v235\n"
    "global_load_dword v36, v39, s[42:43]\n"
    "v_mfma_f32_16x16x32_bf16 v[228:231], a[156:159], a[24:27], v[228:231]\n"
    "s_waitcnt vmcnt(21) lgkmcnt(14)\n"
    "v_mfma_f32_16x16x32_bf16 v[104:107], a[160:163], a[4:7], v[104:107]\n"
    "s_waitcnt vmcnt(20)\n"
    "v_mfma_f32_16x16x32_bf16 v[136:139], a[160:163], a[12:15], v[136:139]\n"
    "s_barrier\n"
    "s_waitcnt vmcnt(19)\n"
    "v_mfma_f32_16x16x32_bf16 v[168:171], a[160:163], a[20:23], v[168:171]\n"
    "ds_read_b64_tr_b16 a[128:129], v236 offset:16384\n"
    "s_waitcnt vmcnt(18)\n"
    "v_mfma_f32_16x16x32_bf16 v[200:203], a[160:163], a[28:31], v[200:203]\n"
    "ds_read_b64_tr_b16 a[130:131], v236 offset:17408\n"
    "s_waitcnt lgkmcnt(14)\n"
    "v_mfma_f32_16x16x32_bf16 v[108:111], a[164:167], a[4:7], v[108:111]\n"
    "v_mfma_f32_16x16x32_bf16 v[140:143], a[164:167], a[12:15], v[140:143]\n"
    "s_mov_b32 s83, 0x4000\n"
    "s_add_u32 m0, s81, 0xc000\n"
    "ds_read_b64_tr_b16 a[132:133], v237 offset:16384\n"
    "buffer_load_dwordx4 v6, s[56:59], s83 offen lds\n"
    "v_mfma_f32_16x16x32_bf16 v[172:175], a[164:167], a[20:23], v[172:175]\n"
    "v_mfma_f32_16x16x32_bf16 v[204:207], a[164:167], a[28:31], v[204:207]\n"
    "ds_read_b64_tr_b16 a[134:135], v237 offset:17408\n"
    "s_waitcnt lgkmcnt(14)\n"
    "v_mfma_f32_16x16x32_bf16 v[112:115], a[168:171], a[4:7], v[112:115]\n"
    "v_mfma_f32_16x16x32_bf16 v[144:147], a[168:171], a[12:15], v[144:147]\n"
    "ds_read_b64_tr_b16 a[136:137], v238 offset:16384\n"
    "v_mfma_f32_16x16x32_bf16 v[176:179], a[168:171], a[20:23], v[176:179]\n"
    "ds_read_b64_tr_b16 a[138:139], v238 offset:17408\n"
    "v_mfma_f32_16x16x32_bf16 v[208:211], a[168:171], a[28:31], v[208:211]\n"
    "s_add_u32 m0, s81, 0xc400\n"
    "s_nop 0\n"
    "buffer_load_dwordx4 v7, s[56:59], s83 offen lds\n"
    "s_waitcnt lgkmcnt(14)\n"
    "v_mfma_f32_16x16x32_bf16 v[116:119], a[172:175], a[4:7], v[116:119]\n"
    "ds_read_b64_tr_b16 a[140:141], v239 offset:16384\n"
    "v_mfma_f32_16x16x32_bf16 v[148:151], a[172:175], a[12:15], v[148:151]\n"
    "v_mfma_f32_16x16x32_bf16 v[180:183], a[172:175], a[20:23], v[180:183]\n"
    "ds_read_b64_tr_b16 a[142:143], v239 offset:17408\n"
    "v_mfma_f32_16x16x32_bf16 v[212:215], a[172:175], a[28:31], v[212:215]\n"
    "s_waitcnt lgkmcnt(14)\n"
    "v_mfma_f32_16x16x32_bf16 v[120:123], a[176:179], a[4:7], v[120:123]\n"
    "ds_read_b64_tr_b16 a[144:145], v240 offset:16384\n"
    "v_mfma_f32_16x16x32_bf16 v[152:155], a[176:179], a[12:15], v[152:155]\n"
    "s_add_u32 m0, s81, 0xe000\n"
    "ds_read_b64_tr_b16 a[146:147], v240 offset:17408\n"
    "buffer_load_dwordx4 v8, s[56:59], s83 offen lds\n"
    "v_mfma_f32_16x16x32_bf16 v[184:187], a[176:179], a[20:23], v[184:187]\n"
    "v_mfma_f32_16x16x32_bf16 v[216:219], a[176:179], a[28:31], v[216:219]\n"
    "ds_read_b64_tr_b16 a[148:149], v241 offset:16384\n"
    "s_waitcnt lgkmcnt(15)\n"
    "v_mfma_f32_16x16x32_bf16 v[124:127], a[180:183], a[4:7], v[124:127]\n"
    "v_mfma_f32_16x16x32_bf16 v[156:159], a[180:183], a[12:15], v[156:159]\n"
    "ds_read_b64_tr_b16 a[150:151], v241 offset:17408\n"
    "v_mfma_f32_16x16x32_bf16 v[188:191], a[180:183], a[20:23], v[188:191]\n"
    "v_mfma_f32_16x16x32_bf16 v[220:223], a[180:183], a[28:31], v[220:223]\n"
    "s_add_u32 m0, s81, 0xe400\n"
    "ds_read_b64_tr_b16 a[152:153], v242 offset:16384\n"
    "buffer_load_dwordx4 v9, s[56:59], s83 offen lds\n"
    "s_waitcnt lgkmcnt(15)\n"
    "v_mfma_f32_16x16x32_bf16 v[128:131], a[184:187], a[4:7], v[128:131]\n"
    "ds_read_b64_tr_b16 a[154:155], v242 offset:17408\n"
    "v_mfma_f32_16x16x32_bf16 v[160:163], a[184:187], a[12:15], v[160:163]\n"
    "v_mfma_f32_16x16x32_bf16 v[192:195], a[184:187], a[20:23], v[192:195]\n"
    "ds_read_b64_tr_b16 a[156:157], v243 offset:16384\n"
    "v_mfma_f32_16x16x32_bf16 v[224:227], a[184:187], a[28:31], v[224:227]\n"
    "s_waitcnt lgkmcnt(15)\n"
    "v_mfma_f32_16x16x32_bf16 v[132:135], a[188:191], a[4:7], v[132:135]\n"
    "ds_read_b64_tr_b16 a[158:159], v243 offset:17408\n"
    "v_mfma_f32_16x16x32_bf16 v[164:167], a[188:191], a[12:15], v[164:167]\n"
    "v_mfma_f32_16x16x32_bf16 v[196:199], a[188:191], a[20:23], v[196:199]\n"
    "v_mfma_f32_16x16x32_bf16 v[228:231], a[188:191], a[28:31], v[228:231]\n"
    "s_waitcnt vmcnt(16) lgkmcnt(14)\n"
    "v_mfma_f32_16x16x32_bf16 v[104:107], a[128:131], a[32:35], v[104:107]\n"
    "ds_read_b64_tr_b16 a[160:161], v236 offset:24576\n"
    "s_add_u32 s84, s96, 0\n"
    "s_mov_b64 s[72:73], s[16:17]\n"
    "s_waitcnt vmcnt(15)\n"
    "v_mfma_f32_16x16x32_bf16 v[136:139], a[128:131], a[40:43], v[136:139]\n"
    "ds_read_b64_tr_b16 a[162:163], v236 offset:25600\n"
    "s_mov_b32 s89, 0x500\n"
    "s_mov_b32 s90, 40\n"
    "s_waitcnt vmcnt(14)\n"
    "v_mfma_f32_16x16x32_bf16 v[168:171], a[128:131], a[48:51], v[168:171]\n"
    "s_mov_b32 s85, 0\n"
    "s_mov_b32 s97, 0x199a\n"
    "s_waitcnt vmcnt(13)\n"
    "v_mfma_f32_16x16x32_bf16 v[200:203], a[128:131], a[56:59], v[200:203]\n"
    "ds_read_b64_tr_b16 a[164:165], v237 offset:24576\n"
    "s_mov_b32 s91, 10\n"
    "s_mov_b32 s92, 0x500000\n"
    "s_waitcnt lgkmcnt(15)\n"
    "v_mfma_f32_16x16x32_bf16 v[108:111], a[132:135], a[32:35], v[108:111]\n"
    "s_sub_u32 s93, s84, 0\n"
    "s_mov_b32 s94, s93\n"
    "v_mfma_f32_16x16x32_bf16 v[140:143], a[132:135], a[40:43], v[140:143]\n"
    "ds_read_b64_tr_b16 a[166:167], v237 offset:25600\n"
    "s_mul_i32 s93, s93, s97\n"
    "s_lshr_b32 s93, s93, 16\n"
    "v_mfma_f32_16x16x32_bf16 v[172:175], a[132:135], a[48:51], v[172:175]\n"
    "s_mul_i32 s46, s93, s91\n"
    "s_sub_u32 s94, s94, s46\n"
    "v_mfma_f32_16x16x32_bf16 v[204:207], a[132:135], a[56:59], v[204:207]\n"
    "ds_read_b64_tr_b16 a[168:169], v238 offset:24576\n"
    "s_mul_i32 s46, s93, s89\n"
    "s_lshl_b32 s46, s46, 5\n"
    "s_waitcnt lgkmcnt(15)\n"
    "v_mfma_f32_16x16x32_bf16 v[112:115], a[136:139], a[32:35], v[112:115]\n"
    "s_lshl_b32 s47, s94, 7\n"
    "s_add_u32 s46, s46, s47\n"
    "v_mfma_f32_16x16x32_bf16 v[144:147], a[136:139], a[40:43], v[144:147]\n"
    "ds_read_b64_tr_b16 a[170:171], v238 offset:25600\n"
    "s_add_u32 s72, s72, s46\n"
    "s_addc_u32 s73, s73, 0\n"
    "v_mfma_f32_16x16x32_bf16 v[176:179], a[136:139], a[48:51], v[176:179]\n"
    "s_sub_u32 s74, s92, s46\n"
    "s_mov_b32 s75, 0x20000\n"
    "v_mfma_f32_16x16x32_bf16 v[208:211], a[136:139], a[56:59], v[208:211]\n"
    "ds_read_b64_tr_b16 a[172:173], v239 offset:24576\n"
    "s_mul_i32 s46, s93, s90\n"
    "s_lshl_b32 s47, s94, 2\n"
    "s_waitcnt lgkmcnt(15)\n"
    "v_mfma_f32_16x16x32_bf16 v[116:119], a[140:143], a[32:35], v[116:119]\n"
    "s_add_u32 s46, s46, s47\n"
    "s_lshl_b32 s46, s46, 10\n"
    "v_mfma_f32_16x16x32_bf16 v[148:151], a[140:143], a[40:43], v[148:151]\n"
    "ds_read_b64_tr_b16 a[174:175], v239 offset:25600\n"
    "s_add_u32 s46, s46, s85\n"
    "s_add_u32 s46, s46, 0x3000000\n"
    "v_mfma_f32_16x16x32_bf16 v[180:183], a[140:143], a[48:51], v[180:183]\n"
    "s_add_u32 s52, s26, s46\n"
    "s_addc_u32 s53, s27, 0\n"
    "v_mfma_f32_16x16x32_bf16 v[212:215], a[140:143], a[56:59], v[212:215]\n"
    "ds_read_b64_tr_b16 a[176:177], v240 offset:24576\n"
    "s_mov_b32 s54, 0x1000\n"
    "s_mov_b32 s55, 0x20000\n"
    "s_waitcnt lgkmcnt(15)\n"
    "v_mfma_f32_16x16x32_bf16 v[120:123], a[144:147], a[32:35], v[120:123]\n"
    "ds_read_b64_tr_b16 a[178:179], v240 offset:25600\n"
    "s_lshl_b32 s95, s89, 3\n"
    "v_lshrrev_b32 v232, 7, v1\n"
    "v_mfma_f32_16x16x32_bf16 v[152:155], a[144:147], a[40:43], v[152:155]\n"
    "v_mul_u32_u24 v232, s89, v232\n"
    "v_and_b32 v37, 0x70, v1\n"
    "v_mfma_f32_16x16x32_bf16 v[184:187], a[144:147], a[48:51], v[184:187]\n"
    "ds_read_b64_tr_b16 a[180:181], v241 offset:24576\n"
    "v_add_u32 v232, v232, v37\n"
    "v_mfma_f32_16x16x32_bf16 v[216:219], a[144:147], a[56:59], v[216:219]\n"
    "s_waitcnt lgkmcnt(15)\n"
    "v_mfma_f32_16x16x32_bf16 v[124:127], a[148:151], a[32:35], v[124:127]\n"
    "ds_read_b64_tr_b16 a[182:183], v241 offset:25600\n"
    "v_mfma_f32_16x16x32_bf16 v[156:159], a[148:151], a[40:43], v[156:159]\n"
    "v_mfma_f32_16x16x32_bf16 v[188:191], a[148:151], a[48:51], v[188:191]\n"
    "ds_read_b64_tr_b16 a[184:185], v242 offset:24576\n"
    "v_mfma_f32_16x16x32_bf16 v[220:223], a[148:151], a[56:59], v[220:223]\n"
    "s_waitcnt lgkmcnt(15)\n"
    "v_mfma_f32_16x16x32_bf16 v[128:131], a[152:155], a[32:35], v[128:131]\n"
    "ds_read_b64_tr_b16 a[186:187], v242 offset:25600\n"
    "v_mfma_f32_16x16x32_bf16 v[160:163], a[152:155], a[40:43], v[160:163]\n"
    "v_mfma_f32_16x16x32_bf16 v[192:195], a[152:155], a[48:51], v[192:195]\n"
    "ds_read_b64_tr_b16 a[188:189], v243 offset:24576\n"
    "v_mfma_f32_16x16x32_bf16 v[224:227], a[152:155], a[56:59], v[224:227]\n"
    "s_waitcnt lgkmcnt(15)\n"
    "v_mfma_f32_16x16x32_bf16 v[132:135], a[156:159], a[32:35], v[132:135]\n"
    "ds_read_b64_tr_b16 a[190:191], v243 offset:25600\n"
    "v_mfma_f32_16x16x32_bf16 v[164:167], a[156:159], a[40:43], v[164:167]\n"
    "v_mfma_f32_16x16x32_bf16 v[196:199], a[156:159], a[48:51], v[196:199]\n"
    "v_mfma_f32_16x16x32_bf16 v[228:231], a[156:159], a[56:59], v[228:231]\n"
    "s_waitcnt vmcnt(12) lgkmcnt(14)\n"
    "v_mfma_f32_16x16x32_bf16 v[104:107], a[160:163], a[36:39], v[104:107]\n"
    "s_waitcnt vmcnt(11)\n"
    "v_mfma_f32_16x16x32_bf16 v[136:139], a[160:163], a[44:47], v[136:139]\n"
    "s_barrier\n"
    "s_waitcnt vmcnt(10)\n"
    "v_mfma_f32_16x16x32_bf16 v[168:171], a[160:163], a[52:55], v[168:171]\n"
    "s_waitcnt vmcnt(9)\n"
    "v_mfma_f32_16x16x32_bf16 v[200:203], a[160:163], a[60:63], v[200:203]\n"
    "s_waitcnt lgkmcnt(12)\n"
    "v_mfma_f32_16x16x32_bf16 v[108:111], a[164:167], a[36:39], v[108:111]\n"
    "ds_read_b128 a[192:195], v22 offset:32768\n"
    "v_mfma_f32_16x16x32_bf16 v[140:143], a[164:167], a[44:47], v[140:143]\n"
    "s_mov_b32 s83, 0x4000\n"
    "s_add_u32 m0, s80, 0x14000\n"
    "s_nop 0\n"
    "buffer_load_dwordx4 v10, s[60:63], s83 offen lds\n"
    "v_mfma_f32_16x16x32_bf16 v[172:175], a[164:167], a[52:55], v[172:175]\n"
    "v_mfma_f32_16x16x32_bf16 v[204:207], a[164:167], a[60:63], v[204:207]\n"
    "ds_read_b128 a[196:199], v23 offset:32768\n"
    "s_waitcnt lgkmcnt(12)\n"
    "v_mfma_f32_16x16x32_bf16 v[112:115], a[168:171], a[36:39], v[112:115]\n"
    "v_mfma_f32_16x16x32_bf16 v[144:147], a[168:171], a[44:47], v[144:147]\n"
    "v_mfma_f32_16x16x32_bf16 v[176:179], a[168:171], a[52:55], v[176:179]\n"
    "ds_read_b128 a[200:203], v22 offset:33280\n"
    "v_mfma_f32_16x16x32_bf16 v[208:211], a[168:171], a[60:63], v[208:211]\n"
    "s_add_u32 m0, s80, 0x14400\n"
    "s_nop 0\n"
    "buffer_load_dwordx4 v11, s[60:63], s83 offen lds\n"
    "s_waitcnt lgkmcnt(11)\n"
    "v_mfma_f32_16x16x32_bf16 v[116:119], a[172:175], a[36:39], v[116:119]\n"
    "v_mfma_f32_16x16x32_bf16 v[148:151], a[172:175], a[44:47], v[148:151]\n"
    "ds_read_b128 a[204:207], v23 offset:33280\n"
    "v_mfma_f32_16x16x32_bf16 v[180:183], a[172:175], a[52:55], v[180:183]\n"
    "v_mfma_f32_16x16x32_bf16 v[212:215], a[172:175], a[60:63], v[212:215]\n"
    "s_waitcnt lgkmcnt(10)\n"
    "v_mfma_f32_16x16x32_bf16 v[120:123], a[176:179], a[36:39], v[120:123]\n"
    "ds_read_b128 a[208:211], v22 offset:33792\n"
    "v_mfma_f32_16x16x32_bf16 v[152:155], a[176:179], a[44:47], v[152:155]\n"
    "s_add_u32 m0, s80, 0x14800\n"
    "s_nop 0\n"
    "buffer_load_dwordx4 v12, s[60:63], s83 offen lds\n"
    "v_mfma_f32_16x16x32_bf16 v[184:187], a[176:179], a[52:55], v[184:187]\n"
    "v_mfma_f32_16x16x32_bf16 v[216:219], a[176:179], a[60:63], v[216:219]\n"
    "ds_read_b128 a[212:215], v23 offset:33792\n"
    "s_waitcnt lgkmcnt(10)\n"
    "v_mfma_f32_16x16x32_bf16 v[124:127], a[180:183], a[36:39], v[124:127]\n"
    "v_mfma_f32_16x16x32_bf16 v[156:159], a[180:183], a[44:47], v[156:159]\n"
    "v_mfma_f32_16x16x32_bf16 v[188:191], a[180:183], a[52:55], v[188:191]\n"
    "ds_read_b128 a[216:219], v22 offset:34304\n"
    "v_mfma_f32_16x16x32_bf16 v[220:223], a[180:183], a[60:63], v[220:223]\n"
    "s_add_u32 m0, s80, 0x14c00\n"
    "s_nop 0\n"
    "buffer_load_dwordx4 v13, s[60:63], s83 offen lds\n"
    "s_waitcnt lgkmcnt(9)\n"
    "v_mfma_f32_16x16x32_bf16 v[128:131], a[184:187], a[36:39], v[128:131]\n"
    "v_mfma_f32_16x16x32_bf16 v[160:163], a[184:187], a[44:47], v[160:163]\n"
    "ds_read_b128 a[220:223], v23 offset:34304\n"
    "v_mfma_f32_16x16x32_bf16 v[192:195], a[184:187], a[52:55], v[192:195]\n"
    "v_mfma_f32_16x16x32_bf16 v[224:227], a[184:187], a[60:63], v[224:227]\n"
    "s_waitcnt lgkmcnt(8)\n"
    "v_mfma_f32_16x16x32_bf16 v[132:135], a[188:191], a[36:39], v[132:135]\n"
    "v_mfma_f32_16x16x32_bf16 v[164:167], a[188:191], a[44:47], v[164:167]\n"
    "v_mfma_f32_16x16x32_bf16 v[196:199], a[188:191], a[52:55], v[196:199]\n"
    "v_mfma_f32_16x16x32_bf16 v[228:231], a[188:191], a[60:63], v[228:231]\n"
    "ds_read_b128 a[128:131], v22 offset:40960\n"
    "ds_read_b128 a[132:135], v23 offset:40960\n"
    "ds_read_b128 a[136:139], v22 offset:41472\n"
    "ds_read_b128 a[140:143], v23 offset:41472\n"
    "ds_read_b128 a[144:147], v22 offset:41984\n"
    "ds_read_b128 a[148:151], v23 offset:41984\n"
    "ds_read_b128 a[152:155], v22 offset:42496\n"
    "ds_read_b128 a[156:159], v23 offset:42496\n"
    "; ==== P2b: attention over the last 512 keys (32x32x16 MFMA), weight re-tiling as a side job\n"
    "v_pk_mul_f32 v[104:105], v[104:105], s[86:87] op_sel_hi:[1,0]\n"
    "v_pk_mul_f32 v[136:137], v[136:137], s[86:87] op_sel_hi:[1,0]\n"
    "v_pk_mul_f32 v[106:107], v[106:107], s[86:87] op_sel_hi:[1,0]\n"
    "v_pk_mul_f32 v[138:139], v[138:139], s[86:87] op_sel_hi:[1,0]\n"
    "v_pk_mul_f32 v[108:109], v[108:109], s[86:87] op_sel_hi:[1,0]\n"
    "v_pk_mul_f32 v[140:141], v[140:141], s[86:87] op_sel_hi:[1,0]\n"
    "v_pk_mul_f32 v[110:111], v[110:111], s[86:87] op_sel_hi:[1,0]\n"
    "v_pk_mul_f32 v[142:143], v[142:143], s[86:87] op_sel_hi:[1,0]\n"
    "v_cvt_pk_bf16_f32 v40, v104, v105\n"
    "v_cvt_pk_bf16_f32 v42, v136, v137\n"
    "v_cvt_pk_bf16_f32 v41, v106, v107\n"
    "v_cvt_pk_bf16_f32 v43, v138, v139\n"
    "v_pk_mul_f32 v[112:113], v[112:113], s[86:87] op_sel_hi:[1,0]\n"
    "v_pk_mul_f32 v[144:145], v[144:145], s[86:87] op_sel_hi:[1,0]\n"
    "v_pk_mul_f32 v[114:115], v[114:115], s[86:87] op_sel_hi:[1,0]\n"
    "v_pk_mul_f32 v[146:147], v[146:147], s[86:87] op_sel_hi:[1,0]\n"
    "v_cvt_pk_bf16_f32 v44, v108, v109\n"
    "v_cvt_pk_bf16_f32 v46, v140, v141\n"
    "v_cvt_pk_bf16_f32 v45, v110, v111\n"
    "v_cvt_pk_bf16_f32 v47, v142, v143\n"
    "v_permlane16_swap_b32 v40, v42\n"
    "v_permlane16_swap_b32 v41, v43\n"
    "s_waitcnt lgkmcnt(15)\n"
    "s_nop 0\n"
    "v_mfma_f32_32x32x16_bf16 v[236:251], a[192:195], v[40:43], 0\n"
    "v_permlane16_swap_b32 v44, v46\n"
    "v_permlane16_swap_b32 v45, v47\n"
    "v_cvt_pk_bf16_f32 v48, v112, v113\n"
    "v_cvt_pk_bf16_f32 v50, v144, v145\n"
    "v_cvt_pk_bf16_f32 v49, v114, v115\n"
    "v_cvt_pk_bf16_f32 v51, v146, v147\n"
    "v_pk_mul_f32 v[116:117], v[116:117], s[86:87] op_sel_hi:[1,0]\n"
    "v_pk_mul_f32 v[148:149], v[148:149], s[86:87] op_sel_hi:[1,0]\n"
    "v_pk_mul_f32 v[118:119], v[118:119], s[86:87] op_sel_hi:[1,0]\n"
    "v_pk_mul_f32 v[150:151], v[150:151], s[86:87] op_sel_hi:[1,0]\n"
    "s_waitcnt lgkmcnt(14)\n"
    "v_mfma_f32_32x32x16_bf16 v[236:251], a[196:199], v[44:47], v[236:251]\n"
    "s_mov_b32 s83, 0x8000\n"
    "s_add_u32 m0, s81, 0\n"
    "v_permlane16_swap_b32 v48, v50\n"
    "v_permlane16_swap_b32 v49, v51\n"
    "v_cvt_pk_bf16_f32 v52, v116, v117\n"
    "v_cvt_pk_bf16_f32 v54, v148, v149\n"
    "v_cvt_pk_bf16_f32 v53, v118, v119\n"
    "v_cvt_pk_bf16_f32 v55, v150, v151\n"
    "v_pk_mul_f32 v[120:121], v[120:121], s[86:87] op_sel_hi:[1,0]\n"
    "v_pk_mul_f32 v[152:153], v[152:153], s[86:87] op_sel_hi:[1,0]\n"
    "v_pk_mul_f32 v[122:123], v[122:123], s[86:87] op_sel_hi:[1,0]\n"
    "v_pk_mul_f32 v[154:155], v[154:155], s[86:87] op_sel_hi:[1,0]\n"
    "buffer_load_dwordx4 v6, s[56:59], s83 offen lds\n"
    "s_waitcnt lgkmcnt(13)\n"
    "v_mfma_f32_32x32x16_bf16 v[236:251], a[200:203], v[48:51], v[236:251]\n"
    "v_permlane16_swap_b32 v52, v54\n"
    "v_permlane16_swap_b32 v53, v55\n"
    "v_cvt_pk_bf16_f32 v56, v120, v121\n"
    "v_cvt_pk_bf16_f32 v58, v152, v153\n"
    "v_cvt_pk_bf16_f32 v57, v122, v123\n"
    "v_cvt_pk_bf16_f32 v59, v154, v155\n"
    "v_pk_mul_f32 v[124:125], v[124:125], s[86:87] op_sel_hi:[1,0]\n"
    "v_pk_mul_f32 v[156:157], v[156:157], s[86:87] op_sel_hi:[1,0]\n"
    "v_pk_mul_f32 v[126:127], v[126:127], s[86:87] op_sel_hi:[1,0]\n"
    "v_pk_mul_f32 v[158:159], v[158:159], s[86:87] op_sel_hi:[1,0]\n"
    "s_waitcnt lgkmcnt(12)\n"
    "v_mfma_f32_32x32x16_bf16 v[236:251], a[204:207], v[52:55], v[236:251]\n"
    "s_add_u32 m0, s81, 0x400\n"
    "v_permlane16_swap_b32 v56, v58\n"
    "v_permlane16_swap_b32 v57, v59\n"
    "v_cvt_pk_bf16_f32 v60, v124, v125\n"
    "v_cvt_pk_bf16_f32 v62, v156, v157\n"
    "v_cvt_pk_bf16_f32 v61, v126, v127\n"
    "v_cvt_pk_bf16_f32 v63, v158, v159\n"
    "v_pk_mul_f32 v[128:129], v[128:129], s[86:87] op_sel_hi:[1,0]\n"
    "v_pk_mul_f32 v[160:161], v[160:161], s[86:87] op_sel_hi:[1,0]\n"
    "v_pk_mul_f32 v[130:131], v[130:131], s[86:87] op_sel_hi:[1,0]\n"
    "v_pk_mul_f32 v[162:163], v[162:163], s[86:87] op_sel_hi:[1,0]\n"
    "buffer_load_dwordx4 v7, s[56:59], s83 offen lds\n"
    "s_waitcnt lgkmcnt(11)\n"
    "v_mfma_f32_32x32x16_bf16 v[236:251], a[208:211], v[56:59], v[236:251]\n"
    "v_permlane16_swap_b32 v60, v62\n"
    "v_permlane16_swap_b32 v61, v63\n"
    "v_cvt_pk_bf16_f32 v64, v128, v129\n"
    "v_cvt_pk_bf16_f32 v66, v160, v161\n"
    "v_cvt_pk_bf16_f32 v65, v130, v131\n"
    "v_cvt_pk_bf16_f32 v67, v162, v163\n"
    "v_pk_mul_f32 v[132:133], v[132:133], s[86:87] op_sel_hi:[1,0]\n"
    "v_pk_mul_f32 v[164:165], v[164:165], s[86:87] op_sel_hi:[1,0]\n"
    "v_pk_mul_f32 v[134:135], v[134:135], s[86:87] op_sel_hi:[1,0]\n"
    "v_pk_mul_f32 v[166:167], v[166:167], s[86:87] op_sel_hi:[1,0]\n"
    "s_waitcnt lgkmcnt(10)\n"
    "v_mfma_f32_32x32x16_bf16 v[236:251], a[212:215], v[60:63], v[236:251]\n"
    "s_add_u32 m0, s81, 0x2000\n"
    "v_permlane16_swap_b32 v64, v66\n"
    "v_permlane16_swap_b32 v65, v67\n"
    "v_cvt_pk_bf16_f32 v68, v132, v133\n"
    "v_cvt_pk_bf16_f32 v70, v164, v165\n"
    "v_cvt_pk_bf16_f32 v69, v134, v135\n"
    "v_cvt_pk_bf16_f32 v71, v166, v167\n"
    "v_pk_mul_f32 v[168:169], v[168:169], s[86:87] op_sel_hi:[1,0]\n"
    "v_pk_mul_f32 v[200:201], v[200:201], s[86:87] op_sel_hi:[1,0]\n"
    "v_pk_mul_f32 v[170:171], v[170:171], s[86:87] op_sel_hi:[1,0]\n"
    "v_pk_mul_f32 v[202:203], v[202:203], s[86:87] op_sel_hi:[1,0]\n"
    "buffer_load_dwordx4 v8, s[56:59], s83 offen lds\n"
    "s_waitcnt lgkmcnt(9)\n"
    "v_mfma_f32_32x32x16_bf16 v[236:251], a[216:219], v[64:67], v[236:251]\n"
    "v_permlane16_swap_b32 v68, v70\n"
    "v_permlane16_swap_b32 v69, v71\n"
    "v_cvt_pk_bf16_f32 v72, v168, v169\n"
    "v_cvt_pk_bf16_f32 v74, v200, v201\n"
    "v_cvt_pk_bf16_f32 v73, v170, v171\n"
    "v_cvt_pk_bf16_f32 v75, v202, v203\n"
    "v_pk_mul_f32 v[172:173], v[172:173], s[86:87] op_sel_hi:[1,0]\n"
    "v_pk_mul_f32 v[204:205], v[204:205], s[86:87] op_sel_hi:[1,0]\n"
    "v_pk_mul_f32 v[174:175], v[174:175], s[86:87] op_sel_hi:[1,0]\n"
    "v_pk_mul_f32 v[206:207], v[206:207], s[86:87] op_sel_hi:[1,0]\n"
    "s_waitcnt lgkmcnt(8)\n"
    "v_mfma_f32_32x32x16_bf16 v[236:251], a[220:223], v[68:71], v[236:251]\n"
    "s_add_u32 m0, s81, 0x2400\n"
    "v_permlane16_swap_b32 v72, v74\n"
    "v_permlane16_swap_b32 v73, v75\n"
    "v_cvt_pk_bf16_f32 v76, v172, v173\n"
    "v_cvt_pk_bf16_f32 v78, v204, v205\n"
    "v_cvt_pk_bf16_f32 v77, v174, v175\n"
    "v_cvt_pk_bf16_f32 v79, v206, v207\n"
    "v_pk_mul_f32 v[176:177], v[176:177], s[86:87] op_sel_hi:[1,0]\n"
    "v_pk_mul_f32 v[208:209], v[208:209], s[86:87] op_sel_hi:[1,0]\n"
    "v_pk_mul_f32 v[178:179], v[178:179], s[86:87] op_sel_hi:[1,0]\n"
    "v_pk_mul_f32 v[210:211], v[210:211], s[86:87] op_sel_hi:[1,0]\n"
    "buffer_load_dwordx4 v9, s[56:59], s83 offen lds\n"
    "v_mfma_f32_32x32x16_bf16 v[120:135], a[192:195], v[72:75], 0\n"
    "v_permlane16_swap_b32 v76, v78\n"
    "v_permlane16_swap_b32 v77, v79\n"
    "v_cvt_pk_bf16_f32 v80, v176, v177\n"
    "v_cvt_pk_bf16_f32 v82, v208, v209\n"
    "v_cvt_pk_bf16_f32 v81, v178, v179\n"
    "v_cvt_pk_bf16_f32 v83, v210, v211\n"
    "v_pk_mul_f32 v[180:181], v[180:181], s[86:87] op_sel_hi:[1,0]\n"
    "v_pk_mul_f32 v[212:213], v[212:213], s[86:87] op_sel_hi:[1,0]\n"
    "v_pk_mul_f32 v[182:183], v[182:183], s[86:87] op_sel_hi:[1,0]\n"
    "v_pk_mul_f32 v[214:215], v[214:215], s[86:87] op_sel_hi:[1,0]\n"
    "v_mfma_f32_32x32x16_bf16 v[120:135], a[196:199], v[76:79], v[120:135]\n"
    "v_permlane16_swap_b32 v80, v82\n"
    "v_permlane16_swap_b32 v81, v83\n"
    "v_cvt_pk_bf16_f32 v84, v180, v181\n"
    "v_cvt_pk_bf16_f32 v86, v212, v213\n"
    "v_cvt_pk_bf16_f32 v85, v182, v183\n"
    "v_cvt_pk_bf16_f32 v87, v214, v215\n"
    "v_pk_mul_f32 v[184:185], v[184:185], s[86:87] op_sel_hi:[1,0]\n"
    "v_pk_mul_f32 v[216:217], v[216:217], s[86:87] op_sel_hi:[1,0]\n"
    "v_pk_mul_f32 v[186:187], v[186:187], s[86:87] op_sel_hi:[1,0]\n"
    "v_pk_mul_f32 v[218:219], v[218:219], s[86:87] op_sel_hi:[1,0]\n"
    "v_mfma_f32_32x32x16_bf16 v[120:135], a[200:203], v[80:83], v[120:135]\n"
    "v_permlane16_swap_b32 v84, v86\n"
    "v_permlane16_swap_b32 v85, v87\n"
    "v_cvt_pk_bf16_f32 v88, v184, v185\n"
    "v_cvt_pk_bf16_f32 v90, v216, v217\n"
    "v_cvt_pk_bf16_f32 v89, v186, v187\n"
    "v_cvt_pk_bf16_f32 v91, v218, v219\n"
    "v_pk_mul_f32 v[188:189], v[188:189], s[86:87] op_sel_hi:[1,0]\n"
    "v_pk_mul_f32 v[220:221], v[220:221], s[86:87] op_sel_hi:[1,0]\n"
    "v_pk_mul_f32 v[190:191], v[190:191], s[86:87] op_sel_hi:[1,0]\n"
    "v_pk_mul_f32 v[222:223], v[222:223], s[86:87] op_sel_hi:[1,0]\n"
    "v_mfma_f32_32x32x16_bf16 v[120:135], a[204:207], v[84:87], v[120:135]\n"
    "v_permlane16_swap_b32 v88, v90\n"
    "v_permlane16_swap_b32 v89, v91\n"
    "v_cvt_pk_bf16_f32 v92, v188, v189\n"
    "v_cvt_pk_bf16_f32 v94, v220, v221\n"
    "v_cvt_pk_bf16_f32 v93, v190, v191\n"
    "v_cvt_pk_bf16_f32 v95, v222, v223\n"
    "v_pk_mul_f32 v[192:193], v[192:193], s[86:87] op_sel_hi:[1,0]\n"
    "v_pk_mul_f32 v[224:225], v[224:225], s[86:87] op_sel_hi:[1,0]\n"
    "v_pk_mul_f32 v[194:195], v[194:195], s[86:87] op_sel_hi:[1,0]\n"
    "v_pk_mul_f32 v[226:227], v[226:227], s[86:87] op_sel_hi:[1,0]\n"
    "v_max3_f32 v29, v236, v237, v238\n"
    "v_max3_f32 v30, v239, v240, v241\n"
    "v_max3_f32 v29, v29, v242, v243\n"
    "v_max3_f32 v30, v30, v244, v245\n"
    "v_mfma_f32_32x32x16_bf16 v[120:135], a[208:211], v[88:91], v[120:135]\n"
    "v_permlane16_swap_b32 v92, v94\n"
    "v_permlane16_swap_b32 v93, v95\n"
    "v_cvt_pk_bf16_f32 v96, v192, v193\n"
    "v_cvt_pk_bf16_f32 v98, v224, v225\n"
    "v_cvt_pk_bf16_f32 v97, v194, v195\n"
    "v_cvt_pk_bf16_f32 v99, v226, v227\n"
    "v_pk_mul_f32 v[196:197], v[196:197], s[86:87] op_sel_hi:[1,0]\n"
    "v_pk_mul_f32 v[228:229], v[228:229], s[86:87] op_sel_hi:[1,0]\n"
    "v_pk_mul_f32 v[198:199], v[198:199], s[86:87] op_sel_hi:[1,0]\n"
    "v_pk_mul_f32 v[230:231], v[230:231], s[86:87] op_sel_hi:[1,0]\n"
    "v_max3_f32 v29, v29, v246, v247\n"
    "v_max3_f32 v30, v30, v248, v249\n"
    "v_max3_f32 v29, v29, v250, v251\n"
    "v_max_f32 v29, v29, v30\n"
    "v_mfma_f32_32x32x16_bf16 v[120:135], a[212:215], v[92:95], v[120:135]\n"
    "v_permlane16_swap_b32 v96, v98\n"
    "v_permlane16_swap_b32 v97, v99\n"
    "v_cvt_pk_bf16_f32 v100, v196, v197\n"
    "v_cvt_pk_bf16_f32 v102, v228, v229\n"
    "v_cvt_pk_bf16_f32 v101, v198, v199\n"
    "v_cvt_pk_bf16_f32 v103, v230, v231\n"
    "v_mov_b32 v30, v29\n"
    "v_mov_b32 v25, 0\n"
    "s_nop 0\n"
    "v_permlane32_swap_b32 v29, v30\n"
    "v_max_f32 v27, v29, v30\n"
    "v_mfma_f32_32x32x16_bf16 v[120:135], a[216:219], v[96:99], v[120:135]\n"
    "v_permlane16_swap_b32 v100, v102\n"
    "v_permlane16_swap_b32 v101, v103\n"
    "v_sub_f32 v29, 0, v27\n"
    "v_mov_b32 v200, v29\n"
    "v_mov_b32 v201, v29\n"
    "v_mov_b32 v202, v29\n"
    "v_mov_b32 v203, v29\n"
    "v_mov_b32 v204, v29\n"
    "v_mov_b32 v205, v29\n"
    "v_mov_b32 v206, v29\n"
    "v_mov_b32 v207, v29\n"
    "v_mfma_f32_32x32x16_bf16 v[120:135], a[220:223], v[100:103], v[120:135]\n"
    "v_mov_b32 v208, v29\n"
    "v_mov_b32 v209, v29\n"
    "v_mov_b32 v210, v29\n"
    "v_mov_b32 v211, v29\n"
    "v_mov_b32 v212, v29\n"
    "v_mov_b32 v213, v29\n"
    "v_mov_b32 v214, v29\n"
    "v_mov_b32 v215, v29\n"
    "s_waitcnt vmcnt(8)\n"
    "s_waitcnt lgkmcnt(0)\n"
    "s_barrier\n"
    "v_mfma_f32_32x32x16_bf16 v[136:151], a[128:131], v[40:43], v[200:215]\n"
    "v_sub_f32 v236, v236, v27\n"
    "v_sub_f32 v237, v237, v27\n"
    "v_sub_f32 v238, v238, v27\n"
    "v_sub_f32 v239, v239, v27\n"
    "ds_read_b64_tr_b16 a[160:161], v18 offset:0\n"
    "v_mfma_f32_32x32x16_bf16 v[136:151], a[132:135], v[44:47], v[136:151]\n"
    "v_sub_f32 v240, v240, v27\n"
    "v_sub_f32 v241, v241, v27\n"
    "v_sub_f32 v242, v242, v27\n"
    "v_sub_f32 v243, v243, v27\n"
    "ds_read_b64_tr_b16 a[162:163], v18 offset:1024\n"
    "v_mfma_f32_32x32x16_bf16 v[136:151], a[136:139], v[48:51], v[136:151]\n"
    "v_sub_f32 v244, v244, v27\n"
    "v_sub_f32 v245, v245, v27\n"
    "v_sub_f32 v246, v246, v27\n"
    "v_sub_f32 v247, v247, v27\n"
    "v_max3_f32 v31, v120, v121, v122\n"
    "v_max3_f32 v32, v123, v124, v125\n"
    "v_max3_f32 v31, v31, v126, v127\n"
    "v_max3_f32 v32, v32, v128, v129\n"
    "ds_read_b64_tr_b16 a[164:165], v19 offset:0\n"
    "v_mfma_f32_32x32x16_bf16 v[136:151], a[140:143], v[52:55], v[136:151]\n"
    "v_sub_f32 v248, v248, v27\n"
    "v_sub_f32 v249, v249, v27\n"
    "v_sub_f32 v250, v250, v27\n"
    "v_sub_f32 v251, v251, v27\n"
    "v_max3_f32 v31, v31, v130, v131\n"
    "v_max3_f32 v32, v32, v132, v133\n"
    "v_max3_f32 v31, v31, v134, v135\n"
    "v_max_f32 v31, v31, v32\n"
    "ds_read_b64_tr_b16 a[166:167], v19 offset:1024\n"
    "v_mfma_f32_32x32x16_bf16 v[136:151], a[144:147], v[56:59], v[136:151]\n"
    "v_mov_b32 v32, v31\n"
    "v_mov_b32 v26, 0\n"
    "s_nop 0\n"
    "v_permlane32_swap_b32 v31, v32\n"
    "v_max_f32 v28, v31, v32\n"
    "ds_read_b64_tr_b16 a[168:169], v20 offset:0\n"
    "v_mfma_f32_32x32x16_bf16 v[136:151], a[148:151], v[60:63], v[136:151]\n"
    "v_sub_f32 v31, 0, v28\n"
    "ds_read_b64_tr_b16 a[170:171], v20 offset:1024\n"
    "v_exp_f32 v236, v236\n"
    "v_exp_f32 v237, v237\n"
    "v_mfma_f32_32x32x16_bf16 v[136:151], a[152:155], v[64:67], v[136:151]\n"
    "v_mov_b32 v216, v31\n"
    "v_mov_b32 v217, v31\n"
    "v_mov_b32 v218, v31\n"
    "v_mov_b32 v219, v31\n"
    "v_mov_b32 v220, v31\n"
    "v_mov_b32 v221, v31\n"
    "v_mov_b32 v222, v31\n"
    "v_mov_b32 v223, v31\n"
    "ds_read_b64_tr_b16 a[172:173], v21 offset:0\n"
    "v_add_f32 v25, v25, v236\n"
    "v_add_f32 v25, v25, v237\n"
    "v_exp_f32 v238, v238\n"
    "v_exp_f32 v239, v239\n"
    "v_cvt_pk_bf16_f32 v168, v236, v237\n"
    "v_mfma_f32_32x32x16_bf16 v[136:151], a[156:159], v[68:71], v[136:151]\n"
    "v_mov_b32 v224, v31\n"
    "v_mov_b32 v225, v31\n"
    "v_mov_b32 v226, v31\n"
    "v_mov_b32 v227, v31\n"
    "v_mov_b32 v228, v31\n"
    "v_mov_b32 v229, v31\n"
    "v_mov_b32 v230, v31\n"
    "v_mov_b32 v231, v31\n"
    "ds_read_b64_tr_b16 a[174:175], v21 offset:1024\n"
    "v_add_f32 v25, v25, v238\n"
    "v_add_f32 v25, v25, v239\n"
    "v_exp_f32 v240, v240\n"
    "v_cvt_pk_bf16_f32 v169, v238, v239\n"
    "v_mfma_f32_32x32x16_bf16 v[152:167], a[128:131], v[72:75], v[216:231]\n"
    "ds_read_b64_tr_b16 a[176:177], v18 offset:4096\n"
    "v_add_f32 v25, v25, v240\n"
    "v_exp_f32 v241, v241\n"
    "v_exp_f32 v242, v242\n"
    "v_mfma_f32_32x32x16_bf16 v[152:167], a[132:135], v[76:79], v[152:167]\n"
    "ds_read_b64_tr_b16 a[178:179], v18 offset:5120\n"
    "v_add_f32 v25, v25, v241\n"
    "v_add_f32 v25, v25, v242\n"
    "v_exp_f32 v243, v243\n"
    "v_cvt_pk_bf16_f32 v170, v240, v241\n"
    "v_mfma_f32_32x32x16_bf16 v[152:167], a[136:139], v[80:83], v[152:167]\n"
    "ds_read_b64_tr_b16 a[180:181], v19 offset:4096\n"
    "v_add_f32 v25, v25, v243\n"
    "v_exp_f32 v244, v244\n"
    "v_exp_f32 v245, v245\n"
    "v_cvt_pk_bf16_f32 v171, v242, v243\n"
    "v_sub_f32 v120, v120, v28\n"
    "v_sub_f32 v121, v121, v28\n"
    "v_sub_f32 v122, v122, v28\n"
    "v_mfma_f32_32x32x16_bf16 v[152:167], a[140:143], v[84:87], v[152:167]\n"
    "ds_read_b64_tr_b16 a[182:183], v19 offset:5120\n"
    "v_add_f32 v25, v25, v244\n"
    "v_add_f32 v25, v25, v245\n"
    "v_exp_f32 v246, v246\n"
    "v_exp_f32 v247, v247\n"
    "v_cvt_pk_bf16_f32 v172, v244, v245\n"
    "v_sub_f32 v123, v123, v28\n"
    "v_sub_f32 v124, v124, v28\n"
    "v_sub_f32 v125, v125, v28\n"
    "v_mfma_f32_32x32x16_bf16 v[152:167], a[144:147], v[88:91], v[152:167]\n"
    "ds_read_b64_tr_b16 a[184:185], v20 offset:4096\n"
    "v_add_f32 v25, v25, v246\n"
    "v_add_f32 v25, v25, v247\n"
    "v_exp_f32 v248, v248\n"
    "v_cvt_pk_bf16_f32 v173, v246, v247\n"
    "v_sub_f32 v126, v126, v28\n"
    "v_sub_f32 v127, v127, v28\n"
    "v_sub_f32 v128, v128, v28\n"
    "v_mfma_f32_32x32x16_bf16 v[152:167], a[148:151], v[92:95], v[152:167]\n"
    "ds_read_b64_tr_b16 a[186:187], v20 offset:5120\n"
    "v_add_f32 v25, v25, v248\n"
    "v_exp_f32 v249, v249\n"
    "v_exp_f32 v250, v250\n"
    "v_sub_f32 v129, v129, v28\n"
    "v_sub_f32 v130, v130, v28\n"
    "v_sub_f32 v131, v131, v28\n"
    "v_mfma_f32_32x32x16_bf16 v[152:167], a[152:155], v[96:99], v[152:167]\n"
    "ds_read_b64_tr_b16 a[188:189], v21 offset:4096\n"
    "v_add_f32 v25, v25, v249\n"
    "v_add_f32 v25, v25, v250\n"
    "v_exp_f32 v251, v251\n"
    "v_cvt_pk_bf16_f32 v174, v248, v249\n"
    "v_sub_f32 v132, v132, v28\n"
    "v_sub_f32 v133, v133, v28\n"
    "v_sub_f32 v134, v134, v28\n"
    "v_mfma_f32_32x32x16_bf16 v[152:167], a[156:159], v[100:103], v[152:167]\n"
    "ds_read_b64_tr_b16 a[190:191], v21 offset:5120\n"
    "v_add_f32 v25, v25, v251\n"
    "v_cvt_pk_bf16_f32 v175, v250, v251\n"
    "v_sub_f32 v135, v135, v28\n"
    "s_waitcnt lgkmcnt(14)\n"
    "v_mfma_f32_32x32x16_bf16 a[0:15], a[160:163], v[168:171], 0\n"
    "ds_read_b128 a[128:131], v22 offset:49152\n"
    "v_exp_f32 v120, v120\n"
    "v_exp_f32 v121, v121\n"
    "v_exp_f32 v122, v122\n"
    "s_waitcnt lgkmcnt(13)\n"
    "v_mfma_f32_32x32x16_bf16 a[32:47], a[164:167], v[168:171], 0\n"
    "ds_read_b128 a[132:135], v23 offset:49152\n"
    "v_add_f32 v26, v26, v120\n"
    "v_add_f32 v26, v26, v121\n"
    "v_add_f32 v26, v26, v122\n"
    "v_exp_f32 v123, v123\n"
    "v_exp_f32 v124, v124\n"
    "v_exp_f32 v125, v125\n"
    "v_cvt_pk_bf16_f32 v176, v120, v121\n"
    "s_waitcnt lgkmcnt(12)\n"
    "v_mfma_f32_32x32x16_bf16 a[64:79], a[168:171], v[168:171], 0\n"
    "ds_read_b128 a[136:139], v22 offset:49664\n"
    "v_add_f32 v26, v26, v123\n"
    "v_add_f32 v26, v26, v124\n"
    "v_add_f32 v26, v26, v125\n"
    "v_exp_f32 v126, v126\n"
    "v_exp_f32 v127, v127\n"
    "v_cvt_pk_bf16_f32 v177, v122, v123\n"
    "v_cvt_pk_bf16_f32 v178, v124, v125\n"
    "s_waitcnt lgkmcnt(11)\n"
    "v_mfma_f32_32x32x16_bf16 a[96:111], a[172:175], v[168:171], 0\n"
    "ds_read_b128 a[140:143], v23 offset:49664\n"
    "v_add_f32 v26, v26, v126\n"
    "v_add_f32 v26, v26, v127\n"
    "v_exp_f32 v128, v128\n"
    "v_exp_f32 v129, v129\n"
    "v_exp_f32 v130, v130\n"
    "v_cvt_pk_bf16_f32 v179, v126, v127\n"
    "s_waitcnt lgkmcnt(10)\n"
    "v_mfma_f32_32x32x16_bf16 a[0:15], a[176:179], v[172:175], a[0:15]\n"
    "ds_read_b128 a[144:147], v22 offset:50176\n"
    "v_add_f32 v26, v26, v128\n"
    "v_add_f32 v26, v26, v129\n"
    "v_add_f32 v26, v26, v130\n"
    "v_exp_f32 v131, v131\n"
    "v_exp_f32 v132, v132\n"
    "v_exp_f32 v133, v133\n"
    "v_cvt_pk_bf16_f32 v180, v128, v129\n"
    "s_waitcnt lgkmcnt(9)\n"
    "v_mfma_f32_32x32x16_bf16 a[32:47], a[180:183], v[172:175], a[32:47]\n"
    "ds_read_b128 a[148:151], v23 offset:50176\n"
    "v_add_f32 v26, v26, v131\n"
    "v_add_f32 v26, v26, v132\n"
    "v_add_f32 v26, v26, v133\n"
    "v_exp_f32 v134, v134\n"
    "v_exp_f32 v135, v135\n"
    "v_cvt_pk_bf16_f32 v181, v130, v131\n"
    "v_cvt_pk_bf16_f32 v182, v132, v133\n"
    "s_waitcnt lgkmcnt(8)\n"
    "v_mfma_f32_32x32x16_bf16 a[64:79], a[184:187], v[172:175], a[64:79]\n"
    "ds_read_b128 a[152:155], v22 offset:50688\n"
    "v_add_f32 v26, v26, v134\n"
    "v_add_f32 v26, v26, v135\n"
    "v_cvt_pk_bf16_f32 v183, v134, v135\n"
    "s_waitcnt lgkmcnt(7)\n"
    "v_mfma_f32_32x32x16_bf16 a[96:111], a[188:191], v[172:175], a[96:111]\n"
    "ds_read_b128 a[156:159], v23 offset:50688\n"
    "v_exp_f32 v136, v136\n"
    "v_exp_f32 v137, v137\n"
    "v_mfma_f32_32x32x16_bf16 a[16:31], a[160:163], v[176:179], 0\n"
    "v_exp_f32 v138, v138\n"
    "v_add_f32 v25, v25, v136\n"
    "v_add_f32 v25, v25, v137\n"
    "v_cvt_pk_bf16_f32 v184, v136, v137\n"
    "v_mfma_f32_32x32x16_bf16 a[48:63], a[164:167], v[176:179], 0\n"
    "v_exp_f32 v139, v139\n"
    "v_add_f32 v25, v25, v138\n"
    "v_mfma_f32_32x32x16_bf16 a[80:95], a[168:171], v[176:179], 0\n"
    "v_exp_f32 v140, v140\n"
    "v_exp_f32 v141, v141\n"
    "v_add_f32 v25, v25, v139\n"
    "v_cvt_pk_bf16_f32 v185, v138, v139\n"
    "v_lshrrev_b32 v39, 4, v1\n"
    "v_min_u32 v38, 59, v39\n"
    "v_mfma_f32_32x32x16_bf16 a[112:127], a[172:175], v[176:179], 0\n"
    "v_exp_f32 v142, v142\n"
    "v_add_f32 v25, v25, v140\n"
    "v_add_f32 v25, v25, v141\n"
    "v_cvt_pk_bf16_f32 v186, v140, v141\n"
    "v_lshlrev_b32 v38, 20, v38\n"
    "global_load_dword v36, v38, s[26:27]\n"
    "v_mfma_f32_32x32x16_bf16 a[16:31], a[176:179], v[180:183], a[16:31]\n"
    "v_exp_f32 v143, v143\n"
    "v_add_f32 v25, v25, v142\n"
    "v_min_u32 v38, 2, v39\n"
    "v_lshlrev_b32 v38, 20, v38\n"
    "v_mfma_f32_32x32x16_bf16 a[48:63], a[180:183], v[180:183], a[48:63]\n"
    "v_exp_f32 v144, v144\n"
    "v_add_f32 v25, v25, v143\n"
    "v_cvt_pk_bf16_f32 v187, v142, v143\n"
    "global_load_dword v36, v38, s[24:25]\n"
    "v_mfma_f32_32x32x16_bf16 a[80:95], a[184:187], v[180:183], a[80:95]\n"
    "v_add_u32 v39, 0x14000, v235\n"
    "global_load_dword v36, v39, s[42:43]\n"
    "v_exp_f32 v145, v145\n"
    "v_exp_f32 v146, v146\n"
    "v_add_f32 v25, v25, v144\n"
    "v_mfma_f32_32x32x16_bf16 a[112:127], a[188:191], v[180:183], a[112:127]\n"
    "v_exp_f32 v147, v147\n"
    "v_add_f32 v25, v25, v145\n"
    "v_add_f32 v25, v25, v146\n"
    "v_cvt_pk_bf16_f32 v188, v144, v145\n"
    "s_waitcnt lgkmcnt(7)\n"
    "v_mfma_f32_32x32x16_bf16 v[104:119], a[128:131], v[40:43], v[200:215]\n"
    "s_mov_b32 s83, 0xc000\n"
    "s_add_u32 m0, s81, 0x4000\n"
    "ds_read_b64_tr_b16 a[160:161], v18 offset:8192\n"
    "v_exp_f32 v148, v148\n"
    "v_add_f32 v25, v25, v147\n"
    "v_cvt_pk_bf16_f32 v189, v146, v147\n"
    "buffer_load_dwordx4 v6, s[56:59], s83 offen lds\n"
    "s_waitcnt lgkmcnt(7)\n"
    "v_mfma_f32_32x32x16_bf16 v[104:119], a[132:135], v[44:47], v[104:119]\n"
    "ds_read_b64_tr_b16 a[162:163], v18 offset:9216\n"
    "v_exp_f32 v149, v149\n"
    "v_exp_f32 v150, v150\n"
    "v_add_f32 v25, v25, v148\n"
    "s_waitcnt lgkmcnt(7)\n"
    "v_mfma_f32_32x32x16_bf16 v[104:119], a[136:139], v[48:51], v[104:119]\n"
    "s_add_u32 m0, s81, 0x4400\n"
    "ds_read_b64_tr_b16 a[164:165], v19 offset:8192\n"
    "v_exp_f32 v151, v151\n"
    "v_add_f32 v25, v25, v149\n"
    "v_add_f32 v25, v25, v150\n"
    "v_cvt_pk_bf16_f32 v190, v148, v149\n"
    "buffer_load_dwordx4 v7, s[56:59], s83 offen lds\n"
    "s_waitcnt lgkmcnt(7)\n"
    "v_mfma_f32_32x32x16_bf16 v[104:119], a[140:143], v[52:55], v[104:119]\n"
    "ds_read_b64_tr_b16 a[166:167], v19 offset:9216\n"
    "v_exp_f32 v152, v152\n"
    "v_add_f32 v25, v25, v151\n"
    "v_cvt_pk_bf16_f32 v191, v150, v151\n"
    "s_waitcnt lgkmcnt(7)\n"
    "v_mfma_f32_32x32x16_bf16 v[104:119], a[144:147], v[56:59], v[104:119]\n"
    "s_add_u32 m0, s81, 0x6000\n"
    "ds_read_b64_tr_b16 a[168:169], v20 offset:8192\n"
    "v_exp_f32 v153, v153\n"
    "v_add_f32 v26, v26, v152\n"
    "buffer_load_dwordx4 v8, s[56:59], s83 offen lds\n"
    "s_waitcnt lgkmcnt(7)\n"
    "v_mfma_f32_32x32x16_bf16 v[104:119], a[148:151], v[60:63], v[104:119]\n"
    "ds_read_b64_tr_b16 a[170:171], v20 offset:9216\n"
    "v_exp_f32 v154, v154\n"
    "v_exp_f32 v155, v155\n"
    "v_add_f32 v26, v26, v153\n"
    "v_cvt_pk_bf16_f32 v192, v152, v153\n"
    "s_waitcnt lgkmcnt(7)\n"
    "v_mfma_f32_32x32x16_bf16 v[104:119], a[152:155], v[64:67], v[104:119]\n"
    "s_add_u32 m0, s81, 0x6400\n"
    "ds_read_b64_tr_b16 a[172:173], v21 offset:8192\n"
    "v_exp_f32 v156, v156\n"
    "v_add_f32 v26, v26, v154\n"
    "v_add_f32 v26, v26, v155\n"
    "v_cvt_pk_bf16_f32 v193, v154, v155\n"
    "buffer_load_dwordx4 v9, s[56:59], s83 offen lds\n"
    "s_waitcnt lgkmcnt(7)\n"
    "v_mfma_f32_32x32x16_bf16 v[104:119], a[156:159], v[68:71], v[104:119]\n"
    "ds_read_b64_tr_b16 a[174:175], v21 offset:9216\n"
    "v_exp_f32 v157, v157\n"
    "v_add_f32 v26, v26, v156\n"
    "v_mfma_f32_32x32x16_bf16 v[120:135], a[128:131], v[72:75], v[216:231]\n"
    "s_mov_b32 s83, 0x8000\n"
    "s_add_u32 m0, s80, 0x18000\n"
    "ds_read_b64_tr_b16 a[176:177], v18 offset:12288\n"
    "v_exp_f32 v158, v158\n"
    "v_exp_f32 v159, v159\n"
    "v_add_f32 v26, v26, v157\n"
    "v_cvt_pk_bf16_f32 v194, v156, v157\n"
    "buffer_load_dwordx4 v10, s[60:63], s83 offen lds\n"
    "v_mfma_f32_32x32x16_bf16 v[120:135], a[132:135], v[76:79], v[120:135]\n"
    "ds_read_b64_tr_b16 a[178:179], v18 offset:13312\n"
    "v_exp_f32 v160, v160\n"
    "v_add_f32 v26, v26, v158\n"
    "v_add_f32 v26, v26, v159\n"
    "v_cvt_pk_bf16_f32 v195, v158, v159\n"
    "v_mfma_f32_32x32x16_bf16 v[120:135], a[136:139], v[80:83], v[120:135]\n"
    "s_add_u32 m0, s80, 0x18400\n"
    "ds_read_b64_tr_b16 a[180:181], v19 offset:12288\n"
    "v_exp_f32 v161, v161\n"
    "v_add_f32 v26, v26, v160\n"
    "buffer_load_dwordx4 v11, s[60:63], s83 offen lds\n"
    "v_mfma_f32_32x32x16_bf16 v[120:135], a[140:143], v[84:87], v[120:135]\n"
    "ds_read_b64_tr_b16 a[182:183], v19 offset:13312\n"
    "v_exp_f32 v162, v162\n"
    "v_add_f32 v26, v26, v161\n"
    "v_cvt_pk_bf16_f32 v196, v160, v161\n"
    "v_mfma_f32_32x32x16_bf16 v[120:135], a[144:147], v[88:91], v[120:135]\n"
    "s_add_u32 m0, s80, 0x18800\n"
    "ds_read_b64_tr_b16 a[184:185], v20 offset:12288\n"
    "v_exp_f32 v163, v163\n"
    "v_exp_f32 v164, v164\n"
    "v_add_f32 v26, v26, v162\n"
    "v_exp_f32 v104, v104\n"
    "buffer_load_dwordx4 v12, s[60:63], s83 offen lds\n"
    "v_mfma_f32_32x32x16_bf16 v[120:135], a[148:151], v[92:95], v[120:135]\n"
    "ds_read_b64_tr_b16 a[186:187], v20 offset:13312\n"
    "v_exp_f32 v165, v165\n"
    "v_add_f32 v26, v26, v163\n"
    "v_add_f32 v26, v26, v164\n"
    "v_cvt_pk_bf16_f32 v197, v162, v163\n"
    "v_exp_f32 v105, v105\n"
    "v_add_f32 v25, v25, v104\n"
    "v_mfma_f32_32x32x16_bf16 v[120:135], a[152:155], v[96:99], v[120:135]\n"
    "s_add_u32 m0, s80, 0x18c00\n"
    "ds_read_b64_tr_b16 a[188:189], v21 offset:12288\n"
    "v_exp_f32 v166, v166\n"
    "v_add_f32 v26, v26, v165\n"
    "v_cvt_pk_bf16_f32 v198, v164, v165\n"
    "v_exp_f32 v106, v106\n"
    "v_add_f32 v25, v25, v105\n"
    "v_cvt_pk_bf16_f32 v168, v104, v105\n"
    "buffer_load_dwordx4 v13, s[60:63], s83 offen lds\n"
    "v_mfma_f32_32x32x16_bf16 v[120:135], a[156:159], v[100:103], v[120:135]\n"
    "ds_read_b64_tr_b16 a[190:191], v21 offset:13312\n"
    "v_exp_f32 v167, v167\n"
    "v_add_f32 v26, v26, v166\n"
    "v_exp_f32 v107, v107\n"
    "v_add_f32 v25, v25, v106\n"
    "s_waitcnt lgkmcnt(14)\n"
    "v_mfma_f32_32x32x16_bf16 a[0:15], a[160:163], v[184:187], a[0:15]\n"
    "s_mul_i32 s93, s95, 0\n"
    "s_add_u32 m0, s82, 0\n"
    "ds_read_b128 a[128:131], v22 offset:57344\n"
    "v_add_f32 v26, v26, v167\n"
    "v_cvt_pk_bf16_f32 v199, v166, v167\n"
    "v_exp_f32 v108, v108\n"
    "v_add_f32 v25, v25, v107\n"
    "v_cvt_pk_bf16_f32 v169, v106, v107\n"
    "buffer_load_dwordx4 v232, s[72:75], s93 offen lds\n"
    "v_mfma_f32_32x32x16_bf16 a[16:31], a[160:163], v[192:195], a[16:31]\n"
    "ds_read_b128 a[132:135], v23 offset:57344\n"
    "v_exp_f32 v109, v109\n"
    "v_add_f32 v25, v25, v108\n"
    "s_waitcnt lgkmcnt(14)\n"
    "v_mfma_f32_32x32x16_bf16 a[32:47], a[164:167], v[184:187], a[32:47]\n"
    "s_mul_i32 s93, s95, 1\n"
    "s_add_u32 m0, s82, 0x400\n"
    "ds_read_b128 a[136:139], v22 offset:57856\n"
    "v_exp_f32 v110, v110\n"
    "v_add_f32 v25, v25, v109\n"
    "v_cvt_pk_bf16_f32 v170, v108, v109\n"
    "buffer_load_dwordx4 v232, s[72:75], s93 offen lds\n"
    "v_mfma_f32_32x32x16_bf16 a[48:63], a[164:167], v[192:195], a[48:63]\n"
    "ds_read_b128 a[140:143], v23 offset:57856\n"
    "v_exp_f32 v111, v111\n"
    "v_add_f32 v25, v25, v110\n"
    "s_waitcnt lgkmcnt(14)\n"
    "v_mfma_f32_32x32x16_bf16 a[64:79], a[168:171], v[184:187], a[64:79]\n"
    "s_mul_i32 s93, s95, 2\n"
    "s_add_u32 m0, s82, 0x800\n"
    "ds_read_b128 a[144:147], v22 offset:58368\n"
    "v_exp_f32 v112, v112\n"
    "v_add_f32 v25, v25, v111\n"
    "v_cvt_pk_bf16_f32 v171, v110, v111\n"
    "buffer_load_dwordx4 v232, s[72:75], s93 offen lds\n"
    "v_mfma_f32_32x32x16_bf16 a[80:95], a[168:171], v[192:195], a[80:95]\n"
    "ds_read_b128 a[148:151], v23 offset:58368\n"
    "v_exp_f32 v113, v113\n"
    "v_add_f32 v25, v25, v112\n"
    "s_waitcnt lgkmcnt(14)\n"
    "v_mfma_f32_32x32x16_bf16 a[96:111], a[172:175], v[184:187], a[96:111]\n"
    "s_mul_i32 s93, s95, 3\n"
    "s_add_u32 m0, s82, 0xc00\n"
    "ds_read_b128 a[152:155], v22 offset:58880\n"
    "v_exp_f32 v114, v114\n"
    "v_add_f32 v25, v25, v113\n"
    "v_cvt_pk_bf16_f32 v172, v112, v113\n"
    "buffer_load_dwordx4 v232, s[72:75], s93 offen lds\n"
    "v_mfma_f32_32x32x16_bf16 a[112:127], a[172:175], v[192:195], a[112:127]\n"
    "ds_read_b128 a[156:159], v23 offset:58880\n"
    "v_exp_f32 v115, v115\n"
    "v_add_f32 v25, v25, v114\n"
    "s_waitcnt lgkmcnt(14)\n"
    "v_mfma_f32_32x32x16_bf16 a[0:15], a[176:179], v[188:191], a[0:15]\n"
    "s_add_u32 s84, s96, 0x400\n"
    "s_mov_b64 s[72:73], s[16:17]\n"
    "s_mov_b32 s89, 0x500\n"
    "s_mov_b32 s90, 40\n"
    "v_exp_f32 v116, v116\n"
    "v_add_f32 v25, v25, v115\n"
    "v_cvt_pk_bf16_f32 v173, v114, v115\n"
    "s_mul_i32 s46, s96, 0x2800\n"
    "s_add_u32 s46, s46, 0x1e70000\n"
    "v_mfma_f32_32x32x16_bf16 a[16:31], a[176:179], v[196:199], a[16:31]\n"
    "s_mov_b32 s85, 0\n"
    "s_mov_b32 s97, 0x199a\n"
    "s_mov_b32 s91, 10\n"
    "s_mov_b32 s92, 0x500000\n"
    "v_exp_f32 v117, v117\n"
    "v_add_f32 v25, v25, v116\n"
    "v_add_u32 v35, s46, v1\n"
    "v_mov_b32 v252, -1\n"
    "s_waitcnt lgkmcnt(12)\n"
    "v_mfma_f32_32x32x16_bf16 a[32:47], a[180:183], v[188:191], a[32:47]\n"
    "s_sub_u32 s93, s84, 0\n"
    "s_sub_u32 s94, s84, 0x500\n"
    "s_cmp_ge_u32 s84, 0x500\n"
    "s_cselect_b64 s[72:73], s[18:19], s[72:73]\n"
    "v_exp_f32 v118, v118\n"
    "v_add_f32 v25, v25, v117\n"
    "v_cvt_pk_bf16_f32 v174, v116, v117\n"
    "v_mov_b32 v253, -1\n"
    "v_mov_b32 v254, -1\n"
    "v_mfma_f32_32x32x16_bf16 a[48:63], a[180:183], v[196:199], a[48:63]\n"
    "s_cselect_b32 s93, s94, s93\n"
    "s_cselect_b32 s89, 0x500, s89\n"
    "s_cselect_b32 s90, 40, s90\n"
    "s_cselect_b32 s85, 0x500000, s85\n"
    "v_exp_f32 v119, v119\n"
    "v_add_f32 v25, v25, v118\n"
    "v_mov_b32 v255, -1\n"
    "s_waitcnt lgkmcnt(10)\n"
    "v_mfma_f32_32x32x16_bf16 a[64:79], a[184:187], v[188:191], a[64:79]\n"
    "s_cselect_b32 s97, 0x199a, s97\n"
    "s_cselect_b32 s91, 10, s91\n"
    "s_cselect_b32 s92, 0x21c000, s92\n"
    "s_sub_u32 s94, s84, 0x71c\n"
    "v_exp_f32 v120, v120\n"
    "v_add_f32 v25, v25, v119\n"
    "v_cvt_pk_bf16_f32 v175, v118, v119\n"
    "v_mfma_f32_32x32x16_bf16 a[80:95], a[184:187], v[196:199], a[80:95]\n"
    "s_cmp_ge_u32 s84, 0x71c\n"
    "s_cselect_b64 s[72:73], s[20:21], s[72:73]\n"
    "s_cselect_b32 s93, s94, s93\n"
    "s_cselect_b32 s89, 0xd80, s89\n"
    "v_exp_f32 v121, v121\n"
    "v_add_f32 v26, v26, v120\n"
    "s_waitcnt lgkmcnt(8)\n"
    "v_mfma_f32_32x32x16_bf16 a[96:111], a[188:191], v[188:191], a[96:111]\n"
    "s_cselect_b32 s90, 0x6c, s90\n"
    "s_cselect_b32 s85, 0x71c000, s85\n"
    "s_cselect_b32 s97, 0x97c, s97\n"
    "s_cselect_b32 s91, 27, s91\n"
    "v_exp_f32 v122, v122\n"
    "v_add_f32 v26, v26, v121\n"
    "v_cvt_pk_bf16_f32 v176, v120, v121\n"
    "v_mfma_f32_32x32x16_bf16 a[112:127], a[188:191], v[196:199], a[112:127]\n"
    "s_waitcnt vmcnt(15)\n"
    "s_waitcnt lgkmcnt(0)\n"
    "s_barrier\n"
    "s_cselect_b32 s92, 0x21c000, s92\n"
    "s_mov_b32 s94, s93\n"
    "s_mul_i32 s93, s93, s97\n"
    "s_lshr_b32 s93, s93, 16\n"
    "v_exp_f32 v123, v123\n"
    "v_add_f32 v26, v26, v122\n"
    "v_mfma_f32_32x32x16_bf16 v[136:151], a[128:131], v[40:43], v[200:215]\n"
    "s_mov_b32 s83, 0x10000\n"
    "s_add_u32 m0, s81, 0x8000\n"
    "s_mul_i32 s46, s93, s91\n"
    "s_sub_u32 s94, s94, s46\n"
    "s_mul_i32 s46, s93, s89\n"
    "s_lshl_b32 s46, s46, 5\n"
    "ds_read_b64_tr_b16 a[160:161], v18 offset:16384\n"
    "v_exp_f32 v124, v124\n"
    "v_add_f32 v26, v26, v123\n"
    "v_cvt_pk_bf16_f32 v177, v122, v123\n"
    "buffer_load_dwordx4 v6, s[56:59], s83 offen lds\n"
    "v_mfma_f32_32x32x16_bf16 v[136:151], a[132:135], v[44:47], v[136:151]\n"
    "s_lshl_b32 s47, s94, 7\n"
    "s_add_u32 s46, s46, s47\n"
    "s_add_u32 s72, s72, s46\n"
    "s_addc_u32 s73, s73, 0\n"
    "ds_read_b64_tr_b16 a[162:163], v18 offset:17408\n"
    "v_exp_f32 v125, v125\n"
    "v_add_f32 v26, v26, v124\n"
    "v_mfma_f32_32x32x16_bf16 v[136:151], a[136:139], v[48:51], v[136:151]\n"
    "s_add_u32 m0, s81, 0x8400\n"
    "s_sub_u32 s74, s92, s46\n"
    "s_mov_b32 s75, 0x20000\n"
    "s_mul_i32 s46, s93, s90\n"
    "s_lshl_b32 s47, s94, 2\n"
    "ds_read_b64_tr_b16 a[164:165], v19 offset:16384\n"
    "v_exp_f32 v126, v126\n"
    "v_add_f32 v26, v26, v125\n"
    "v_cvt_pk_bf16_f32 v178, v124, v125\n"
    "buffer_load_dwordx4 v7, s[56:59], s83 offen lds\n"
    "v_mfma_f32_32x32x16_bf16 v[136:151], a[140:143], v[52:55], v[136:151]\n"
    "s_add_u32 s46, s46, s47\n"
    "s_lshl_b32 s46, s46, 10\n"
    "s_add_u32 s46, s46, s85\n"
    "s_add_u32 s46, s46, 0x3000000\n"
    "ds_read_b64_tr_b16 a[166:167], v19 offset:17408\n"
    "v_exp_f32 v127, v127\n"
    "v_add_f32 v26, v26, v126\n"
    "v_mfma_f32_32x32x16_bf16 v[136:151], a[144:147], v[56:59], v[136:151]\n"
    "s_add_u32 m0, s81, 0xa000\n"
    "s_add_u32 s64, s26, s46\n"
    "s_addc_u32 s65, s27, 0\n"
    "s_mov_b32 s66, 0x1000\n"
    "s_mov_b32 s67, 0x20000\n"
    "ds_read_b64_tr_b16 a[168:169], v20 offset:16384\n"
    "v_exp_f32 v128, v128\n"
    "v_add_f32 v26, v26, v127\n"
    "v_cvt_pk_bf16_f32 v179, v126, v127\n"
    "buffer_load_dwordx4 v8, s[56:59], s83 offen lds\n"
    "v_mfma_f32_32x32x16_bf16 v[136:151], a[148:151], v[60:63], v[136:151]\n"
    "s_lshl_b32 s95, s89, 3\n"
    "v_lshrrev_b32 v232, 7, v1\n"
    "v_mul_u32_u24 v232, s89, v232\n"
    "v_and_b32 v37, 0x70, v1\n"
    "ds_read_b64_tr_b16 a[170:171], v20 offset:17408\n"
    "v_exp_f32 v129, v129\n"
    "v_add_f32 v26, v26, v128\n"
    "v_mfma_f32_32x32x16_bf16 v[136:151], a[152:155], v[64:67], v[136:151]\n"
    "s_add_u32 m0, s81, 0xa400\n"
    "v_add_u32 v232, v232, v37\n"
    "ds_read_b64_tr_b16 a[172:173], v21 offset:16384\n"
    "v_exp_f32 v130, v130\n"
    "v_add_f32 v26, v26, v129\n"
    "v_cvt_pk_bf16_f32 v180, v128, v129\n"
    "buffer_load_dwordx4 v9, s[56:59], s83 offen lds\n"
    "v_mfma_f32_32x32x16_bf16 v[136:151], a[156:159], v[68:71], v[136:151]\n"
    "ds_read_b64_tr_b16 a[174:175], v21 offset:17408\n"
    "v_exp_f32 v131, v131\n"
    "v_add_f32 v26, v26, v130\n"
    "v_mfma_f32_32x32x16_bf16 v[152:167], a[128:131], v[72:75], v[216:231]\n"
    "s_mov_b32 s83, 0xc000\n"
    "s_add_u32 m0, s80, 0x1c000\n"
    "ds_read_b64_tr_b16 a[176:177], v18 offset:20480\n"
    "v_exp_f32 v132, v132\n"
    "v_add_f32 v26, v26, v131\n"
    "v_cvt_pk_bf16_f32 v181, v130, v131\n"
    "buffer_load_dwordx4 v10, s[60:63], s83 offen lds\n"
    "v_mfma_f32_32x32x16_bf16 v[152:167], a[132:135], v[76:79], v[152:167]\n"
    "ds_read_b64_tr_b16 a[178:179], v18 offset:21504\n"
    "v_exp_f32 v133, v133\n"
    "v_add_f32 v26, v26, v132\n"
    "v_mfma_f32_32x32x16_bf16 v[152:167], a[136:139], v[80:83], v[152:167]\n"
    "s_add_u32 m0, s80, 0x1c400\n"
    "ds_read_b64_tr_b16 a[180:181], v19 offset:20480\n"
    "v_exp_f32 v134, v134\n"
    "v_add_f32 v26, v26, v133\n"
    "v_cvt_pk_bf16_f32 v182, v132, v133\n"
    "buffer_load_dwordx4 v11, s[60:63], s83 offen lds\n"
    "v_mfma_f32_32x32x16_bf16 v[152:167], a[140:143], v[84:87], v[152:167]\n"
    "ds_read_b64_tr_b16 a[182:183], v19 offset:21504\n"
    "v_exp_f32 v135, v135\n"
    "v_add_f32 v26, v26, v134\n"
    "v_mfma_f32_32x32x16_bf16 v[152:167], a[144:147], v[88:91], v[152:167]\n"
    "s_add_u32 m0, s80, 0x1c800\n"
    "ds_read_b64_tr_b16 a[184:185], v20 offset:20480\n"
    "v_add_f32 v26, v26, v135\n"
    "v_cvt_pk_bf16_f32 v183, v134, v135\n"
    "v_exp_f32 v136, v136\n"
    "buffer_load_dwordx4 v12, s[60:63], s83 offen lds\n"
    "v_mfma_f32_32x32x16_bf16 v[152:167], a[148:151], v[92:95], v[152:167]\n"
    "ds_read_b64_tr_b16 a[186:187], v20 offset:21504\n"
    "v_exp_f32 v137, v137\n"
    "v_add_f32 v25, v25, v136\n"
    "v_mfma_f32_32x32x16_bf16 v[152:167], a[152:155], v[96:99], v[152:167]\n"
    "s_add_u32 m0, s80, 0x1cc00\n"
    "ds_read_b64_tr_b16 a[188:189], v21 offset:20480\n"
    "v_exp_f32 v138, v138\n"
    "v_add_f32 v25, v25, v137\n"
    "v_cvt_pk_bf16_f32 v184, v136, v137\n"
    "buffer_load_dwordx4 v13, s[60:63], s83 offen lds\n"
    "v_mfma_f32_32x32x16_bf16 v[152:167], a[156:159], v[100:103], v[152:167]\n"
    "ds_read_b64_tr_b16 a[190:191], v21 offset:21504\n"
    "v_exp_f32 v139, v139\n"
    "v_add_f32 v25, v25, v138\n"
    "s_waitcnt lgkmcnt(14)\n"
    "v_mfma_f32_32x32x16_bf16 a[0:15], a[160:163], v[168:171], a[0:15]\n"
    "s_mul_i32 s93, s95, 0\n"
    "s_add_u32 m0, s82, 0x1000\n"
    "ds_read_b128 a[128:131], v22 offset:0\n"
    "v_exp_f32 v140, v140\n"
    "v_add_f32 v25, v25, v139\n"
    "v_cvt_pk_bf16_f32 v185, v138, v139\n"
    "buffer_load_dwordx4 v232, s[72:75], s93 offen lds\n"
    "v_mfma_f32_32x32x16_bf16 a[16:31], a[160:163], v[176:179], a[16:31]\n"
    "ds_read_b128 a[132:135], v23 offset:0\n"
    "v_exp_f32 v141, v141\n"
    "v_add_f32 v25, v25, v140\n"
    "s_waitcnt lgkmcnt(14)\n"
    "v_mfma_f32_32x32x16_bf16 a[32:47], a[164:167], v[168:171], a[32:47]\n"
    "s_mul_i32 s93, s95, 1\n"
    "s_add_u32 m0, s82, 0x1400\n"
    "ds_read_b128 a[136:139], v22 offset:512\n"
    "v_exp_f32 v142, v142\n"
    "v_add_f32 v25, v25, v141\n"
    "v_cvt_pk_bf16_f32 v186, v140, v141\n"
    "buffer_load_dwordx4 v232, s[72:75], s93 offen lds\n"
    "v_mfma_f32_32x32x16_bf16 a[48:63], a[164:167], v[176:179], a[48:63]\n"
    "ds_read_b128 a[140:143], v23 offset:512\n"
    "v_exp_f32 v143, v143\n"
    "v_add_f32 v25, v25, v142\n"
    "s_waitcnt lgkmcnt(14)\n"
    "v_mfma_f32_32x32x16_bf16 a[64:79], a[168:171], v[168:171], a[64:79]\n"
    "s_mul_i32 s93, s95, 2\n"
    "s_add_u32 m0, s82, 0x1800\n"
    "ds_read_b128 a[144:147], v22 offset:1024\n"
    "v_exp_f32 v144, v144\n"
    "v_add_f32 v25, v25, v143\n"
    "v_cvt_pk_bf16_f32 v187, v142, v143\n"
    "buffer_load_dwordx4 v232, s[72:75], s93 offen lds\n"
    "v_mfma_f32_32x32x16_bf16 a[80:95], a[168:171], v[176:179], a[80:95]\n"
    "ds_read_b128 a[148:151], v23 offset:1024\n"
    "v_exp_f32 v145, v145\n"
    "v_add_f32 v25, v25, v144\n"
    "s_waitcnt lgkmcnt(14)\n"
    "v_mfma_f32_32x32x16_bf16 a[96:111], a[172:175], v[168:171], a[96:111]\n"
    "s_mul_i32 s93, s95, 3\n"
    "s_add_u32 m0, s82, 0x1c00\n"
    "ds_read_b128 a[152:155], v22 offset:1536\n"
    "v_exp_f32 v146, v146\n"
    "v_add_f32 v25, v25, v145\n"
    "v_cvt_pk_bf16_f32 v188, v144, v145\n"
    "buffer_load_dwordx4 v232, s[72:75], s93 offen lds\n"
    "v_mfma_f32_32x32x16_bf16 a[112:127], a[172:175], v[176:179], a[112:127]\n"
    "ds_read_b128 a[156:159], v23 offset:1536\n"
    "v_exp_f32 v147, v147\n"
    "v_add_f32 v25, v25, v146\n"
    "s_waitcnt lgkmcnt(14)\n"
    "v_mfma_f32_32x32x16_bf16 a[0:15], a[176:179], v[172:175], a[0:15]\n"
    "v_exp_f32 v148, v148\n"
    "v_add_f32 v25, v25, v147\n"
    "v_cvt_pk_bf16_f32 v189, v146, v147\n"
    "v_mfma_f32_32x32x16_bf16 a[16:31], a[176:179], v[180:183], a[16:31]\n"
    "v_exp_f32 v149, v149\n"
    "v_add_f32 v25, v25, v148\n"
    "global_store_dwordx4 v35, v[252:255], s[26:27] offset:0 sc1\n"
    "s_waitcnt lgkmcnt(12)\n"
    "v_mfma_f32_32x32x16_bf16 a[32:47], a[180:183], v[172:175], a[32:47]\n"
    "v_exp_f32 v150, v150\n"
    "v_add_f32 v25, v25, v149\n"
    "v_cvt_pk_bf16_f32 v190, v148, v149\n"
    "global_store_dwordx4 v35, v[252:255], s[26:27] offset:1024 sc1\n"
    "v_mfma_f32_32x32x16_bf16 a[48:63], a[180:183], v[180:183], a[48:63]\n"
    "v_exp_f32 v151, v151\n"
    "v_add_f32 v25, v25, v150\n"
    "global_store_dwordx4 v35, v[252:255], s[26:27] offset:2048 sc1\n"
    "s_waitcnt lgkmcnt(10)\n"
    "v_mfma_f32_32x32x16_bf16 a[64:79], a[184:187], v[172:175], a[64:79]\n"
    "v_exp_f32 v152, v152\n"
    "v_add_f32 v25, v25, v151\n"
    "v_cvt_pk_bf16_f32 v191, v150, v151\n"
    "global_store_dwordx4 v35, v[252:255], s[26:27] offset:3072 sc1\n"
    "v_mfma_f32_32x32x16_bf16 a[80:95], a[184:187], v[180:183], a[80:95]\n"
    "v_exp_f32 v153, v153\n"
    "v_add_f32 v26, v26, v152\n"
    "v_add_u32 v35, 0x1000, v35\n"
    "global_store_dwordx4 v35, v[252:255], s[26:27] offset:0 sc1\n"
    "s_waitcnt lgkmcnt(8)\n"
    "v_mfma_f32_32x32x16_bf16 a[96:111], a[188:191], v[172:175], a[96:111]\n"
    "v_add_u32 v39, 0x18000, v235\n"
    "global_load_dword v36, v39, s[42:43]\n"
    "v_exp_f32 v154, v154\n"
    "v_add_f32 v26, v26, v153\n"
    "v_cvt_pk_bf16_f32 v192, v152, v153\n"
    "global_store_dwordx4 v35, v[252:255], s[26:27] offset:1024 sc1\n"
    "v_mfma_f32_32x32x16_bf16 a[112:127], a[188:191], v[180:183], a[112:127]\n"
    "v_exp_f32 v155, v155\n"
    "v_add_f32 v26, v26, v154\n"
    "global_store_dwordx4 v35, v[252:255], s[26:27] offset:2048 sc1\n"
    "s_waitcnt lgkmcnt(7)\n"
    "v_mfma_f32_32x32x16_bf16 v[104:119], a[128:131], v[40:43], v[200:215]\n"
    "ds_read_b64_tr_b16 a[160:161], v18 offset:24576\n"
    "v_exp_f32 v156, v156\n"
    "v_add_f32 v26, v26, v155\n"
    "v_cvt_pk_bf16_f32 v193, v154, v155\n"
    "global_store_dwordx4 v35, v[252:255], s[26:27] offset:3072 sc1\n"
    "s_waitcnt lgkmcnt(7)\n"
    "v_mfma_f32_32x32x16_bf16 v[104:119], a[132:135], v[44:47], v[104:119]\n"
    "ds_read_b64_tr_b16 a[162:163], v18 offset:25600\n"
    "v_exp_f32 v157, v157\n"
    "v_add_f32 v26, v26, v156\n"
    "s_waitcnt lgkmcnt(7)\n"
    "v_mfma_f32_32x32x16_bf16 v[104:119], a[136:139], v[48:51], v[104:119]\n"
    "ds_read_b64_tr_b16 a[164:165], v19 offset:24576\n"
    "v_exp_f32 v158, v158\n"
    "v_add_f32 v26, v26, v157\n"
    "v_cvt_pk_bf16_f32 v194, v156, v157\n"
    "v_add_u32 v35, 0x1000, v35\n"
    "global_store_dwordx4 v35, v[252:255], s[26:27] offset:0 sc1\n"
    "s_waitcnt lgkmcnt(7)\n"
    "v_mfma_f32_32x32x16_bf16 v[104:119], a[140:143], v[52:55], v[104:119]\n"
    "ds_read_b64_tr_b16 a[166:167], v19 offset:25600\n"
    "v_exp_f32 v159, v159\n"
    "v_add_f32 v26, v26, v158\n"
    "s_waitcnt lgkmcnt(7)\n"
    "v_mfma_f32_32x32x16_bf16 v[104:119], a[144:147], v[56:59], v[104:119]\n"
    "ds_read_b64_tr_b16 a[168:169], v20 offset:24576\n"
    "v_exp_f32 v160, v160\n"
    "v_add_f32 v26, v26, v159\n"
    "v_cvt_pk_bf16_f32 v195, v158, v159\n"
    "global_store_dwordx4 v35, v[252:255], s[26:27] offset:1024 sc1\n"
    "s_waitcnt lgkmcnt(7)\n"
    "v_mfma_f32_32x32x16_bf16 v[104:119], a[148:151], v[60:63], v[104:119]\n"
    "ds_read_b64_tr_b16 a[170:171], v20 offset:25600\n"
    "v_exp_f32 v161, v161\n"
    "v_add_f32 v26, v26, v160\n"
    "s_waitcnt lgkmcnt(7)\n"
    "v_mfma_f32_32x32x16_bf16 v[104:119], a[152:155], v[64:67], v[104:119]\n"
    "ds_read_b64_tr_b16 a[172:173], v21 offset:24576\n"
    "v_exp_f32 v162, v162\n"
    "v_add_f32 v26, v26, v161\n"
    "v_cvt_pk_bf16_f32 v196, v160, v161\n"
    "s_waitcnt lgkmcnt(7)\n"
    "v_mfma_f32_32x32x16_bf16 v[104:119], a[156:159], v[68:71], v[104:119]\n"
    "ds_read_b64_tr_b16 a[174:175], v21 offset:25600\n"
    "v_exp_f32 v163, v163\n"
    "v_add_f32 v26, v26, v162\n"
    "v_mfma_f32_32x32x16_bf16 v[120:135], a[128:131], v[72:75], v[216:231]\n"
    "ds_read_b64_tr_b16 a[176:177], v18 offset:28672\n"
    "v_exp_f32 v164, v164\n"
    "v_add_f32 v26, v26, v163\n"
    "v_cvt_pk_bf16_f32 v197, v162, v163\n"
    "v_mfma_f32_32x32x16_bf16 v[120:135], a[132:135], v[76:79], v[120:135]\n"
    "ds_read_b64_tr_b16 a[178:179], v18 offset:29696\n"
    "v_exp_f32 v165, v165\n"
    "v_add_f32 v26, v26, v164\n"
    "v_mfma_f32_32x32x16_bf16 v[120:135], a[136:139], v[80:83], v[120:135]\n"
    "ds_read_b64_tr_b16 a[180:181], v19 offset:28672\n"
    "v_exp_f32 v166, v166\n"
    "v_add_f32 v26, v26, v165\n"
    "v_cvt_pk_bf16_f32 v198, v164, v165\n"
    "v_mfma_f32_32x32x16_bf16 v[120:135], a[140:143], v[84:87], v[120:135]\n"
    "ds_read_b64_tr_b16 a[182:183], v19 offset:29696\n"
    "v_exp_f32 v167, v167\n"
    "v_add_f32 v26, v26, v166\n"
    "v_mfma_f32_32x32x16_bf16 v[120:135], a[144:147], v[88:91], v[120:135]\n"
    "ds_read_b64_tr_b16 a[184:185], v20 offset:28672\n"
    "v_add_f32 v26, v26, v167\n"
    "v_cvt_pk_bf16_f32 v199, v166, v167\n"
    "v_exp_f32 v104, v104\n"
    "v_mfma_f32_32x32x16_bf16 v[120:135], a[148:151], v[92:95], v[120:135]\n"
    "ds_read_b64_tr_b16 a[186:187], v20 offset:29696\n"
    "v_exp_f32 v105, v105\n"
    "v_add_f32 v25, v25, v104\n"
    "v_mfma_f32_32x32x16_bf16 v[120:135], a[152:155], v[96:99], v[120:135]\n"
    "ds_read_b64_tr_b16 a[188:189], v21 offset:28672\n"
    "v_exp_f32 v106, v106\n"
    "v_add_f32 v25, v25, v105\n"
    "v_cvt_pk_bf16_f32 v168, v104, v105\n"
    "v_mfma_f32_32x32x16_bf16 v[120:135], a[156:159], v[100:103], v[120:135]\n"
    "ds_read_b64_tr_b16 a[190:191], v21 offset:29696\n"
    "v_exp_f32 v107, v107\n"
    "v_add_f32 v25, v25, v106\n"
    "s_waitcnt lgkmcnt(14)\n"
    "v_mfma_f32_32x32x16_bf16 a[0:15], a[160:163], v[184:187], a[0:15]\n"
    "ds_read_b128 a[128:131], v22 offset:8192\n"
    "s_add_u32 s84, s96, 0x800\n"
    "s_mov_b64 s[72:73], s[20:21]\n"
    "s_mov_b32 s89, 0xd80\n"
    "s_mov_b32 s90, 0x6c\n"
    "v_exp_f32 v108, v108\n"
    "v_add_f32 v25, v25, v107\n"
    "v_cvt_pk_bf16_f32 v169, v106, v107\n"
    "v_mfma_f32_32x32x16_bf16 a[16:31], a[160:163], v[192:195], a[16:31]\n"
    "ds_read_b128 a[132:135], v23 offset:8192\n"
    "s_mov_b32 s85, 0x71c000\n"
    "s_mov_b32 s97, 0x97c\n"
    "s_mov_b32 s91, 27\n"
    "s_mov_b32 s92, 0x21c000\n"
    "v_exp_f32 v109, v109\n"
    "v_add_f32 v25, v25, v108\n"
    "s_waitcnt lgkmcnt(14)\n"
    "v_mfma_f32_32x32x16_bf16 a[32:47], a[164:167], v[184:187], a[32:47]\n"
    "ds_read_b128 a[136:139], v22 offset:8704\n"
    "s_sub_u32 s93, s84, 0x71c\n"
    "s_sub_u32 s94, s84, 0x938\n"
    "s_cmp_ge_u32 s84, 0x938\n"
    "s_cselect_b64 s[72:73], s[22:23], s[72:73]\n"
    "v_exp_f32 v110, v110\n"
    "v_add_f32 v25, v25, v109\n"
    "v_cvt_pk_bf16_f32 v170, v108, v109\n"
    "v_mfma_f32_32x32x16_bf16 a[48:63], a[164:167], v[192:195], a[48:63]\n"
    "ds_read_b128 a[140:143], v23 offset:8704\n"
    "s_cselect_b32 s93, s94, s93\n"
    "s_cselect_b32 s89, 0xd80, s89\n"
    "s_cselect_b32 s90, 0x6c, s90\n"
    "s_cselect_b32 s85, 0x938000, s85\n"
    "v_exp_f32 v111, v111\n"
    "v_add_f32 v25, v25, v110\n"
    "s_waitcnt lgkmcnt(14)\n"
    "v_mfma_f32_32x32x16_bf16 a[64:79], a[168:171], v[184:187], a[64:79]\n"
    "ds_read_b128 a[144:147], v22 offset:9216\n"
    "s_cselect_b32 s97, 0x97c, s97\n"
    "s_cselect_b32 s91, 27, s91\n"
    "s_cselect_b32 s92, 0x21c000, s92\n"
    "s_mov_b32 s94, s93\n"
    "v_exp_f32 v112, v112\n"
    "v_add_f32 v25, v25, v111\n"
    "v_cvt_pk_bf16_f32 v171, v110, v111\n"
    "v_mfma_f32_32x32x16_bf16 a[80:95], a[168:171], v[192:195], a[80:95]\n"
    "ds_read_b128 a[148:151], v23 offset:9216\n"
    "s_mul_i32 s93, s93, s97\n"
    "s_lshr_b32 s93, s93, 16\n"
    "s_mul_i32 s46, s93, s91\n"
    "s_sub_u32 s94, s94, s46\n"
    "v_exp_f32 v113, v113\n"
    "v_add_f32 v25, v25, v112\n"
    "s_waitcnt lgkmcnt(14)\n"
    "v_mfma_f32_32x32x16_bf16 a[96:111], a[172:175], v[184:187], a[96:111]\n"
    "ds_read_b128 a[152:155], v22 offset:9728\n"
    "s_mul_i32 s46, s93, s89\n"
    "s_lshl_b32 s46, s46, 5\n"
    "s_lshl_b32 s47, s94, 7\n"
    "s_add_u32 s46, s46, s47\n"
    "v_exp_f32 v114, v114\n"
    "v_add_f32 v25, v25, v113\n"
    "v_cvt_pk_bf16_f32 v172, v112, v113\n"
    "v_mfma_f32_32x32x16_bf16 a[112:127], a[172:175], v[192:195], a[112:127]\n"
    "ds_read_b128 a[156:159], v23 offset:9728\n"
    "s_add_u32 s72, s72, s46\n"
    "s_addc_u32 s73, s73, 0\n"
    "s_sub_u32 s74, s92, s46\n"
    "s_mov_b32 s75, 0x20000\n"
    "v_exp_f32 v115, v115\n"
    "v_add_f32 v25, v25, v114\n"
    "s_waitcnt lgkmcnt(14)\n"
    "v_mfma_f32_32x32x16_bf16 a[0:15], a[176:179], v[188:191], a[0:15]\n"
    "s_mul_i32 s46, s93, s90\n"
    "s_lshl_b32 s47, s94, 2\n"
    "s_add_u32 s46, s46, s47\n"
    "s_lshl_b32 s46, s46, 10\n"
    "v_exp_f32 v116, v116\n"
    "v_add_f32 v25, v25, v115\n"
    "v_cvt_pk_bf16_f32 v173, v114, v115\n"
    "v_mfma_f32_32x32x16_bf16 a[16:31], a[176:179], v[196:199], a[16:31]\n"
    "s_add_u32 s46, s46, s85\n"
    "s_add_u32 s46, s46, 0x3000000\n"
    "s_add_u32 s76, s26, s46\n"
    "s_addc_u32 s77, s27, 0\n"
    "v_exp_f32 v117, v117\n"
    "v_add_f32 v25, v25, v116\n"
    "s_waitcnt lgkmcnt(12)\n"
    "v_mfma_f32_32x32x16_bf16 a[32:47], a[180:183], v[188:191], a[32:47]\n"
    "s_mov_b32 s78, 0x1000\n"
    "s_mov_b32 s79, 0x20000\n"
    "s_cmp_lt_u32 s84, 0xb54\n"
    "s_cselect_b32 s74, s74, 0\n"
    "v_exp_f32 v118, v118\n"
    "v_add_f32 v25, v25, v117\n"
    "v_cvt_pk_bf16_f32 v174, v116, v117\n"
    "v_mfma_f32_32x32x16_bf16 a[48:63], a[180:183], v[196:199], a[48:63]\n"
    "s_cselect_b32 s78, s78, 0\n"
    "s_lshl_b32 s95, s89, 3\n"
    "v_lshrrev_b32 v232, 7, v1\n"
    "v_mul_u32_u24 v232, s89, v232\n"
    "v_exp_f32 v119, v119\n"
    "v_add_f32 v25, v25, v118\n"
    "s_waitcnt lgkmcnt(10)\n"
    "v_mfma_f32_32x32x16_bf16 a[64:79], a[184:187], v[188:191], a[64:79]\n"
    "v_and_b32 v37, 0x70, v1\n"
    "v_add_u32 v232, v232, v37\n"
    "v_exp_f32 v120, v120\n"
    "v_add_f32 v25, v25, v119\n"
    "v_cvt_pk_bf16_f32 v175, v118, v119\n"
    "v_mfma_f32_32x32x16_bf16 a[80:95], a[184:187], v[196:199], a[80:95]\n"
    "v_exp_f32 v121, v121\n"
    "v_add_f32 v26, v26, v120\n"
    "s_waitcnt lgkmcnt(8)\n"
    "v_mfma_f32_32x32x16_bf16 a[96:111], a[188:191], v[188:191], a[96:111]\n"
    "v_exp_f32 v122, v122\n"
    "v_add_f32 v26, v26, v121\n"
    "v_cvt_pk_bf16_f32 v176, v120, v121\n"
    "v_mfma_f32_32x32x16_bf16 a[112:127], a[188:191], v[196:199], a[112:127]\n"
    "s_waitcnt vmcnt(27)\n"
    "s_waitcnt lgkmcnt(0)\n"
    "s_barrier\n"
    "v_exp_f32 v123, v123\n"
    "v_add_f32 v26, v26, v122\n"
    "v_mfma_f32_32x32x16_bf16 v[136:151], a[128:131], v[40:43], v[200:215]\n"
    "s_mov_b32 s83, 0x14000\n"
    "s_add_u32 m0, s81, 0xc000\n"
    "ds_read_b64_tr_b16 a[160:161], v18 offset:32768\n"
    "v_exp_f32 v124, v124\n"
    "v_add_f32 v26, v26, v123\n"
    "v_cvt_pk_bf16_f32 v177, v122, v123\n"
    "buffer_load_dwordx4 v6, s[56:59], s83 offen lds\n"
    "v_mfma_f32_32x32x16_bf16 v[136:151], a[132:135], v[44:47], v[136:151]\n"
    "ds_read_b64_tr_b16 a[162:163], v18 offset:33792\n"
    "v_exp_f32 v125, v125\n"
    "v_add_f32 v26, v26, v124\n"
    "v_mfma_f32_32x32x16_bf16 v[136:151], a[136:139], v[48:51], v[136:151]\n"
    "s_add_u32 m0, s81, 0xc400\n"
    "ds_read_b64_tr_b16 a[164:165], v19 offset:32768\n"
    "v_exp_f32 v126, v126\n"
    "v_add_f32 v26, v26, v125\n"
    "v_cvt_pk_bf16_f32 v178, v124, v125\n"
    "buffer_load_dwordx4 v7, s[56:59], s83 offen lds\n"
    "v_mfma_f32_32x32x16_bf16 v[136:151], a[140:143], v[52:55], v[136:151]\n"
    "ds_read_b64_tr_b16 a[166:167], v19 offset:33792\n"
    "v_exp_f32 v127, v127\n"
    "v_add_f32 v26, v26, v126\n"
    "v_mfma_f32_32x32x16_bf16 v[136:151], a[144:147], v[56:59], v[136:151]\n"
    "s_add_u32 m0, s81, 0xe000\n"
    "ds_read_b64_tr_b16 a[168:169], v20 offset:32768\n"
    "v_exp_f32 v128, v128\n"
    "v_add_f32 v26, v26, v127\n"
    "v_cvt_pk_bf16_f32 v179, v126, v127\n"
    "buffer_load_dwordx4 v8, s[56:59], s83 offen lds\n"
    "v_mfma_f32_32x32x16_bf16 v[136:151], a[148:151], v[60:63], v[136:151]\n"
    "ds_read_b64_tr_b16 a[170:171], v20 offset:33792\n"
    "v_exp_f32 v129, v129\n"
    "v_add_f32 v26, v26, v128\n"
    "v_mfma_f32_32x32x16_bf16 v[136:151], a[152:155], v[64:67], v[136:151]\n"
    "s_add_u32 m0, s81, 0xe400\n"
    "ds_read_b64_tr_b16 a[172:173], v21 offset:32768\n"
    "v_exp_f32 v130, v130\n"
    "v_add_f32 v26, v26, v129\n"
    "v_cvt_pk_bf16_f32 v180, v128, v129\n"
    "buffer_load_dwordx4 v9, s[56:59], s83 offen lds\n"
    "v_mfma_f32_32x32x16_bf16 v[136:151], a[156:159], v[68:71], v[136:151]\n"
    "ds_read_b64_tr_b16 a[174:175], v21 offset:33792\n"
    "v_exp_f32 v131, v131\n"
    "v_add_f32 v26, v26, v130\n"
    "v_mfma_f32_32x32x16_bf16 v[152:167], a[128:131], v[72:75], v[216:231]\n"
    "s_mov_b32 s83, 0x10000\n"
    "s_add_u32 m0, s80, 0x10000\n"
    "ds_read_b64_tr_b16 a[176:177], v18 offset:36864\n"
    "v_exp_f32 v132, v132\n"
    "v_add_f32 v26, v26, v131\n"
    "v_cvt_pk_bf16_f32 v181, v130, v131\n"
    "buffer_load_dwordx4 v10, s[60:63], s83 offen lds\n"
    "v_mfma_f32_32x32x16_bf16 v[152:167], a[132:135], v[76:79], v[152:167]\n"
    "ds_read_b64_tr_b16 a[178:179], v18 offset:37888\n"
    "v_exp_f32 v133, v133\n"
    "v_add_f32 v26, v26, v132\n"
    "v_mfma_f32_32x32x16_bf16 v[152:167], a[136:139], v[80:83], v[152:167]\n"
    "s_add_u32 m0, s80, 0x10400\n"
    "ds_read_b64_tr_b16 a[180:181], v19 offset:36864\n"
    "v_exp_f32 v134, v134\n"
    "v_add_f32 v26, v26, v133\n"
    "v_cvt_pk_bf16_f32 v182, v132, v133\n"
    "buffer_load_dwordx4 v11, s[60:63], s83 offen lds\n"
    "v_mfma_f32_32x32x16_bf16 v[152:167], a[140:143], v[84:87], v[152:167]\n"
    "ds_read_b64_tr_b16 a[182:183], v19 offset:37888\n"
    "v_exp_f32 v135, v135\n"
    "v_add_f32 v26, v26, v134\n"
    "v_mfma_f32_32x32x16_bf16 v[152:167], a[144:147], v[88:91], v[152:167]\n"
    "s_add_u32 m0, s80, 0x10800\n"
    "ds_read_b64_tr_b16 a[184:185], v20 offset:36864\n"
    "v_add_f32 v26, v26, v135\n"
    "v_cvt_pk_bf16_f32 v183, v134, v135\n"
    "v_exp_f32 v136, v136\n"
    "buffer_load_dwordx4 v12, s[60:63], s83 offen lds\n"
    "v_mfma_f32_32x32x16_bf16 v[152:167], a[148:151], v[92:95], v[152:167]\n"
    "ds_read_b64_tr_b16 a[186:187], v20 offset:37888\n"
    "v_exp_f32 v137, v137\n"
    "v_add_f32 v25, v25, v136\n"
    "v_mfma_f32_32x32x16_bf16 v[152:167], a[152:155], v[96:99], v[152:167]\n"
    "s_add_u32 m0, s80, 0x10c00\n"
    "ds_read_b64_tr_b16 a[188:189], v21 offset:36864\n"
    "v_exp_f32 v138, v138\n"
    "v_add_f32 v25, v25, v137\n"
    "v_cvt_pk_bf16_f32 v184, v136, v137\n"
    "buffer_load_dwordx4 v13, s[60:63], s83 offen lds\n"
    "v_mfma_f32_32x32x16_bf16 v[152:167], a[156:159], v[100:103], v[152:167]\n"
    "ds_read_b64_tr_b16 a[190:191], v21 offset:37888\n"
    "v_exp_f32 v139, v139\n"
    "v_add_f32 v25, v25, v138\n"
    "s_waitcnt lgkmcnt(14)\n"
    "v_mfma_f32_32x32x16_bf16 a[0:15], a[160:163], v[168:171], a[0:15]\n"
    "ds_read_b128 a[128:131], v22 offset:16384\n"
    "s_waitcnt vmcnt(31)\n"
    "ds_read_b64_tr_b16 a[192:193], v233 offset:0\n"
    "v_exp_f32 v140, v140\n"
    "v_add_f32 v25, v25, v139\n"
    "v_cvt_pk_bf16_f32 v185, v138, v139\n"
    "v_mfma_f32_32x32x16_bf16 a[16:31], a[160:163], v[176:179], a[16:31]\n"
    "ds_read_b128 a[132:135], v23 offset:16384\n"
    "ds_read_b64_tr_b16 a[194:195], v233 offset:512\n"
    "v_exp_f32 v141, v141\n"
    "v_add_f32 v25, v25, v140\n"
    "s_waitcnt lgkmcnt(15)\n"
    "v_mfma_f32_32x32x16_bf16 a[32:47], a[164:167], v[168:171], a[32:47]\n"
    "ds_read_b128 a[136:139], v22 offset:16896\n"
    "ds_read_b64_tr_b16 a[196:197], v233 offset:8\n"
    "v_exp_f32 v142, v142\n"
    "v_add_f32 v25, v25, v141\n"
    "v_cvt_pk_bf16_f32 v186, v140, v141\n"
    "v_mfma_f32_32x32x16_bf16 a[48:63], a[164:167], v[176:179], a[48:63]\n"
    "ds_read_b128 a[140:143], v23 offset:16896\n"
    "ds_read_b64_tr_b16 a[198:199], v233 offset:520\n"
    "v_exp_f32 v143, v143\n"
    "v_add_f32 v25, v25, v142\n"
    "s_waitcnt lgkmcnt(15)\n"
    "v_mfma_f32_32x32x16_bf16 a[64:79], a[168:171], v[168:171], a[64:79]\n"
    "ds_read_b128 a[144:147], v22 offset:17408\n"
    "ds_read_b64_tr_b16 a[200:201], v233 offset:64\n"
    "v_exp_f32 v144, v144\n"
    "v_add_f32 v25, v25, v143\n"
    "v_cvt_pk_bf16_f32 v187, v142, v143\n"
    "v_mfma_f32_32x32x16_bf16 a[80:95], a[168:171], v[176:179], a[80:95]\n"
    "ds_read_b128 a[148:151], v23 offset:17408\n"
    "ds_read_b64_tr_b16 a[202:203], v233 offset:576\n"
    "v_exp_f32 v145, v145\n"
    "v_add_f32 v25, v25, v144\n"
    "v_mfma_f32_32x32x16_bf16 a[96:111], a[172:175], v[168:171], a[96:111]\n"
    "ds_read_b128 a[152:155], v22 offset:17920\n"
    "ds_read_b64_tr_b16 a[204:205], v233 offset:72\n"
    "v_exp_f32 v146, v146\n"
    "v_add_f32 v25, v25, v145\n"
    "v_cvt_pk_bf16_f32 v188, v144, v145\n"
    "v_mfma_f32_32x32x16_bf16 a[112:127], a[172:175], v[176:179], a[112:127]\n"
    "ds_read_b128 a[156:159], v23 offset:17920\n"
    "ds_read_b64_tr_b16 a[206:207], v233 offset:584\n"
    "v_exp_f32 v147, v147\n"
    "v_add_f32 v25, v25, v146\n"
    "s_waitcnt lgkmcnt(15)\n"
    "v_mfma_f32_32x32x16_bf16 a[0:15], a[176:179], v[172:175], a[0:15]\n"
    "v_exp_f32 v148, v148\n"
    "v_add_f32 v25, v25, v147\n"
    "v_cvt_pk_bf16_f32 v189, v146, v147\n"
    "v_mfma_f32_32x32x16_bf16 a[16:31], a[176:179], v[180:183], a[16:31]\n"
    "v_exp_f32 v149, v149\n"
    "v_add_f32 v25, v25, v148\n"
    "v_mfma_f32_32x32x16_bf16 a[32:47], a[180:183], v[172:175], a[32:47]\n"
    "v_exp_f32 v150, v150\n"
    "v_add_f32 v25, v25, v149\n"
    "v_cvt_pk_bf16_f32 v190, v148, v149\n"
    "v_mfma_f32_32x32x16_bf16 a[48:63], a[180:183], v[180:183], a[48:63]\n"
    "v_exp_f32 v151, v151\n"
    "v_add_f32 v25, v25, v150\n"
    "v_mfma_f32_32x32x16_bf16 a[64:79], a[184:187], v[172:175], a[64:79]\n"
    "v_exp_f32 v152, v152\n"
    "v_add_f32 v25, v25, v151\n"
    "v_cvt_pk_bf16_f32 v191, v150, v151\n"
    "v_mfma_f32_32x32x16_bf16 a[80:95], a[184:187], v[180:183], a[80:95]\n"
    "v_exp_f32 v153, v153\n"
    "v_add_f32 v26, v26, v152\n"
    "v_mfma_f32_32x32x16_bf16 a[96:111], a[188:191], v[172:175], a[96:111]\n"
    "v_add_u32 v39, 0x1c000, v235\n"
    "global_load_dword v36, v39, s[42:43]\n"
    "v_exp_f32 v154, v154\n"
    "v_add_f32 v26, v26, v153\n"
    "v_cvt_pk_bf16_f32 v192, v152, v153\n"
    "v_mfma_f32_32x32x16_bf16 a[112:127], a[188:191], v[180:183], a[112:127]\n"
    "v_exp_f32 v155, v155\n"
    "v_add_f32 v26, v26, v154\n"
    "v_mfma_f32_32x32x16_bf16 v[104:119], a[128:131], v[40:43], v[200:215]\n"
    "s_waitcnt lgkmcnt(0)\n"
    "ds_read_b64_tr_b16 a[160:161], v18 offset:40960\n"
    "v_exp_f32 v156, v156\n"
    "v_add_f32 v26, v26, v155\n"
    "v_cvt_pk_bf16_f32 v193, v154, v155\n"
    "v_mfma_f32_32x32x16_bf16 v[104:119], a[132:135], v[44:47], v[104:119]\n"
    "s_mul_i32 s93, s95, 0\n"
    "s_add_u32 m0, s82, 0\n"
    "ds_read_b64_tr_b16 a[162:163], v18 offset:41984\n"
    "v_exp_f32 v157, v157\n"
    "v_add_f32 v26, v26, v156\n"
    "buffer_load_dwordx4 v232, s[72:75], s93 offen lds\n"
    "v_mfma_f32_32x32x16_bf16 v[104:119], a[136:139], v[48:51], v[104:119]\n"
    "ds_read_b64_tr_b16 a[164:165], v19 offset:40960\n"
    "v_exp_f32 v158, v158\n"
    "v_add_f32 v26, v26, v157\n"
    "v_cvt_pk_bf16_f32 v194, v156, v157\n"
    "v_mfma_f32_32x32x16_bf16 v[104:119], a[140:143], v[52:55], v[104:119]\n"
    "s_mul_i32 s93, s95, 1\n"
    "s_add_u32 m0, s82, 0x400\n"
    "ds_read_b64_tr_b16 a[166:167], v19 offset:41984\n"
    "v_exp_f32 v159, v159\n"
    "v_add_f32 v26, v26, v158\n"
    "buffer_load_dwordx4 v232, s[72:75], s93 offen lds\n"
    "v_mfma_f32_32x32x16_bf16 v[104:119], a[144:147], v[56:59], v[104:119]\n"
    "ds_read_b64_tr_b16 a[168:169], v20 offset:40960\n"
    "v_exp_f32 v160, v160\n"
    "v_add_f32 v26, v26, v159\n"
    "v_cvt_pk_bf16_f32 v195, v158, v159\n"
    "v_mfma_f32_32x32x16_bf16 v[104:119], a[148:151], v[60:63], v[104:119]\n"
    "s_mul_i32 s93, s95, 2\n"
    "s_add_u32 m0, s82, 0x800\n"
    "ds_read_b64_tr_b16 a[170:171], v20 offset:41984\n"
    "v_exp_f32 v161, v161\n"
    "v_add_f32 v26, v26, v160\n"
    "buffer_load_dwordx4 v232, s[72:75], s93 offen lds\n"
    "v_mfma_f32_32x32x16_bf16 v[104:119], a[152:155], v[64:67], v[104:119]\n"
    "ds_read_b64_tr_b16 a[172:173], v21 offset:40960\n"
    "v_exp_f32 v162, v162\n"
    "v_add_f32 v26, v26, v161\n"
    "v_cvt_pk_bf16_f32 v196, v160, v161\n"
    "v_mfma_f32_32x32x16_bf16 v[104:119], a[156:159], v[68:71], v[104:119]\n"
    "s_mul_i32 s93, s95, 3\n"
    "s_add_u32 m0, s82, 0xc00\n"
    "ds_read_b64_tr_b16 a[174:175], v21 offset:41984\n"
    "v_exp_f32 v163, v163\n"
    "v_add_f32 v26, v26, v162\n"
    "buffer_load_dwordx4 v232, s[72:75], s93 offen lds\n"
    "v_mfma_f32_32x32x16_bf16 v[120:135], a[128:131], v[72:75], v[216:231]\n"
    "ds_read_b64_tr_b16 a[176:177], v18 offset:45056\n"
    "v_exp_f32 v164, v164\n"
    "v_add_f32 v26, v26, v163\n"
    "v_cvt_pk_bf16_f32 v197, v162, v163\n"
    "v_mfma_f32_32x32x16_bf16 v[120:135], a[132:135], v[76:79], v[120:135]\n"
    "ds_read_b64_tr_b16 a[178:179], v18 offset:46080\n"
    "v_exp_f32 v165, v165\n"
    "v_add_f32 v26, v26, v164\n"
    "v_mfma_f32_32x32x16_bf16 v[120:135], a[136:139], v[80:83], v[120:135]\n"
    "ds_read_b64_tr_b16 a[180:181], v19 offset:45056\n"
    "v_exp_f32 v166, v166\n"
    "v_add_f32 v26, v26, v165\n"
    "v_cvt_pk_bf16_f32 v198, v164, v165\n"
    "v_mfma_f32_32x32x16_bf16 v[120:135], a[140:143], v[84:87], v[120:135]\n"
    "ds_read_b64_tr_b16 a[182:183], v19 offset:46080\n"
    "v_exp_f32 v167, v167\n"
    "v_add_f32 v26, v26, v166\n"
    "v_mfma_f32_32x32x16_bf16 v[120:135], a[144:147], v[88:91], v[120:135]\n"
    "ds_read_b64_tr_b16 a[184:185], v20 offset:45056\n"
    "v_add_f32 v26, v26, v167\n"
    "v_cvt_pk_bf16_f32 v199, v166, v167\n"
    "v_exp_f32 v104, v104\n"
    "v_mfma_f32_32x32x16_bf16 v[120:135], a[148:151], v[92:95], v[120:135]\n"
    "ds_read_b64_tr_b16 a[186:187], v20 offset:46080\n"
    "v_exp_f32 v105, v105\n"
    "v_add_f32 v25, v25, v104\n"
    "v_mfma_f32_32x32x16_bf16 v[120:135], a[152:155], v[96:99], v[120:135]\n"
    "ds_read_b64_tr_b16 a[188:189], v21 offset:45056\n"
    "v_exp_f32 v106, v106\n"
    "v_add_f32 v25, v25, v105\n"
    "v_cvt_pk_bf16_f32 v168, v104, v105\n"
    "v_mfma_f32_32x32x16_bf16 v[120:135], a[156:159], v[100:103], v[120:135]\n"
    "ds_read_b64_tr_b16 a[190:191], v21 offset:46080\n"
    "v_exp_f32 v107, v107\n"
    "v_add_f32 v25, v25, v106\n"
    "s_waitcnt lgkmcnt(14)\n"
    "v_mfma_f32_32x32x16_bf16 a[0:15], a[160:163], v[184:187], a[0:15]\n"
    "ds_read_b128 a[128:131], v22 offset:24576\n"
    "v_exp_f32 v108, v108\n"
    "v_add_f32 v25, v25, v107\n"
    "v_cvt_pk_bf16_f32 v169, v106, v107\n"
    "v_mfma_f32_32x32x16_bf16 a[16:31], a[160:163], v[192:195], a[16:31]\n"
    "ds_read_b128 a[132:135], v23 offset:24576\n"
    "v_exp_f32 v109, v109\n"
    "v_add_f32 v25, v25, v108\n"
    "s_waitcnt lgkmcnt(14)\n"
    "v_mfma_f32_32x32x16_bf16 a[32:47], a[164:167], v[184:187], a[32:47]\n"
    "ds_read_b128 a[136:139], v22 offset:25088\n"
    "v_exp_f32 v110, v110\n"
    "v_add_f32 v25, v25, v109\n"
    "v_cvt_pk_bf16_f32 v170, v108, v109\n"
    "v_mfma_f32_32x32x16_bf16 a[48:63], a[164:167], v[192:195], a[48:63]\n"
    "ds_read_b128 a[140:143], v23 offset:25088\n"
    "v_exp_f32 v111, v111\n"
    "v_add_f32 v25, v25, v110\n"
    "s_waitcnt lgkmcnt(14)\n"
    "v_mfma_f32_32x32x16_bf16 a[64:79], a[168:171], v[184:187], a[64:79]\n"
    "ds_read_b128 a[144:147], v22 offset:25600\n"
    "v_exp_f32 v112, v112\n"
    "v_add_f32 v25, v25, v111\n"
    "v_cvt_pk_bf16_f32 v171, v110, v111\n"
    "v_mfma_f32_32x32x16_bf16 a[80:95], a[168:171], v[192:195], a[80:95]\n"
    "ds_read_b128 a[148:151], v23 offset:25600\n"
    "v_exp_f32 v113, v113\n"
    "v_add_f32 v25, v25, v112\n"
    "s_waitcnt lgkmcnt(14)\n"
    "v_mfma_f32_32x32x16_bf16 a[96:111], a[172:175], v[184:187], a[96:111]\n"
    "ds_read_b128 a[152:155], v22 offset:26112\n"
    "v_exp_f32 v114, v114\n"
    "v_add_f32 v25, v25, v113\n"
    "v_cvt_pk_bf16_f32 v172, v112, v113\n"
    "v_mfma_f32_32x32x16_bf16 a[112:127], a[172:175], v[192:195], a[112:127]\n"
    "ds_read_b128 a[156:159], v23 offset:26112\n"
    "v_exp_f32 v115, v115\n"
    "v_add_f32 v25, v25, v114\n"
    "s_waitcnt lgkmcnt(14)\n"
    "v_mfma_f32_32x32x16_bf16 a[0:15], a[176:179], v[188:191], a[0:15]\n"
    "v_exp_f32 v116, v116\n"
    "v_add_f32 v25, v25, v115\n"
    "v_cvt_pk_bf16_f32 v173, v114, v115\n"
    "v_mfma_f32_32x32x16_bf16 a[16:31], a[176:179], v[196:199], a[16:31]\n"
    "v_exp_f32 v117, v117\n"
    "v_add_f32 v25, v25, v116\n"
    "s_waitcnt lgkmcnt(12)\n"
    "v_mfma_f32_32x32x16_bf16 a[32:47], a[180:183], v[188:191], a[32:47]\n"
    "v_exp_f32 v118, v118\n"
    "v_add_f32 v25, v25, v117\n"
    "v_cvt_pk_bf16_f32 v174, v116, v117\n"
    "v_mfma_f32_32x32x16_bf16 a[48:63], a[180:183], v[196:199], a[48:63]\n"
    "v_exp_f32 v119, v119\n"
    "v_add_f32 v25, v25, v118\n"
    "s_waitcnt lgkmcnt(10)\n"
    "v_mfma_f32_32x32x16_bf16 a[64:79], a[184:187], v[188:191], a[64:79]\n"
    "v_exp_f32 v120, v120\n"
    "v_add_f32 v25, v25, v119\n"
    "v_cvt_pk_bf16_f32 v175, v118, v119\n"
    "v_mfma_f32_32x32x16_bf16 a[80:95], a[184:187], v[196:199], a[80:95]\n"
    "v_exp_f32 v121, v121\n"
    "v_add_f32 v26, v26, v120\n"
    "s_waitcnt lgkmcnt(8)\n"
    "v_mfma_f32_32x32x16_bf16 a[96:111], a[188:191], v[188:191], a[96:111]\n"
    "v_exp_f32 v122, v122\n"
    "v_add_f32 v26, v26, v121\n"
    "v_cvt_pk_bf16_f32 v176, v120, v121\n"
    "v_mfma_f32_32x32x16_bf16 a[112:127], a[188:191], v[196:199], a[112:127]\n"
    "s_waitcnt vmcnt(28)\n"
    "s_waitcnt lgkmcnt(0)\n"
    "s_barrier\n"
    "v_exp_f32 v123, v123\n"
    "v_add_f32 v26, v26, v122\n"
    "v_mfma_f32_32x32x16_bf16 v[136:151], a[128:131], v[40:43], v[200:215]\n"
    "s_mov_b32 s83, 0x18000\n"
    "s_add_u32 m0, s81, 0\n"
    "ds_read_b64_tr_b16 a[160:161], v18 offset:49152\n"
    "v_exp_f32 v124, v124\n"
    "v_add_f32 v26, v26, v123\n"
    "v_cvt_pk_bf16_f32 v177, v122, v123\n"
    "buffer_load_dwordx4 v6, s[56:59], s83 offen lds\n"
    "v_mfma_f32_32x32x16_bf16 v[136:151], a[132:135], v[44:47], v[136:151]\n"
    "ds_read_b64_tr_b16 a[162:163], v18 offset:50176\n"
    "v_exp_f32 v125, v125\n"
    "v_add_f32 v26, v26, v124\n"
    "v_mfma_f32_32x32x16_bf16 v[136:151], a[136:139], v[48:51], v[136:151]\n"
    "s_add_u32 m0, s81, 0x400\n"
    "ds_read_b64_tr_b16 a[164:165], v19 offset:49152\n"
    "v_exp_f32 v126, v126\n"
    "v_add_f32 v26, v26, v125\n"
    "v_cvt_pk_bf16_f32 v178, v124, v125\n"
    "buffer_load_dwordx4 v7, s[56:59], s83 offen lds\n"
    "v_mfma_f32_32x32x16_bf16 v[136:151], a[140:143], v[52:55], v[136:151]\n"
    "ds_read_b64_tr_b16 a[166:167], v19 offset:50176\n"
    "v_exp_f32 v127, v127\n"
    "v_add_f32 v26, v26, v126\n"
    "v_mfma_f32_32x32x16_bf16 v[136:151], a[144:147], v[56:59], v[136:151]\n"
    "s_add_u32 m0, s81, 0x2000\n"
    "ds_read_b64_tr_b16 a[168:169], v20 offset:49152\n"
    "v_exp_f32 v128, v128\n"
    "v_add_f32 v26, v26, v127\n"
    "v_cvt_pk_bf16_f32 v179, v126, v127\n"
    "buffer_load_dwordx4 v8, s[56:59], s83 offen lds\n"
    "v_mfma_f32_32x32x16_bf16 v[136:151], a[148:151], v[60:63], v[136:151]\n"
    "ds_read_b64_tr_b16 a[170:171], v20 offset:50176\n"
    "v_exp_f32 v129, v129\n"
    "v_add_f32 v26, v26, v128\n"
    "v_mfma_f32_32x32x16_bf16 v[136:151], a[152:155], v[64:67], v[136:151]\n"
    "s_add_u32 m0, s81, 0x2400\n"
    "ds_read_b64_tr_b16 a[172:173], v21 offset:49152\n"
    "v_exp_f32 v130, v130\n"
    "v_add_f32 v26, v26, v129\n"
    "v_cvt_pk_bf16_f32 v180, v128, v129\n"
    "buffer_load_dwordx4 v9, s[56:59], s83 offen lds\n"
    "v_mfma_f32_32x32x16_bf16 v[136:151], a[156:159], v[68:71], v[136:151]\n"
    "ds_read_b64_tr_b16 a[174:175], v21 offset:50176\n"
    "v_exp_f32 v131, v131\n"
    "v_add_f32 v26, v26, v130\n"
    "v_mfma_f32_32x32x16_bf16 v[152:167], a[128:131], v[72:75], v[216:231]\n"
    "s_mov_b32 s83, 0x14000\n"
    "s_add_u32 m0, s80, 0x14000\n"
    "ds_read_b64_tr_b16 a[176:177], v18 offset:53248\n"
    "v_exp_f32 v132, v132\n"
    "v_add_f32 v26, v26, v131\n"
    "v_cvt_pk_bf16_f32 v181, v130, v131\n"
    "buffer_load_dwordx4 v10, s[60:63], s83 offen lds\n"
    "v_mfma_f32_32x32x16_bf16 v[152:167], a[132:135], v[76:79], v[152:167]\n"
    "ds_read_b64_tr_b16 a[178:179], v18 offset:54272\n"
    "v_exp_f32 v133, v133\n"
    "v_add_f32 v26, v26, v132\n"
    "v_mfma_f32_32x32x16_bf16 v[152:167], a[136:139], v[80:83], v[152:167]\n"
    "s_add_u32 m0, s80, 0x14400\n"
    "ds_read_b64_tr_b16 a[180:181], v19 offset:53248\n"
    "v_exp_f32 v134, v134\n"
    "v_add_f32 v26, v26, v133\n"
    "v_cvt_pk_bf16_f32 v182, v132, v133\n"
    "buffer_load_dwordx4 v11, s[60:63], s83 offen lds\n"
    "v_mfma_f32_32x32x16_bf16 v[152:167], a[140:143], v[84:87], v[152:167]\n"
    "ds_read_b64_tr_b16 a[182:183], v19 offset:54272\n"
    "v_exp_f32 v135, v135\n"
    "v_add_f32 v26, v26, v134\n"
    "v_mfma_f32_32x32x16_bf16 v[152:167], a[144:147], v[88:91], v[152:167]\n"
    "s_add_u32 m0, s80, 0x14800\n"
    "ds_read_b64_tr_b16 a[184:185], v20 offset:53248\n"
    "v_add_f32 v26, v26, v135\n"
    "v_cvt_pk_bf16_f32 v183, v134, v135\n"
    "v_exp_f32 v136, v136\n"
    "buffer_load_dwordx4 v12, s[60:63], s83 offen lds\n"
    "v_mfma_f32_32x32x16_bf16 v[152:167], a[148:151], v[92:95], v[152:167]\n"
    "ds_read_b64_tr_b16 a[186:187], v20 offset:54272\n"
    "v_exp_f32 v137, v137\n"
    "v_add_f32 v25, v25, v136\n"
    "v_mfma_f32_32x32x16_bf16 v[152:167], a[152:155], v[96:99], v[152:167]\n"
    "s_add_u32 m0, s80, 0x14c00\n"
    "ds_read_b64_tr_b16 a[188:189], v21 offset:53248\n"
    "v_exp_f32 v138, v138\n"
    "v_add_f32 v25, v25, v137\n"
    "v_cvt_pk_bf16_f32 v184, v136, v137\n"
    "buffer_load_dwordx4 v13, s[60:63], s83 offen lds\n"
    "v_mfma_f32_32x32x16_bf16 v[152:167], a[156:159], v[100:103], v[152:167]\n"
    "ds_read_b64_tr_b16 a[190:191], v21 offset:54272\n"
    "v_exp_f32 v139, v139\n"
    "v_add_f32 v25, v25, v138\n"
    "s_waitcnt lgkmcnt(14)\n"
    "v_mfma_f32_32x32x16_bf16 a[0:15], a[160:163], v[168:171], a[0:15]\n"
    "ds_read_b128 a[128:131], v22 offset:32768\n"
    "s_waitcnt vmcnt(32)\n"
    "ds_read_b64_tr_b16 a[208:209], v233 offset:4096\n"
    "v_exp_f32 v140, v140\n"
    "v_add_f32 v25, v25, v139\n"
    "v_cvt_pk_bf16_f32 v185, v138, v139\n"
    "v_mfma_f32_32x32x16_bf16 a[16:31], a[160:163], v[176:179], a[16:31]\n"
    "ds_read_b128 a[132:135], v23 offset:32768\n"
    "ds_read_b64_tr_b16 a[210:211], v233 offset:4608\n"
    "v_exp_f32 v141, v141\n"
    "v_add_f32 v25, v25, v140\n"
    "s_waitcnt lgkmcnt(15)\n"
    "v_mfma_f32_32x32x16_bf16 a[32:47], a[164:167], v[168:171], a[32:47]\n"
    "ds_read_b128 a[136:139], v22 offset:33280\n"
    "ds_read_b64_tr_b16 a[212:213], v233 offset:4104\n"
    "v_exp_f32 v142, v142\n"
    "v_add_f32 v25, v25, v141\n"
    "v_cvt_pk_bf16_f32 v186, v140, v141\n"
    "v_mfma_f32_32x32x16_bf16 a[48:63], a[164:167], v[176:179], a[48:63]\n"
    "ds_read_b128 a[140:143], v23 offset:33280\n"
    "ds_read_b64_tr_b16 a[214:215], v233 offset:4616\n"
    "v_exp_f32 v143, v143\n"
    "v_add_f32 v25, v25, v142\n"
    "s_waitcnt lgkmcnt(15)\n"
    "v_mfma_f32_32x32x16_bf16 a[64:79], a[168:171], v[168:171], a[64:79]\n"
    "ds_read_b128 a[144:147], v22 offset:33792\n"
    "ds_read_b64_tr_b16 a[216:217], v233 offset:4160\n"
    "v_exp_f32 v144, v144\n"
    "v_add_f32 v25, v25, v143\n"
    "v_cvt_pk_bf16_f32 v187, v142, v143\n"
    "v_mfma_f32_32x32x16_bf16 a[80:95], a[168:171], v[176:179], a[80:95]\n"
    "ds_read_b128 a[148:151], v23 offset:33792\n"
    "ds_read_b64_tr_b16 a[218:219], v233 offset:4672\n"
    "v_exp_f32 v145, v145\n"
    "v_add_f32 v25, v25, v144\n"
    "v_mfma_f32_32x32x16_bf16 a[96:111], a[172:175], v[168:171], a[96:111]\n"
    "ds_read_b128 a[152:155], v22 offset:34304\n"
    "ds_read_b64_tr_b16 a[220:221], v233 offset:4168\n"
    "v_exp_f32 v146, v146\n"
    "v_add_f32 v25, v25, v145\n"
    "v_cvt_pk_bf16_f32 v188, v144, v145\n"
    "v_mfma_f32_32x32x16_bf16 a[112:127], a[172:175], v[176:179], a[112:127]\n"
    "ds_read_b128 a[156:159], v23 offset:34304\n"
    "ds_read_b64_tr_b16 a[222:223], v233 offset:4680\n"
    "v_exp_f32 v147, v147\n"
    "v_add_f32 v25, v25, v146\n"
    "s_waitcnt lgkmcnt(15)\n"
    "v_mfma_f32_32x32x16_bf16 a[0:15], a[176:179], v[172:175], a[0:15]\n"
    "v_exp_f32 v148, v148\n"
    "v_add_f32 v25, v25, v147\n"
    "v_cvt_pk_bf16_f32 v189, v146, v147\n"
    "v_mfma_f32_32x32x16_bf16 a[16:31], a[176:179], v[180:183], a[16:31]\n"
    "v_exp_f32 v149, v149\n"
    "v_add_f32 v25, v25, v148\n"
    "v_mfma_f32_32x32x16_bf16 a[32:47], a[180:183], v[172:175], a[32:47]\n"
    "v_exp_f32 v150, v150\n"
    "v_add_f32 v25, v25, v149\n"
    "v_cvt_pk_bf16_f32 v190, v148, v149\n"
    "v_mfma_f32_32x32x16_bf16 a[48:63], a[180:183], v[180:183], a[48:63]\n"
    "v_exp_f32 v151, v151\n"
    "v_add_f32 v25, v25, v150\n"
    "v_mfma_f32_32x32x16_bf16 a[64:79], a[184:187], v[172:175], a[64:79]\n"
    "v_exp_f32 v152, v152\n"
    "v_add_f32 v25, v25, v151\n"
    "v_cvt_pk_bf16_f32 v191, v150, v151\n"
    "v_mfma_f32_32x32x16_bf16 a[80:95], a[184:187], v[180:183], a[80:95]\n"
    "v_exp_f32 v153, v153\n"
    "v_add_f32 v26, v26, v152\n"
    "v_mfma_f32_32x32x16_bf16 a[96:111], a[188:191], v[172:175], a[96:111]\n"
    "v_exp_f32 v154, v154\n"
    "v_add_f32 v26, v26, v153\n"
    "v_cvt_pk_bf16_f32 v192, v152, v153\n"
    "v_mfma_f32_32x32x16_bf16 a[112:127], a[188:191], v[180:183], a[112:127]\n"
    "v_exp_f32 v155, v155\n"
    "v_add_f32 v26, v26, v154\n"
    "v_mfma_f32_32x32x16_bf16 v[104:119], a[128:131], v[40:43], v[200:215]\n"
    "ds_read_b64_tr_b16 a[160:161], v18 offset:57344\n"
    "v_exp_f32 v156, v156\n"
    "v_add_f32 v26, v26, v155\n"
    "v_cvt_pk_bf16_f32 v193, v154, v155\n"
    "s_waitcnt lgkmcnt(14)\n"
    "v_mfma_f32_32x32x16_bf16 v[104:119], a[132:135], v[44:47], v[104:119]\n"
    "ds_read_b64_tr_b16 a[162:163], v18 offset:58368\n"
    "v_exp_f32 v157, v157\n"
    "v_add_f32 v26, v26, v156\n"
    "s_waitcnt lgkmcnt(13)\n"
    "v_mfma_f32_32x32x16_bf16 v[104:119], a[136:139], v[48:51], v[104:119]\n"
    "ds_read_b64_tr_b16 a[164:165], v19 offset:57344\n"
    "v_exp_f32 v158, v158\n"
    "v_add_f32 v26, v26, v157\n"
    "v_cvt_pk_bf16_f32 v194, v156, v157\n"
    "s_waitcnt lgkmcnt(12)\n"
    "v_mfma_f32_32x32x16_bf16 v[104:119], a[140:143], v[52:55], v[104:119]\n"
    "ds_read_b64_tr_b16 a[166:167], v19 offset:58368\n"
    "v_exp_f32 v159, v159\n"
    "v_add_f32 v26, v26, v158\n"
    "s_waitcnt lgkmcnt(11)\n"
    "v_mfma_f32_32x32x16_bf16 v[104:119], a[144:147], v[56:59], v[104:119]\n"
    "ds_read_b64_tr_b16 a[168:169], v20 offset:57344\n"
    "v_exp_f32 v160, v160\n"
    "v_add_f32 v26, v26, v159\n"
    "v_cvt_pk_bf16_f32 v195, v158, v159\n"
    "s_waitcnt lgkmcnt(10)\n"
    "v_mfma_f32_32x32x16_bf16 v[104:119], a[148:151], v[60:63], v[104:119]\n"
    "ds_read_b64_tr_b16 a[170:171], v20 offset:58368\n"
    "v_exp_f32 v161, v161\n"
    "v_add_f32 v26, v26, v160\n"
    "s_waitcnt lgkmcnt(9)\n"
    "v_mfma_f32_32x32x16_bf16 v[104:119], a[152:155], v[64:67], v[104:119]\n"
    "ds_read_b64_tr_b16 a[172:173], v21 offset:57344\n"
    "v_exp_f32 v162, v162\n"
    "v_add_f32 v26, v26, v161\n"
    "v_cvt_pk_bf16_f32 v196, v160, v161\n"
    "s_waitcnt lgkmcnt(8)\n"
    "v_mfma_f32_32x32x16_bf16 v[104:119], a[156:159], v[68:71], v[104:119]\n"
    "ds_read_b64_tr_b16 a[174:175], v21 offset:58368\n"
    "v_exp_f32 v163, v163\n"
    "v_add_f32 v26, v26, v162\n"
    "v_mfma_f32_32x32x16_bf16 v[120:135], a[128:131], v[72:75], v[216:231]\n"
    "ds_read_b64_tr_b16 a[176:177], v18 offset:61440\n"
    "v_exp_f32 v164, v164\n"
    "v_add_f32 v26, v26, v163\n"
    "v_cvt_pk_bf16_f32 v197, v162, v163\n"
    "v_mfma_f32_32x32x16_bf16 v[120:135], a[132:135], v[76:79], v[120:135]\n"
    "ds_read_b64_tr_b16 a[178:179], v18 offset:62464\n"
    "v_exp_f32 v165, v165\n"
    "v_add_f32 v26, v26, v164\n"
    "v_mfma_f32_32x32x16_bf16 v[120:135], a[136:139], v[80:83], v[120:135]\n"
    "ds_read_b64_tr_b16 a[180:181], v19 offset:61440\n"
    "v_exp_f32 v166, v166\n"
    "v_add_f32 v26, v26, v165\n"
    "v_cvt_pk_bf16_f32 v198, v164, v165\n"
    "v_mfma_f32_32x32x16_bf16 v[120:135], a[140:143], v[84:87], v[120:135]\n"
    "ds_read_b64_tr_b16 a[182:183], v19 offset:62464\n"
    "v_exp_f32 v167, v167\n"
    "v_add_f32 v26, v26, v166\n"
    "v_mfma_f32_32x32x16_bf16 v[120:135], a[144:147], v[88:91], v[120:135]\n"
    "ds_read_b64_tr_b16 a[184:185], v20 offset:61440\n"
    "v_add_f32 v26, v26, v167\n"
    "v_cvt_pk_bf16_f32 v199, v166, v167\n"
    "v_exp_f32 v104, v104\n"
    "v_mfma_f32_32x32x16_bf16 v[120:135], a[148:151], v[92:95], v[120:135]\n"
    "ds_read_b64_tr_b16 a[186:187], v20 offset:62464\n"
    "v_exp_f32 v105, v105\n"
    "v_add_f32 v25, v25, v104\n"
    "v_mfma_f32_32x32x16_bf16 v[120:135], a[152:155], v[96:99], v[120:135]\n"
    "ds_read_b64_tr_b16 a[188:189], v21 offset:61440\n"
    "v_exp_f32 v106, v106\n"
    "v_add_f32 v25, v25, v105\n"
    "v_cvt_pk_bf16_f32 v168, v104, v105\n"
    "v_mfma_f32_32x32x16_bf16 v[120:135], a[156:159], v[100:103], v[120:135]\n"
    "ds_read_b64_tr_b16 a[190:191], v21 offset:62464\n"
    "v_exp_f32 v107, v107\n"
    "v_add_f32 v25, v25, v106\n"
    "s_waitcnt lgkmcnt(14)\n"
    "v_mfma_f32_32x32x16_bf16 a[0:15], a[160:163], v[184:187], a[0:15]\n"
    "ds_read_b128 a[128:131], v22 offset:40960\n"
    "v_exp_f32 v108, v108\n"
    "v_add_f32 v25, v25, v107\n"
    "v_cvt_pk_bf16_f32 v169, v106, v107\n"
    "v_mfma_f32_32x32x16_bf16 a[16:31], a[160:163], v[192:195], a[16:31]\n"
    "ds_read_b128 a[132:135], v23 offset:40960\n"
    "v_exp_f32 v109, v109\n"
    "v_add_f32 v25, v25, v108\n"
    "s_waitcnt lgkmcnt(14)\n"
    "v_mfma_f32_32x32x16_bf16 a[32:47], a[164:167], v[184:187], a[32:47]\n"
    "ds_read_b128 a[136:139], v22 offset:41472\n"
    "v_exp_f32 v110, v110\n"
    "v_add_f32 v25, v25, v109\n"
    "v_cvt_pk_bf16_f32 v170, v108, v109\n"
    "v_mfma_f32_32x32x16_bf16 a[48:63], a[164:167], v[192:195], a[48:63]\n"
    "ds_read_b128 a[140:143], v23 offset:41472\n"
    "v_exp_f32 v111, v111\n"
    "v_add_f32 v25, v25, v110\n"
    "s_waitcnt lgkmcnt(14)\n"
    "v_mfma_f32_32x32x16_bf16 a[64:79], a[168:171], v[184:187], a[64:79]\n"
    "ds_read_b128 a[144:147], v22 offset:41984\n"
    "v_exp_f32 v112, v112\n"
    "v_add_f32 v25, v25, v111\n"
    "v_cvt_pk_bf16_f32 v171, v110, v111\n"
    "v_mfma_f32_32x32x16_bf16 a[80:95], a[168:171], v[192:195], a[80:95]\n"
    "ds_read_b128 a[148:151], v23 offset:41984\n"
    "v_exp_f32 v113, v113\n"
    "v_add_f32 v25, v25, v112\n"
    "s_waitcnt lgkmcnt(14)\n"
    "v_mfma_f32_32x32x16_bf16 a[96:111], a[172:175], v[184:187], a[96:111]\n"
    "ds_read_b128 a[152:155], v22 offset:42496\n"
    "v_exp_f32 v114, v114\n"
    "v_add_f32 v25, v25, v113\n"
    "v_cvt_pk_bf16_f32 v172, v112, v113\n"
    "v_mfma_f32_32x32x16_bf16 a[112:127], a[172:175], v[192:195], a[112:127]\n"
    "ds_read_b128 a[156:159], v23 offset:42496\n"
    "v_exp_f32 v115, v115\n"
    "v_add_f32 v25, v25, v114\n"
    "s_waitcnt lgkmcnt(14)\n"
    "v_mfma_f32_32x32x16_bf16 a[0:15], a[176:179], v[188:191], a[0:15]\n"
    "v_exp_f32 v116, v116\n"
    "v_add_f32 v25, v25, v115\n"
    "v_cvt_pk_bf16_f32 v173, v114, v115\n"
    "v_mfma_f32_32x32x16_bf16 a[16:31], a[176:179], v[196:199], a[16:31]\n"
    "v_exp_f32 v117, v117\n"
    "v_add_f32 v25, v25, v116\n"
    "s_waitcnt lgkmcnt(12)\n"
    "v_mfma_f32_32x32x16_bf16 a[32:47], a[180:183], v[188:191], a[32:47]\n"
    "v_exp_f32 v118, v118\n"
    "v_add_f32 v25, v25, v117\n"
    "v_cvt_pk_bf16_f32 v174, v116, v117\n"
    "v_mfma_f32_32x32x16_bf16 a[48:63], a[180:183], v[196:199], a[48:63]\n"
    "v_exp_f32 v119, v119\n"
    "v_add_f32 v25, v25, v118\n"
    "s_waitcnt lgkmcnt(10)\n"
    "v_mfma_f32_32x32x16_bf16 a[64:79], a[184:187], v[188:191], a[64:79]\n"
    "v_exp_f32 v120, v120\n"
    "v_add_f32 v25, v25, v119\n"
    "v_cvt_pk_bf16_f32 v175, v118, v119\n"
    "v_mfma_f32_32x32x16_bf16 a[80:95], a[184:187], v[196:199], a[80:95]\n"
    "v_exp_f32 v121, v121\n"
    "v_add_f32 v26, v26, v120\n"
    "s_waitcnt lgkmcnt(8)\n"
    "v_mfma_f32_32x32x16_bf16 a[96:111], a[188:191], v[188:191], a[96:111]\n"
    "v_exp_f32 v122, v122\n"
    "v_add_f32 v26, v26, v121\n"
    "v_cvt_pk_bf16_f32 v176, v120, v121\n"
    "v_mfma_f32_32x32x16_bf16 a[112:127], a[188:191], v[196:199], a[112:127]\n"
    "s_waitcnt vmcnt(13)\n"
    "s_waitcnt lgkmcnt(0)\n"
    "s_barrier\n"
    "v_exp_f32 v123, v123\n"
    "v_add_f32 v26, v26, v122\n"
    "v_mfma_f32_32x32x16_bf16 v[136:151], a[128:131], v[40:43], v[200:215]\n"
    "s_mov_b32 s83, 0x1c000\n"
    "s_add_u32 m0, s81, 0x4000\n"
    "ds_read_b64_tr_b16 a[160:161], v18 offset:0\n"
    "v_exp_f32 v124, v124\n"
    "v_add_f32 v26, v26, v123\n"
    "v_cvt_pk_bf16_f32 v177, v122, v123\n"
    "buffer_load_dwordx4 v6, s[56:59], s83 offen lds\n"
    "v_mfma_f32_32x32x16_bf16 v[136:151], a[132:135], v[44:47], v[136:151]\n"
    "ds_read_b64_tr_b16 a[162:163], v18 offset:1024\n"
    "v_exp_f32 v125, v125\n"
    "v_add_f32 v26, v26, v124\n"
    "v_mfma_f32_32x32x16_bf16 v[136:151], a[136:139], v[48:51], v[136:151]\n"
    "s_add_u32 m0, s81, 0x4400\n"
    "ds_read_b64_tr_b16 a[164:165], v19 offset:0\n"
    "v_exp_f32 v126, v126\n"
    "v_add_f32 v26, v26, v125\n"
    "v_cvt_pk_bf16_f32 v178, v124, v125\n"
    "buffer_load_dwordx4 v7, s[56:59], s83 offen lds\n"
    "v_mfma_f32_32x32x16_bf16 v[136:151], a[140:143], v[52:55], v[136:151]\n"
    "ds_read_b64_tr_b16 a[166:167], v19 offset:1024\n"
    "v_exp_f32 v127, v127\n"
    "v_add_f32 v26, v26, v126\n"
    "v_mfma_f32_32x32x16_bf16 v[136:151], a[144:147], v[56:59], v[136:151]\n"
    "s_add_u32 m0, s81, 0x6000\n"
    "ds_read_b64_tr_b16 a[168:169], v20 offset:0\n"
    "v_exp_f32 v128, v128\n"
    "v_add_f32 v26, v26, v127\n"
    "v_cvt_pk_bf16_f32 v179, v126, v127\n"
    "buffer_load_dwordx4 v8, s[56:59], s83 offen lds\n"
    "v_mfma_f32_32x32x16_bf16 v[136:151], a[148:151], v[60:63], v[136:151]\n"
    "ds_read_b64_tr_b16 a[170:171], v20 offset:1024\n"
    "v_exp_f32 v129, v129\n"
    "v_add_f32 v26, v26, v128\n"
    "v_mfma_f32_32x32x16_bf16 v[136:151], a[152:155], v[64:67], v[136:151]\n"
    "s_add_u32 m0, s81, 0x6400\n"
    "ds_read_b64_tr_b16 a[172:173], v21 offset:0\n"
    "v_exp_f32 v130, v130\n"
    "v_add_f32 v26, v26, v129\n"
    "v_cvt_pk_bf16_f32 v180, v128, v129\n"
    "buffer_load_dwordx4 v9, s[56:59], s83 offen lds\n"
    "v_mfma_f32_32x32x16_bf16 v[136:151], a[156:159], v[68:71], v[136:151]\n"
    "ds_read_b64_tr_b16 a[174:175], v21 offset:1024\n"
    "v_exp_f32 v131, v131\n"
    "v_add_f32 v26, v26, v130\n"
    "v_mfma_f32_32x32x16_bf16 v[152:167], a[128:131], v[72:75], v[216:231]\n"
    "s_mov_b32 s83, 0x18000\n"
    "s_add_u32 m0, s80, 0x18000\n"
    "ds_read_b64_tr_b16 a[176:177], v18 offset:4096\n"
    "v_exp_f32 v132, v132\n"
    "v_add_f32 v26, v26, v131\n"
    "v_cvt_pk_bf16_f32 v181, v130, v131\n"
    "buffer_load_dwordx4 v10, s[60:63], s83 offen lds\n"
    "v_mfma_f32_32x32x16_bf16 v[152:167], a[132:135], v[76:79], v[152:167]\n"
    "ds_read_b64_tr_b16 a[178:179], v18 offset:5120\n"
    "v_exp_f32 v133, v133\n"
    "v_add_f32 v26, v26, v132\n"
    "v_mfma_f32_32x32x16_bf16 v[152:167], a[136:139], v[80:83], v[152:167]\n"
    "s_add_u32 m0, s80, 0x18400\n"
    "ds_read_b64_tr_b16 a[180:181], v19 offset:4096\n"
    "v_exp_f32 v134, v134\n"
    "v_add_f32 v26, v26, v133\n"
    "v_cvt_pk_bf16_f32 v182, v132, v133\n"
    "buffer_load_dwordx4 v11, s[60:63], s83 offen lds\n"
    "v_mfma_f32_32x32x16_bf16 v[152:167], a[140:143], v[84:87], v[152:167]\n"
    "ds_read_b64_tr_b16 a[182:183], v19 offset:5120\n"
    "v_exp_f32 v135, v135\n"
    "v_add_f32 v26, v26, v134\n"
    "v_mfma_f32_32x32x16_bf16 v[152:167], a[144:147], v[88:91], v[152:167]\n"
    "s_add_u32 m0, s80, 0x18800\n"
    "ds_read_b64_tr_b16 a[184:185], v20 offset:4096\n"
    "v_add_f32 v26, v26, v135\n"
    "v_cvt_pk_bf16_f32 v183, v134, v135\n"
    "v_exp_f32 v136, v136\n"
    "buffer_load_dwordx4 v12, s[60:63], s83 offen lds\n"
    "v_mfma_f32_32x32x16_bf16 v[152:167], a[148:151], v[92:95], v[152:167]\n"
    "ds_read_b64_tr_b16 a[186:187], v20 offset:5120\n"
    "v_exp_f32 v137, v137\n"
    "v_add_f32 v25, v25, v136\n"
    "v_mfma_f32_32x32x16_bf16 v[152:167], a[152:155], v[96:99], v[152:167]\n"
    "s_add_u32 m0, s80, 0x18c00\n"
    "ds_read_b64_tr_b16 a[188:189], v21 offset:4096\n"
    "v_exp_f32 v138, v138\n"
    "v_add_f32 v25, v25, v137\n"
    "v_cvt_pk_bf16_f32 v184, v136, v137\n"
    "buffer_load_dwordx4 v13, s[60:63], s83 offen lds\n"
    "v_mfma_f32_32x32x16_bf16 v[152:167], a[156:159], v[100:103], v[152:167]\n"
    "ds_read_b64_tr_b16 a[190:191], v21 offset:5120\n"
    "v_exp_f32 v139, v139\n"
    "v_add_f32 v25, v25, v138\n"
    "s_waitcnt lgkmcnt(14)\n"
    "v_mfma_f32_32x32x16_bf16 a[0:15], a[160:163], v[168:171], a[0:15]\n"
    "ds_read_b128 a[128:131], v22 offset:49152\n"
    "s_waitcnt vmcnt(16)\n"
    "ds_read_b64_tr_b16 v[236:237], v233 offset:0\n"
    "v_exp_f32 v140, v140\n"
    "v_add_f32 v25, v25, v139\n"
    "v_cvt_pk_bf16_f32 v185, v138, v139\n"
    "v_mfma_f32_32x32x16_bf16 a[16:31], a[160:163], v[176:179], a[16:31]\n"
    "ds_read_b128 a[132:135], v23 offset:49152\n"
    "buffer_store_dwordx4 a[192:195], v1, s[52:55], 0 offen offset:0 sc1\n"
    "ds_read_b64_tr_b16 v[238:239], v233 offset:512\n"
    "v_exp_f32 v141, v141\n"
    "v_add_f32 v25, v25, v140\n"
    "s_waitcnt lgkmcnt(15)\n"
    "v_mfma_f32_32x32x16_bf16 a[32:47], a[164:167], v[168:171], a[32:47]\n"
    "ds_read_b128 a[136:139], v22 offset:49664\n"
    "ds_read_b64_tr_b16 v[240:241], v233 offset:8\n"
    "v_exp_f32 v142, v142\n"
    "v_add_f32 v25, v25, v141\n"
    "v_cvt_pk_bf16_f32 v186, v140, v141\n"
    "v_mfma_f32_32x32x16_bf16 a[48:63], a[164:167], v[176:179], a[48:63]\n"
    "ds_read_b128 a[140:143], v23 offset:49664\n"
    "buffer_store_dwordx4 a[196:199], v1, s[52:55], 0 offen offset:1024 sc1\n"
    "ds_read_b64_tr_b16 v[242:243], v233 offset:520\n"
    "v_exp_f32 v143, v143\n"
    "v_add_f32 v25, v25, v142\n"
    "s_waitcnt lgkmcnt(15)\n"
    "v_mfma_f32_32x32x16_bf16 a[64:79], a[168:171], v[168:171], a[64:79]\n"
    "ds_read_b128 a[144:147], v22 offset:50176\n"
    "ds_read_b64_tr_b16 v[244:245], v233 offset:64\n"
    "v_exp_f32 v144, v144\n"
    "v_add_f32 v25, v25, v143\n"
    "v_cvt_pk_bf16_f32 v187, v142, v143\n"
    "v_mfma_f32_32x32x16_bf16 a[80:95], a[168:171], v[176:179], a[80:95]\n"
    "ds_read_b128 a[148:151], v23 offset:50176\n"
    "buffer_store_dwordx4 a[200:203], v1, s[52:55], 0 offen offset:2048 sc1\n"
    "ds_read_b64_tr_b16 v[246:247], v233 offset:576\n"
    "v_exp_f32 v145, v145\n"
    "v_add_f32 v25, v25, v144\n"
    "v_mfma_f32_32x32x16_bf16 a[96:111], a[172:175], v[168:171], a[96:111]\n"
    "ds_read_b128 a[152:155], v22 offset:50688\n"
    "ds_read_b64_tr_b16 v[248:249], v233 offset:72\n"
    "v_exp_f32 v146, v146\n"
    "v_add_f32 v25, v25, v145\n"
    "v_cvt_pk_bf16_f32 v188, v144, v145\n"
    "v_mfma_f32_32x32x16_bf16 a[112:127], a[172:175], v[176:179], a[112:127]\n"
    "ds_read_b128 a[156:159], v23 offset:50688\n"
    "buffer_store_dwordx4 a[204:207], v1, s[52:55], 0 offen offset:3072 sc1\n"
    "ds_read_b64_tr_b16 v[250:251], v233 offset:584\n"
    "v_exp_f32 v147, v147\n"
    "v_add_f32 v25, v25, v146\n"
    "s_waitcnt lgkmcnt(15)\n"
    "v_mfma_f32_32x32x16_bf16 a[0:15], a[176:179], v[172:175], a[0:15]\n"
    "buffer_store_dwordx4 a[208:211], v1, s[64:67], 0 offen offset:0 sc1\n"
    "v_exp_f32 v148, v148\n"
    "v_add_f32 v25, v25, v147\n"
    "v_cvt_pk_bf16_f32 v189, v146, v147\n"
    "v_mfma_f32_32x32x16_bf16 a[16:31], a[176:179], v[180:183], a[16:31]\n"
    "v_exp_f32 v149, v149\n"
    "v_add_f32 v25, v25, v148\n"
    "v_mfma_f32_32x32x16_bf16 a[32:47], a[180:183], v[172:175], a[32:47]\n"
    "buffer_store_dwordx4 a[212:215], v1, s[64:67], 0 offen offset:1024 sc1\n"
    "v_exp_f32 v150, v150\n"
    "v_add_f32 v25, v25, v149\n"
    "v_cvt_pk_bf16_f32 v190, v148, v149\n"
    "v_mfma_f32_32x32x16_bf16 a[48:63], a[180:183], v[180:183], a[48:63]\n"
    "v_exp_f32 v151, v151\n"
    "v_add_f32 v25, v25, v150\n"
    "v_mfma_f32_32x32x16_bf16 a[64:79], a[184:187], v[172:175], a[64:79]\n"
    "buffer_store_dwordx4 a[216:219], v1, s[64:67], 0 offen offset:2048 sc1\n"
    "v_exp_f32 v152, v152\n"
    "v_add_f32 v25, v25, v151\n"
    "v_cvt_pk_bf16_f32 v191, v150, v151\n"
    "v_mfma_f32_32x32x16_bf16 a[80:95], a[184:187], v[180:183], a[80:95]\n"
    "v_exp_f32 v153, v153\n"
    "v_add_f32 v26, v26, v152\n"
    "v_mfma_f32_32x32x16_bf16 a[96:111], a[188:191], v[172:175], a[96:111]\n"
    "buffer_store_dwordx4 a[220:223], v1, s[64:67], 0 offen offset:3072 sc1\n"
    "v_exp_f32 v154, v154\n"
    "v_add_f32 v26, v26, v153\n"
    "v_cvt_pk_bf16_f32 v192, v152, v153\n"
    "v_mfma_f32_32x32x16_bf16 a[112:127], a[188:191], v[180:183], a[112:127]\n"
    "v_exp_f32 v155, v155\n"
    "v_add_f32 v26, v26, v154\n"
    "v_mfma_f32_32x32x16_bf16 v[104:119], a[128:131], v[40:43], v[200:215]\n"
    "ds_read_b64_tr_b16 a[160:161], v18 offset:8192\n"
    "v_exp_f32 v156, v156\n"
    "v_add_f32 v26, v26, v155\n"
    "v_cvt_pk_bf16_f32 v193, v154, v155\n"
    "s_waitcnt lgkmcnt(14)\n"
    "v_mfma_f32_32x32x16_bf16 v[104:119], a[132:135], v[44:47], v[104:119]\n"
    "ds_read_b64_tr_b16 a[162:163], v18 offset:9216\n"
    "v_exp_f32 v157, v157\n"
    "v_add_f32 v26, v26, v156\n"
    "s_waitcnt lgkmcnt(13)\n"
    "v_mfma_f32_32x32x16_bf16 v[104:119], a[136:139], v[48:51], v[104:119]\n"
    "ds_read_b64_tr_b16 a[164:165], v19 offset:8192\n"
    "v_exp_f32 v158, v158\n"
    "v_add_f32 v26, v26, v157\n"
    "v_cvt_pk_bf16_f32 v194, v156, v157\n"
    "s_waitcnt lgkmcnt(12)\n"
    "v_mfma_f32_32x32x16_bf16 v[104:119], a[140:143], v[52:55], v[104:119]\n"
    "ds_read_b64_tr_b16 a[166:167], v19 offset:9216\n"
    "v_exp_f32 v159, v159\n"
    "v_add_f32 v26, v26, v158\n"
    "s_waitcnt lgkmcnt(11)\n"
    "v_mfma_f32_32x32x16_bf16 v[104:119], a[144:147], v[56:59], v[104:119]\n"
    "ds_read_b64_tr_b16 a[168:169], v20 offset:8192\n"
    "v_exp_f32 v160, v160\n"
    "v_add_f32 v26, v26, v159\n"
    "v_cvt_pk_bf16_f32 v195, v158, v159\n"
    "s_waitcnt lgkmcnt(10)\n"
    "v_mfma_f32_32x32x16_bf16 v[104:119], a[148:151], v[60:63], v[104:119]\n"
    "ds_read_b64_tr_b16 a[170:171], v20 offset:9216\n"
    "v_exp_f32 v161, v161\n"
    "v_add_f32 v26, v26, v160\n"
    "s_waitcnt lgkmcnt(9)\n"
    "v_mfma_f32_32x32x16_bf16 v[104:119], a[152:155], v[64:67], v[104:119]\n"
    "ds_read_b64_tr_b16 a[172:173], v21 offset:8192\n"
    "v_exp_f32 v162, v162\n"
    "v_add_f32 v26, v26, v161\n"
    "v_cvt_pk_bf16_f32 v196, v160, v161\n"
    "s_waitcnt lgkmcnt(8)\n"
    "v_mfma_f32_32x32x16_bf16 v[104:119], a[156:159], v[68:71], v[104:119]\n"
    "ds_read_b64_tr_b16 a[174:175], v21 offset:9216\n"
    "v_exp_f32 v163, v163\n"
    "v_add_f32 v26, v26, v162\n"
    "v_mfma_f32_32x32x16_bf16 v[120:135], a[128:131], v[72:75], v[216:231]\n"
    "ds_read_b64_tr_b16 a[176:177], v18 offset:12288\n"
    "v_exp_f32 v164, v164\n"
    "v_add_f32 v26, v26, v163\n"
    "v_cvt_pk_bf16_f32 v197, v162, v163\n"
    "v_mfma_f32_32x32x16_bf16 v[120:135], a[132:135], v[76:79], v[120:135]\n"
    "ds_read_b64_tr_b16 a[178:179], v18 offset:13312\n"
    "v_exp_f32 v165, v165\n"
    "v_add_f32 v26, v26, v164\n"
    "v_mfma_f32_32x32x16_bf16 v[120:135], a[136:139], v[80:83], v[120:135]\n"
    "ds_read_b64_tr_b16 a[180:181], v19 offset:12288\n"
    "v_exp_f32 v166, v166\n"
    "v_add_f32 v26, v26, v165\n"
    "v_cvt_pk_bf16_f32 v198, v164, v165\n"
    "v_mfma_f32_32x32x16_bf16 v[120:135], a[140:143], v[84:87], v[120:135]\n"
    "ds_read_b64_tr_b16 a[182:183], v19 offset:13312\n"
    "v_exp_f32 v167, v167\n"
    "v_add_f32 v26, v26, v166\n"
    "v_mfma_f32_32x32x16_bf16 v[120:135], a[144:147], v[88:91], v[120:135]\n"
    "ds_read_b64_tr_b16 a[184:185], v20 offset:12288\n"
    "v_add_f32 v26, v26, v167\n"
    "v_cvt_pk_bf16_f32 v199, v166, v167\n"
    "v_exp_f32 v104, v104\n"
    "v_mfma_f32_32x32x16_bf16 v[120:135], a[148:151], v[92:95], v[120:135]\n"
    "ds_read_b64_tr_b16 a[186:187], v20 offset:13312\n"
    "v_exp_f32 v105, v105\n"
    "v_add_f32 v25, v25, v104\n"
    "v_mfma_f32_32x32x16_bf16 v[120:135], a[152:155], v[96:99], v[120:135]\n"
    "ds_read_b64_tr_b16 a[188:189], v21 offset:12288\n"
    "v_exp_f32 v106, v106\n"
    "v_add_f32 v25, v25, v105\n"
    "v_cvt_pk_bf16_f32 v168, v104, v105\n"
    "v_mfma_f32_32x32x16_bf16 v[120:135], a[156:159], v[100:103], v[120:135]\n"
    "ds_read_b64_tr_b16 a[190:191], v21 offset:13312\n"
    "v_exp_f32 v107, v107\n"
    "v_add_f32 v25, v25, v106\n"
    "s_waitcnt lgkmcnt(14)\n"
    "v_mfma_f32_32x32x16_bf16 a[0:15], a[160:163], v[184:187], a[0:15]\n"
    "ds_read_b128 a[128:131], v22 offset:57344\n"
    "buffer_store_dwordx4 v[236:239], v1, s[76:79], 0 offen offset:0 sc1\n"
    "v_exp_f32 v108, v108\n"
    "v_add_f32 v25, v25, v107\n"
    "v_cvt_pk_bf16_f32 v169, v106, v107\n"
    "v_mfma_f32_32x32x16_bf16 a[16:31], a[160:163], v[192:195], a[16:31]\n"
    "ds_read_b128 a[132:135], v23 offset:57344\n"
    "v_exp_f32 v109, v109\n"
    "v_add_f32 v25, v25, v108\n"
    "s_waitcnt lgkmcnt(14)\n"
    "v_mfma_f32_32x32x16_bf16 a[32:47], a[164:167], v[184:187], a[32:47]\n"
    "ds_read_b128 a[136:139], v22 offset:57856\n"
    "buffer_store_dwordx4 v[240:243], v1, s[76:79], 0 offen offset:1024 sc1\n"
    "v_exp_f32 v110, v110\n"
    "v_add_f32 v25, v25, v109\n"
    "v_cvt_pk_bf16_f32 v170, v108, v109\n"
    "v_mfma_f32_32x32x16_bf16 a[48:63], a[164:167], v[192:195], a[48:63]\n"
    "ds_read_b128 a[140:143], v23 offset:57856\n"
    "v_exp_f32 v111, v111\n"
    "v_add_f32 v25, v25, v110\n"
    "s_waitcnt lgkmcnt(14)\n"
    "v_mfma_f32_32x32x16_bf16 a[64:79], a[168:171], v[184:187], a[64:79]\n"
    "ds_read_b128 a[144:147], v22 offset:58368\n"
    "buffer_store_dwordx4 v[244:247], v1, s[76:79], 0 offen offset:2048 sc1\n"
    "v_exp_f32 v112, v112\n"
    "v_add_f32 v25, v25, v111\n"
    "v_cvt_pk_bf16_f32 v171, v110, v111\n"
    "v_mfma_f32_32x32x16_bf16 a[80:95], a[168:171], v[192:195], a[80:95]\n"
    "ds_read_b128 a[148:151], v23 offset:58368\n"
    "v_exp_f32 v113, v113\n"
    "v_add_f32 v25, v25, v112\n"
    "s_waitcnt lgkmcnt(14)\n"
    "v_mfma_f32_32x32x16_bf16 a[96:111], a[172:175], v[184:187], a[96:111]\n"
    "ds_read_b128 a[152:155], v22 offset:58880\n"
    "buffer_store_dwordx4 v[248:251], v1, s[76:79], 0 offen offset:3072 sc1\n"
    "v_exp_f32 v114, v114\n"
    "v_add_f32 v25, v25, v113\n"
    "v_cvt_pk_bf16_f32 v172, v112, v113\n"
    "v_mfma_f32_32x32x16_bf16 a[112:127], a[172:175], v[192:195], a[112:127]\n"
    "ds_read_b128 a[156:159], v23 offset:58880\n"
    "v_exp_f32 v115, v115\n"
    "v_add_f32 v25, v25, v114\n"
    "s_waitcnt lgkmcnt(14)\n"
    "v_mfma_f32_32x32x16_bf16 a[0:15], a[176:179], v[188:191], a[0:15]\n"
    "v_exp_f32 v116, v116\n"
    "v_add_f32 v25, v25, v115\n"
    "v_cvt_pk_bf16_f32 v173, v114, v115\n"
    "v_mfma_f32_32x32x16_bf16 a[16:31], a[176:179], v[196:199], a[16:31]\n"
    "v_exp_f32 v117, v117\n"
    "v_add_f32 v25, v25, v116\n"
    "s_waitcnt lgkmcnt(12)\n"
    "v_mfma_f32_32x32x16_bf16 a[32:47], a[180:183], v[188:191], a[32:47]\n"
    "v_exp_f32 v118, v118\n"
    "v_add_f32 v25, v25, v117\n"
    "v_cvt_pk_bf16_f32 v174, v116, v117\n"
    "v_mfma_f32_32x32x16_bf16 a[48:63], a[180:183], v[196:199], a[48:63]\n"
    "v_exp_f32 v119, v119\n"
    "v_add_f32 v25, v25, v118\n"
    "s_waitcnt lgkmcnt(10)\n"
    "v_mfma_f32_32x32x16_bf16 a[64:79], a[184:187], v[188:191], a[64:79]\n"
    "v_exp_f32 v120, v120\n"
    "v_add_f32 v25, v25, v119\n"
    "v_cvt_pk_bf16_f32 v175, v118, v119\n"
    "v_mfma_f32_32x32x16_bf16 a[80:95], a[184:187], v[196:199], a[80:95]\n"
    "v_exp_f32 v121, v121\n"
    "v_add_f32 v26, v26, v120\n"
    "s_waitcnt lgkmcnt(8)\n"
    "v_mfma_f32_32x32x16_bf16 a[96:111], a[188:191], v[188:191], a[96:111]\n"
    "v_exp_f32 v122, v122\n"
    "v_add_f32 v26, v26, v121\n"
    "v_cvt_pk_bf16_f32 v176, v120, v121\n"
    "v_mfma_f32_32x32x16_bf16 a[112:127], a[188:191], v[196:199], a[112:127]\n"
    "s_waitcnt vmcnt(20)\n"
    "s_waitcnt lgkmcnt(0)\n"
    "s_barrier\n"
    "v_exp_f32 v123, v123\n"
    "v_add_f32 v26, v26, v122\n"
    "v_mfma_f32_32x32x16_bf16 v[136:151], a[128:131], v[40:43], v[200:215]\n"
    "s_mov_b32 s83, 0x1c000\n"
    "s_add_u32 m0, s80, 0x1c000\n"
    "ds_read_b64_tr_b16 a[160:161], v18 offset:16384\n"
    "v_exp_f32 v124, v124\n"
    "v_add_f32 v26, v26, v123\n"
    "v_cvt_pk_bf16_f32 v177, v122, v123\n"
    "buffer_load_dwordx4 v10, s[60:63], s83 offen lds\n"
    "v_mfma_f32_32x32x16_bf16 v[136:151], a[132:135], v[44:47], v[136:151]\n"
    "ds_read_b64_tr_b16 a[162:163], v18 offset:17408\n"
    "v_exp_f32 v125, v125\n"
    "v_add_f32 v26, v26, v124\n"
    "v_mfma_f32_32x32x16_bf16 v[136:151], a[136:139], v[48:51], v[136:151]\n"
    "s_add_u32 m0, s80, 0x1c400\n"
    "ds_read_b64_tr_b16 a[164:165], v19 offset:16384\n"
    "v_exp_f32 v126, v126\n"
    "v_add_f32 v26, v26, v125\n"
    "v_cvt_pk_bf16_f32 v178, v124, v125\n"
    "buffer_load_dwordx4 v11, s[60:63], s83 offen lds\n"
    "v_mfma_f32_32x32x16_bf16 v[136:151], a[140:143], v[52:55], v[136:151]\n"
    "ds_read_b64_tr_b16 a[166:167], v19 offset:17408\n"
    "v_exp_f32 v127, v127\n"
    "v_add_f32 v26, v26, v126\n"
    "v_mfma_f32_32x32x16_bf16 v[136:151], a[144:147], v[56:59], v[136:151]\n"
    "s_add_u32 m0, s80, 0x1c800\n"
    "ds_read_b64_tr_b16 a[168:169], v20 offset:16384\n"
    "v_exp_f32 v128, v128\n"
    "v_add_f32 v26, v26, v127\n"
    "v_cvt_pk_bf16_f32 v179, v126, v127\n"
    "buffer_load_dwordx4 v12, s[60:63], s83 offen lds\n"
    "v_mfma_f32_32x32x16_bf16 v[136:151], a[148:151], v[60:63], v[136:151]\n"
    "ds_read_b64_tr_b16 a[170:171], v20 offset:17408\n"
    "v_exp_f32 v129, v129\n"
    "v_add_f32 v26, v26, v128\n"
    "v_mfma_f32_32x32x16_bf16 v[136:151], a[152:155], v[64:67], v[136:151]\n"
    "s_add_u32 m0, s80, 0x1cc00\n"
    "ds_read_b64_tr_b16 a[172:173], v21 offset:16384\n"
    "v_exp_f32 v130, v130\n"
    "v_add_f32 v26, v26, v129\n"
    "v_cvt_pk_bf16_f32 v180, v128, v129\n"
    "buffer_load_dwordx4 v13, s[60:63], s83 offen lds\n"
    "v_mfma_f32_32x32x16_bf16 v[136:151], a[156:159], v[68:71], v[136:151]\n"
    "ds_read_b64_tr_b16 a[174:175], v21 offset:17408\n"
    "v_exp_f32 v131, v131\n"
    "v_add_f32 v26, v26, v130\n"
    "v_mfma_f32_32x32x16_bf16 v[152:167], a[128:131], v[72:75], v[216:231]\n"
    "ds_read_b64_tr_b16 a[176:177], v18 offset:20480\n"
    "v_exp_f32 v132, v132\n"
    "v_add_f32 v26, v26, v131\n"
    "v_cvt_pk_bf16_f32 v181, v130, v131\n"
    "v_mfma_f32_32x32x16_bf16 v[152:167], a[132:135], v[76:79], v[152:167]\n"
    "ds_read_b64_tr_b16 a[178:179], v18 offset:21504\n"
    "v_exp_f32 v133, v133\n"
    "v_add_f32 v26, v26, v132\n"
    "v_mfma_f32_32x32x16_bf16 v[152:167], a[136:139], v[80:83], v[152:167]\n"
    "ds_read_b64_tr_b16 a[180:181], v19 offset:20480\n"
    "v_exp_f32 v134, v134\n"
    "v_add_f32 v26, v26, v133\n"
    "v_cvt_pk_bf16_f32 v182, v132, v133\n"
    "v_mfma_f32_32x32x16_bf16 v[152:167], a[140:143], v[84:87], v[152:167]\n"
    "ds_read_b64_tr_b16 a[182:183], v19 offset:21504\n"
    "v_exp_f32 v135, v135\n"
    "v_add_f32 v26, v26, v134\n"
    "v_mfma_f32_32x32x16_bf16 v[152:167], a[144:147], v[88:91], v[152:167]\n"
    "ds_read_b64_tr_b16 a[184:185], v20 offset:20480\n"
    "v_add_f32 v26, v26, v135\n"
    "v_cvt_pk_bf16_f32 v183, v134, v135\n"
    "v_exp_f32 v136, v136\n"
    "v_mfma_f32_32x32x16_bf16 v[152:167], a[148:151], v[92:95], v[152:167]\n"
    "ds_read_b64_tr_b16 a[186:187], v20 offset:21504\n"
    "v_exp_f32 v137, v137\n"
    "v_add_f32 v25, v25, v136\n"
    "v_mfma_f32_32x32x16_bf16 v[152:167], a[152:155], v[96:99], v[152:167]\n"
    "ds_read_b64_tr_b16 a[188:189], v21 offset:20480\n"
    "v_exp_f32 v138, v138\n"
    "v_add_f32 v25, v25, v137\n"
    "v_cvt_pk_bf16_f32 v184, v136, v137\n"
    "v_mfma_f32_32x32x16_bf16 v[152:167], a[156:159], v[100:103], v[152:167]\n"
    "ds_read_b64_tr_b16 a[190:191], v21 offset:21504\n"
    "v_exp_f32 v139, v139\n"
    "v_add_f32 v25, v25, v138\n"
    "s_waitcnt lgkmcnt(14)\n"
    "v_mfma_f32_32x32x16_bf16 a[0:15], a[160:163], v[168:171], a[0:15]\n"
    "ds_read_b128 a[128:131], v22 offset:0\n"
    "v_exp_f32 v140, v140\n"
    "v_add_f32 v25, v25, v139\n"
    "v_cvt_pk_bf16_f32 v185, v138, v139\n"
    "v_mfma_f32_32x32x16_bf16 a[16:31], a[160:163], v[176:179], a[16:31]\n"
    "ds_read_b128 a[132:135], v23 offset:0\n"
    "v_exp_f32 v141, v141\n"
    "v_add_f32 v25, v25, v140\n"
    "s_waitcnt lgkmcnt(14)\n"
    "v_mfma_f32_32x32x16_bf16 a[32:47], a[164:167], v[168:171], a[32:47]\n"
    "ds_read_b128 a[136:139], v22 offset:512\n"
    "v_exp_f32 v142, v142\n"
    "v_add_f32 v25, v25, v141\n"
    "v_cvt_pk_bf16_f32 v186, v140, v141\n"
    "v_mfma_f32_32x32x16_bf16 a[48:63], a[164:167], v[176:179], a[48:63]\n"
    "ds_read_b128 a[140:143], v23 offset:512\n"
    "v_exp_f32 v143, v143\n"
    "v_add_f32 v25, v25, v142\n"
    "s_waitcnt lgkmcnt(14)\n"
    "v_mfma_f32_32x32x16_bf16 a[64:79], a[168:171], v[168:171], a[64:79]\n"
    "ds_read_b128 a[144:147], v22 offset:1024\n"
    "v_exp_f32 v144, v144\n"
    "v_add_f32 v25, v25, v143\n"
    "v_cvt_pk_bf16_f32 v187, v142, v143\n"
    "v_mfma_f32_32x32x16_bf16 a[80:95], a[168:171], v[176:179], a[80:95]\n"
    "ds_read_b128 a[148:151], v23 offset:1024\n"
    "v_exp_f32 v145, v145\n"
    "v_add_f32 v25, v25, v144\n"
    "s_waitcnt lgkmcnt(14)\n"
    "v_mfma_f32_32x32x16_bf16 a[96:111], a[172:175], v[168:171], a[96:111]\n"
    "ds_read_b128 a[152:155], v22 offset:1536\n"
    "v_exp_f32 v146, v146\n"
    "v_add_f32 v25, v25, v145\n"
    "v_cvt_pk_bf16_f32 v188, v144, v145\n"
    "v_mfma_f32_32x32x16_bf16 a[112:127], a[172:175], v[176:179], a[112:127]\n"
    "ds_read_b128 a[156:159], v23 offset:1536\n"
    "v_exp_f32 v147, v147\n"
    "v_add_f32 v25, v25, v146\n"
    "s_waitcnt lgkmcnt(14)\n"
    "v_mfma_f32_32x32x16_bf16 a[0:15], a[176:179], v[172:175], a[0:15]\n"
    "v_exp_f32 v148, v148\n"
    "v_add_f32 v25, v25, v147\n"
    "v_cvt_pk_bf16_f32 v189, v146, v147\n"
    "v_mfma_f32_32x32x16_bf16 a[16:31], a[176:179], v[180:183], a[16:31]\n"
    "v_exp_f32 v149, v149\n"
    "v_add_f32 v25, v25, v148\n"
    "s_waitcnt lgkmcnt(12)\n"
    "v_mfma_f32_32x32x16_bf16 a[32:47], a[180:183], v[172:175], a[32:47]\n"
    "v_exp_f32 v150, v150\n"
    "v_add_f32 v25, v25, v149\n"
    "v_cvt_pk_bf16_f32 v190, v148, v149\n"
    "v_mfma_f32_32x32x16_bf16 a[48:63], a[180:183], v[180:183], a[48:63]\n"
    "v_exp_f32 v151, v151\n"
    "v_add_f32 v25, v25, v150\n"
    "s_waitcnt lgkmcnt(10)\n"
    "v_mfma_f32_32x32x16_bf16 a[64:79], a[184:187], v[172:175], a[64:79]\n"
    "v_exp_f32 v152, v152\n"
    "v_add_f32 v25, v25, v151\n"
    "v_cvt_pk_bf16_f32 v191, v150, v151\n"
    "v_mfma_f32_32x32x16_bf16 a[80:95], a[184:187], v[180:183], a[80:95]\n"
    "v_exp_f32 v153, v153\n"
    "v_add_f32 v26, v26, v152\n"
    "s_waitcnt lgkmcnt(8)\n"
    "v_mfma_f32_32x32x16_bf16 a[96:111], a[188:191], v[172:175], a[96:111]\n"
    "v_exp_f32 v154, v154\n"
    "v_add_f32 v26, v26, v153\n"
    "v_cvt_pk_bf16_f32 v192, v152, v153\n"
    "v_mfma_f32_32x32x16_bf16 a[112:127], a[188:191], v[180:183], a[112:127]\n"
    "v_exp_f32 v155, v155\n"
    "v_add_f32 v26, v26, v154\n"
    "s_waitcnt lgkmcnt(7)\n"
    "v_mfma_f32_32x32x16_bf16 v[104:119], a[128:131], v[40:43], v[200:215]\n"
    "ds_read_b64_tr_b16 a[160:161], v18 offset:24576\n"
    "v_exp_f32 v156, v156\n"
    "v_add_f32 v26, v26, v155\n"
    "v_cvt_pk_bf16_f32 v193, v154, v155\n"
    "s_waitcnt lgkmcnt(7)\n"
    "v_mfma_f32_32x32x16_bf16 v[104:119], a[132:135], v[44:47], v[104:119]\n"
    "ds_read_b64_tr_b16 a[162:163], v18 offset:25600\n"
    "v_exp_f32 v157, v157\n"
    "v_add_f32 v26, v26, v156\n"
    "s_waitcnt lgkmcnt(7)\n"
    "v_mfma_f32_32x32x16_bf16 v[104:119], a[136:139], v[48:51], v[104:119]\n"
    "ds_read_b64_tr_b16 a[164:165], v19 offset:24576\n"
    "v_exp_f32 v158, v158\n"
    "v_add_f32 v26, v26, v157\n"
    "v_cvt_pk_bf16_f32 v194, v156, v157\n"
    "s_waitcnt lgkmcnt(7)\n"
    "v_mfma_f32_32x32x16_bf16 v[104:119], a[140:143], v[52:55], v[104:119]\n"
    "ds_read_b64_tr_b16 a[166:167], v19 offset:25600\n"
    "v_exp_f32 v159, v159\n"
    "v_add_f32 v26, v26, v158\n"
    "s_waitcnt lgkmcnt(7)\n"
    "v_mfma_f32_32x32x16_bf16 v[104:119], a[144:147], v[56:59], v[104:119]\n"
    "ds_read_b64_tr_b16 a[168:169], v20 offset:24576\n"
    "v_exp_f32 v160, v160\n"
    "v_add_f32 v26, v26, v159\n"
    "v_cvt_pk_bf16_f32 v195, v158, v159\n"
    "s_waitcnt lgkmcnt(7)\n"
    "v_mfma_f32_32x32x16_bf16 v[104:119], a[148:151], v[60:63], v[104:119]\n"
    "ds_read_b64_tr_b16 a[170:171], v20 offset:25600\n"
    "v_exp_f32 v161, v161\n"
    "v_add_f32 v26, v26, v160\n"
    "s_waitcnt lgkmcnt(7)\n"
    "v_mfma_f32_32x32x16_bf16 v[104:119], a[152:155], v[64:67], v[104:119]\n"
    "ds_read_b64_tr_b16 a[172:173], v21 offset:24576\n"
    "v_exp_f32 v162, v162\n"
    "v_add_f32 v26, v26, v161\n"
    "v_cvt_pk_bf16_f32 v196, v160, v161\n"
    "s_waitcnt lgkmcnt(7)\n"
    "v_mfma_f32_32x32x16_bf16 v[104:119], a[156:159], v[68:71], v[104:119]\n"
    "ds_read_b64_tr_b16 a[174:175], v21 offset:25600\n"
    "v_exp_f32 v163, v163\n"
    "v_add_f32 v26, v26, v162\n"
    "v_mfma_f32_32x32x16_bf16 v[120:135], a[128:131], v[72:75], v[216:231]\n"
    "ds_read_b64_tr_b16 a[176:177], v18 offset:28672\n"
    "v_exp_f32 v164, v164\n"
    "v_add_f32 v26, v26, v163\n"
    "v_cvt_pk_bf16_f32 v197, v162, v163\n"
    "v_mfma_f32_32x32x16_bf16 v[120:135], a[132:135], v[76:79], v[120:135]\n"
    "ds_read_b64_tr_b16 a[178:179], v18 offset:29696\n"
    "v_exp_f32 v165, v165\n"
    "v_add_f32 v26, v26, v164\n"
    "v_mfma_f32_32x32x16_bf16 v[120:135], a[136:139], v[80:83], v[120:135]\n"
    "ds_read_b64_tr_b16 a[180:181], v19 offset:28672\n"
    "v_exp_f32 v166, v166\n"
    "v_add_f32 v26, v26, v165\n"
    "v_cvt_pk_bf16_f32 v198, v164, v165\n"
    "v_mfma_f32_32x32x16_bf16 v[120:135], a[140:143], v[84:87], v[120:135]\n"
    "ds_read_b64_tr_b16 a[182:183], v19 offset:29696\n"
    "v_exp_f32 v167, v167\n"
    "v_add_f32 v26, v26, v166\n"
    "v_mfma_f32_32x32x16_bf16 v[120:135], a[144:147], v[88:91], v[120:135]\n"
    "ds_read_b64_tr_b16 a[184:185], v20 offset:28672\n"
    "v_add_f32 v26, v26, v167\n"
    "v_cvt_pk_bf16_f32 v199, v166, v167\n"
    "v_exp_f32 v104, v104\n"
    "v_mfma_f32_32x32x16_bf16 v[120:135], a[148:151], v[92:95], v[120:135]\n"
    "ds_read_b64_tr_b16 a[186:187], v20 offset:29696\n"
    "v_exp_f32 v105, v105\n"
    "v_add_f32 v25, v25, v104\n"
    "v_mfma_f32_32x32x16_bf16 v[120:135], a[152:155], v[96:99], v[120:135]\n"
    "ds_read_b64_tr_b16 a[188:189], v21 offset:28672\n"
    "v_exp_f32 v106, v106\n"
    "v_add_f32 v25, v25, v105\n"
    "v_cvt_pk_bf16_f32 v168, v104, v105\n"
    "v_mfma_f32_32x32x16_bf16 v[120:135], a[156:159], v[100:103], v[120:135]\n"
    "ds_read_b64_tr_b16 a[190:191], v21 offset:29696\n"
    "v_exp_f32 v107, v107\n"
    "v_add_f32 v25, v25, v106\n"
    "s_waitcnt lgkmcnt(14)\n"
    "v_mfma_f32_32x32x16_bf16 a[0:15], a[160:163], v[184:187], a[0:15]\n"
    "ds_read_b128 a[128:131], v22 offset:8192\n"
    "v_exp_f32 v108, v108\n"
    "v_add_f32 v25, v25, v107\n"
    "v_cvt_pk_bf16_f32 v169, v106, v107\n"
    "v_mfma_f32_32x32x16_bf16 a[16:31], a[160:163], v[192:195], a[16:31]\n"
    "ds_read_b128 a[132:135], v23 offset:8192\n"
    "v_exp_f32 v109, v109\n"
    "v_add_f32 v25, v25, v108\n"
    "s_waitcnt lgkmcnt(14)\n"
    "v_mfma_f32_32x32x16_bf16 a[32:47], a[164:167], v[184:187], a[32:47]\n"
    "ds_read_b128 a[136:139], v22 offset:8704\n"
    "v_exp_f32 v110, v110\n"
    "v_add_f32 v25, v25, v109\n"
    "v_cvt_pk_bf16_f32 v170, v108, v109\n"
    "v_mfma_f32_32x32x16_bf16 a[48:63], a[164:167], v[192:195], a[48:63]\n"
    "ds_read_b128 a[140:143], v23 offset:8704\n"
    "v_exp_f32 v111, v111\n"
    "v_add_f32 v25, v25, v110\n"
    "s_waitcnt lgkmcnt(14)\n"
    "v_mfma_f32_32x32x16_bf16 a[64:79], a[168:171], v[184:187], a[64:79]\n"
    "ds_read_b128 a[144:147], v22 offset:9216\n"
    "v_exp_f32 v112, v112\n"
    "v_add_f32 v25, v25, v111\n"
    "v_cvt_pk_bf16_f32 v171, v110, v111\n"
    "v_mfma_f32_32x32x16_bf16 a[80:95], a[168:171], v[192:195], a[80:95]\n"
    "ds_read_b128 a[148:151], v23 offset:9216\n"
    "v_exp_f32 v113, v113\n"
    "v_add_f32 v25, v25, v112\n"
    "s_waitcnt lgkmcnt(14)\n"
    "v_mfma_f32_32x32x16_bf16 a[96:111], a[172:175], v[184:187], a[96:111]\n"
    "ds_read_b128 a[152:155], v22 offset:9728\n"
    "v_exp_f32 v114, v114\n"
    "v_add_f32 v25, v25, v113\n"
    "v_cvt_pk_bf16_f32 v172, v112, v113\n"
    "v_mfma_f32_32x32x16_bf16 a[112:127], a[172:175], v[192:195], a[112:127]\n"
    "ds_read_b128 a[156:159], v23 offset:9728\n"
    "v_exp_f32 v115, v115\n"
    "v_add_f32 v25, v25, v114\n"
    "s_waitcnt lgkmcnt(14)\n"
    "v_mfma_f32_32x32x16_bf16 a[0:15], a[176:179], v[188:191], a[0:15]\n"
    "v_exp_f32 v116, v116\n"
    "v_add_f32 v25, v25, v115\n"
    "v_cvt_pk_bf16_f32 v173, v114, v115\n"
    "v_mfma_f32_32x32x16_bf16 a[16:31], a[176:179], v[196:199], a[16:31]\n"
    "v_exp_f32 v117, v117\n"
    "v_add_f32 v25, v25, v116\n"
    "s_waitcnt lgkmcnt(12)\n"
    "v_mfma_f32_32x32x16_bf16 a[32:47], a[180:183], v[188:191], a[32:47]\n"
    "v_exp_f32 v118, v118\n"
    "v_add_f32 v25, v25, v117\n"
    "v_cvt_pk_bf16_f32 v174, v116, v117\n"
    "v_mfma_f32_32x32x16_bf16 a[48:63], a[180:183], v[196:199], a[48:63]\n"
    "v_exp_f32 v119, v119\n"
    "v_add_f32 v25, v25, v118\n"
    "s_waitcnt lgkmcnt(10)\n"
    "v_mfma_f32_32x32x16_bf16 a[64:79], a[184:187], v[188:191], a[64:79]\n"
    "v_exp_f32 v120, v120\n"
    "v_add_f32 v25, v25, v119\n"
    "v_cvt_pk_bf16_f32 v175, v118, v119\n"
    "v_mfma_f32_32x32x16_bf16 a[80:95], a[184:187], v[196:199], a[80:95]\n"
    "v_exp_f32 v121, v121\n"
    "v_add_f32 v26, v26, v120\n"
    "s_waitcnt lgkmcnt(8)\n"
    "v_mfma_f32_32x32x16_bf16 a[96:111], a[188:191], v[188:191], a[96:111]\n"
    "v_exp_f32 v122, v122\n"
    "v_add_f32 v26, v26, v121\n"
    "v_cvt_pk_bf16_f32 v176, v120, v121\n"
    "v_mfma_f32_32x32x16_bf16 a[112:127], a[188:191], v[196:199], a[112:127]\n"
    "s_waitcnt vmcnt(16)\n"
    "s_waitcnt vmcnt(4)\n"
    "s_waitcnt lgkmcnt(0)\n"
    "s_barrier\n"
    "v_exp_f32 v123, v123\n"
    "v_add_f32 v26, v26, v122\n"
    "v_mfma_f32_32x32x16_bf16 v[136:151], a[128:131], v[40:43], v[200:215]\n"
    "s_lshl_b32 s46, %1, 3\n"
    "s_add_u32 s46, s46, 0x8000\n"
    "v_mov_b32 v30, s40\n"
    "v_mov_b32 v31, s41\n"
    "v_lshlrev_b32 v29, 7, v1\n"
    "v_add_u32 v29, s46, v29\n"
    "s_mov_b64 exec, 0xff\n"
    "global_store_dwordx2 v29, v[30:31], s[26:27] offset:0 sc1\n"
    "s_mov_b64 exec, -1\n"
    "ds_read_b64_tr_b16 a[160:161], v18 offset:32768\n"
    "v_exp_f32 v124, v124\n"
    "v_add_f32 v26, v26, v123\n"
    "v_cvt_pk_bf16_f32 v177, v122, v123\n"
    "v_mfma_f32_32x32x16_bf16 v[136:151], a[132:135], v[44:47], v[136:151]\n"
    "ds_read_b64_tr_b16 a[162:163], v18 offset:33792\n"
    "v_exp_f32 v125, v125\n"
    "v_add_f32 v26, v26, v124\n"
    "v_mfma_f32_32x32x16_bf16 v[136:151], a[136:139], v[48:51], v[136:151]\n"
    "ds_read_b64_tr_b16 a[164:165], v19 offset:32768\n"
    "v_exp_f32 v126, v126\n"
    "v_add_f32 v26, v26, v125\n"
    "v_cvt_pk_bf16_f32 v178, v124, v125\n"
    "v_mfma_f32_32x32x16_bf16 v[136:151], a[140:143], v[52:55], v[136:151]\n"
    "ds_read_b64_tr_b16 a[166:167], v19 offset:33792\n"
    "v_exp_f32 v127, v127\n"
    "v_add_f32 v26, v26, v126\n"
    "v_mfma_f32_32x32x16_bf16 v[136:151], a[144:147], v[56:59], v[136:151]\n"
    "ds_read_b64_tr_b16 a[168:169], v20 offset:32768\n"
    "v_exp_f32 v128, v128\n"
    "v_add_f32 v26, v26, v127\n"
    "v_cvt_pk_bf16_f32 v179, v126, v127\n"
    "v_mfma_f32_32x32x16_bf16 v[136:151], a[148:151], v[60:63], v[136:151]\n"
    "ds_read_b64_tr_b16 a[170:171], v20 offset:33792\n"
    "v_exp_f32 v129, v129\n"
    "v_add_f32 v26, v26, v128\n"
    "v_mfma_f32_32x32x16_bf16 v[136:151], a[152:155], v[64:67], v[136:151]\n"
    "ds_read_b64_tr_b16 a[172:173], v21 offset:32768\n"
    "v_exp_f32 v130, v130\n"
    "v_add_f32 v26, v26, v129\n"
    "v_cvt_pk_bf16_f32 v180, v128, v129\n"
    "v_mfma_f32_32x32x16_bf16 v[136:151], a[156:159], v[68:71], v[136:151]\n"
    "ds_read_b64_tr_b16 a[174:175], v21 offset:33792\n"
    "v_exp_f32 v131, v131\n"
    "v_add_f32 v26, v26, v130\n"
    "v_mfma_f32_32x32x16_bf16 v[152:167], a[128:131], v[72:75], v[216:231]\n"
    "ds_read_b64_tr_b16 a[176:177], v18 offset:36864\n"
    "v_exp_f32 v132, v132\n"
    "v_add_f32 v26, v26, v131\n"
    "v_cvt_pk_bf16_f32 v181, v130, v131\n"
    "v_mfma_f32_32x32x16_bf16 v[152:167], a[132:135], v[76:79], v[152:167]\n"
    "ds_read_b64_tr_b16 a[178:179], v18 offset:37888\n"
    "v_exp_f32 v133, v133\n"
    "v_add_f32 v26, v26, v132\n"
    "v_mfma_f32_32x32x16_bf16 v[152:167], a[136:139], v[80:83], v[152:167]\n"
    "ds_read_b64_tr_b16 a[180:181], v19 offset:36864\n"
    "v_exp_f32 v134, v134\n"
    "v_add_f32 v26, v26, v133\n"
    "v_cvt_pk_bf16_f32 v182, v132, v133\n"
    "v_mfma_f32_32x32x16_bf16 v[152:167], a[140:143], v[84:87], v[152:167]\n"
    "ds_read_b64_tr_b16 a[182:183], v19 offset:37888\n"
    "v_exp_f32 v135, v135\n"
    "v_add_f32 v26, v26, v134\n"
    "v_mfma_f32_32x32x16_bf16 v[152:167], a[144:147], v[88:91], v[152:167]\n"
    "ds_read_b64_tr_b16 a[184:185], v20 offset:36864\n"
    "v_add_f32 v26, v26, v135\n"
    "v_cvt_pk_bf16_f32 v183, v134, v135\n"
    "v_exp_f32 v136, v136\n"
    "v_mfma_f32_32x32x16_bf16 v[152:167], a[148:151], v[92:95], v[152:167]\n"
    "ds_read_b64_tr_b16 a[186:187], v20 offset:37888\n"
    "v_exp_f32 v137, v137\n"
    "v_add_f32 v25, v25, v136\n"
    "v_mfma_f32_32x32x16_bf16 v[152:167], a[152:155], v[96:99], v[152:167]\n"
    "ds_read_b64_tr_b16 a[188:189], v21 offset:36864\n"
    "v_exp_f32 v138, v138\n"
    "v_add_f32 v25, v25, v137\n"
    "v_cvt_pk_bf16_f32 v184, v136, v137\n"
    "v_mfma_f32_32x32x16_bf16 v[152:167], a[156:159], v[100:103], v[152:167]\n"
    "ds_read_b64_tr_b16 a[190:191], v21 offset:37888\n"
    "v_exp_f32 v139, v139\n"
    "v_add_f32 v25, v25, v138\n"
    "s_waitcnt lgkmcnt(14)\n"
    "v_mfma_f32_32x32x16_bf16 a[0:15], a[160:163], v[168:171], a[0:15]\n"
    "ds_read_b128 a[128:131], v22 offset:16384\n"
    "v_exp_f32 v140, v140\n"
    "v_add_f32 v25, v25, v139\n"
    "v_cvt_pk_bf16_f32 v185, v138, v139\n"
    "v_mfma_f32_32x32x16_bf16 a[16:31], a[160:163], v[176:179], a[16:31]\n"
    "ds_read_b128 a[132:135], v23 offset:16384\n"
    "v_exp_f32 v141, v141\n"
    "v_add_f32 v25, v25, v140\n"
    "s_waitcnt lgkmcnt(14)\n"
    "v_mfma_f32_32x32x16_bf16 a[32:47], a[164:167], v[168:171], a[32:47]\n"
    "ds_read_b128 a[136:139], v22 offset:16896\n"
    "v_exp_f32 v142, v142\n"
    "v_add_f32 v25, v25, v141\n"
    "v_cvt_pk_bf16_f32 v186, v140, v141\n"
    "v_mfma_f32_32x32x16_bf16 a[48:63], a[164:167], v[176:179], a[48:63]\n"
    "ds_read_b128 a[140:143], v23 offset:16896\n"
    "v_exp_f32 v143, v143\n"
    "v_add_f32 v25, v25, v142\n"
    "s_waitcnt lgkmcnt(14)\n"
    "v_mfma_f32_32x32x16_bf16 a[64:79], a[168:171], v[168:171], a[64:79]\n"
    "ds_read_b128 a[144:147], v22 offset:17408\n"
    "v_exp_f32 v144, v144\n"
    "v_add_f32 v25, v25, v143\n"
    "v_cvt_pk_bf16_f32 v187, v142, v143\n"
    "v_mfma_f32_32x32x16_bf16 a[80:95], a[168:171], v[176:179], a[80:95]\n"
    "ds_read_b128 a[148:151], v23 offset:17408\n"
    "v_exp_f32 v145, v145\n"
    "v_add_f32 v25, v25, v144\n"
    "s_waitcnt lgkmcnt(14)\n"
    "v_mfma_f32_32x32x16_bf16 a[96:111], a[172:175], v[168:171], a[96:111]\n"
    "ds_read_b128 a[152:155], v22 offset:17920\n"
    "v_exp_f32 v146, v146\n"
    "v_add_f32 v25, v25, v145\n"
    "v_cvt_pk_bf16_f32 v188, v144, v145\n"
    "v_mfma_f32_32x32x16_bf16 a[112:127], a[172:175], v[176:179], a[112:127]\n"
    "ds_read_b128 a[156:159], v23 offset:17920\n"
    "v_exp_f32 v147, v147\n"
    "v_add_f32 v25, v25, v146\n"
    "s_waitcnt lgkmcnt(14)\n"
    "v_mfma_f32_32x32x16_bf16 a[0:15], a[176:179], v[172:175], a[0:15]\n"
    "v_exp_f32 v148, v148\n"
    "v_add_f32 v25, v25, v147\n"
    "v_cvt_pk_bf16_f32 v189, v146, v147\n"
    "v_mfma_f32_32x32x16_bf16 a[16:31], a[176:179], v[180:183], a[16:31]\n"
    "v_exp_f32 v149, v149\n"
    "v_add_f32 v25, v25, v148\n"
    "s_waitcnt lgkmcnt(12)\n"
    "v_mfma_f32_32x32x16_bf16 a[32:47], a[180:183], v[172:175], a[32:47]\n"
    "v_exp_f32 v150, v150\n"
    "v_add_f32 v25, v25, v149\n"
    "v_cvt_pk_bf16_f32 v190, v148, v149\n"
    "v_mfma_f32_32x32x16_bf16 a[48:63], a[180:183], v[180:183], a[48:63]\n"
    "v_exp_f32 v151, v151\n"
    "v_add_f32 v25, v25, v150\n"
    "s_waitcnt lgkmcnt(10)\n"
    "v_mfma_f32_32x32x16_bf16 a[64:79], a[184:187], v[172:175], a[64:79]\n"
    "v_exp_f32 v152, v152\n"
    "v_add_f32 v25, v25, v151\n"
    "v_cvt_pk_bf16_f32 v191, v150, v151\n"
    "v_mfma_f32_32x32x16_bf16 a[80:95], a[184:187], v[180:183], a[80:95]\n"
    "v_exp_f32 v153, v153\n"
    "v_add_f32 v26, v26, v152\n"
    "s_waitcnt lgkmcnt(8)\n"
    "v_mfma_f32_32x32x16_bf16 a[96:111], a[188:191], v[172:175], a[96:111]\n"
    "v_exp_f32 v154, v154\n"
    "v_add_f32 v26, v26, v153\n"
    "v_cvt_pk_bf16_f32 v192, v152, v153\n"
    "v_mfma_f32_32x32x16_bf16 a[112:127], a[188:191], v[180:183], a[112:127]\n"
    "v_exp_f32 v155, v155\n"
    "v_add_f32 v26, v26, v154\n"
    "s_waitcnt lgkmcnt(7)\n"
    "v_mfma_f32_32x32x16_bf16 v[104:119], a[128:131], v[40:43], v[200:215]\n"
    "ds_read_b64_tr_b16 a[160:161], v18 offset:40960\n"
    "v_exp_f32 v156, v156\n"
    "v_add_f32 v26, v26, v155\n"
    "v_cvt_pk_bf16_f32 v193, v154, v155\n"
    "s_waitcnt lgkmcnt(7)\n"
    "v_mfma_f32_32x32x16_bf16 v[104:119], a[132:135], v[44:47], v[104:119]\n"
    "ds_read_b64_tr_b16 a[162:163], v18 offset:41984\n"
    "v_exp_f32 v157, v157\n"
    "v_add_f32 v26, v26, v156\n"
    "s_waitcnt lgkmcnt(7)\n"
    "v_mfma_f32_32x32x16_bf16 v[104:119], a[136:139], v[48:51], v[104:119]\n"
    "ds_read_b64_tr_b16 a[164:165], v19 offset:40960\n"
    "v_exp_f32 v158, v158\n"
    "v_add_f32 v26, v26, v157\n"
    "v_cvt_pk_bf16_f32 v194, v156, v157\n"
    "s_waitcnt lgkmcnt(7)\n"
    "v_mfma_f32_32x32x16_bf16 v[104:119], a[140:143], v[52:55], v[104:119]\n"
    "ds_read_b64_tr_b16 a[166:167], v19 offset:41984\n"
    "v_exp_f32 v159, v159\n"
    "v_add_f32 v26, v26, v158\n"
    "s_waitcnt lgkmcnt(7)\n"
    "v_mfma_f32_32x32x16_bf16 v[104:119], a[144:147], v[56:59], v[104:119]\n"
    "ds_read_b64_tr_b16 a[168:169], v20 offset:40960\n"
    "v_exp_f32 v160, v160\n"
    "v_add_f32 v26, v26, v159\n"
    "v_cvt_pk_bf16_f32 v195, v158, v159\n"
    "s_waitcnt lgkmcnt(7)\n"
    "v_mfma_f32_32x32x16_bf16 v[104:119], a[148:151], v[60:63], v[104:119]\n"
    "ds_read_b64_tr_b16 a[170:171], v20 offset:41984\n"
    "v_exp_f32 v161, v161\n"
    "v_add_f32 v26, v26, v160\n"
    "s_waitcnt lgkmcnt(7)\n"
    "v_mfma_f32_32x32x16_bf16 v[104:119], a[152:155], v[64:67], v[104:119]\n"
    "ds_read_b64_tr_b16 a[172:173], v21 offset:40960\n"
    "v_exp_f32 v162, v162\n"
    "v_add_f32 v26, v26, v161\n"
    "v_cvt_pk_bf16_f32 v196, v160, v161\n"
    "s_waitcnt lgkmcnt(7)\n"
    "v_mfma_f32_32x32x16_bf16 v[104:119], a[156:159], v[68:71], v[104:119]\n"
    "ds_read_b64_tr_b16 a[174:175], v21 offset:41984\n"
    "v_exp_f32 v163, v163\n"
    "v_add_f32 v26, v26, v162\n"
    "v_mfma_f32_32x32x16_bf16 v[120:135], a[128:131], v[72:75], v[216:231]\n"
    "ds_read_b64_tr_b16 a[176:177], v18 offset:45056\n"
    "v_exp_f32 v164, v164\n"
    "v_add_f32 v26, v26, v163\n"
    "v_cvt_pk_bf16_f32 v197, v162, v163\n"
    "v_mfma_f32_32x32x16_bf16 v[120:135], a[132:135], v[76:79], v[120:135]\n"
    "ds_read_b64_tr_b16 a[178:179], v18 offset:46080\n"
    "v_exp_f32 v165, v165\n"
    "v_add_f32 v26, v26, v164\n"
    "v_mfma_f32_32x32x16_bf16 v[120:135], a[136:139], v[80:83], v[120:135]\n"
    "ds_read_b64_tr_b16 a[180:181], v19 offset:45056\n"
    "v_exp_f32 v166, v166\n"
    "v_add_f32 v26, v26, v165\n"
    "v_cvt_pk_bf16_f32 v198, v164, v165\n"
    "v_mfma_f32_32x32x16_bf16 v[120:135], a[140:143], v[84:87], v[120:135]\n"
    "ds_read_b64_tr_b16 a[182:183], v19 offset:46080\n"
    "v_exp_f32 v167, v167\n"
    "v_add_f32 v26, v26, v166\n"
    "v_mfma_f32_32x32x16_bf16 v[120:135], a[144:147], v[88:91], v[120:135]\n"
    "ds_read_b64_tr_b16 a[184:185], v20 offset:45056\n"
    "v_add_f32 v26, v26, v167\n"
    "v_cvt_pk_bf16_f32 v199, v166, v167\n"
    "v_exp_f32 v104, v104\n"
    "v_mfma_f32_32x32x16_bf16 v[120:135], a[148:151], v[92:95], v[120:135]\n"
    "ds_read_b64_tr_b16 a[186:187], v20 offset:46080\n"
    "v_exp_f32 v105, v105\n"
    "v_add_f32 v25, v25, v104\n"
    "v_mfma_f32_32x32x16_bf16 v[120:135], a[152:155], v[96:99], v[120:135]\n"
    "ds_read_b64_tr_b16 a[188:189], v21 offset:45056\n"
    "v_exp_f32 v106, v106\n"
    "v_add_f32 v25, v25, v105\n"
    "v_cvt_pk_bf16_f32 v168, v104, v105\n"
    "v_mfma_f32_32x32x16_bf16 v[120:135], a[156:159], v[100:103], v[120:135]\n"
    "ds_read_b64_tr_b16 a[190:191], v21 offset:46080\n"
    "v_exp_f32 v107, v107\n"
    "v_add_f32 v25, v25, v106\n"
    "s_waitcnt lgkmcnt(14)\n"
    "v_mfma_f32_32x32x16_bf16 a[0:15], a[160:163], v[184:187], a[0:15]\n"
    "ds_read_b128 a[128:131], v22 offset:24576\n"
    "v_exp_f32 v108, v108\n"
    "v_add_f32 v25, v25, v107\n"
    "v_cvt_pk_bf16_f32 v169, v106, v107\n"
    "v_mfma_f32_32x32x16_bf16 a[16:31], a[160:163], v[192:195], a[16:31]\n"
    "ds_read_b128 a[132:135], v23 offset:24576\n"
    "v_exp_f32 v109, v109\n"
    "v_add_f32 v25, v25, v108\n"
    "s_waitcnt lgkmcnt(14)\n"
    "v_mfma_f32_32x32x16_bf16 a[32:47], a[164:167], v[184:187], a[32:47]\n"
    "ds_read_b128 a[136:139], v22 offset:25088\n"
    "v_exp_f32 v110, v110\n"
    "v_add_f32 v25, v25, v109\n"
    "v_cvt_pk_bf16_f32 v170, v108, v109\n"
    "v_mfma_f32_32x32x16_bf16 a[48:63], a[164:167], v[192:195], a[48:63]\n"
    "ds_read_b128 a[140:143], v23 offset:25088\n"
    "v_exp_f32 v111, v111\n"
    "v_add_f32 v25, v25, v110\n"
    "s_waitcnt lgkmcnt(14)\n"
    "v_mfma_f32_32x32x16_bf16 a[64:79], a[168:171], v[184:187], a[64:79]\n"
    "ds_read_b128 a[144:147], v22 offset:25600\n"
    "v_exp_f32 v112, v112\n"
    "v_add_f32 v25, v25, v111\n"
    "v_cvt_pk_bf16_f32 v171, v110, v111\n"
    "v_mfma_f32_32x32x16_bf16 a[80:95], a[168:171], v[192:195], a[80:95]\n"
    "ds_read_b128 a[148:151], v23 offset:25600\n"
    "v_exp_f32 v113, v113\n"
    "v_add_f32 v25, v25, v112\n"
    "s_waitcnt lgkmcnt(14)\n"
    "v_mfma_f32_32x32x16_bf16 a[96:111], a[172:175], v[184:187], a[96:111]\n"
    "ds_read_b128 a[152:155], v22 offset:26112\n"
    "v_exp_f32 v114, v114\n"
    "v_add_f32 v25, v25, v113\n"
    "v_cvt_pk_bf16_f32 v172, v112, v113\n"
    "v_mfma_f32_32x32x16_bf16 a[112:127], a[172:175], v[192:195], a[112:127]\n"
    "ds_read_b128 a[156:159], v23 offset:26112\n"
    "v_exp_f32 v115, v115\n"
    "v_add_f32 v25, v25, v114\n"
    "s_waitcnt lgkmcnt(14)\n"
    "v_mfma_f32_32x32x16_bf16 a[0:15], a[176:179], v[188:191], a[0:15]\n"
    "v_exp_f32 v116, v116\n"
    "v_add_f32 v25, v25, v115\n"
    "v_cvt_pk_bf16_f32 v173, v114, v115\n"
    "v_mfma_f32_32x32x16_bf16 a[16:31], a[176:179], v[196:199], a[16:31]\n"
    "v_exp_f32 v117, v117\n"
    "v_add_f32 v25, v25, v116\n"
    "s_waitcnt lgkmcnt(12)\n"
    "v_mfma_f32_32x32x16_bf16 a[32:47], a[180:183], v[188:191], a[32:47]\n"
    "v_exp_f32 v118, v118\n"
    "v_add_f32 v25, v25, v117\n"
    "v_cvt_pk_bf16_f32 v174, v116, v117\n"
    "v_mfma_f32_32x32x16_bf16 a[48:63], a[180:183], v[196:199], a[48:63]\n"
    "v_exp_f32 v119, v119\n"
    "v_add_f32 v25, v25, v118\n"
    "s_waitcnt lgkmcnt(10)\n"
    "v_mfma_f32_32x32x16_bf16 a[64:79], a[184:187], v[188:191], a[64:79]\n"
    "v_exp_f32 v120, v120\n"
    "v_add_f32 v25, v25, v119\n"
    "v_cvt_pk_bf16_f32 v175, v118, v119\n"
    "v_mfma_f32_32x32x16_bf16 a[80:95], a[184:187], v[196:199], a[80:95]\n"
    "v_exp_f32 v121, v121\n"
    "v_add_f32 v26, v26, v120\n"
    "s_waitcnt lgkmcnt(8)\n"
    "v_mfma_f32_32x32x16_bf16 a[96:111], a[188:191], v[188:191], a[96:111]\n"
    "v_exp_f32 v122, v122\n"
    "v_add_f32 v26, v26, v121\n"
    "v_cvt_pk_bf16_f32 v176, v120, v121\n"
    "v_mfma_f32_32x32x16_bf16 a[112:127], a[188:191], v[196:199], a[112:127]\n"
    "s_waitcnt vmcnt(1)\n"
    "s_waitcnt lgkmcnt(0)\n"
    "s_barrier\n"
    "v_exp_f32 v123, v123\n"
    "v_add_f32 v26, v26, v122\n"
    "v_mfma_f32_32x32x16_bf16 v[136:151], a[128:131], v[40:43], v[200:215]\n"
    "ds_read_b64_tr_b16 a[160:161], v18 offset:49152\n"
    "v_exp_f32 v124, v124\n"
    "v_add_f32 v26, v26, v123\n"
    "v_cvt_pk_bf16_f32 v177, v122, v123\n"
    "v_mfma_f32_32x32x16_bf16 v[136:151], a[132:135], v[44:47], v[136:151]\n"
    "ds_read_b64_tr_b16 a[162:163], v18 offset:50176\n"
    "v_exp_f32 v125, v125\n"
    "v_add_f32 v26, v26, v124\n"
    "v_mfma_f32_32x32x16_bf16 v[136:151], a[136:139], v[48:51], v[136:151]\n"
    "ds_read_b64_tr_b16 a[164:165], v19 offset:49152\n"
    "v_exp_f32 v126, v126\n"
    "v_add_f32 v26, v26, v125\n"
    "v_cvt_pk_bf16_f32 v178, v124, v125\n"
    "v_mfma_f32_32x32x16_bf16 v[136:151], a[140:143], v[52:55], v[136:151]\n"
    "ds_read_b64_tr_b16 a[166:167], v19 offset:50176\n"
    "v_exp_f32 v127, v127\n"
    "v_add_f32 v26, v26, v126\n"
    "v_mfma_f32_32x32x16_bf16 v[136:151], a[144:147], v[56:59], v[136:151]\n"
    "ds_read_b64_tr_b16 a[168:169], v20 offset:49152\n"
    "v_exp_f32 v128, v128\n"
    "v_add_f32 v26, v26, v127\n"
    "v_cvt_pk_bf16_f32 v179, v126, v127\n"
    "v_mfma_f32_32x32x16_bf16 v[136:151], a[148:151], v[60:63], v[136:151]\n"
    "ds_read_b64_tr_b16 a[170:171], v20 offset:50176\n"
    "v_exp_f32 v129, v129\n"
    "v_add_f32 v26, v26, v128\n"
    "v_mfma_f32_32x32x16_bf16 v[136:151], a[152:155], v[64:67], v[136:151]\n"
    "ds_read_b64_tr_b16 a[172:173], v21 offset:49152\n"
    "v_exp_f32 v130, v130\n"
    "v_add_f32 v26, v26, v129\n"
    "v_cvt_pk_bf16_f32 v180, v128, v129\n"
    "v_mfma_f32_32x32x16_bf16 v[136:151], a[156:159], v[68:71], v[136:151]\n"
    "ds_read_b64_tr_b16 a[174:175], v21 offset:50176\n"
    "v_exp_f32 v131, v131\n"
    "v_add_f32 v26, v26, v130\n"
    "v_mfma_f32_32x32x16_bf16 v[152:167], a[128:131], v[72:75], v[216:231]\n"
    "ds_read_b64_tr_b16 a[176:177], v18 offset:53248\n"
    "v_exp_f32 v132, v132\n"
    "v_add_f32 v26, v26, v131\n"
    "v_cvt_pk_bf16_f32 v181, v130, v131\n"
    "v_mfma_f32_32x32x16_bf16 v[152:167], a[132:135], v[76:79], v[152:167]\n"
    "ds_read_b64_tr_b16 a[178:179], v18 offset:54272\n"
    "v_exp_f32 v133, v133\n"
    "v_add_f32 v26, v26, v132\n"
    "v_mfma_f32_32x32x16_bf16 v[152:167], a[136:139], v[80:83], v[152:167]\n"
    "ds_read_b64_tr_b16 a[180:181], v19 offset:53248\n"
    "v_exp_f32 v134, v134\n"
    "v_add_f32 v26, v26, v133\n"
    "v_cvt_pk_bf16_f32 v182, v132, v133\n"
    "v_mfma_f32_32x32x16_bf16 v[152:167], a[140:143], v[84:87], v[152:167]\n"
    "ds_read_b64_tr_b16 a[182:183], v19 offset:54272\n"
    "v_exp_f32 v135, v135\n"
    "v_add_f32 v26, v26, v134\n"
    "v_exp_f32 v136, v136\n"
    "v_exp_f32 v137, v137\n"
    "v_mfma_f32_32x32x16_bf16 v[152:167], a[144:147], v[88:91], v[152:167]\n"
    "ds_read_b64_tr_b16 a[184:185], v20 offset:53248\n"
    "v_add_f32 v26, v26, v135\n"
    "v_cvt_pk_bf16_f32 v183, v134, v135\n"
    "v_exp_f32 v138, v138\n"
    "v_exp_f32 v139, v139\n"
    "v_add_f32 v25, v25, v136\n"
    "v_add_f32 v25, v25, v137\n"
    "v_cvt_pk_bf16_f32 v184, v136, v137\n"
    "v_mfma_f32_32x32x16_bf16 v[152:167], a[148:151], v[92:95], v[152:167]\n"
    "ds_read_b64_tr_b16 a[186:187], v20 offset:54272\n"
    "v_exp_f32 v140, v140\n"
    "v_exp_f32 v141, v141\n"
    "v_add_f32 v25, v25, v138\n"
    "v_add_f32 v25, v25, v139\n"
    "v_cvt_pk_bf16_f32 v185, v138, v139\n"
    "v_mfma_f32_32x32x16_bf16 v[152:167], a[152:155], v[96:99], v[152:167]\n"
    "ds_read_b64_tr_b16 a[188:189], v21 offset:53248\n"
    "v_exp_f32 v142, v142\n"
    "v_exp_f32 v143, v143\n"
    "v_add_f32 v25, v25, v140\n"
    "v_add_f32 v25, v25, v141\n"
    "v_cvt_pk_bf16_f32 v186, v140, v141\n"
    "v_mfma_f32_32x32x16_bf16 v[152:167], a[156:159], v[100:103], v[152:167]\n"
    "ds_read_b64_tr_b16 a[190:191], v21 offset:54272\n"
    "v_exp_f32 v144, v144\n"
    "v_exp_f32 v145, v145\n"
    "v_add_f32 v25, v25, v142\n"
    "v_add_f32 v25, v25, v143\n"
    "v_cvt_pk_bf16_f32 v187, v142, v143\n"
    "s_waitcnt lgkmcnt(14)\n"
    "v_mfma_f32_32x32x16_bf16 a[0:15], a[160:163], v[168:171], a[0:15]\n"
    "ds_read_b64_tr_b16 a[224:225], v18 offset:57344\n"
    "v_exp_f32 v146, v146\n"
    "v_exp_f32 v147, v147\n"
    "v_add_f32 v25, v25, v144\n"
    "v_add_f32 v25, v25, v145\n"
    "v_cvt_pk_bf16_f32 v188, v144, v145\n"
    "v_mfma_f32_32x32x16_bf16 a[16:31], a[160:163], v[176:179], a[16:31]\n"
    "ds_read_b64_tr_b16 a[226:227], v18 offset:58368\n"
    "v_exp_f32 v148, v148\n"
    "v_exp_f32 v149, v149\n"
    "v_add_f32 v25, v25, v146\n"
    "v_add_f32 v25, v25, v147\n"
    "v_cvt_pk_bf16_f32 v189, v146, v147\n"
    "s_waitcnt lgkmcnt(14)\n"
    "v_mfma_f32_32x32x16_bf16 a[32:47], a[164:167], v[168:171], a[32:47]\n"
    "ds_read_b64_tr_b16 a[228:229], v19 offset:57344\n"
    "v_exp_f32 v150, v150\n"
    "v_exp_f32 v151, v151\n"
    "v_add_f32 v25, v25, v148\n"
    "v_add_f32 v25, v25, v149\n"
    "v_cvt_pk_bf16_f32 v190, v148, v149\n"
    "v_mfma_f32_32x32x16_bf16 a[48:63], a[164:167], v[176:179], a[48:63]\n"
    "ds_read_b64_tr_b16 a[230:231], v19 offset:58368\n"
    "v_exp_f32 v152, v152\n"
    "v_exp_f32 v153, v153\n"
    "v_add_f32 v25, v25, v150\n"
    "v_add_f32 v25, v25, v151\n"
    "v_cvt_pk_bf16_f32 v191, v150, v151\n"
    "s_waitcnt lgkmcnt(14)\n"
    "v_mfma_f32_32x32x16_bf16 a[64:79], a[168:171], v[168:171], a[64:79]\n"
    "ds_read_b64_tr_b16 a[232:233], v20 offset:57344\n"
    "v_exp_f32 v154, v154\n"
    "v_exp_f32 v155, v155\n"
    "v_add_f32 v26, v26, v152\n"
    "v_add_f32 v26, v26, v153\n"
    "v_cvt_pk_bf16_f32 v192, v152, v153\n"
    "v_mfma_f32_32x32x16_bf16 a[80:95], a[168:171], v[176:179], a[80:95]\n"
    "ds_read_b64_tr_b16 a[234:235], v20 offset:58368\n"
    "v_exp_f32 v156, v156\n"
    "v_exp_f32 v157, v157\n"
    "v_add_f32 v26, v26, v154\n"
    "v_add_f32 v26, v26, v155\n"
    "v_cvt_pk_bf16_f32 v193, v154, v155\n"
    "s_waitcnt lgkmcnt(14)\n"
    "v_mfma_f32_32x32x16_bf16 a[96:111], a[172:175], v[168:171], a[96:111]\n"
    "ds_read_b64_tr_b16 a[236:237], v21 offset:57344\n"
    "v_exp_f32 v158, v158\n"
    "v_exp_f32 v159, v159\n"
    "v_add_f32 v26, v26, v156\n"
    "v_add_f32 v26, v26, v157\n"
    "v_cvt_pk_bf16_f32 v194, v156, v157\n"
    "v_mfma_f32_32x32x16_bf16 a[112:127], a[172:175], v[176:179], a[112:127]\n"
    "ds_read_b64_tr_b16 a[238:239], v21 offset:58368\n"
    "v_exp_f32 v160, v160\n"
    "v_exp_f32 v161, v161\n"
    "v_add_f32 v26, v26, v158\n"
    "v_add_f32 v26, v26, v159\n"
    "v_cvt_pk_bf16_f32 v195, v158, v159\n"
    "s_waitcnt lgkmcnt(14)\n"
    "v_mfma_f32_32x32x16_bf16 a[0:15], a[176:179], v[172:175], a[0:15]\n"
    "ds_read_b64_tr_b16 a[240:241], v18 offset:61440\n"
    "v_exp_f32 v162, v162\n"
    "v_exp_f32 v163, v163\n"
    "v_add_f32 v26, v26, v160\n"
    "v_add_f32 v26, v26, v161\n"
    "v_cvt_pk_bf16_f32 v196, v160, v161\n"
    "v_mfma_f32_32x32x16_bf16 a[16:31], a[176:179], v[180:183], a[16:31]\n"
    "ds_read_b64_tr_b16 a[242:243], v18 offset:62464\n"
    "v_exp_f32 v164, v164\n"
    "v_exp_f32 v165, v165\n"
    "v_add_f32 v26, v26, v162\n"
    "v_add_f32 v26, v26, v163\n"
    "v_cvt_pk_bf16_f32 v197, v162, v163\n"
    "s_waitcnt lgkmcnt(14)\n"
    "v_mfma_f32_32x32x16_bf16 a[32:47], a[180:183], v[172:175], a[32:47]\n"
    "ds_read_b64_tr_b16 a[244:245], v19 offset:61440\n"
    "v_exp_f32 v166, v166\n"
    "v_exp_f32 v167, v167\n"
    "v_add_f32 v26, v26, v164\n"
    "v_add_f32 v26, v26, v165\n"
    "v_cvt_pk_bf16_f32 v198, v164, v165\n"
    "v_mfma_f32_32x32x16_bf16 a[48:63], a[180:183], v[180:183], a[48:63]\n"
    "ds_read_b64_tr_b16 a[246:247], v19 offset:62464\n"
    "v_add_f32 v26, v26, v166\n"
    "v_add_f32 v26, v26, v167\n"
    "v_cvt_pk_bf16_f32 v199, v166, v167\n"
    "s_waitcnt lgkmcnt(14)\n"
    "v_mfma_f32_32x32x16_bf16 a[64:79], a[184:187], v[172:175], a[64:79]\n"
    "ds_read_b64_tr_b16 a[248:249], v20 offset:61440\n"
    "v_mov_b32 v37, v25\n"
    "s_nop 1\n"
    "v_permlane32_swap_b32 v37, v25\n"
    "v_add_f32 v25, v25, v37\n"
    "v_rcp_f32 v30, v25\n"
    "v_mov_b32 v37, v26\n"
    "v_mfma_f32_32x32x16_bf16 a[80:95], a[184:187], v[180:183], a[80:95]\n"
    "ds_read_b64_tr_b16 a[250:251], v20 offset:62464\n"
    "v_permlane32_swap_b32 v37, v26\n"
    "v_add_f32 v26, v26, v37\n"
    "v_rcp_f32 v32, v26\n"
    "v_mov_b32 v35, 0x58800000\n"
    "v_cmp_nle_f32 vcc, v25, v35\n"
    "s_waitcnt lgkmcnt(14)\n"
    "v_mfma_f32_32x32x16_bf16 a[96:111], a[188:191], v[172:175], a[96:111]\n"
    "ds_read_b64_tr_b16 a[252:253], v21 offset:61440\n"
    "s_nop 0\n"
    "s_mov_b64 s[50:51], vcc\n"
    "v_cmp_nle_f32 vcc, v26, v35\n"
    "s_nop 3\n"
    "s_or_b64 s[50:51], s[50:51], vcc\n"
    "s_cmp_lg_u64 s[50:51], 0\n"
    "s_cselect_b32 s49, 1, 0\n"
    "v_mfma_f32_32x32x16_bf16 a[112:127], a[188:191], v[180:183], a[112:127]\n"
    "ds_read_b64_tr_b16 a[254:255], v21 offset:62464\n"
    "s_cmp_eq_u32 s98, 0\n"
    "s_cselect_b32 s49, s49, 0\n"
    "s_lshl_b32 s89, s34, 2\n"
    "s_add_u32 s89, s89, 0x20040\n"
    "v_mov_b32 v36, s49\n"
    "s_waitcnt lgkmcnt(14)\n"
    "v_mfma_f32_32x32x16_bf16 a[0:15], a[224:227], v[184:187], a[0:15]\n"
    "v_mov_b32 v38, s89\n"
    "ds_write_b32 v38, v36 offset:0\n"
    "v_mfma_f32_32x32x16_bf16 a[16:31], a[224:227], v[192:195], a[16:31]\n"
    "s_waitcnt lgkmcnt(7)\n"
    "v_mfma_f32_32x32x16_bf16 a[0:15], a[240:243], v[188:191], a[0:15]\n"
    "v_mfma_f32_32x32x16_bf16 a[16:31], a[240:243], v[196:199], a[16:31]\n"
    "v_mfma_f32_32x32x16_bf16 a[32:47], a[228:231], v[184:187], a[32:47]\n"
    "v_mfma_f32_32x32x16_bf16 a[48:63], a[228:231], v[192:195], a[48:63]\n"
    "s_waitcnt lgkmcnt(5)\n"
    "v_mfma_f32_32x32x16_bf16 a[32:47], a[244:247], v[188:191], a[32:47]\n"
    "s_nop 6\n"
    "v_accvgpr_read_b32 v104, a0\n"
    "v_accvgpr_read_b32 v105, a1\n"
    "v_accvgpr_read_b32 v106, a2\n"
    "v_accvgpr_read_b32 v107, a3\n"
    "v_accvgpr_read_b32 v108, a4\n"
    "v_accvgpr_read_b32 v109, a5\n"
    "v_accvgpr_read_b32 v110, a6\n"
    "v_accvgpr_read_b32 v111, a7\n"
    "v_mfma_f32_32x32x16_bf16 a[48:63], a[244:247], v[196:199], a[48:63]\n"
    "v_pk_mul_f32 v[104:105], v[104:105], v[30:31] op_sel_hi:[1,0]\n"
    "v_pk_mul_f32 v[106:107], v[106:107], v[30:31] op_sel_hi:[1,0]\n"
    "v_pk_mul_f32 v[108:109], v[108:109], v[30:31] op_sel_hi:[1,0]\n"
    "v_pk_mul_f32 v[110:111], v[110:111], v[30:31] op_sel_hi:[1,0]\n"
    "v_cvt_pk_bf16_f32 v168, v104, v105\n"
    "v_cvt_pk_bf16_f32 v169, v106, v107\n"
    "v_cvt_pk_bf16_f32 v170, v108, v109\n"
    "v_mfma_f32_32x32x16_bf16 a[64:79], a[232:235], v[184:187], a[64:79]\n"
    "v_cvt_pk_bf16_f32 v171, v110, v111\n"
    "s_mov_b32 s47, 0\n"
    "buffer_store_dwordx4 v[168:171], v24, s[68:71], s47 offen offset:0 sc1\n"
    "v_accvgpr_read_b32 v112, a8\n"
    "v_accvgpr_read_b32 v113, a9\n"
    "v_accvgpr_read_b32 v114, a10\n"
    "v_accvgpr_read_b32 v115, a11\n"
    "v_mfma_f32_32x32x16_bf16 a[80:95], a[232:235], v[192:195], a[80:95]\n"
    "v_accvgpr_read_b32 v116, a12\n"
    "v_accvgpr_read_b32 v117, a13\n"
    "v_accvgpr_read_b32 v118, a14\n"
    "v_accvgpr_read_b32 v119, a15\n"
    "v_pk_mul_f32 v[112:113], v[112:113], v[30:31] op_sel_hi:[1,0]\n"
    "v_pk_mul_f32 v[114:115], v[114:115], v[30:31] op_sel_hi:[1,0]\n"
    "v_pk_mul_f32 v[116:117], v[116:117], v[30:31] op_sel_hi:[1,0]\n"
    "s_waitcnt lgkmcnt(3)\n"
    "v_mfma_f32_32x32x16_bf16 a[64:79], a[248:251], v[188:191], a[64:79]\n"
    "v_pk_mul_f32 v[118:119], v[118:119], v[30:31] op_sel_hi:[1,0]\n"
    "v_cvt_pk_bf16_f32 v172, v112, v113\n"
    "v_cvt_pk_bf16_f32 v173, v114, v115\n"
    "v_cvt_pk_bf16_f32 v174, v116, v117\n"
    "v_cvt_pk_bf16_f32 v175, v118, v119\n"
    "s_mov_b32 s47, 0\n"
    "buffer_store_dwordx4 v[172:175], v24, s[68:71], s47 offen offset:512 sc1\n"
    "v_mfma_f32_32x32x16_bf16 a[80:95], a[248:251], v[196:199], a[80:95]\n"
    "v_accvgpr_read_b32 v120, a16\n"
    "v_accvgpr_read_b32 v121, a17\n"
    "v_accvgpr_read_b32 v122, a18\n"
    "v_accvgpr_read_b32 v123, a19\n"
    "v_accvgpr_read_b32 v124, a20\n"
    "v_accvgpr_read_b32 v125, a21\n"
    "v_accvgpr_read_b32 v126, a22\n"
    "v_accvgpr_read_b32 v127, a23\n"
    "v_mfma_f32_32x32x16_bf16 a[96:111], a[236:239], v[184:187], a[96:111]\n"
    "v_pk_mul_f32 v[120:121], v[120:121], v[32:33] op_sel_hi:[1,0]\n"
    "v_pk_mul_f32 v[122:123], v[122:123], v[32:33] op_sel_hi:[1,0]\n"
    "v_pk_mul_f32 v[124:125], v[124:125], v[32:33] op_sel_hi:[1,0]\n"
    "v_pk_mul_f32 v[126:127], v[126:127], v[32:33] op_sel_hi:[1,0]\n"
    "v_cvt_pk_bf16_f32 v176, v120, v121\n"
    "v_cvt_pk_bf16_f32 v177, v122, v123\n"
    "v_cvt_pk_bf16_f32 v178, v124, v125\n"
    "v_mfma_f32_32x32x16_bf16 a[112:127], a[236:239], v[192:195], a[112:127]\n"
    "v_cvt_pk_bf16_f32 v179, v126, v127\n"
    "s_mov_b32 s47, 0\n"
    "buffer_store_dwordx4 v[176:179], v24, s[68:71], s47 offen offset:2048 sc1\n"
    "v_accvgpr_read_b32 v128, a24\n"
    "v_accvgpr_read_b32 v129, a25\n"
    "v_accvgpr_read_b32 v130, a26\n"
    "v_accvgpr_read_b32 v131, a27\n"
    "s_waitcnt lgkmcnt(1)\n"
    "v_mfma_f32_32x32x16_bf16 a[96:111], a[252:255], v[188:191], a[96:111]\n"
    "v_accvgpr_read_b32 v132, a28\n"
    "v_accvgpr_read_b32 v133, a29\n"
    "v_accvgpr_read_b32 v134, a30\n"
    "v_accvgpr_read_b32 v135, a31\n"
    "v_pk_mul_f32 v[128:129], v[128:129], v[32:33] op_sel_hi:[1,0]\n"
    "v_pk_mul_f32 v[130:131], v[130:131], v[32:33] op_sel_hi:[1,0]\n"
    "v_pk_mul_f32 v[132:133], v[132:133], v[32:33] op_sel_hi:[1,0]\n"
    "v_mfma_f32_32x32x16_bf16 a[112:127], a[252:255], v[196:199], a[112:127]\n"
    "v_pk_mul_f32 v[134:135], v[134:135], v[32:33] op_sel_hi:[1,0]\n"
    "v_cvt_pk_bf16_f32 v180, v128, v129\n"
    "v_cvt_pk_bf16_f32 v181, v130, v131\n"
    "v_cvt_pk_bf16_f32 v182, v132, v133\n"
    "v_cvt_pk_bf16_f32 v183, v134, v135\n"
    "s_mov_b32 s47, 0\n"
    "buffer_store_dwordx4 v[180:183], v24, s[68:71], s47 offen offset:2560 sc1\n"
    "v_accvgpr_read_b32 v104, a32\n"
    "v_accvgpr_read_b32 v105, a33\n"
    "v_accvgpr_read_b32 v106, a34\n"
    "v_accvgpr_read_b32 v107, a35\n"
    "v_accvgpr_read_b32 v108, a36\n"
    "v_accvgpr_read_b32 v109, a37\n"
    "v_accvgpr_read_b32 v110, a38\n"
    "v_accvgpr_read_b32 v111, a39\n"
    "v_pk_mul_f32 v[104:105], v[104:105], v[30:31] op_sel_hi:[1,0]\n"
    "v_pk_mul_f32 v[106:107], v[106:107], v[30:31] op_sel_hi:[1,0]\n"
    "v_pk_mul_f32 v[108:109], v[108:109], v[30:31] op_sel_hi:[1,0]\n"
    "v_pk_mul_f32 v[110:111], v[110:111], v[30:31] op_sel_hi:[1,0]\n"
    "v_cvt_pk_bf16_f32 v168, v104, v105\n"
    "v_cvt_pk_bf16_f32 v169, v106, v107\n"
    "v_cvt_pk_bf16_f32 v170, v108, v109\n"
    "v_cvt_pk_bf16_f32 v171, v110, v111\n"
    "s_mov_b32 s47, 0x1000\n"
    "buffer_store_dwordx4 v[168:171], v24, s[68:71], s47 offen offset:0 sc1\n"
    "v_accvgpr_read_b32 v112, a40\n"
    "v_accvgpr_read_b32 v113, a41\n"
    "v_accvgpr_read_b32 v114, a42\n"
    "v_accvgpr_read_b32 v115, a43\n"
    "v_accvgpr_read_b32 v116, a44\n"
    "v_accvgpr_read_b32 v117, a45\n"
    "v_accvgpr_read_b32 v118, a46\n"
    "v_accvgpr_read_b32 v119, a47\n"
    "v_pk_mul_f32 v[112:113], v[112:113], v[30:31] op_sel_hi:[1,0]\n"
    "v_pk_mul_f32 v[114:115], v[114:115], v[30:31] op_sel_hi:[1,0]\n"
    "v_pk_mul_f32 v[116:117], v[116:117], v[30:31] op_sel_hi:[1,0]\n"
    "v_pk_mul_f32 v[118:119], v[118:119], v[30:31] op_sel_hi:[1,0]\n"
    "v_cvt_pk_bf16_f32 v172, v112, v113\n"
    "v_cvt_pk_bf16_f32 v173, v114, v115\n"
    "v_cvt_pk_bf16_f32 v174, v116, v117\n"
    "v_cvt_pk_bf16_f32 v175, v118, v119\n"
    "s_mov_b32 s47, 0x1000\n"
    "buffer_store_dwordx4 v[172:175], v24, s[68:71], s47 offen offset:512 sc1\n"
    "v_accvgpr_read_b32 v120, a48\n"
    "v_accvgpr_read_b32 v121, a49\n"
    "v_accvgpr_read_b32 v122, a50\n"
    "v_accvgpr_read_b32 v123, a51\n"
    "v_accvgpr_read_b32 v124, a52\n"
    "v_accvgpr_read_b32 v125, a53\n"
    "v_accvgpr_read_b32 v126, a54\n"
    "v_accvgpr_read_b32 v127, a55\n"
    "v_pk_mul_f32 v[120:121], v[120:121], v[32:33] op_sel_hi:[1,0]\n"
    "v_pk_mul_f32 v[122:123], v[122:123], v[32:33] op_sel_hi:[1,0]\n"
    "v_pk_mul_f32 v[124:125], v[124:125], v[32:33] op_sel_hi:[1,0]\n"
    "v_pk_mul_f32 v[126:127], v[126:127], v[32:33] op_sel_hi:[1,0]\n"
    "v_cvt_pk_bf16_f32 v176, v120, v121\n"
    "v_cvt_pk_bf16_f32 v177, v122, v123\n"
    "v_cvt_pk_bf16_f32 v178, v124, v125\n"
    "v_cvt_pk_bf16_f32 v179, v126, v127\n"
    "s_mov_b32 s47, 0x1000\n"
    "buffer_store_dwordx4 v[176:179], v24, s[68:71], s47 offen offset:2048 sc1\n"
    "v_accvgpr_read_b32 v128, a56\n"
    "v_accvgpr_read_b32 v129, a57\n"
    "v_accvgpr_read_b32 v130, a58\n"
    "v_accvgpr_read_b32 v131, a59\n"
    "v_accvgpr_read_b32 v132, a60\n"
    "v_accvgpr_read_b32 v133, a61\n"
    "v_accvgpr_read_b32 v134, a62\n"
    "v_accvgpr_read_b32 v135, a63\n"
    "v_pk_mul_f32 v[128:129], v[128:129], v[32:33] op_sel_hi:[1,0]\n"
    "v_pk_mul_f32 v[130:131], v[130:131], v[32:33] op_sel_hi:[1,0]\n"
    "v_pk_mul_f32 v[132:133], v[132:133], v[32:33] op_sel_hi:[1,0]\n"
    "v_pk_mul_f32 v[134:135], v[134:135], v[32:33] op_sel_hi:[1,0]\n"
    "v_cvt_pk_bf16_f32 v180, v128, v129\n"
    "v_cvt_pk_bf16_f32 v181, v130, v131\n"
    "v_cvt_pk_bf16_f32 v182, v132, v133\n"
    "v_cvt_pk_bf16_f32 v183, v134, v135\n"
    "s_mov_b32 s47, 0x1000\n"
    "buffer_store_dwordx4 v[180:183], v24, s[68:71], s47 offen offset:2560 sc1\n"
    "v_accvgpr_read_b32 v104, a64\n"
    "v_accvgpr_read_b32 v105, a65\n"
    "v_accvgpr_read_b32 v106, a66\n"
    "v_accvgpr_read_b32 v107, a67\n"
    "v_accvgpr_read_b32 v108, a68\n"
    "v_accvgpr_read_b32 v109, a69\n"
    "v_accvgpr_read_b32 v110, a70\n"
    "v_accvgpr_read_b32 v111, a71\n"
    "v_pk_mul_f32 v[104:105], v[104:105], v[30:31] op_sel_hi:[1,0]\n"
    "v_pk_mul_f32 v[106:107], v[106:107], v[30:31] op_sel_hi:[1,0]\n"
    "v_pk_mul_f32 v[108:109], v[108:109], v[30:31] op_sel_hi:[1,0]\n"
    "v_pk_mul_f32 v[110:111], v[110:111], v[30:31] op_sel_hi:[1,0]\n"
    "v_cvt_pk_bf16_f32 v168, v104, v105\n"
    "v_cvt_pk_bf16_f32 v169, v106, v107\n"
    "v_cvt_pk_bf16_f32 v170, v108, v109\n"
    "v_cvt_pk_bf16_f32 v171, v110, v111\n"
    "s_mov_b32 s47, 0x2000\n"
    "buffer_store_dwordx4 v[168:171], v24, s[68:71], s47 offen offset:0 sc1\n"
    "v_accvgpr_read_b32 v112, a72\n"
    "v_accvgpr_read_b32 v113, a73\n"
    "v_accvgpr_read_b32 v114, a74\n"
    "v_accvgpr_read_b32 v115, a75\n"
    "v_accvgpr_read_b32 v116, a76\n"
    "v_accvgpr_read_b32 v117, a77\n"
    "v_accvgpr_read_b32 v118, a78\n"
    "v_accvgpr_read_b32 v119, a79\n"
    "v_pk_mul_f32 v[112:113], v[112:113], v[30:31] op_sel_hi:[1,0]\n"
    "v_pk_mul_f32 v[114:115], v[114:115], v[30:31] op_sel_hi:[1,0]\n"
    "v_pk_mul_f32 v[116:117], v[116:117], v[30:31] op_sel_hi:[1,0]\n"
    "v_pk_mul_f32 v[118:119], v[118:119], v[30:31] op_sel_hi:[1,0]\n"
    "v_cvt_pk_bf16_f32 v172, v112, v113\n"
    "v_cvt_pk_bf16_f32 v173, v114, v115\n"
    "v_cvt_pk_bf16_f32 v174, v116, v117\n"
    "v_cvt_pk_bf16_f32 v175, v118, v119\n"
    "s_mov_b32 s47, 0x2000\n"
    "buffer_store_dwordx4 v[172:175], v24, s[68:71], s47 offen offset:512 sc1\n"
    "v_accvgpr_read_b32 v120, a80\n"
    "v_accvgpr_read_b32 v121, a81\n"
    "v_accvgpr_read_b32 v122, a82\n"
    "v_accvgpr_read_b32 v123, a83\n"
    "v_accvgpr_read_b32 v124, a84\n"
    "v_accvgpr_read_b32 v125, a85\n"
    "v_accvgpr_read_b32 v126, a86\n"
    "v_accvgpr_read_b32 v127, a87\n"
    "v_pk_mul_f32 v[120:121], v[120:121], v[32:33] op_sel_hi:[1,0]\n"
    "v_pk_mul_f32 v[122:123], v[122:123], v[32:33] op_sel_hi:[1,0]\n"
    "v_pk_mul_f32 v[124:125], v[124:125], v[32:33] op_sel_hi:[1,0]\n"
    "v_pk_mul_f32 v[126:127], v[126:127], v[32:33] op_sel_hi:[1,0]\n"
    "v_cvt_pk_bf16_f32 v176, v120, v121\n"
    "v_cvt_pk_bf16_f32 v177, v122, v123\n"
    "v_cvt_pk_bf16_f32 v178, v124, v125\n"
    "v_cvt_pk_bf16_f32 v179, v126, v127\n"
    "s_mov_b32 s47, 0x2000\n"
    "buffer_store_dwordx4 v[176:179], v24, s[68:71], s47 offen offset:2048 sc1\n"
    "v_accvgpr_read_b32 v128, a88\n"
    "v_accvgpr_read_b32 v129, a89\n"
    "v_accvgpr_read_b32 v130, a90\n"
    "v_accvgpr_read_b32 v131, a91\n"
    "v_accvgpr_read_b32 v132, a92\n"
    "v_accvgpr_read_b32 v133, a93\n"
    "v_accvgpr_read_b32 v134, a94\n"
    "v_accvgpr_read_b32 v135, a95\n"
    "v_pk_mul_f32 v[128:129], v[128:129], v[32:33] op_sel_hi:[1,0]\n"
    "v_pk_mul_f32 v[130:131], v[130:131], v[32:33] op_sel_hi:[1,0]\n"
    "v_pk_mul_f32 v[132:133], v[132:133], v[32:33] op_sel_hi:[1,0]\n"
    "v_pk_mul_f32 v[134:135], v[134:135], v[32:33] op_sel_hi:[1,0]\n"
    "v_cvt_pk_bf16_f32 v180, v128, v129\n"
    "v_cvt_pk_bf16_f32 v181, v130, v131\n"
    "v_cvt_pk_bf16_f32 v182, v132, v133\n"
    "v_cvt_pk_bf16_f32 v183, v134, v135\n"
    "s_mov_b32 s47, 0x2000\n"
    "buffer_store_dwordx4 v[180:183], v24, s[68:71], s47 offen offset:2560 sc1\n"
    "v_accvgpr_read_b32 v104, a96\n"
    "v_accvgpr_read_b32 v105, a97\n"
    "v_accvgpr_read_b32 v106, a98\n"
    "v_accvgpr_read_b32 v107, a99\n"
    "v_accvgpr_read_b32 v108, a100\n"
    "v_accvgpr_read_b32 v109, a101\n"
    "v_accvgpr_read_b32 v110, a102\n"
    "v_accvgpr_read_b32 v111, a103\n"
    "v_pk_mul_f32 v[104:105], v[104:105], v[30:31] op_sel_hi:[1,0]\n"
    "v_pk_mul_f32 v[106:107], v[106:107], v[30:31] op_sel_hi:[1,0]\n"
    "v_pk_mul_f32 v[108:109], v[108:109], v[30:31] op_sel_hi:[1,0]\n"
    "v_pk_mul_f32 v[110:111], v[110:111], v[30:31] op_sel_hi:[1,0]\n"
    "v_cvt_pk_bf16_f32 v168, v104, v105\n"
    "v_cvt_pk_bf16_f32 v169, v106, v107\n"
    "v_cvt_pk_bf16_f32 v170, v108, v109\n"
    "v_cvt_pk_bf16_f32 v171, v110, v111\n"
    "s_mov_b32 s47, 0x3000\n"
    "buffer_store_dwordx4 v[168:171], v24, s[68:71], s47 offen offset:0 sc1\n"
    "v_accvgpr_read_b32 v112, a104\n"
    "v_accvgpr_read_b32 v113, a105\n"
    "v_accvgpr_read_b32 v114, a106\n"
    "v_accvgpr_read_b32 v115, a107\n"
    "v_accvgpr_read_b32 v116, a108\n"
    "v_accvgpr_read_b32 v117, a109\n"
    "v_accvgpr_read_b32 v118, a110\n"
    "v_accvgpr_read_b32 v119, a111\n"
    "v_pk_mul_f32 v[112:113], v[112:113], v[30:31] op_sel_hi:[1,0]\n"
    "v_pk_mul_f32 v[114:115], v[114:115], v[30:31] op_sel_hi:[1,0]\n"
    "v_pk_mul_f32 v[116:117], v[116:117], v[30:31] op_sel_hi:[1,0]\n"
    "v_pk_mul_f32 v[118:119], v[118:119], v[30:31] op_sel_hi:[1,0]\n"
    "v_cvt_pk_bf16_f32 v172, v112, v113\n"
    "v_cvt_pk_bf16_f32 v173, v114, v115\n"
    "v_cvt_pk_bf16_f32 v174, v116, v117\n"
    "v_cvt_pk_bf16_f32 v175, v118, v119\n"
    "s_mov_b32 s47, 0x3000\n"
    "buffer_store_dwordx4 v[172:175], v24, s[68:71], s47 offen offset:512 sc1\n"
    "v_accvgpr_read_b32 v120, a112\n"
    "v_accvgpr_read_b32 v121, a113\n"
    "v_accvgpr_read_b32 v122, a114\n"
    "v_accvgpr_read_b32 v123, a115\n"
    "v_accvgpr_read_b32 v124, a116\n"
    "v_accvgpr_read_b32 v125, a117\n"
    "v_accvgpr_read_b32 v126, a118\n"
    "v_accvgpr_read_b32 v127, a119\n"
    "v_pk_mul_f32 v[120:121], v[120:121], v[32:33] op_sel_hi:[1,0]\n"
    "v_pk_mul_f32 v[122:123], v[122:123], v[32:33] op_sel_hi:[1,0]\n"
    "v_pk_mul_f32 v[124:125], v[124:125], v[32:33] op_sel_hi:[1,0]\n"
    "v_pk_mul_f32 v[126:127], v[126:127], v[32:33] op_sel_hi:[1,0]\n"
    "v_cvt_pk_bf16_f32 v176, v120, v121\n"
    "v_cvt_pk_bf16_f32 v177, v122, v123\n"
    "v_cvt_pk_bf16_f32 v178, v124, v125\n"
    "v_cvt_pk_bf16_f32 v179, v126, v127\n"
    "s_mov_b32 s47, 0x3000\n"
    "buffer_store_dwordx4 v[176:179], v24, s[68:71], s47 offen offset:2048 sc1\n"
    "v_accvgpr_read_b32 v128, a120\n"
    "v_accvgpr_read_b32 v129, a121\n"
    "v_accvgpr_read_b32 v130, a122\n"
    "v_accvgpr_read_b32 v131, a123\n"
    "v_accvgpr_read_b32 v132, a124\n"
    "v_accvgpr_read_b32 v133, a125\n"
    "v_accvgpr_read_b32 v134, a126\n"
    "v_accvgpr_read_b32 v135, a127\n"
    "v_pk_mul_f32 v[128:129], v[128:129], v[32:33] op_sel_hi:[1,0]\n"
    "v_pk_mul_f32 v[130:131], v[130:131], v[32:33] op_sel_hi:[1,0]\n"
    "v_pk_mul_f32 v[132:133], v[132:133], v[32:33] op_sel_hi:[1,0]\n"
    "v_pk_mul_f32 v[134:135], v[134:135], v[32:33] op_sel_hi:[1,0]\n"
    "v_cvt_pk_bf16_f32 v180, v128, v129\n"
    "v_cvt_pk_bf16_f32 v181, v130, v131\n"
    "v_cvt_pk_bf16_f32 v182, v132, v133\n"
    "v_cvt_pk_bf16_f32 v183, v134, v135\n"
    "s_mov_b32 s47, 0x3000\n"
    "buffer_store_dwordx4 v[180:183], v24, s[68:71], s47 offen offset:2560 sc1\n"
    "Lepi7_%=:\n"
    "; ==== S1: publish O (flag F1) after the re-tiled weights of all workgroups are known complete (flags FR)\n"
    "s_lshl_b32 s46, s88, 8\n"
    "s_lshl_b32 s47, s87, 3\n"
    "s_add_u32 s46, s46, s47\n"
    "s_add_u32 s46, s46, 0x1000\n"
    "v_mov_b32 v36, s40\n"
    "v_mov_b32 v37, s41\n"
    "v_mov_b32 v38, s46\n"
    "s_waitcnt vmcnt(0) lgkmcnt(0)\n"
    "s_and_b32 s49, %1, 7\n"
    "s_lshl_b32 s49, s49, 11\n"
    "s_add_u32 s92, s49, 0x8000\n"
    "s_add_u32 s93, s49, 0x8200\n"
    "s_add_u32 s94, s49, 0x8400\n"
    "s_add_u32 s95, s49, 0x8600\n"
    "s_cmp_eq_u32 s34, 0\n"
    "s_cbranch_scc0 Lpollskip11_%=\n"
    "v_lshrrev_b32 v242, 4, v1\n"
    "v_lshlrev_b32 v242, 3, v242\n"
    "v_add_u32 v242, s92, v242\n"
    "v_lshrrev_b32 v246, 4, v1\n"
    "v_lshlrev_b32 v246, 3, v246\n"
    "v_add_u32 v246, s93, v246\n"
    "v_lshrrev_b32 v250, 4, v1\n"
    "v_lshlrev_b32 v250, 3, v250\n"
    "v_add_u32 v250, s94, v250\n"
    "v_lshrrev_b32 v254, 4, v1\n"
    "v_lshlrev_b32 v254, 3, v254\n"
    "v_add_u32 v254, s95, v254\n"
    "s_mov_b32 s44, 0\n"
    "Lpoll9_%=:\n"
    "global_load_dwordx2 v[240:241], v242, s[26:27] sc1\n"
    "global_load_dwordx2 v[244:245], v246, s[26:27] sc1\n"
    "global_load_dwordx2 v[248:249], v250, s[26:27] sc1\n"
    "global_load_dwordx2 v[252:253], v254, s[26:27] sc1\n"
    "s_waitcnt vmcnt(0)\n"
    "v_cmp_ne_u64 vcc, s[40:41], v[240:241]\n"
    "s_nop 4\n"
    "s_mov_b64 s[50:51], vcc\n"
    "v_cmp_ne_u64 vcc, s[40:41], v[244:245]\n"
    "s_nop 4\n"
    "s_or_b64 s[50:51], s[50:51], vcc\n"
    "v_cmp_ne_u64 vcc, s[40:41], v[248:249]\n"
    "s_nop 4\n"
    "s_or_b64 s[50:51], s[50:51], vcc\n"
    "v_cmp_ne_u64 vcc, s[40:41], v[252:253]\n"
    "s_nop 4\n"
    "s_or_b64 s[50:51], s[50:51], vcc\n"
    "s_cmp_eq_u64 s[50:51], 0\n"
    "s_cbranch_scc1 Lpollok10_%=\n"
    "s_add_u32 s44, s44, 1\n"
    "s_cmp_ge_u32 s44, 32768\n"
    "s_cbranch_scc1 Lpollok10_%=\n"
    "s_branch Lpoll9_%=\n"
    "Lpollok10_%=:\n"
    "Lpollskip11_%=:\n"
    "s_barrier\n"
    "v_mov_b32 v34, 0x20040\n"
    "ds_read_b128 v[32:35], v34 offset:0\n"
    "s_waitcnt lgkmcnt(0)\n"
    "v_or_b32 v32, v32, v33\n"
    "v_or_b32 v34, v34, v35\n"
    "v_or_b32 v32, v32, v34\n"
    "s_nop 0\n"
    "v_readfirstlane_b32 s47, v32\n"
    "s_nop 3\n"
    "s_cmp_lg_u32 s47, 0\n"
    "s_cbranch_scc1 Lfb8_%=\n"
    "s_mov_b64 exec, 1\n"
    "global_store_dwordx2 v38, v[36:37], s[26:27] offset:0 sc1\n"
    "s_mov_b64 exec, -1\n"
    "s_mul_i32 s46, s35, 0x140000\n"
    "s_mul_i32 s48, s37, 0x2800\n"
    "s_add_u32 s46, s46, s48\n"
    "s_and_b32 s48, s34, 1\n"
    "s_mul_i32 s48, s48, 0x1400\n"
    "s_add_u32 s46, s46, s48\n"
    "s_lshr_b32 s48, s34, 1\n"
    "s_mul_i32 s48, s48, 0xa000\n"
    "s_add_u32 s46, s46, s48\n"
    "s_sub_u32 s47, 0x500000, s46\n"
    "s_add_u32 s46, s46, 0x3000000\n"
    "s_add_u32 s52, s26, s46\n"
    "s_addc_u32 s53, s27, 0\n"
    "s_mov_b32 s54, s47\n"
    "s_mov_b32 s55, 0x20000\n"
    "s_mul_i32 s64, s34, 0x1400\n"
    "v_add_u32 v2, 0x1000, v1\n"
    "s_mov_b32 s65, 0\n"
    "s_add_u32 m0, s64, 0\n"
    "s_nop 0\n"
    "buffer_load_dwordx4 v1, s[52:55], s65 offen offset:0 sc1 lds\n"
    "buffer_load_dwordx4 v1, s[52:55], s65 offen offset:1024 sc1 lds\n"
    "buffer_load_dwordx4 v1, s[52:55], s65 offen offset:2048 sc1 lds\n"
    "buffer_load_dwordx4 v1, s[52:55], s65 offen offset:3072 sc1 lds\n"
    "s_add_u32 m0, s64, 0x1000\n"
    "s_nop 0\n"
    "buffer_load_dwordx4 v2, s[52:55], s65 offen offset:0 sc1 lds\n"
    "s_mov_b32 s65, 0x14000\n"
    "s_add_u32 m0, s64, 0x5000\n"
    "s_nop 0\n"
    "buffer_load_dwordx4 v1, s[52:55], s65 offen offset:0 sc1 lds\n"
    "buffer_load_dwordx4 v1, s[52:55], s65 offen offset:1024 sc1 lds\n"
    "buffer_load_dwordx4 v1, s[52:55], s65 offen offset:2048 sc1 lds\n"
    "buffer_load_dwordx4 v1, s[52:55], s65 offen offset:3072 sc1 lds\n"
    "s_add_u32 m0, s64, 0x6000\n"
    "s_nop 0\n"
    "buffer_load_dwordx4 v2, s[52:55], s65 offen offset:0 sc1 lds\n"
    "s_mov_b32 s65, 0x28000\n"
    "s_add_u32 m0, s64, 0xa000\n"
    "s_nop 0\n"
    "buffer_load_dwordx4 v1, s[52:55], s65 offen offset:0 sc1 lds\n"
    "buffer_load_dwordx4 v1, s[52:55], s65 offen offset:1024 sc1 lds\n"
    "buffer_load_dwordx4 v1, s[52:55], s65 offen offset:2048 sc1 lds\n"
    "buffer_load_dwordx4 v1, s[52:55], s65 offen offset:3072 sc1 lds\n"
    "s_add_u32 m0, s64, 0xb000\n"
    "s_nop 0\n"
    "buffer_load_dwordx4 v2, s[52:55], s65 offen offset:0 sc1 lds\n"
    "s_lshl_b32 s46, s36, 1\n"
    "s_lshr_b32 s48, s34, 1\n"
    "s_add_u32 s46, s46, s48\n"
    "s_lshl_b32 s46, s46, 2\n"
    "s_add_u32 s46, s46, s35\n"
    "s_lshl_b32 s46, s46, 17\n"
    "s_and_b32 s48, s34, 1\n"
    "s_lshl_b32 s48, s48, 11\n"
    "s_add_u32 s46, s46, s48\n"
    "s_add_u32 s46, s46, 0x2b0000\n"
    "s_add_u32 s56, s26, s46\n"
    "s_addc_u32 s57, s27, 0\n"
    "s_mov_b32 s58, 0x20000\n"
    "s_mov_b32 s59, 0x20000\n"
    "s_lshl_b32 s46, s35, 4\n"
    "s_add_u32 s46, s46, s36\n"
    "s_lshl_b32 s46, s46, 2\n"
    "s_add_u32 s46, s46, s37\n"
    "s_lshl_b32 s46, s46, 2\n"
    "s_add_u32 s46, s46, s34\n"
    "s_mul_i32 s46, s46, 0x2800\n"
    "s_add_u32 s46, s46, 0x1e70000\n"
    "s_add_u32 s60, s26, s46\n"
    "s_addc_u32 s61, s27, 0\n"
    "s_mov_b32 s62, 0x2800\n"
    "s_mov_b32 s63, 0x20000\n"
    "v_add_u32 v3, 0x10000, v1\n"
    "v_add_u32 v200, 0x20000, v1\n"
    "s_lshl_b32 s48, s88, 8\n"
    "s_lshl_b32 s47, s35, 6\n"
    "s_add_u32 s48, s48, s47\n"
    "s_add_u32 s48, s48, 0x1000\n"
    "s_cmp_eq_u32 s34, 0\n"
    "s_cbranch_scc0 Lpollskip14_%=\n"
    "v_lshrrev_b32 v10, 4, v1\n"
    "v_min_u32 v10, 7, v10\n"
    "v_lshlrev_b32 v10, 3, v10\n"
    "v_add_u32 v10, s48, v10\n"
    "s_mov_b32 s44, 0\n"
    "Lpoll12_%=:\n"
    "global_load_dwordx2 v[8:9], v10, s[26:27] sc1\n"
    "s_waitcnt vmcnt(0)\n"
    "v_cmp_ne_u64 vcc, s[40:41], v[8:9]\n"
    "s_nop 4\n"
    "s_mov_b64 s[50:51], vcc\n"
    "s_cmp_eq_u64 s[50:51], 0\n"
    "s_cbranch_scc1 Lpollok13_%=\n"
    "s_add_u32 s44, s44, 1\n"
    "s_cmp_ge_u32 s44, 32768\n"
    "s_cbranch_scc1 Lpollok13_%=\n"
    "s_branch Lpoll12_%=\n"
    "Lpollok13_%=:\n"
    "Lpollskip14_%=:\n"
    "s_barrier\n"
    "; ==== P3: partial O @ wo (128 tokens x 160 features x K slice of 1024), bf16 partial sums\n"
    "s_mov_b32 s66, 0\n"
    "buffer_load_dwordx4 v[136:139], v1, s[56:59], s66 offen offset:0 sc1\n"
    "buffer_load_dwordx4 v[140:143], v1, s[56:59], s66 offen offset:1024 sc1\n"
    "s_mov_b32 s66, 0x1000\n"
    "buffer_load_dwordx4 v[144:147], v1, s[56:59], s66 offen offset:0 sc1\n"
    "buffer_load_dwordx4 v[148:151], v1, s[56:59], s66 offen offset:1024 sc1\n"
    "s_mov_b32 s66, 0x2000\n"
    "buffer_load_dwordx4 v[152:155], v1, s[56:59], s66 offen offset:0 sc1\n"
    "buffer_load_dwordx4 v[156:159], v1, s[56:59], s66 offen offset:1024 sc1\n"
    "s_mov_b32 s66, 0x3000\n"
    "buffer_load_dwordx4 v[160:163], v1, s[56:59], s66 offen offset:0 sc1\n"
    "buffer_load_dwordx4 v[164:167], v1, s[56:59], s66 offen offset:1024 sc1\n"
    "s_waitcnt vmcnt(18)\n"
    "s_barrier\n"
    "ds_read_b128 a[0:3], v1 offset:0\n"
    "ds_read_b128 a[4:7], v1 offset:1024\n"
    "ds_read_b128 a[8:11], v1 offset:2048\n"
    "ds_read_b128 a[12:15], v1 offset:3072\n"
    "ds_read_b128 a[16:19], v1 offset:4096\n"
    "ds_read_b128 a[20:23], v1 offset:5120\n"
    "ds_read_b128 a[24:27], v1 offset:6144\n"
    "ds_read_b128 a[28:31], v1 offset:7168\n"
    "ds_read_b128 a[32:35], v1 offset:8192\n"
    "ds_read_b128 a[36:39], v1 offset:9216\n"
    "s_waitcnt vmcnt(7) lgkmcnt(9)\n"
    "v_mfma_f32_16x16x32_bf16 v[24:27], a[0:3], v[136:139], 0\n"
    "s_waitcnt vmcnt(6)\n"
    "v_mfma_f32_16x16x32_bf16 v[28:31], a[0:3], v[140:143], 0\n"
    "ds_read_b128 a[40:43], v1 offset:10240\n"
    "s_waitcnt lgkmcnt(9)\n"
    "v_mfma_f32_16x16x32_bf16 v[32:35], a[4:7], v[136:139], 0\n"
    "ds_read_b128 a[44:47], v1 offset:11264\n"
    "s_mov_b32 s66, 0x4000\n"
    "buffer_load_dwordx4 v[168:171], v1, s[56:59], s66 offen offset:0 sc1\n"
    "v_mfma_f32_16x16x32_bf16 v[36:39], a[4:7], v[140:143], 0\n"
    "s_waitcnt lgkmcnt(9)\n"
    "v_mfma_f32_16x16x32_bf16 v[40:43], a[8:11], v[136:139], 0\n"
    "ds_read_b128 a[48:51], v1 offset:12288\n"
    "v_mfma_f32_16x16x32_bf16 v[44:47], a[8:11], v[140:143], 0\n"
    "ds_read_b128 a[52:55], v1 offset:13312\n"
    "s_waitcnt lgkmcnt(10)\n"
    "v_mfma_f32_16x16x32_bf16 v[48:51], a[12:15], v[136:139], 0\n"
    "v_mfma_f32_16x16x32_bf16 v[52:55], a[12:15], v[140:143], 0\n"
    "ds_read_b128 a[56:59], v1 offset:14336\n"
    "s_waitcnt lgkmcnt(10)\n"
    "v_mfma_f32_16x16x32_bf16 v[56:59], a[16:19], v[136:139], 0\n"
    "ds_read_b128 a[60:63], v1 offset:15360\n"
    "buffer_load_dwordx4 v[172:175], v1, s[56:59], s66 offen offset:1024 sc1\n"
    "v_mfma_f32_16x16x32_bf16 v[60:63], a[16:19], v[140:143], 0\n"
    "s_waitcnt lgkmcnt(10)\n"
    "v_mfma_f32_16x16x32_bf16 v[64:67], a[20:23], v[136:139], 0\n"
    "ds_read_b128 a[64:67], v1 offset:16384\n"
    "v_mfma_f32_16x16x32_bf16 v[68:71], a[20:23], v[140:143], 0\n"
    "ds_read_b128 a[68:71], v1 offset:17408\n"
    "s_waitcnt lgkmcnt(11)\n"
    "v_mfma_f32_16x16x32_bf16 v[72:75], a[24:27], v[136:139], 0\n"
    "v_mfma_f32_16x16x32_bf16 v[76:79], a[24:27], v[140:143], 0\n"
    "ds_read_b128 a[72:75], v1 offset:18432\n"
    "s_waitcnt lgkmcnt(11)\n"
    "v_mfma_f32_16x16x32_bf16 v[80:83], a[28:31], v[136:139], 0\n"
    "ds_read_b128 a[76:79], v1 offset:19456\n"
    "v_mfma_f32_16x16x32_bf16 v[84:87], a[28:31], v[140:143], 0\n"
    "s_waitcnt lgkmcnt(11)\n"
    "v_mfma_f32_16x16x32_bf16 v[88:91], a[32:35], v[136:139], 0\n"
    "v_mfma_f32_16x16x32_bf16 v[92:95], a[32:35], v[140:143], 0\n"
    "s_waitcnt lgkmcnt(10)\n"
    "v_mfma_f32_16x16x32_bf16 v[96:99], a[36:39], v[136:139], 0\n"
    "v_mfma_f32_16x16x32_bf16 v[100:103], a[36:39], v[140:143], 0\n"
    "s_waitcnt vmcnt(7) lgkmcnt(9)\n"
    "v_mfma_f32_16x16x32_bf16 v[24:27], a[40:43], v[144:147], v[24:27]\n"
    "s_waitcnt lgkmcnt(0)\n"
    "s_barrier\n"
    "s_waitcnt vmcnt(6)\n"
    "v_mfma_f32_16x16x32_bf16 v[28:31], a[40:43], v[148:151], v[28:31]\n"
    "ds_read_b128 a[0:3], v1 offset:20480\n"
    "v_mfma_f32_16x16x32_bf16 v[32:35], a[44:47], v[144:147], v[32:35]\n"
    "ds_read_b128 a[4:7], v1 offset:21504\n"
    "s_mov_b32 s66, 0x5000\n"
    "buffer_load_dwordx4 v[136:139], v1, s[56:59], s66 offen offset:0 sc1\n"
    "v_mfma_f32_16x16x32_bf16 v[36:39], a[44:47], v[148:151], v[36:39]\n"
    "s_mov_b32 s65, 0x3c000\n"
    "s_add_u32 m0, s64, 0xf000\n"
    "s_nop 0\n"
    "buffer_load_dwordx4 v1, s[52:55], s65 offen offset:0 sc1 lds\n"
    "v_mfma_f32_16x16x32_bf16 v[40:43], a[48:51], v[144:147], v[40:43]\n"
    "ds_read_b128 a[8:11], v1 offset:22528\n"
    "v_mfma_f32_16x16x32_bf16 v[44:47], a[48:51], v[148:151], v[44:47]\n"
    "ds_read_b128 a[12:15], v1 offset:23552\n"
    "buffer_load_dwordx4 v1, s[52:55], s65 offen offset:1024 sc1 lds\n"
    "v_mfma_f32_16x16x32_bf16 v[48:51], a[52:55], v[144:147], v[48:51]\n"
    "v_mfma_f32_16x16x32_bf16 v[52:55], a[52:55], v[148:151], v[52:55]\n"
    "ds_read_b128 a[16:19], v1 offset:24576\n"
    "v_mfma_f32_16x16x32_bf16 v[56:59], a[56:59], v[144:147], v[56:59]\n"
    "ds_read_b128 a[20:23], v1 offset:25600\n"
    "buffer_load_dwordx4 v[140:143], v1, s[56:59], s66 offen offset:1024 sc1\n"
    "buffer_load_dwordx4 v1, s[52:55], s65 offen offset:2048 sc1 lds\n"
    "v_mfma_f32_16x16x32_bf16 v[60:63], a[56:59], v[148:151], v[60:63]\n"
    "buffer_load_dwordx4 v1, s[52:55], s65 offen offset:3072 sc1 lds\n"
    "v_mfma_f32_16x16x32_bf16 v[64:67], a[60:63], v[144:147], v[64:67]\n"
    "ds_read_b128 a[24:27], v1 offset:26624\n"
    "v_mfma_f32_16x16x32_bf16 v[68:71], a[60:63], v[148:151], v[68:71]\n"
    "s_add_u32 m0, s64, 0x10000\n"
    "ds_read_b128 a[28:31], v1 offset:27648\n"
    "buffer_load_dwordx4 v2, s[52:55], s65 offen offset:0 sc1 lds\n"
    "v_mfma_f32_16x16x32_bf16 v[72:75], a[64:67], v[144:147], v[72:75]\n"
    "v_mfma_f32_16x16x32_bf16 v[76:79], a[64:67], v[148:151], v[76:79]\n"
    "ds_read_b128 a[32:35], v1 offset:28672\n"
    "v_mfma_f32_16x16x32_bf16 v[80:83], a[68:71], v[144:147], v[80:83]\n"
    "ds_read_b128 a[36:39], v1 offset:29696\n"
    "v_mfma_f32_16x16x32_bf16 v[84:87], a[68:71], v[148:151], v[84:87]\n"
    "v_mfma_f32_16x16x32_bf16 v[88:91], a[72:75], v[144:147], v[88:91]\n"
    "v_mfma_f32_16x16x32_bf16 v[92:95], a[72:75], v[148:151], v[92:95]\n"
    "v_mfma_f32_16x16x32_bf16 v[96:99], a[76:79], v[144:147], v[96:99]\n"
    "v_mfma_f32_16x16x32_bf16 v[100:103], a[76:79], v[148:151], v[100:103]\n"
    "s_waitcnt vmcnt(12) lgkmcnt(9)\n"
    "v_mfma_f32_16x16x32_bf16 v[24:27], a[0:3], v[152:155], v[24:27]\n"
    "s_waitcnt vmcnt(11)\n"
    "v_mfma_f32_16x16x32_bf16 v[28:31], a[0:3], v[156:159], v[28:31]\n"
    "ds_read_b128 a[40:43], v1 offset:30720\n"
    "s_waitcnt lgkmcnt(9)\n"
    "v_mfma_f32_16x16x32_bf16 v[32:35], a[4:7], v[152:155], v[32:35]\n"
    "ds_read_b128 a[44:47], v1 offset:31744\n"
    "s_mov_b32 s66, 0x6000\n"
    "buffer_load_dwordx4 v[144:147], v1, s[56:59], s66 offen offset:0 sc1\n"
    "v_mfma_f32_16x16x32_bf16 v[36:39], a[4:7], v[156:159], v[36:39]\n"
    "s_waitcnt lgkmcnt(9)\n"
    "v_mfma_f32_16x16x32_bf16 v[40:43], a[8:11], v[152:155], v[40:43]\n"
    "ds_read_b128 a[48:51], v1 offset:32768\n"
    "v_mfma_f32_16x16x32_bf16 v[44:47], a[8:11], v[156:159], v[44:47]\n"
    "ds_read_b128 a[52:55], v1 offset:33792\n"
    "s_waitcnt lgkmcnt(10)\n"
    "v_mfma_f32_16x16x32_bf16 v[48:51], a[12:15], v[152:155], v[48:51]\n"
    "v_mfma_f32_16x16x32_bf16 v[52:55], a[12:15], v[156:159], v[52:55]\n"
    "ds_read_b128 a[56:59], v1 offset:34816\n"
    "s_waitcnt lgkmcnt(10)\n"
    "v_mfma_f32_16x16x32_bf16 v[56:59], a[16:19], v[152:155], v[56:59]\n"
    "ds_read_b128 a[60:63], v1 offset:35840\n"
    "buffer_load_dwordx4 v[148:151], v1, s[56:59], s66 offen offset:1024 sc1\n"
    "v_mfma_f32_16x16x32_bf16 v[60:63], a[16:19], v[156:159], v[60:63]\n"
    "s_waitcnt lgkmcnt(10)\n"
    "v_mfma_f32_16x16x32_bf16 v[64:67], a[20:23], v[152:155], v[64:67]\n"
    "ds_read_b128 a[64:67], v1 offset:36864\n"
    "v_mfma_f32_16x16x32_bf16 v[68:71], a[20:23], v[156:159], v[68:71]\n"
    "ds_read_b128 a[68:71], v1 offset:37888\n"
    "s_waitcnt lgkmcnt(11)\n"
    "v_mfma_f32_16x16x32_bf16 v[72:75], a[24:27], v[152:155], v[72:75]\n"
    "v_mfma_f32_16x16x32_bf16 v[76:79], a[24:27], v[156:159], v[76:79]\n"
    "ds_read_b128 a[72:75], v1 offset:38912\n"
    "s_waitcnt lgkmcnt(11)\n"
    "v_mfma_f32_16x16x32_bf16 v[80:83], a[28:31], v[152:155], v[80:83]\n"
    "ds_read_b128 a[76:79], v1 offset:39936\n"
    "v_mfma_f32_16x16x32_bf16 v[84:87], a[28:31], v[156:159], v[84:87]\n"
    "s_waitcnt lgkmcnt(11)\n"
    "v_mfma_f32_16x16x32_bf16 v[88:91], a[32:35], v[152:155], v[88:91]\n"
    "v_mfma_f32_16x16x32_bf16 v[92:95], a[32:35], v[156:159], v[92:95]\n"
    "s_waitcnt lgkmcnt(10)\n"
    "v_mfma_f32_16x16x32_bf16 v[96:99], a[36:39], v[152:155], v[96:99]\n"
    "v_mfma_f32_16x16x32_bf16 v[100:103], a[36:39], v[156:159], v[100:103]\n"
    "s_waitcnt vmcnt(12) lgkmcnt(9)\n"
    "v_mfma_f32_16x16x32_bf16 v[24:27], a[40:43], v[160:163], v[24:27]\n"
    "s_waitcnt lgkmcnt(0)\n"
    "s_barrier\n"
    "s_waitcnt vmcnt(11)\n"
    "v_mfma_f32_16x16x32_bf16 v[28:31], a[40:43], v[164:167], v[28:31]\n"
    "ds_read_b128 a[0:3], v1 offset:40960\n"
    "v_mfma_f32_16x16x32_bf16 v[32:35], a[44:47], v[160:163], v[32:35]\n"
    "ds_read_b128 a[4:7], v1 offset:41984\n"
    "s_mov_b32 s66, 0x7000\n"
    "buffer_load_dwordx4 v[152:155], v1, s[56:59], s66 offen offset:0 sc1\n"
    "v_mfma_f32_16x16x32_bf16 v[36:39], a[44:47], v[164:167], v[36:39]\n"
    "s_mov_b32 s65, 0x50000\n"
    "s_add_u32 m0, s64, 0\n"
    "s_nop 0\n"
    "buffer_load_dwordx4 v1, s[52:55], s65 offen offset:0 sc1 lds\n"
    "v_mfma_f32_16x16x32_bf16 v[40:43], a[48:51], v[160:163], v[40:43]\n"
    "ds_read_b128 a[8:11], v1 offset:43008\n"
    "v_mfma_f32_16x16x32_bf16 v[44:47], a[48:51], v[164:167], v[44:47]\n"
    "ds_read_b128 a[12:15], v1 offset:44032\n"
    "buffer_load_dwordx4 v1, s[52:55], s65 offen offset:1024 sc1 lds\n"
    "v_mfma_f32_16x16x32_bf16 v[48:51], a[52:55], v[160:163], v[48:51]\n"
    "v_mfma_f32_16x16x32_bf16 v[52:55], a[52:55], v[164:167], v[52:55]\n"
    "ds_read_b128 a[16:19], v1 offset:45056\n"
    "v_mfma_f32_16x16x32_bf16 v[56:59], a[56:59], v[160:163], v[56:59]\n"
    "ds_read_b128 a[20:23], v1 offset:46080\n"
    "buffer_load_dwordx4 v[156:159], v1, s[56:59], s66 offen offset:1024 sc1\n"
    "buffer_load_dwordx4 v1, s[52:55], s65 offen offset:2048 sc1 lds\n"
    "v_mfma_f32_16x16x32_bf16 v[60:63], a[56:59], v[164:167], v[60:63]\n"
    "buffer_load_dwordx4 v1, s[52:55], s65 offen offset:3072 sc1 lds\n"
    "v_mfma_f32_16x16x32_bf16 v[64:67], a[60:63], v[160:163], v[64:67]\n"
    "ds_read_b128 a[24:27], v1 offset:47104\n"
    "v_mfma_f32_16x16x32_bf16 v[68:71], a[60:63], v[164:167], v[68:71]\n"
    "s_add_u32 m0, s64, 0x1000\n"
    "ds_read_b128 a[28:31], v1 offset:48128\n"
    "buffer_load_dwordx4 v2, s[52:55], s65 offen offset:0 sc1 lds\n"
    "v_mfma_f32_16x16x32_bf16 v[72:75], a[64:67], v[160:163], v[72:75]\n"
    "v_mfma_f32_16x16x32_bf16 v[76:79], a[64:67], v[164:167], v[76:79]\n"
    "ds_read_b128 a[32:35], v1 offset:49152\n"
    "v_mfma_f32_16x16x32_bf16 v[80:83], a[68:71], v[160:163], v[80:83]\n"
    "ds_read_b128 a[36:39], v1 offset:50176\n"
    "v_mfma_f32_16x16x32_bf16 v[84:87], a[68:71], v[164:167], v[84:87]\n"
    "v_mfma_f32_16x16x32_bf16 v[88:91], a[72:75], v[160:163], v[88:91]\n"
    "v_mfma_f32_16x16x32_bf16 v[92:95], a[72:75], v[164:167], v[92:95]\n"
    "v_mfma_f32_16x16x32_bf16 v[96:99], a[76:79], v[160:163], v[96:99]\n"
    "v_mfma_f32_16x16x32_bf16 v[100:103], a[76:79], v[164:167], v[100:103]\n"
    "s_waitcnt vmcnt(17) lgkmcnt(9)\n"
    "v_mfma_f32_16x16x32_bf16 v[24:27], a[0:3], v[168:171], v[24:27]\n"
    "s_waitcnt vmcnt(16)\n"
    "v_mfma_f32_16x16x32_bf16 v[28:31], a[0:3], v[172:175], v[28:31]\n"
    "ds_read_b128 a[40:43], v1 offset:51200\n"
    "s_waitcnt lgkmcnt(9)\n"
    "v_mfma_f32_16x16x32_bf16 v[32:35], a[4:7], v[168:171], v[32:35]\n"
    "ds_read_b128 a[44:47], v1 offset:52224\n"
    "s_mov_b32 s66, 0x8000\n"
    "buffer_load_dwordx4 v[160:163], v1, s[56:59], s66 offen offset:0 sc1\n"
    "v_mfma_f32_16x16x32_bf16 v[36:39], a[4:7], v[172:175], v[36:39]\n"
    "s_waitcnt lgkmcnt(9)\n"
    "v_mfma_f32_16x16x32_bf16 v[40:43], a[8:11], v[168:171], v[40:43]\n"
    "ds_read_b128 a[48:51], v1 offset:53248\n"
    "v_mfma_f32_16x16x32_bf16 v[44:47], a[8:11], v[172:175], v[44:47]\n"
    "ds_read_b128 a[52:55], v1 offset:54272\n"
    "s_waitcnt lgkmcnt(10)\n"
    "v_mfma_f32_16x16x32_bf16 v[48:51], a[12:15], v[168:171], v[48:51]\n"
    "v_mfma_f32_16x16x32_bf16 v[52:55], a[12:15], v[172:175], v[52:55]\n"
    "ds_read_b128 a[56:59], v1 offset:55296\n"
    "s_waitcnt lgkmcnt(10)\n"
    "v_mfma_f32_16x16x32_bf16 v[56:59], a[16:19], v[168:171], v[56:59]\n"
    "ds_read_b128 a[60:63], v1 offset:56320\n"
    "buffer_load_dwordx4 v[164:167], v1, s[56:59], s66 offen offset:1024 sc1\n"
    "v_mfma_f32_16x16x32_bf16 v[60:63], a[16:19], v[172:175], v[60:63]\n"
    "s_waitcnt lgkmcnt(10)\n"
    "v_mfma_f32_16x16x32_bf16 v[64:67], a[20:23], v[168:171], v[64:67]\n"
    "ds_read_b128 a[64:67], v1 offset:57344\n"
    "v_mfma_f32_16x16x32_bf16 v[68:71], a[20:23], v[172:175], v[68:71]\n"
    "ds_read_b128 a[68:71], v1 offset:58368\n"
    "s_waitcnt lgkmcnt(11)\n"
    "v_mfma_f32_16x16x32_bf16 v[72:75], a[24:27], v[168:171], v[72:75]\n"
    "v_mfma_f32_16x16x32_bf16 v[76:79], a[24:27], v[172:175], v[76:79]\n"
    "ds_read_b128 a[72:75], v1 offset:59392\n"
    "s_waitcnt lgkmcnt(11)\n"
    "v_mfma_f32_16x16x32_bf16 v[80:83], a[28:31], v[168:171], v[80:83]\n"
    "ds_read_b128 a[76:79], v1 offset:60416\n"
    "v_mfma_f32_16x16x32_bf16 v[84:87], a[28:31], v[172:175], v[84:87]\n"
    "s_waitcnt lgkmcnt(11)\n"
    "v_mfma_f32_16x16x32_bf16 v[88:91], a[32:35], v[168:171], v[88:91]\n"
    "v_mfma_f32_16x16x32_bf16 v[92:95], a[32:35], v[172:175], v[92:95]\n"
    "s_waitcnt lgkmcnt(10)\n"
    "v_mfma_f32_16x16x32_bf16 v[96:99], a[36:39], v[168:171], v[96:99]\n"
    "v_mfma_f32_16x16x32_bf16 v[100:103], a[36:39], v[172:175], v[100:103]\n"
    "s_waitcnt vmcnt(17) lgkmcnt(9)\n"
    "v_mfma_f32_16x16x32_bf16 v[24:27], a[40:43], v[136:139], v[24:27]\n"
    "s_waitcnt vmcnt(11)\n"
    "s_waitcnt lgkmcnt(0)\n"
    "s_barrier\n"
    "v_mfma_f32_16x16x32_bf16 v[28:31], a[40:43], v[140:143], v[28:31]\n"
    "ds_read_b128 a[0:3], v1 offset:61440\n"
    "v_mfma_f32_16x16x32_bf16 v[32:35], a[44:47], v[136:139], v[32:35]\n"
    "ds_read_b128 a[4:7], v1 offset:62464\n"
    "s_mov_b32 s66, 0x9000\n"
    "buffer_load_dwordx4 v[168:171], v1, s[56:59], s66 offen offset:0 sc1\n"
    "v_mfma_f32_16x16x32_bf16 v[36:39], a[44:47], v[140:143], v[36:39]\n"
    "s_mov_b32 s65, 0x64000\n"
    "s_add_u32 m0, s64, 0x5000\n"
    "s_nop 0\n"
    "buffer_load_dwordx4 v1, s[52:55], s65 offen offset:0 sc1 lds\n"
    "v_mfma_f32_16x16x32_bf16 v[40:43], a[48:51], v[136:139], v[40:43]\n"
    "ds_read_b128 a[8:11], v1 offset:63488\n"
    "v_mfma_f32_16x16x32_bf16 v[44:47], a[48:51], v[140:143], v[44:47]\n"
    "ds_read_b128 a[12:15], v1 offset:64512\n"
    "buffer_load_dwordx4 v1, s[52:55], s65 offen offset:1024 sc1 lds\n"
    "v_mfma_f32_16x16x32_bf16 v[48:51], a[52:55], v[136:139], v[48:51]\n"
    "v_mfma_f32_16x16x32_bf16 v[52:55], a[52:55], v[140:143], v[52:55]\n"
    "ds_read_b128 a[16:19], v3 offset:0\n"
    "v_mfma_f32_16x16x32_bf16 v[56:59], a[56:59], v[136:139], v[56:59]\n"
    "ds_read_b128 a[20:23], v3 offset:1024\n"
    "buffer_load_dwordx4 v[172:175], v1, s[56:59], s66 offen offset:1024 sc1\n"
    "buffer_load_dwordx4 v1, s[52:55], s65 offen offset:2048 sc1 lds\n"
    "v_mfma_f32_16x16x32_bf16 v[60:63], a[56:59], v[140:143], v[60:63]\n"
    "buffer_load_dwordx4 v1, s[52:55], s65 offen offset:3072 sc1 lds\n"
    "v_mfma_f32_16x16x32_bf16 v[64:67], a[60:63], v[136:139], v[64:67]\n"
    "ds_read_b128 a[24:27], v3 offset:2048\n"
    "v_mfma_f32_16x16x32_bf16 v[68:71], a[60:63], v[140:143], v[68:71]\n"
    "s_add_u32 m0, s64, 0x6000\n"
    "ds_read_b128 a[28:31], v3 offset:3072\n"
    "buffer_load_dwordx4 v2, s[52:55], s65 offen offset:0 sc1 lds\n"
    "v_mfma_f32_16x16x32_bf16 v[72:75], a[64:67], v[136:139], v[72:75]\n"
    "v_mfma_f32_16x16x32_bf16 v[76:79], a[64:67], v[140:143], v[76:79]\n"
    "ds_read_b128 a[32:35], v3 offset:4096\n"
    "v_mfma_f32_16x16x32_bf16 v[80:83], a[68:71], v[136:139], v[80:83]\n"
    "ds_read_b128 a[36:39], v3 offset:5120\n"
    "v_mfma_f32_16x16x32_bf16 v[84:87], a[68:71], v[140:143], v[84:87]\n"
    "v_mfma_f32_16x16x32_bf16 v[88:91], a[72:75], v[136:139], v[88:91]\n"
    "v_mfma_f32_16x16x32_bf16 v[92:95], a[72:75], v[140:143], v[92:95]\n"
    "v_mfma_f32_16x16x32_bf16 v[96:99], a[76:79], v[136:139], v[96:99]\n"
    "v_mfma_f32_16x16x32_bf16 v[100:103], a[76:79], v[140:143], v[100:103]\n"
    "s_waitcnt vmcnt(17) lgkmcnt(9)\n"
    "v_mfma_f32_16x16x32_bf16 v[24:27], a[0:3], v[144:147], v[24:27]\n"
    "s_waitcnt vmcnt(16)\n"
    "v_mfma_f32_16x16x32_bf16 v[28:31], a[0:3], v[148:151], v[28:31]\n"
    "ds_read_b128 a[40:43], v3 offset:6144\n"
    "s_waitcnt lgkmcnt(9)\n"
    "v_mfma_f32_16x16x32_bf16 v[32:35], a[4:7], v[144:147], v[32:35]\n"
    "ds_read_b128 a[44:47], v3 offset:7168\n"
    "s_mov_b32 s66, 0xa000\n"
    "buffer_load_dwordx4 v[136:139], v1, s[56:59], s66 offen offset:0 sc1\n"
    "v_mfma_f32_16x16x32_bf16 v[36:39], a[4:7], v[148:151], v[36:39]\n"
    "s_waitcnt lgkmcnt(9)\n"
    "v_mfma_f32_16x16x32_bf16 v[40:43], a[8:11], v[144:147], v[40:43]\n"
    "ds_read_b128 a[48:51], v3 offset:8192\n"
    "v_mfma_f32_16x16x32_bf16 v[44:47], a[8:11], v[148:151], v[44:47]\n"
    "ds_read_b128 a[52:55], v3 offset:9216\n"
    "s_waitcnt lgkmcnt(10)\n"
    "v_mfma_f32_16x16x32_bf16 v[48:51], a[12:15], v[144:147], v[48:51]\n"
    "v_mfma_f32_16x16x32_bf16 v[52:55], a[12:15], v[148:151], v[52:55]\n"
    "ds_read_b128 a[56:59], v3 offset:10240\n"
    "s_waitcnt lgkmcnt(10)\n"
    "v_mfma_f32_16x16x32_bf16 v[56:59], a[16:19], v[144:147], v[56:59]\n"
    "ds_read_b128 a[60:63], v3 offset:11264\n"
    "buffer_load_dwordx4 v[140:143], v1, s[56:59], s66 offen offset:1024 sc1\n"
    "v_mfma_f32_16x16x32_bf16 v[60:63], a[16:19], v[148:151], v[60:63]\n"
    "s_waitcnt lgkmcnt(10)\n"
    "v_mfma_f32_16x16x32_bf16 v[64:67], a[20:23], v[144:147], v[64:67]\n"
    "ds_read_b128 a[64:67], v3 offset:12288\n"
    "v_mfma_f32_16x16x32_bf16 v[68:71], a[20:23], v[148:151], v[68:71]\n"
    "ds_read_b128 a[68:71], v3 offset:13312\n"
    "s_waitcnt lgkmcnt(11)\n"
    "v_mfma_f32_16x16x32_bf16 v[72:75], a[24:27], v[144:147], v[72:75]\n"
    "v_mfma_f32_16x16x32_bf16 v[76:79], a[24:27], v[148:151], v[76:79]\n"
    "ds_read_b128 a[72:75], v3 offset:14336\n"
    "s_waitcnt lgkmcnt(11)\n"
    "v_mfma_f32_16x16x32_bf16 v[80:83], a[28:31], v[144:147], v[80:83]\n"
    "ds_read_b128 a[76:79], v3 offset:15360\n"
    "v_mfma_f32_16x16x32_bf16 v[84:87], a[28:31], v[148:151], v[84:87]\n"
    "s_waitcnt lgkmcnt(11)\n"
    "v_mfma_f32_16x16x32_bf16 v[88:91], a[32:35], v[144:147], v[88:91]\n"
    "v_mfma_f32_16x16x32_bf16 v[92:95], a[32:35], v[148:151], v[92:95]\n"
    "s_waitcnt lgkmcnt(10)\n"
    "v_mfma_f32_16x16x32_bf16 v[96:99], a[36:39], v[144:147], v[96:99]\n"
    "v_mfma_f32_16x16x32_bf16 v[100:103], a[36:39], v[148:151], v[100:103]\n"
    "s_waitcnt vmcnt(17) lgkmcnt(9)\n"
    "v_mfma_f32_16x16x32_bf16 v[24:27], a[40:43], v[152:155], v[24:27]\n"
    "s_waitcnt vmcnt(11)\n"
    "s_waitcnt lgkmcnt(0)\n"
    "s_barrier\n"
    "v_mfma_f32_16x16x32_bf16 v[28:31], a[40:43], v[156:159], v[28:31]\n"
    "ds_read_b128 a[0:3], v1 offset:0\n"
    "v_mfma_f32_16x16x32_bf16 v[32:35], a[44:47], v[152:155], v[32:35]\n"
    "ds_read_b128 a[4:7], v1 offset:1024\n"
    "s_mov_b32 s66, 0xb000\n"
    "buffer_load_dwordx4 v[144:147], v1, s[56:59], s66 offen offset:0 sc1\n"
    "v_mfma_f32_16x16x32_bf16 v[36:39], a[44:47], v[156:159], v[36:39]\n"
    "s_mov_b32 s65, 0x78000\n"
    "s_add_u32 m0, s64, 0xa000\n"
    "s_nop 0\n"
    "buffer_load_dwordx4 v1, s[52:55], s65 offen offset:0 sc1 lds\n"
    "v_mfma_f32_16x16x32_bf16 v[40:43], a[48:51], v[152:155], v[40:43]\n"
    "ds_read_b128 a[8:11], v1 offset:2048\n"
    "v_mfma_f32_16x16x32_bf16 v[44:47], a[48:51], v[156:159], v[44:47]\n"
    "ds_read_b128 a[12:15], v1 offset:3072\n"
    "buffer_load_dwordx4 v1, s[52:55], s65 offen offset:1024 sc1 lds\n"
    "v_mfma_f32_16x16x32_bf16 v[48:51], a[52:55], v[152:155], v[48:51]\n"
    "v_mfma_f32_16x16x32_bf16 v[52:55], a[52:55], v[156:159], v[52:55]\n"
    "ds_read_b128 a[16:19], v1 offset:4096\n"
    "v_mfma_f32_16x16x32_bf16 v[56:59], a[56:59], v[152:155], v[56:59]\n"
    "ds_read_b128 a[20:23], v1 offset:5120\n"
    "buffer_load_dwordx4 v[148:151], v1, s[56:59], s66 offen offset:1024 sc1\n"
    "buffer_load_dwordx4 v1, s[52:55], s65 offen offset:2048 sc1 lds\n"
    "v_mfma_f32_16x16x32_bf16 v[60:63], a[56:59], v[156:159], v[60:63]\n"
    "buffer_load_dwordx4 v1, s[52:55], s65 offen offset:3072 sc1 lds\n"
    "v_mfma_f32_16x16x32_bf16 v[64:67], a[60:63], v[152:155], v[64:67]\n"
    "ds_read_b128 a[24:27], v1 offset:6144\n"
    "v_mfma_f32_16x16x32_bf16 v[68:71], a[60:63], v[156:159], v[68:71]\n"
    "s_add_u32 m0, s64, 0xb000\n"
    "ds_read_b128 a[28:31], v1 offset:7168\n"
    "buffer_load_dwordx4 v2, s[52:55], s65 offen offset:0 sc1 lds\n"
    "v_mfma_f32_16x16x32_bf16 v[72:75], a[64:67], v[152:155], v[72:75]\n"
    "v_mfma_f32_16x16x32_bf16 v[76:79], a[64:67], v[156:159], v[76:79]\n"
    "ds_read_b128 a[32:35], v1 offset:8192\n"
    "v_mfma_f32_16x16x32_bf16 v[80:83], a[68:71], v[152:155], v[80:83]\n"
    "ds_read_b128 a[36:39], v1 offset:9216\n"
    "v_mfma_f32_16x16x32_bf16 v[84:87], a[68:71], v[156:159], v[84:87]\n"
    "v_mfma_f32_16x16x32_bf16 v[88:91], a[72:75], v[152:155], v[88:91]\n"
    "v_mfma_f32_16x16x32_bf16 v[92:95], a[72:75], v[156:159], v[92:95]\n"
    "v_mfma_f32_16x16x32_bf16 v[96:99], a[76:79], v[152:155], v[96:99]\n"
    "v_mfma_f32_16x16x32_bf16 v[100:103], a[76:79], v[156:159], v[100:103]\n"
    "s_waitcnt vmcnt(17) lgkmcnt(9)\n"
    "v_mfma_f32_16x16x32_bf16 v[24:27], a[0:3], v[160:163], v[24:27]\n"
    "s_waitcnt vmcnt(16)\n"
    "v_mfma_f32_16x16x32_bf16 v[28:31], a[0:3], v[164:167], v[28:31]\n"
    "ds_read_b128 a[40:43], v1 offset:10240\n"
    "s_waitcnt lgkmcnt(9)\n"
    "v_mfma_f32_16x16x32_bf16 v[32:35], a[4:7], v[160:163], v[32:35]\n"
    "ds_read_b128 a[44:47], v1 offset:11264\n"
    "s_mov_b32 s66, 0xc000\n"
    "buffer_load_dwordx4 v[152:155], v1, s[56:59], s66 offen offset:0 sc1\n"
    "v_mfma_f32_16x16x32_bf16 v[36:39], a[4:7], v[164:167], v[36:39]\n"
    "s_waitcnt lgkmcnt(9)\n"
    "v_mfma_f32_16x16x32_bf16 v[40:43], a[8:11], v[160:163], v[40:43]\n"
    "ds_read_b128 a[48:51], v1 offset:12288\n"
    "v_mfma_f32_16x16x32_bf16 v[44:47], a[8:11], v[164:167], v[44:47]\n"
    "ds_read_b128 a[52:55], v1 offset:13312\n"
    "s_waitcnt lgkmcnt(10)\n"
    "v_mfma_f32_16x16x32_bf16 v[48:51], a[12:15], v[160:163], v[48:51]\n"
    "v_mfma_f32_16x16x32_bf16 v[52:55], a[12:15], v[164:167], v[52:55]\n"
    "ds_read_b128 a[56:59], v1 offset:14336\n"
    "s_waitcnt lgkmcnt(10)\n"
    "v_mfma_f32_16x16x32_bf16 v[56:59], a[16:19], v[160:163], v[56:59]\n"
    "ds_read_b128 a[60:63], v1 offset:15360\n"
    "buffer_load_dwordx4 v[156:159], v1, s[56:59], s66 offen offset:1024 sc1\n"
    "v_mfma_f32_16x16x32_bf16 v[60:63], a[16:19], v[164:167], v[60:63]\n"
    "s_waitcnt lgkmcnt(10)\n"
    "v_mfma_f32_16x16x32_bf16 v[64:67], a[20:23], v[160:163], v[64:67]\n"
    "ds_read_b128 a[64:67], v1 offset:16384\n"
    "v_mfma_f32_16x16x32_bf16 v[68:71], a[20:23], v[164:167], v[68:71]\n"
    "ds_read_b128 a[68:71], v1 offset:17408\n"
    "s_waitcnt lgkmcnt(11)\n"
    "v_mfma_f32_16x16x32_bf16 v[72:75], a[24:27], v[160:163], v[72:75]\n"
    "v_mfma_f32_16x16x32_bf16 v[76:79], a[24:27], v[164:167], v[76:79]\n"
    "ds_read_b128 a[72:75], v1 offset:18432\n"
    "s_waitcnt lgkmcnt(11)\n"
    "v_mfma_f32_16x16x32_bf16 v[80:83], a[28:31], v[160:163], v[80:83]\n"
    "ds_read_b128 a[76:79], v1 offset:19456\n"
    "v_mfma_f32_16x16x32_bf16 v[84:87], a[28:31], v[164:167], v[84:87]\n"
    "s_waitcnt lgkmcnt(11)\n"
    "v_mfma_f32_16x16x32_bf16 v[88:91], a[32:35], v[160:163], v[88:91]\n"
    "v_mfma_f32_16x16x32_bf16 v[92:95], a[32:35], v[164:167], v[92:95]\n"
    "s_waitcnt lgkmcnt(10)\n"
    "v_mfma_f32_16x16x32_bf16 v[96:99], a[36:39], v[160:163], v[96:99]\n"
    "v_mfma_f32_16x16x32_bf16 v[100:103], a[36:39], v[164:167], v[100:103]\n"
    "s_waitcnt vmcnt(17) lgkmcnt(9)\n"
    "v_mfma_f32_16x16x32_bf16 v[24:27], a[40:43], v[168:171], v[24:27]\n"
    "s_waitcnt vmcnt(11)\n"
    "s_waitcnt lgkmcnt(0)\n"
    "s_barrier\n"
    "v_mfma_f32_16x16x32_bf16 v[28:31], a[40:43], v[172:175], v[28:31]\n"
    "ds_read_b128 a[0:3], v1 offset:20480\n"
    "v_mfma_f32_16x16x32_bf16 v[32:35], a[44:47], v[168:171], v[32:35]\n"
    "ds_read_b128 a[4:7], v1 offset:21504\n"
    "s_mov_b32 s66, 0xd000\n"
    "buffer_load_dwordx4 v[160:163], v1, s[56:59], s66 offen offset:0 sc1\n"
    "v_mfma_f32_16x16x32_bf16 v[36:39], a[44:47], v[172:175], v[36:39]\n"
    "s_mov_b32 s65, 0x8c000\n"
    "s_add_u32 m0, s64, 0xf000\n"
    "s_nop 0\n"
    "buffer_load_dwordx4 v1, s[52:55], s65 offen offset:0 sc1 lds\n"
    "v_mfma_f32_16x16x32_bf16 v[40:43], a[48:51], v[168:171], v[40:43]\n"
    "ds_read_b128 a[8:11], v1 offset:22528\n"
    "v_mfma_f32_16x16x32_bf16 v[44:47], a[48:51], v[172:175], v[44:47]\n"
    "ds_read_b128 a[12:15], v1 offset:23552\n"
    "buffer_load_dwordx4 v1, s[52:55], s65 offen offset:1024 sc1 lds\n"
    "v_mfma_f32_16x16x32_bf16 v[48:51], a[52:55], v[168:171], v[48:51]\n"
    "v_mfma_f32_16x16x32_bf16 v[52:55], a[52:55], v[172:175], v[52:55]\n"
    "ds_read_b128 a[16:19], v1 offset:24576\n"
    "v_mfma_f32_16x16x32_bf16 v[56:59], a[56:59], v[168:171], v[56:59]\n"
    "ds_read_b128 a[20:23], v1 offset:25600\n"
    "buffer_load_dwordx4 v[164:167], v1, s[56:59], s66 offen offset:1024 sc1\n"
    "buffer_load_dwordx4 v1, s[52:55], s65 offen offset:2048 sc1 lds\n"
    "v_mfma_f32_16x16x32_bf16 v[60:63], a[56:59], v[172:175], v[60:63]\n"
    "buffer_load_dwordx4 v1, s[52:55], s65 offen offset:3072 sc1 lds\n"
    "v_mfma_f32_16x16x32_bf16 v[64:67], a[60:63], v[168:171], v[64:67]\n"
    "ds_read_b128 a[24:27], v1 offset:26624\n"
    "v_mfma_f32_16x16x32_bf16 v[68:71], a[60:63], v[172:175], v[68:71]\n"
    "s_add_u32 m0, s64, 0x10000\n"
    "ds_read_b128 a[28:31], v1 offset:27648\n"
    "buffer_load_dwordx4 v2, s[52:55], s65 offen offset:0 sc1 lds\n"
    "v_mfma_f32_16x16x32_bf16 v[72:75], a[64:67], v[168:171], v[72:75]\n"
    "v_mfma_f32_16x16x32_bf16 v[76:79], a[64:67], v[172:175], v[76:79]\n"
    "ds_read_b128 a[32:35], v1 offset:28672\n"
    "v_mfma_f32_16x16x32_bf16 v[80:83], a[68:71], v[168:171], v[80:83]\n"
    "ds_read_b128 a[36:39], v1 offset:29696\n"
    "v_mfma_f32_16x16x32_bf16 v[84:87], a[68:71], v[172:175], v[84:87]\n"
    "v_mfma_f32_16x16x32_bf16 v[88:91], a[72:75], v[168:171], v[88:91]\n"
    "v_mfma_f32_16x16x32_bf16 v[92:95], a[72:75], v[172:175], v[92:95]\n"
    "v_mfma_f32_16x16x32_bf16 v[96:99], a[76:79], v[168:171], v[96:99]\n"
    "v_mfma_f32_16x16x32_bf16 v[100:103], a[76:79], v[172:175], v[100:103]\n"
    "s_waitcnt vmcnt(17) lgkmcnt(9)\n"
    "v_mfma_f32_16x16x32_bf16 v[24:27], a[0:3], v[136:139], v[24:27]\n"
    "s_waitcnt vmcnt(16)\n"
    "v_mfma_f32_16x16x32_bf16 v[28:31], a[0:3], v[140:143], v[28:31]\n"
    "ds_read_b128 a[40:43], v1 offset:30720\n"
    "s_waitcnt lgkmcnt(9)\n"
    "v_mfma_f32_16x16x32_bf16 v[32:35], a[4:7], v[136:139], v[32:35]\n"
    "ds_read_b128 a[44:47], v1 offset:31744\n"
    "s_mov_b32 s66, 0xe000\n"
    "buffer_load_dwordx4 v[168:171], v1, s[56:59], s66 offen offset:0 sc1\n"
    "v_mfma_f32_16x16x32_bf16 v[36:39], a[4:7], v[140:143], v[36:39]\n"
    "s_waitcnt lgkmcnt(9)\n"
    "v_mfma_f32_16x16x32_bf16 v[40:43], a[8:11], v[136:139], v[40:43]\n"
    "ds_read_b128 a[48:51], v1 offset:32768\n"
    "v_mfma_f32_16x16x32_bf16 v[44:47], a[8:11], v[140:143], v[44:47]\n"
    "ds_read_b128 a[52:55], v1 offset:33792\n"
    "s_waitcnt lgkmcnt(10)\n"
    "v_mfma_f32_16x16x32_bf16 v[48:51], a[12:15], v[136:139], v[48:51]\n"
    "v_mfma_f32_16x16x32_bf16 v[52:55], a[12:15], v[140:143], v[52:55]\n"
    "ds_read_b128 a[56:59], v1 offset:34816\n"
    "s_waitcnt lgkmcnt(10)\n"
    "v_mfma_f32_16x16x32_bf16 v[56:59], a[16:19], v[136:139], v[56:59]\n"
    "ds_read_b128 a[60:63], v1 offset:35840\n"
    "buffer_load_dwordx4 v[172:175], v1, s[56:59], s66 offen offset:1024 sc1\n"
    "v_mfma_f32_16x16x32_bf16 v[60:63], a[16:19], v[140:143], v[60:63]\n"
    "s_waitcnt lgkmcnt(10)\n"
    "v_mfma_f32_16x16x32_bf16 v[64:67], a[20:23], v[136:139], v[64:67]\n"
    "ds_read_b128 a[64:67], v1 offset:36864\n"
    "v_mfma_f32_16x16x32_bf16 v[68:71], a[20:23], v[140:143], v[68:71]\n"
    "ds_read_b128 a[68:71], v1 offset:37888\n"
    "s_waitcnt lgkmcnt(11)\n"
    "v_mfma_f32_16x16x32_bf16 v[72:75], a[24:27], v[136:139], v[72:75]\n"
    "v_mfma_f32_16x16x32_bf16 v[76:79], a[24:27], v[140:143], v[76:79]\n"
    "ds_read_b128 a[72:75], v1 offset:38912\n"
    "s_waitcnt lgkmcnt(11)\n"
    "v_mfma_f32_16x16x32_bf16 v[80:83], a[28:31], v[136:139], v[80:83]\n"
    "ds_read_b128 a[76:79], v1 offset:39936\n"
    "v_mfma_f32_16x16x32_bf16 v[84:87], a[28:31], v[140:143], v[84:87]\n"
    "s_waitcnt lgkmcnt(11)\n"
    "v_mfma_f32_16x16x32_bf16 v[88:91], a[32:35], v[136:139], v[88:91]\n"
    "v_mfma_f32_16x16x32_bf16 v[92:95], a[32:35], v[140:143], v[92:95]\n"
    "s_waitcnt lgkmcnt(10)\n"
    "v_mfma_f32_16x16x32_bf16 v[96:99], a[36:39], v[136:139], v[96:99]\n"
    "v_mfma_f32_16x16x32_bf16 v[100:103], a[36:39], v[140:143], v[100:103]\n"
    "s_waitcnt vmcnt(17) lgkmcnt(9)\n"
    "v_mfma_f32_16x16x32_bf16 v[24:27], a[40:43], v[144:147], v[24:27]\n"
    "s_waitcnt vmcnt(11)\n"
    "s_waitcnt lgkmcnt(0)\n"
    "s_barrier\n"
    "v_mfma_f32_16x16x32_bf16 v[28:31], a[40:43], v[148:151], v[28:31]\n"
    "ds_read_b128 a[0:3], v1 offset:40960\n"
    "v_mfma_f32_16x16x32_bf16 v[32:35], a[44:47], v[144:147], v[32:35]\n"
    "ds_read_b128 a[4:7], v1 offset:41984\n"
    "s_mov_b32 s66, 0xf000\n"
    "buffer_load_dwordx4 v[136:139], v1, s[56:59], s66 offen offset:0 sc1\n"
    "v_mfma_f32_16x16x32_bf16 v[36:39], a[44:47], v[148:151], v[36:39]\n"
    "s_mov_b32 s65, 0xa0000\n"
    "s_add_u32 m0, s64, 0\n"
    "s_nop 0\n"
    "buffer_load_dwordx4 v1, s[52:55], s65 offen offset:0 sc1 lds\n"
    "v_mfma_f32_16x16x32_bf16 v[40:43], a[48:51], v[144:147], v[40:43]\n"
    "ds_read_b128 a[8:11], v1 offset:43008\n"
    "v_mfma_f32_16x16x32_bf16 v[44:47], a[48:51], v[148:151], v[44:47]\n"
    "ds_read_b128 a[12:15], v1 offset:44032\n"
    "buffer_load_dwordx4 v1, s[52:55], s65 offen offset:1024 sc1 lds\n"
    "v_mfma_f32_16x16x32_bf16 v[48:51], a[52:55], v[144:147], v[48:51]\n"
    "v_mfma_f32_16x16x32_bf16 v[52:55], a[52:55], v[148:151], v[52:55]\n"
    "ds_read_b128 a[16:19], v1 offset:45056\n"
    "v_mfma_f32_16x16x32_bf16 v[56:59], a[56:59], v[144:147], v[56:59]\n"
    "ds_read_b128 a[20:23], v1 offset:46080\n"
    "buffer_load_dwordx4 v[140:143], v1, s[56:59], s66 offen offset:1024 sc1\n"
    "buffer_load_dwordx4 v1, s[52:55], s65 offen offset:2048 sc1 lds\n"
    "v_mfma_f32_16x16x32_bf16 v[60:63], a[56:59], v[148:151], v[60:63]\n"
    "buffer_load_dwordx4 v1, s[52:55], s65 offen offset:3072 sc1 lds\n"
    "v_mfma_f32_16x16x32_bf16 v[64:67], a[60:63], v[144:147], v[64:67]\n"
    "ds_read_b128 a[24:27], v1 offset:47104\n"
    "v_mfma_f32_16x16x32_bf16 v[68:71], a[60:63], v[148:151], v[68:71]\n"
    "s_add_u32 m0, s64, 0x1000\n"
    "ds_read_b128 a[28:31], v1 offset:48128\n"
    "buffer_load_dwordx4 v2, s[52:55], s65 offen offset:0 sc1 lds\n"
    "v_mfma_f32_16x16x32_bf16 v[72:75], a[64:67], v[144:147], v[72:75]\n"
    "v_mfma_f32_16x16x32_bf16 v[76:79], a[64:67], v[148:151], v[76:79]\n"
    "ds_read_b128 a[32:35], v1 offset:49152\n"
    "v_mfma_f32_16x16x32_bf16 v[80:83], a[68:71], v[144:147], v[80:83]\n"
    "ds_read_b128 a[36:39], v1 offset:50176\n"
    "v_mfma_f32_16x16x32_bf16 v[84:87], a[68:71], v[148:151], v[84:87]\n"
    "v_mfma_f32_16x16x32_bf16 v[88:91], a[72:75], v[144:147], v[88:91]\n"
    "v_mfma_f32_16x16x32_bf16 v[92:95], a[72:75], v[148:151], v[92:95]\n"
    "v_mfma_f32_16x16x32_bf16 v[96:99], a[76:79], v[144:147], v[96:99]\n"
    "v_mfma_f32_16x16x32_bf16 v[100:103], a[76:79], v[148:151], v[100:103]\n"
    "s_waitcnt vmcnt(17) lgkmcnt(9)\n"
    "v_mfma_f32_16x16x32_bf16 v[24:27], a[0:3], v[152:155], v[24:27]\n"
    "s_waitcnt vmcnt(16)\n"
    "v_mfma_f32_16x16x32_bf16 v[28:31], a[0:3], v[156:159], v[28:31]\n"
    "ds_read_b128 a[40:43], v1 offset:51200\n"
    "s_waitcnt lgkmcnt(9)\n"
    "v_mfma_f32_16x16x32_bf16 v[32:35], a[4:7], v[152:155], v[32:35]\n"
    "ds_read_b128 a[44:47], v1 offset:52224\n"
    "s_mov_b32 s66, 0x10000\n"
    "buffer_load_dwordx4 v[144:147], v1, s[56:59], s66 offen offset:0 sc1\n"
    "v_mfma_f32_16x16x32_bf16 v[36:39], a[4:7], v[156:159], v[36:39]\n"
    "s_waitcnt lgkmcnt(9)\n"
    "v_mfma_f32_16x16x32_bf16 v[40:43], a[8:11], v[152:155], v[40:43]\n"
    "ds_read_b128 a[48:51], v1 offset:53248\n"
    "v_mfma_f32_16x16x32_bf16 v[44:47], a[8:11], v[156:159], v[44:47]\n"
    "ds_read_b128 a[52:55], v1 offset:54272\n"
    "s_waitcnt lgkmcnt(10)\n"
    "v_mfma_f32_16x16x32_bf16 v[48:51], a[12:15], v[152:155], v[48:51]\n"
    "v_mfma_f32_16x16x32_bf16 v[52:55], a[12:15], v[156:159], v[52:55]\n"
    "ds_read_b128 a[56:59], v1 offset:55296\n"
    "s_waitcnt lgkmcnt(10)\n"
    "v_mfma_f32_16x16x32_bf16 v[56:59], a[16:19], v[152:155], v[56:59]\n"
    "ds_read_b128 a[60:63], v1 offset:56320\n"
    "buffer_load_dwordx4 v[148:151], v1, s[56:59], s66 offen offset:1024 sc1\n"
    "v_mfma_f32_16x16x32_bf16 v[60:63], a[16:19], v[156:159], v[60:63]\n"
    "s_waitcnt lgkmcnt(10)\n"
    "v_mfma_f32_16x16x32_bf16 v[64:67], a[20:23], v[152:155], v[64:67]\n"
    "ds_read_b128 a[64:67], v1 offset:57344\n"
    "v_mfma_f32_16x16x32_bf16 v[68:71], a[20:23], v[156:159], v[68:71]\n"
    "ds_read_b128 a[68:71], v1 offset:58368\n"
    "s_waitcnt lgkmcnt(11)\n"
    "v_mfma_f32_16x16x32_bf16 v[72:75], a[24:27], v[152:155], v[72:75]\n"
    "v_mfma_f32_16x16x32_bf16 v[76:79], a[24:27], v[156:159], v[76:79]\n"
    "ds_read_b128 a[72:75], v1 offset:59392\n"
    "s_waitcnt lgkmcnt(11)\n"
    "v_mfma_f32_16x16x32_bf16 v[80:83], a[28:31], v[152:155], v[80:83]\n"
    "ds_read_b128 a[76:79], v1 offset:60416\n"
    "v_mfma_f32_16x16x32_bf16 v[84:87], a[28:31], v[156:159], v[84:87]\n"
    "s_waitcnt lgkmcnt(11)\n"
    "v_mfma_f32_16x16x32_bf16 v[88:91], a[32:35], v[152:155], v[88:91]\n"
    "v_mfma_f32_16x16x32_bf16 v[92:95], a[32:35], v[156:159], v[92:95]\n"
    "s_waitcnt lgkmcnt(10)\n"
    "v_mfma_f32_16x16x32_bf16 v[96:99], a[36:39], v[152:155], v[96:99]\n"
    "v_mfma_f32_16x16x32_bf16 v[100:103], a[36:39], v[156:159], v[100:103]\n"
    "s_waitcnt vmcnt(17) lgkmcnt(9)\n"
    "v_mfma_f32_16x16x32_bf16 v[24:27], a[40:43], v[160:163], v[24:27]\n"
    "s_waitcnt vmcnt(11)\n"
    "s_waitcnt lgkmcnt(0)\n"
    "s_barrier\n"
    "v_mfma_f32_16x16x32_bf16 v[28:31], a[40:43], v[164:167], v[28:31]\n"
    "ds_read_b128 a[0:3], v1 offset:61440\n"
    "v_mfma_f32_16x16x32_bf16 v[32:35], a[44:47], v[160:163], v[32:35]\n"
    "ds_read_b128 a[4:7], v1 offset:62464\n"
    "s_mov_b32 s66, 0x11000\n"
    "buffer_load_dwordx4 v[152:155], v1, s[56:59], s66 offen offset:0 sc1\n"
    "v_mfma_f32_16x16x32_bf16 v[36:39], a[44:47], v[164:167], v[36:39]\n"
    "s_mov_b32 s65, 0xb4000\n"
    "s_add_u32 m0, s64, 0x5000\n"
    "s_nop 0\n"
    "buffer_load_dwordx4 v1, s[52:55], s65 offen offset:0 sc1 lds\n"
    "v_mfma_f32_16x16x32_bf16 v[40:43], a[48:51], v[160:163], v[40:43]\n"
    "ds_read_b128 a[8:11], v1 offset:63488\n"
    "v_mfma_f32_16x16x32_bf16 v[44:47], a[48:51], v[164:167], v[44:47]\n"
    "ds_read_b128 a[12:15], v1 offset:64512\n"
    "buffer_load_dwordx4 v1, s[52:55], s65 offen offset:1024 sc1 lds\n"
    "v_mfma_f32_16x16x32_bf16 v[48:51], a[52:55], v[160:163], v[48:51]\n"
    "v_mfma_f32_16x16x32_bf16 v[52:55], a[52:55], v[164:167], v[52:55]\n"
    "ds_read_b128 a[16:19], v3 offset:0\n"
    "v_mfma_f32_16x16x32_bf16 v[56:59], a[56:59], v[160:163], v[56:59]\n"
    "ds_read_b128 a[20:23], v3 offset:1024\n"
    "buffer_load_dwordx4 v[156:159], v1, s[56:59], s66 offen offset:1024 sc1\n"
    "buffer_load_dwordx4 v1, s[52:55], s65 offen offset:2048 sc1 lds\n"
    "v_mfma_f32_16x16x32_bf16 v[60:63], a[56:59], v[164:167], v[60:63]\n"
    "buffer_load_dwordx4 v1, s[52:55], s65 offen offset:3072 sc1 lds\n"
    "v_mfma_f32_16x16x32_bf16 v[64:67], a[60:63], v[160:163], v[64:67]\n"
    "ds_read_b128 a[24:27], v3 offset:2048\n"
    "v_mfma_f32_16x16x32_bf16 v[68:71], a[60:63], v[164:167], v[68:71]\n"
    "s_add_u32 m0, s64, 0x6000\n"
    "ds_read_b128 a[28:31], v3 offset:3072\n"
    "buffer_load_dwordx4 v2, s[52:55], s65 offen offset:0 sc1 lds\n"
    "v_mfma_f32_16x16x32_bf16 v[72:75], a[64:67], v[160:163], v[72:75]\n"
    "v_mfma_f32_16x16x32_bf16 v[76:79], a[64:67], v[164:167], v[76:79]\n"
    "ds_read_b128 a[32:35], v3 offset:4096\n"
    "v_mfma_f32_16x16x32_bf16 v[80:83], a[68:71], v[160:163], v[80:83]\n"
    "ds_read_b128 a[36:39], v3 offset:5120\n"
    "v_mfma_f32_16x16x32_bf16 v[84:87], a[68:71], v[164:167], v[84:87]\n"
    "v_mfma_f32_16x16x32_bf16 v[88:91], a[72:75], v[160:163], v[88:91]\n"
    "v_mfma_f32_16x16x32_bf16 v[92:95], a[72:75], v[164:167], v[92:95]\n"
    "v_mfma_f32_16x16x32_bf16 v[96:99], a[76:79], v[160:163], v[96:99]\n"
    "v_mfma_f32_16x16x32_bf16 v[100:103], a[76:79], v[164:167], v[100:103]\n"
    "s_waitcnt vmcnt(17) lgkmcnt(9)\n"
    "v_mfma_f32_16x16x32_bf16 v[24:27], a[0:3], v[168:171], v[24:27]\n"
    "s_waitcnt vmcnt(16)\n"
    "v_mfma_f32_16x16x32_bf16 v[28:31], a[0:3], v[172:175], v[28:31]\n"
    "ds_read_b128 a[40:43], v3 offset:6144\n"
    "s_waitcnt lgkmcnt(9)\n"
    "v_mfma_f32_16x16x32_bf16 v[32:35], a[4:7], v[168:171], v[32:35]\n"
    "ds_read_b128 a[44:47], v3 offset:7168\n"
    "s_mov_b32 s66, 0x12000\n"
    "buffer_load_dwordx4 v[160:163], v1, s[56:59], s66 offen offset:0 sc1\n"
    "v_mfma_f32_16x16x32_bf16 v[36:39], a[4:7], v[172:175], v[36:39]\n"
    "s_waitcnt lgkmcnt(9)\n"
    "v_mfma_f32_16x16x32_bf16 v[40:43], a[8:11], v[168:171], v[40:43]\n"
    "ds_read_b128 a[48:51], v3 offset:8192\n"
    "v_mfma_f32_16x16x32_bf16 v[44:47], a[8:11], v[172:175], v[44:47]\n"
    "ds_read_b128 a[52:55], v3 offset:9216\n"
    "s_waitcnt lgkmcnt(10)\n"
    "v_mfma_f32_16x16x32_bf16 v[48:51], a[12:15], v[168:171], v[48:51]\n"
    "v_mfma_f32_16x16x32_bf16 v[52:55], a[12:15], v[172:175], v[52:55]\n"
    "ds_read_b128 a[56:59], v3 offset:10240\n"
    "s_waitcnt lgkmcnt(10)\n"
    "v_mfma_f32_16x16x32_bf16 v[56:59], a[16:19], v[168:171], v[56:59]\n"
    "ds_read_b128 a[60:63], v3 offset:11264\n"
    "buffer_load_dwordx4 v[164:167], v1, s[56:59], s66 offen offset:1024 sc1\n"
    "v_mfma_f32_16x16x32_bf16 v[60:63], a[16:19], v[172:175], v[60:63]\n"
    "s_waitcnt lgkmcnt(10)\n"
    "v_mfma_f32_16x16x32_bf16 v[64:67], a[20:23], v[168:171], v[64:67]\n"
    "ds_read_b128 a[64:67], v3 offset:12288\n"
    "v_mfma_f32_16x16x32_bf16 v[68:71], a[20:23], v[172:175], v[68:71]\n"
    "ds_read_b128 a[68:71], v3 offset:13312\n"
    "s_waitcnt lgkmcnt(11)\n"
    "v_mfma_f32_16x16x32_bf16 v[72:75], a[24:27], v[168:171], v[72:75]\n"
    "v_mfma_f32_16x16x32_bf16 v[76:79], a[24:27], v[172:175], v[76:79]\n"
    "ds_read_b128 a[72:75], v3 offset:14336\n"
    "s_waitcnt lgkmcnt(11)\n"
    "v_mfma_f32_16x16x32_bf16 v[80:83], a[28:31], v[168:171], v[80:83]\n"
    "ds_read_b128 a[76:79], v3 offset:15360\n"
    "v_mfma_f32_16x16x32_bf16 v[84:87], a[28:31], v[172:175], v[84:87]\n"
    "s_waitcnt lgkmcnt(11)\n"
    "v_mfma_f32_16x16x32_bf16 v[88:91], a[32:35], v[168:171], v[88:91]\n"
    "v_mfma_f32_16x16x32_bf16 v[92:95], a[32:35], v[172:175], v[92:95]\n"
    "s_waitcnt lgkmcnt(10)\n"
    "v_mfma_f32_16x16x32_bf16 v[96:99], a[36:39], v[168:171], v[96:99]\n"
    "v_mfma_f32_16x16x32_bf16 v[100:103], a[36:39], v[172:175], v[100:103]\n"
    "s_waitcnt vmcnt(17) lgkmcnt(9)\n"
    "v_mfma_f32_16x16x32_bf16 v[24:27], a[40:43], v[136:139], v[24:27]\n"
    "s_waitcnt vmcnt(11)\n"
    "s_waitcnt lgkmcnt(0)\n"
    "s_barrier\n"
    "v_mfma_f32_16x16x32_bf16 v[28:31], a[40:43], v[140:143], v[28:31]\n"
    "ds_read_b128 a[0:3], v1 offset:0\n"
    "v_mfma_f32_16x16x32_bf16 v[32:35], a[44:47], v[136:139], v[32:35]\n"
    "ds_read_b128 a[4:7], v1 offset:1024\n"
    "s_mov_b32 s66, 0x13000\n"
    "buffer_load_dwordx4 v[168:171], v1, s[56:59], s66 offen offset:0 sc1\n"
    "v_mfma_f32_16x16x32_bf16 v[36:39], a[44:47], v[140:143], v[36:39]\n"
    "s_mov_b32 s65, 0xc8000\n"
    "s_add_u32 m0, s64, 0xa000\n"
    "s_nop 0\n"
    "buffer_load_dwordx4 v1, s[52:55], s65 offen offset:0 sc1 lds\n"
    "v_mfma_f32_16x16x32_bf16 v[40:43], a[48:51], v[136:139], v[40:43]\n"
    "ds_read_b128 a[8:11], v1 offset:2048\n"
    "v_mfma_f32_16x16x32_bf16 v[44:47], a[48:51], v[140:143], v[44:47]\n"
    "ds_read_b128 a[12:15], v1 offset:3072\n"
    "buffer_load_dwordx4 v1, s[52:55], s65 offen offset:1024 sc1 lds\n"
    "v_mfma_f32_16x16x32_bf16 v[48:51], a[52:55], v[136:139], v[48:51]\n"
    "v_mfma_f32_16x16x32_bf16 v[52:55], a[52:55], v[140:143], v[52:55]\n"
    "ds_read_b128 a[16:19], v1 offset:4096\n"
    "v_mfma_f32_16x16x32_bf16 v[56:59], a[56:59], v[136:139], v[56:59]\n"
    "ds_read_b128 a[20:23], v1 offset:5120\n"
    "buffer_load_dwordx4 v[172:175], v1, s[56:59], s66 offen offset:1024 sc1\n"
    "buffer_load_dwordx4 v1, s[52:55], s65 offen offset:2048 sc1 lds\n"
    "v_mfma_f32_16x16x32_bf16 v[60:63], a[56:59], v[140:143], v[60:63]\n"
    "buffer_load_dwordx4 v1, s[52:55], s65 offen offset:3072 sc1 lds\n"
    "v_mfma_f32_16x16x32_bf16 v[64:67], a[60:63], v[136:139], v[64:67]\n"
    "ds_read_b128 a[24:27], v1 offset:6144\n"
    "v_mfma_f32_16x16x32_bf16 v[68:71], a[60:63], v[140:143], v[68:71]\n"
    "s_add_u32 m0, s64, 0xb000\n"
    "ds_read_b128 a[28:31], v1 offset:7168\n"
    "buffer_load_dwordx4 v2, s[52:55], s65 offen offset:0 sc1 lds\n"
    "v_mfma_f32_16x16x32_bf16 v[72:75], a[64:67], v[136:139], v[72:75]\n"
    "v_mfma_f32_16x16x32_bf16 v[76:79], a[64:67], v[140:143], v[76:79]\n"
    "ds_read_b128 a[32:35], v1 offset:8192\n"
    "v_mfma_f32_16x16x32_bf16 v[80:83], a[68:71], v[136:139], v[80:83]\n"
    "ds_read_b128 a[36:39], v1 offset:9216\n"
    "v_mfma_f32_16x16x32_bf16 v[84:87], a[68:71], v[140:143], v[84:87]\n"
    "v_mfma_f32_16x16x32_bf16 v[88:91], a[72:75], v[136:139], v[88:91]\n"
    "v_mfma_f32_16x16x32_bf16 v[92:95], a[72:75], v[140:143], v[92:95]\n"
    "v_mfma_f32_16x16x32_bf16 v[96:99], a[76:79], v[136:139], v[96:99]\n"
    "v_mfma_f32_16x16x32_bf16 v[100:103], a[76:79], v[140:143], v[100:103]\n"
    "s_waitcnt vmcnt(17) lgkmcnt(9)\n"
    "v_mfma_f32_16x16x32_bf16 v[24:27], a[0:3], v[144:147], v[24:27]\n"
    "s_waitcnt vmcnt(16)\n"
    "v_mfma_f32_16x16x32_bf16 v[28:31], a[0:3], v[148:151], v[28:31]\n"
    "ds_read_b128 a[40:43], v1 offset:10240\n"
    "s_waitcnt lgkmcnt(9)\n"
    "v_mfma_f32_16x16x32_bf16 v[32:35], a[4:7], v[144:147], v[32:35]\n"
    "ds_read_b128 a[44:47], v1 offset:11264\n"
    "s_mov_b32 s66, 0x14000\n"
    "buffer_load_dwordx4 v[136:139], v1, s[56:59], s66 offen offset:0 sc1\n"
    "v_mfma_f32_16x16x32_bf16 v[36:39], a[4:7], v[148:151], v[36:39]\n"
    "s_waitcnt lgkmcnt(9)\n"
    "v_mfma_f32_16x16x32_bf16 v[40:43], a[8:11], v[144:147], v[40:43]\n"
    "ds_read_b128 a[48:51], v1 offset:12288\n"
    "v_mfma_f32_16x16x32_bf16 v[44:47], a[8:11], v[148:151], v[44:47]\n"
    "ds_read_b128 a[52:55], v1 offset:13312\n"
    "s_waitcnt lgkmcnt(10)\n"
    "v_mfma_f32_16x16x32_bf16 v[48:51], a[12:15], v[144:147], v[48:51]\n"
    "v_mfma_f32_16x16x32_bf16 v[52:55], a[12:15], v[148:151], v[52:55]\n"
    "ds_read_b128 a[56:59], v1 offset:14336\n"
    "s_waitcnt lgkmcnt(10)\n"
    "v_mfma_f32_16x16x32_bf16 v[56:59], a[16:19], v[144:147], v[56:59]\n"
    "ds_read_b128 a[60:63], v1 offset:15360\n"
    "buffer_load_dwordx4 v[140:143], v1, s[56:59], s66 offen offset:1024 sc1\n"
    "v_mfma_f32_16x16x32_bf16 v[60:63], a[16:19], v[148:151], v[60:63]\n"
    "s_waitcnt lgkmcnt(10)\n"
    "v_mfma_f32_16x16x32_bf16 v[64:67], a[20:23], v[144:147], v[64:67]\n"
    "ds_read_b128 a[64:67], v1 offset:16384\n"
    "v_mfma_f32_16x16x32_bf16 v[68:71], a[20:23], v[148:151], v[68:71]\n"
    "ds_read_b128 a[68:71], v1 offset:17408\n"
    "s_waitcnt lgkmcnt(11)\n"
    "v_mfma_f32_16x16x32_bf16 v[72:75], a[24:27], v[144:147], v[72:75]\n"
    "v_mfma_f32_16x16x32_bf16 v[76:79], a[24:27], v[148:151], v[76:79]\n"
    "ds_read_b128 a[72:75], v1 offset:18432\n"
    "s_waitcnt lgkmcnt(11)\n"
    "v_mfma_f32_16x16x32_bf16 v[80:83], a[28:31], v[144:147], v[80:83]\n"
    "ds_read_b128 a[76:79], v1 offset:19456\n"
    "v_mfma_f32_16x16x32_bf16 v[84:87], a[28:31], v[148:151], v[84:87]\n"
    "s_waitcnt lgkmcnt(11)\n"
    "v_mfma_f32_16x16x32_bf16 v[88:91], a[32:35], v[144:147], v[88:91]\n"
    "v_mfma_f32_16x16x32_bf16 v[92:95], a[32:35], v[148:151], v[92:95]\n"
    "s_waitcnt lgkmcnt(10)\n"
    "v_mfma_f32_16x16x32_bf16 v[96:99], a[36:39], v[144:147], v[96:99]\n"
    "v_mfma_f32_16x16x32_bf16 v[100:103], a[36:39], v[148:151], v[100:103]\n"
    "s_waitcnt vmcnt(17) lgkmcnt(9)\n"
    "v_mfma_f32_16x16x32_bf16 v[24:27], a[40:43], v[152:155], v[24:27]\n"
    "s_waitcnt vmcnt(11)\n"
    "s_waitcnt lgkmcnt(0)\n"
    "s_barrier\n"
    "v_mfma_f32_16x16x32_bf16 v[28:31], a[40:43], v[156:159], v[28:31]\n"
    "ds_read_b128 a[0:3], v1 offset:20480\n"
    "v_mfma_f32_16x16x32_bf16 v[32:35], a[44:47], v[152:155], v[32:35]\n"
    "ds_read_b128 a[4:7], v1 offset:21504\n"
    "s_mov_b32 s66, 0x15000\n"
    "buffer_load_dwordx4 v[144:147], v1, s[56:59], s66 offen offset:0 sc1\n"
    "v_mfma_f32_16x16x32_bf16 v[36:39], a[44:47], v[156:159], v[36:39]\n"
    "s_mov_b32 s65, 0xdc000\n"
    "s_add_u32 m0, s64, 0xf000\n"
    "s_nop 0\n"
    "buffer_load_dwordx4 v1, s[52:55], s65 offen offset:0 sc1 lds\n"
    "v_mfma_f32_16x16x32_bf16 v[40:43], a[48:51], v[152:155], v[40:43]\n"
    "ds_read_b128 a[8:11], v1 offset:22528\n"
    "v_mfma_f32_16x16x32_bf16 v[44:47], a[48:51], v[156:159], v[44:47]\n"
    "ds_read_b128 a[12:15], v1 offset:23552\n"
    "buffer_load_dwordx4 v1, s[52:55], s65 offen offset:1024 sc1 lds\n"
    "v_mfma_f32_16x16x32_bf16 v[48:51], a[52:55], v[152:155], v[48:51]\n"
    "v_mfma_f32_16x16x32_bf16 v[52:55], a[52:55], v[156:159], v[52:55]\n"
    "ds_read_b128 a[16:19], v1 offset:24576\n"
    "v_mfma_f32_16x16x32_bf16 v[56:59], a[56:59], v[152:155], v[56:59]\n"
    "ds_read_b128 a[20:23], v1 offset:25600\n"
    "buffer_load_dwordx4 v[148:151], v1, s[56:59], s66 offen offset:1024 sc1\n"
    "buffer_load_dwordx4 v1, s[52:55], s65 offen offset:2048 sc1 lds\n"
    "v_mfma_f32_16x16x32_bf16 v[60:63], a[56:59], v[156:159], v[60:63]\n"
    "buffer_load_dwordx4 v1, s[52:55], s65 offen offset:3072 sc1 lds\n"
    "v_mfma_f32_16x16x32_bf16 v[64:67], a[60:63], v[152:155], v[64:67]\n"
    "ds_read_b128 a[24:27], v1 offset:26624\n"
    "v_mfma_f32_16x16x32_bf16 v[68:71], a[60:63], v[156:159], v[68:71]\n"
    "s_add_u32 m0, s64, 0x10000\n"
    "ds_read_b128 a[28:31], v1 offset:27648\n"
    "buffer_load_dwordx4 v2, s[52:55], s65 offen offset:0 sc1 lds\n"
    "v_mfma_f32_16x16x32_bf16 v[72:75], a[64:67], v[152:155], v[72:75]\n"
    "v_mfma_f32_16x16x32_bf16 v[76:79], a[64:67], v[156:159], v[76:79]\n"
    "ds_read_b128 a[32:35], v1 offset:28672\n"
    "v_mfma_f32_16x16x32_bf16 v[80:83], a[68:71], v[152:155], v[80:83]\n"
    "ds_read_b128 a[36:39], v1 offset:29696\n"
    "v_mfma_f32_16x16x32_bf16 v[84:87], a[68:71], v[156:159], v[84:87]\n"
    "v_mfma_f32_16x16x32_bf16 v[88:91], a[72:75], v[152:155], v[88:91]\n"
    "v_mfma_f32_16x16x32_bf16 v[92:95], a[72:75], v[156:159], v[92:95]\n"
    "v_mfma_f32_16x16x32_bf16 v[96:99], a[76:79], v[152:155], v[96:99]\n"
    "v_mfma_f32_16x16x32_bf16 v[100:103], a[76:79], v[156:159], v[100:103]\n"
    "s_waitcnt vmcnt(17) lgkmcnt(9)\n"
    "v_mfma_f32_16x16x32_bf16 v[24:27], a[0:3], v[160:163], v[24:27]\n"
    "s_waitcnt vmcnt(16)\n"
    "v_mfma_f32_16x16x32_bf16 v[28:31], a[0:3], v[164:167], v[28:31]\n"
    "ds_read_b128 a[40:43], v1 offset:30720\n"
    "s_waitcnt lgkmcnt(9)\n"
    "v_mfma_f32_16x16x32_bf16 v[32:35], a[4:7], v[160:163], v[32:35]\n"
    "ds_read_b128 a[44:47], v1 offset:31744\n"
    "s_mov_b32 s66, 0x16000\n"
    "buffer_load_dwordx4 v[152:155], v1, s[56:59], s66 offen offset:0 sc1\n"
    "v_mfma_f32_16x16x32_bf16 v[36:39], a[4:7], v[164:167], v[36:39]\n"
    "s_waitcnt lgkmcnt(9)\n"
    "v_mfma_f32_16x16x32_bf16 v[40:43], a[8:11], v[160:163], v[40:43]\n"
    "ds_read_b128 a[48:51], v1 offset:32768\n"
    "v_mfma_f32_16x16x32_bf16 v[44:47], a[8:11], v[164:167], v[44:47]\n"
    "ds_read_b128 a[52:55], v1 offset:33792\n"
    "s_waitcnt lgkmcnt(10)\n"
    "v_mfma_f32_16x16x32_bf16 v[48:51], a[12:15], v[160:163], v[48:51]\n"
    "v_mfma_f32_16x16x32_bf16 v[52:55], a[12:15], v[164:167], v[52:55]\n"
    "ds_read_b128 a[56:59], v1 offset:34816\n"
    "s_waitcnt lgkmcnt(10)\n"
    "v_mfma_f32_16x16x32_bf16 v[56:59], a[16:19], v[160:163], v[56:59]\n"
    "ds_read_b128 a[60:63], v1 offset:35840\n"
    "buffer_load_dwordx4 v[156:159], v1, s[56:59], s66 offen offset:1024 sc1\n"
    "v_mfma_f32_16x16x32_bf16 v[60:63], a[16:19], v[164:167], v[60:63]\n"
    "s_waitcnt lgkmcnt(10)\n"
    "v_mfma_f32_16x16x32_bf16 v[64:67], a[20:23], v[160:163], v[64:67]\n"
    "ds_read_b128 a[64:67], v1 offset:36864\n"
    "v_mfma_f32_16x16x32_bf16 v[68:71], a[20:23], v[164:167], v[68:71]\n"
    "ds_read_b128 a[68:71], v1 offset:37888\n"
    "s_waitcnt lgkmcnt(11)\n"
    "v_mfma_f32_16x16x32_bf16 v[72:75], a[24:27], v[160:163], v[72:75]\n"
    "v_mfma_f32_16x16x32_bf16 v[76:79], a[24:27], v[164:167], v[76:79]\n"
    "ds_read_b128 a[72:75], v1 offset:38912\n"
    "s_waitcnt lgkmcnt(11)\n"
    "v_mfma_f32_16x16x32_bf16 v[80:83], a[28:31], v[160:163], v[80:83]\n"
    "ds_read_b128 a[76:79], v1 offset:39936\n"
    "v_mfma_f32_16x16x32_bf16 v[84:87], a[28:31], v[164:167], v[84:87]\n"
    "s_waitcnt lgkmcnt(11)\n"
    "v_mfma_f32_16x16x32_bf16 v[88:91], a[32:35], v[160:163], v[88:91]\n"
    "v_mfma_f32_16x16x32_bf16 v[92:95], a[32:35], v[164:167], v[92:95]\n"
    "s_waitcnt lgkmcnt(10)\n"
    "v_mfma_f32_16x16x32_bf16 v[96:99], a[36:39], v[160:163], v[96:99]\n"
    "v_mfma_f32_16x16x32_bf16 v[100:103], a[36:39], v[164:167], v[100:103]\n"
    "s_waitcnt vmcnt(17) lgkmcnt(9)\n"
    "v_mfma_f32_16x16x32_bf16 v[24:27], a[40:43], v[168:171], v[24:27]\n"
    "s_waitcnt vmcnt(11)\n"
    "s_waitcnt lgkmcnt(0)\n"
    "s_barrier\n"
    "v_mfma_f32_16x16x32_bf16 v[28:31], a[40:43], v[172:175], v[28:31]\n"
    "ds_read_b128 a[0:3], v1 offset:40960\n"
    "v_mfma_f32_16x16x32_bf16 v[32:35], a[44:47], v[168:171], v[32:35]\n"
    "ds_read_b128 a[4:7], v1 offset:41984\n"
    "s_mov_b32 s66, 0x17000\n"
    "buffer_load_dwordx4 v[160:163], v1, s[56:59], s66 offen offset:0 sc1\n"
    "v_mfma_f32_16x16x32_bf16 v[36:39], a[44:47], v[172:175], v[36:39]\n"
    "s_mov_b32 s65, 0xf0000\n"
    "s_add_u32 m0, s64, 0\n"
    "s_nop 0\n"
    "buffer_load_dwordx4 v1, s[52:55], s65 offen offset:0 sc1 lds\n"
    "v_mfma_f32_16x16x32_bf16 v[40:43], a[48:51], v[168:171], v[40:43]\n"
    "ds_read_b128 a[8:11], v1 offset:43008\n"
    "v_mfma_f32_16x16x32_bf16 v[44:47], a[48:51], v[172:175], v[44:47]\n"
    "ds_read_b128 a[12:15], v1 offset:44032\n"
    "buffer_load_dwordx4 v1, s[52:55], s65 offen offset:1024 sc1 lds\n"
    "v_mfma_f32_16x16x32_bf16 v[48:51], a[52:55], v[168:171], v[48:51]\n"
    "v_mfma_f32_16x16x32_bf16 v[52:55], a[52:55], v[172:175], v[52:55]\n"
    "ds_read_b128 a[16:19], v1 offset:45056\n"
    "v_mfma_f32_16x16x32_bf16 v[56:59], a[56:59], v[168:171], v[56:59]\n"
    "ds_read_b128 a[20:23], v1 offset:46080\n"
    "buffer_load_dwordx4 v[164:167], v1, s[56:59], s66 offen offset:1024 sc1\n"
    "buffer_load_dwordx4 v1, s[52:55], s65 offen offset:2048 sc1 lds\n"
    "v_mfma_f32_16x16x32_bf16 v[60:63], a[56:59], v[172:175], v[60:63]\n"
    "buffer_load_dwordx4 v1, s[52:55], s65 offen offset:3072 sc1 lds\n"
    "v_mfma_f32_16x16x32_bf16 v[64:67], a[60:63], v[168:171], v[64:67]\n"
    "ds_read_b128 a[24:27], v1 offset:47104\n"
    "v_mfma_f32_16x16x32_bf16 v[68:71], a[60:63], v[172:175], v[68:71]\n"
    "s_add_u32 m0, s64, 0x1000\n"
    "ds_read_b128 a[28:31], v1 offset:48128\n"
    "buffer_load_dwordx4 v2, s[52:55], s65 offen offset:0 sc1 lds\n"
    "v_mfma_f32_16x16x32_bf16 v[72:75], a[64:67], v[168:171], v[72:75]\n"
    "v_mfma_f32_16x16x32_bf16 v[76:79], a[64:67], v[172:175], v[76:79]\n"
    "ds_read_b128 a[32:35], v1 offset:49152\n"
    "v_mfma_f32_16x16x32_bf16 v[80:83], a[68:71], v[168:171], v[80:83]\n"
    "ds_read_b128 a[36:39], v1 offset:50176\n"
    "v_mfma_f32_16x16x32_bf16 v[84:87], a[68:71], v[172:175], v[84:87]\n"
    "v_mfma_f32_16x16x32_bf16 v[88:91], a[72:75], v[168:171], v[88:91]\n"
    "v_mfma_f32_16x16x32_bf16 v[92:95], a[72:75], v[172:175], v[92:95]\n"
    "v_mfma_f32_16x16x32_bf16 v[96:99], a[76:79], v[168:171], v[96:99]\n"
    "v_mfma_f32_16x16x32_bf16 v[100:103], a[76:79], v[172:175], v[100:103]\n"
    "s_waitcnt vmcnt(17) lgkmcnt(9)\n"
    "v_mfma_f32_16x16x32_bf16 v[24:27], a[0:3], v[136:139], v[24:27]\n"
    "s_waitcnt vmcnt(16)\n"
    "v_mfma_f32_16x16x32_bf16 v[28:31], a[0:3], v[140:143], v[28:31]\n"
    "ds_read_b128 a[40:43], v1 offset:51200\n"
    "s_waitcnt lgkmcnt(9)\n"
    "v_mfma_f32_16x16x32_bf16 v[32:35], a[4:7], v[136:139], v[32:35]\n"
    "ds_read_b128 a[44:47], v1 offset:52224\n"
    "s_mov_b32 s66, 0x18000\n"
    "buffer_load_dwordx4 v[168:171], v1, s[56:59], s66 offen offset:0 sc1\n"
    "v_mfma_f32_16x16x32_bf16 v[36:39], a[4:7], v[140:143], v[36:39]\n"
    "s_waitcnt lgkmcnt(9)\n"
    "v_mfma_f32_16x16x32_bf16 v[40:43], a[8:11], v[136:139], v[40:43]\n"
    "ds_read_b128 a[48:51], v1 offset:53248\n"
    "v_mfma_f32_16x16x32_bf16 v[44:47], a[8:11], v[140:143], v[44:47]\n"
    "ds_read_b128 a[52:55], v1 offset:54272\n"
    "s_waitcnt lgkmcnt(10)\n"
    "v_mfma_f32_16x16x32_bf16 v[48:51], a[12:15], v[136:139], v[48:51]\n"
    "v_mfma_f32_16x16x32_bf16 v[52:55], a[12:15], v[140:143], v[52:55]\n"
    "ds_read_b128 a[56:59], v1 offset:55296\n"
    "s_waitcnt lgkmcnt(10)\n"
    "v_mfma_f32_16x16x32_bf16 v[56:59], a[16:19], v[136:139], v[56:59]\n"
    "ds_read_b128 a[60:63], v1 offset:56320\n"
    "buffer_load_dwordx4 v[172:175], v1, s[56:59], s66 offen offset:1024 sc1\n"
    "v_mfma_f32_16x16x32_bf16 v[60:63], a[16:19], v[140:143], v[60:63]\n"
    "s_waitcnt lgkmcnt(10)\n"
    "v_mfma_f32_16x16x32_bf16 v[64:67], a[20:23], v[136:139], v[64:67]\n"
    "ds_read_b128 a[64:67], v1 offset:57344\n"
    "v_mfma_f32_16x16x32_bf16 v[68:71], a[20:23], v[140:143], v[68:71]\n"
    "ds_read_b128 a[68:71], v1 offset:58368\n"
    "s_waitcnt lgkmcnt(11)\n"
    "v_mfma_f32_16x16x32_bf16 v[72:75], a[24:27], v[136:139], v[72:75]\n"
    "v_mfma_f32_16x16x32_bf16 v[76:79], a[24:27], v[140:143], v[76:79]\n"
    "ds_read_b128 a[72:75], v1 offset:59392\n"
    "s_waitcnt lgkmcnt(11)\n"
    "v_mfma_f32_16x16x32_bf16 v[80:83], a[28:31], v[136:139], v[80:83]\n"
    "ds_read_b128 a[76:79], v1 offset:60416\n"
    "v_mfma_f32_16x16x32_bf16 v[84:87], a[28:31], v[140:143], v[84:87]\n"
    "s_waitcnt lgkmcnt(11)\n"
    "v_mfma_f32_16x16x32_bf16 v[88:91], a[32:35], v[136:139], v[88:91]\n"
    "v_mfma_f32_16x16x32_bf16 v[92:95], a[32:35], v[140:143], v[92:95]\n"
    "s_waitcnt lgkmcnt(10)\n"
    "v_mfma_f32_16x16x32_bf16 v[96:99], a[36:39], v[136:139], v[96:99]\n"
    "v_mfma_f32_16x16x32_bf16 v[100:103], a[36:39], v[140:143], v[100:103]\n"
    "s_waitcnt vmcnt(17) lgkmcnt(9)\n"
    "v_mfma_f32_16x16x32_bf16 v[24:27], a[40:43], v[144:147], v[24:27]\n"
    "s_waitcnt vmcnt(11)\n"
    "s_waitcnt lgkmcnt(0)\n"
    "s_barrier\n"
    "v_mfma_f32_16x16x32_bf16 v[28:31], a[40:43], v[148:151], v[28:31]\n"
    "ds_read_b128 a[0:3], v1 offset:61440\n"
    "v_mfma_f32_16x16x32_bf16 v[32:35], a[44:47], v[144:147], v[32:35]\n"
    "ds_read_b128 a[4:7], v1 offset:62464\n"
    "s_mov_b32 s66, 0x19000\n"
    "buffer_load_dwordx4 v[136:139], v1, s[56:59], s66 offen offset:0 sc1\n"
    "v_mfma_f32_16x16x32_bf16 v[36:39], a[44:47], v[148:151], v[36:39]\n"
    "s_mov_b32 s65, 0x104000\n"
    "s_add_u32 m0, s64, 0x5000\n"
    "s_nop 0\n"
    "buffer_load_dwordx4 v1, s[52:55], s65 offen offset:0 sc1 lds\n"
    "v_mfma_f32_16x16x32_bf16 v[40:43], a[48:51], v[144:147], v[40:43]\n"
    "ds_read_b128 a[8:11], v1 offset:63488\n"
    "v_mfma_f32_16x16x32_bf16 v[44:47], a[48:51], v[148:151], v[44:47]\n"
    "ds_read_b128 a[12:15], v1 offset:64512\n"
    "buffer_load_dwordx4 v1, s[52:55], s65 offen offset:1024 sc1 lds\n"
    "v_mfma_f32_16x16x32_bf16 v[48:51], a[52:55], v[144:147], v[48:51]\n"
    "v_mfma_f32_16x16x32_bf16 v[52:55], a[52:55], v[148:151], v[52:55]\n"
    "ds_read_b128 a[16:19], v3 offset:0\n"
    "v_mfma_f32_16x16x32_bf16 v[56:59], a[56:59], v[144:147], v[56:59]\n"
    "ds_read_b128 a[20:23], v3 offset:1024\n"
    "buffer_load_dwordx4 v[140:143], v1, s[56:59], s66 offen offset:1024 sc1\n"
    "buffer_load_dwordx4 v1, s[52:55], s65 offen offset:2048 sc1 lds\n"
    "v_mfma_f32_16x16x32_bf16 v[60:63], a[56:59], v[148:151], v[60:63]\n"
    "buffer_load_dwordx4 v1, s[52:55], s65 offen offset:3072 sc1 lds\n"
    "v_mfma_f32_16x16x32_bf16 v[64:67], a[60:63], v[144:147], v[64:67]\n"
    "ds_read_b128 a[24:27], v3 offset:2048\n"
    "v_mfma_f32_16x16x32_bf16 v[68:71], a[60:63], v[148:151], v[68:71]\n"
    "s_add_u32 m0, s64, 0x6000\n"
    "ds_read_b128 a[28:31], v3 offset:3072\n"
    "buffer_load_dwordx4 v2, s[52:55], s65 offen offset:0 sc1 lds\n"
    "v_mfma_f32_16x16x32_bf16 v[72:75], a[64:67], v[144:147], v[72:75]\n"
    "v_mfma_f32_16x16x32_bf16 v[76:79], a[64:67], v[148:151], v[76:79]\n"
    "ds_read_b128 a[32:35], v3 offset:4096\n"
    "v_mfma_f32_16x16x32_bf16 v[80:83], a[68:71], v[144:147], v[80:83]\n"
    "ds_read_b128 a[36:39], v3 offset:5120\n"
    "v_mfma_f32_16x16x32_bf16 v[84:87], a[68:71], v[148:151], v[84:87]\n"
    "v_mfma_f32_16x16x32_bf16 v[88:91], a[72:75], v[144:147], v[88:91]\n"
    "v_mfma_f32_16x16x32_bf16 v[92:95], a[72:75], v[148:151], v[92:95]\n"
    "v_mfma_f32_16x16x32_bf16 v[96:99], a[76:79], v[144:147], v[96:99]\n"
    "v_mfma_f32_16x16x32_bf16 v[100:103], a[76:79], v[148:151], v[100:103]\n"
    "s_waitcnt vmcnt(17) lgkmcnt(9)\n"
    "v_mfma_f32_16x16x32_bf16 v[24:27], a[0:3], v[152:155], v[24:27]\n"
    "s_waitcnt vmcnt(16)\n"
    "v_mfma_f32_16x16x32_bf16 v[28:31], a[0:3], v[156:159], v[28:31]\n"
    "ds_read_b128 a[40:43], v3 offset:6144\n"
    "s_waitcnt lgkmcnt(9)\n"
    "v_mfma_f32_16x16x32_bf16 v[32:35], a[4:7], v[152:155], v[32:35]\n"
    "ds_read_b128 a[44:47], v3 offset:7168\n"
    "s_mov_b32 s66, 0x1a000\n"
    "buffer_load_dwordx4 v[144:147], v1, s[56:59], s66 offen offset:0 sc1\n"
    "v_mfma_f32_16x16x32_bf16 v[36:39], a[4:7], v[156:159], v[36:39]\n"
    "s_waitcnt lgkmcnt(9)\n"
    "v_mfma_f32_16x16x32_bf16 v[40:43], a[8:11], v[152:155], v[40:43]\n"
    "ds_read_b128 a[48:51], v3 offset:8192\n"
    "v_mfma_f32_16x16x32_bf16 v[44:47], a[8:11], v[156:159], v[44:47]\n"
    "ds_read_b128 a[52:55], v3 offset:9216\n"
    "s_waitcnt lgkmcnt(10)\n"
    "v_mfma_f32_16x16x32_bf16 v[48:51], a[12:15], v[152:155], v[48:51]\n"
    "v_mfma_f32_16x16x32_bf16 v[52:55], a[12:15], v[156:159], v[52:55]\n"
    "ds_read_b128 a[56:59], v3 offset:10240\n"
    "s_waitcnt lgkmcnt(10)\n"
    "v_mfma_f32_16x16x32_bf16 v[56:59], a[16:19], v[152:155], v[56:59]\n"
    "ds_read_b128 a[60:63], v3 offset:11264\n"
    "buffer_load_dwordx4 v[148:151], v1, s[56:59], s66 offen offset:1024 sc1\n"
    "v_mfma_f32_16x16x32_bf16 v[60:63], a[16:19], v[156:159], v[60:63]\n"
    "s_waitcnt lgkmcnt(10)\n"
    "v_mfma_f32_16x16x32_bf16 v[64:67], a[20:23], v[152:155], v[64:67]\n"
    "ds_read_b128 a[64:67], v3 offset:12288\n"
    "v_mfma_f32_16x16x32_bf16 v[68:71], a[20:23], v[156:159], v[68:71]\n"
    "ds_read_b128 a[68:71], v3 offset:13312\n"
    "s_waitcnt lgkmcnt(11)\n"
    "v_mfma_f32_16x16x32_bf16 v[72:75], a[24:27], v[152:155], v[72:75]\n"
    "v_mfma_f32_16x16x32_bf16 v[76:79], a[24:27], v[156:159], v[76:79]\n"
    "ds_read_b128 a[72:75], v3 offset:14336\n"
    "s_waitcnt lgkmcnt(11)\n"
    "v_mfma_f32_16x16x32_bf16 v[80:83], a[28:31], v[152:155], v[80:83]\n"
    "ds_read_b128 a[76:79], v3 offset:15360\n"
    "v_mfma_f32_16x16x32_bf16 v[84:87], a[28:31], v[156:159], v[84:87]\n"
    "s_waitcnt lgkmcnt(11)\n"
    "v_mfma_f32_16x16x32_bf16 v[88:91], a[32:35], v[152:155], v[88:91]\n"
    "v_mfma_f32_16x16x32_bf16 v[92:95], a[32:35], v[156:159], v[92:95]\n"
    "s_waitcnt lgkmcnt(10)\n"
    "v_mfma_f32_16x16x32_bf16 v[96:99], a[36:39], v[152:155], v[96:99]\n"
    "v_mfma_f32_16x16x32_bf16 v[100:103], a[36:39], v[156:159], v[100:103]\n"
    "s_waitcnt vmcnt(17) lgkmcnt(9)\n"
    "v_mfma_f32_16x16x32_bf16 v[24:27], a[40:43], v[160:163], v[24:27]\n"
    "s_waitcnt vmcnt(11)\n"
    "s_waitcnt lgkmcnt(0)\n"
    "s_barrier\n"
    "v_mfma_f32_16x16x32_bf16 v[28:31], a[40:43], v[164:167], v[28:31]\n"
    "ds_read_b128 a[0:3], v1 offset:0\n"
    "v_mfma_f32_16x16x32_bf16 v[32:35], a[44:47], v[160:163], v[32:35]\n"
    "ds_read_b128 a[4:7], v1 offset:1024\n"
    "s_mov_b32 s66, 0x1b000\n"
    "buffer_load_dwordx4 v[152:155], v1, s[56:59], s66 offen offset:0 sc1\n"
    "v_mfma_f32_16x16x32_bf16 v[36:39], a[44:47], v[164:167], v[36:39]\n"
    "s_mov_b32 s65, 0x118000\n"
    "s_add_u32 m0, s64, 0xa000\n"
    "s_nop 0\n"
    "buffer_load_dwordx4 v1, s[52:55], s65 offen offset:0 sc1 lds\n"
    "v_mfma_f32_16x16x32_bf16 v[40:43], a[48:51], v[160:163], v[40:43]\n"
    "ds_read_b128 a[8:11], v1 offset:2048\n"
    "v_mfma_f32_16x16x32_bf16 v[44:47], a[48:51], v[164:167], v[44:47]\n"
    "ds_read_b128 a[12:15], v1 offset:3072\n"
    "buffer_load_dwordx4 v1, s[52:55], s65 offen offset:1024 sc1 lds\n"
    "v_mfma_f32_16x16x32_bf16 v[48:51], a[52:55], v[160:163], v[48:51]\n"
    "v_mfma_f32_16x16x32_bf16 v[52:55], a[52:55], v[164:167], v[52:55]\n"
    "ds_read_b128 a[16:19], v1 offset:4096\n"
    "v_mfma_f32_16x16x32_bf16 v[56:59], a[56:59], v[160:163], v[56:59]\n"
    "ds_read_b128 a[20:23], v1 offset:5120\n"
    "buffer_load_dwordx4 v[156:159], v1, s[56:59], s66 offen offset:1024 sc1\n"
    "buffer_load_dwordx4 v1, s[52:55], s65 offen offset:2048 sc1 lds\n"
    "v_mfma_f32_16x16x32_bf16 v[60:63], a[56:59], v[164:167], v[60:63]\n"
    "buffer_load_dwordx4 v1, s[52:55], s65 offen offset:3072 sc1 lds\n"
    "v_mfma_f32_16x16x32_bf16 v[64:67], a[60:63], v[160:163], v[64:67]\n"
    "ds_read_b128 a[24:27], v1 offset:6144\n"
    "v_mfma_f32_16x16x32_bf16 v[68:71], a[60:63], v[164:167], v[68:71]\n"
    "s_add_u32 m0, s64, 0xb000\n"
    "ds_read_b128 a[28:31], v1 offset:7168\n"
    "buffer_load_dwordx4 v2, s[52:55], s65 offen offset:0 sc1 lds\n"
    "v_mfma_f32_16x16x32_bf16 v[72:75], a[64:67], v[160:163], v[72:75]\n"
    "v_mfma_f32_16x16x32_bf16 v[76:79], a[64:67], v[164:167], v[76:79]\n"
    "ds_read_b128 a[32:35], v1 offset:8192\n"
    "v_mfma_f32_16x16x32_bf16 v[80:83], a[68:71], v[160:163], v[80:83]\n"
    "ds_read_b128 a[36:39], v1 offset:9216\n"
    "v_mfma_f32_16x16x32_bf16 v[84:87], a[68:71], v[164:167], v[84:87]\n"
    "v_mfma_f32_16x16x32_bf16 v[88:91], a[72:75], v[160:163], v[88:91]\n"
    "v_mfma_f32_16x16x32_bf16 v[92:95], a[72:75], v[164:167], v[92:95]\n"
    "v_mfma_f32_16x16x32_bf16 v[96:99], a[76:79], v[160:163], v[96:99]\n"
    "v_mfma_f32_16x16x32_bf16 v[100:103], a[76:79], v[164:167], v[100:103]\n"
    "s_waitcnt vmcnt(17) lgkmcnt(9)\n"
    "v_mfma_f32_16x16x32_bf16 v[24:27], a[0:3], v[168:171], v[24:27]\n"
    "s_waitcnt vmcnt(16)\n"
    "v_mfma_f32_16x16x32_bf16 v[28:31], a[0:3], v[172:175], v[28:31]\n"
    "ds_read_b128 a[40:43], v1 offset:10240\n"
    "s_waitcnt lgkmcnt(9)\n"
    "v_mfma_f32_16x16x32_bf16 v[32:35], a[4:7], v[168:171], v[32:35]\n"
    "ds_read_b128 a[44:47], v1 offset:11264\n"
    "s_mov_b32 s66, 0x1c000\n"
    "buffer_load_dwordx4 v[160:163], v1, s[56:59], s66 offen offset:0 sc1\n"
    "v_mfma_f32_16x16x32_bf16 v[36:39], a[4:7], v[172:175], v[36:39]\n"
    "s_waitcnt lgkmcnt(9)\n"
    "v_mfma_f32_16x16x32_bf16 v[40:43], a[8:11], v[168:171], v[40:43]\n"
    "ds_read_b128 a[48:51], v1 offset:12288\n"
    "v_mfma_f32_16x16x32_bf16 v[44:47], a[8:11], v[172:175], v[44:47]\n"
    "ds_read_b128 a[52:55], v1 offset:13312\n"
    "s_waitcnt lgkmcnt(10)\n"
    "v_mfma_f32_16x16x32_bf16 v[48:51], a[12:15], v[168:171], v[48:51]\n"
    "v_mfma_f32_16x16x32_bf16 v[52:55], a[12:15], v[172:175], v[52:55]\n"
    "ds_read_b128 a[56:59], v1 offset:14336\n"
    "s_waitcnt lgkmcnt(10)\n"
    "v_mfma_f32_16x16x32_bf16 v[56:59], a[16:19], v[168:171], v[56:59]\n"
    "ds_read_b128 a[60:63], v1 offset:15360\n"
    "buffer_load_dwordx4 v[164:167], v1, s[56:59], s66 offen offset:1024 sc1\n"
    "v_mfma_f32_16x16x32_bf16 v[60:63], a[16:19], v[172:175], v[60:63]\n"
    "s_waitcnt lgkmcnt(10)\n"
    "v_mfma_f32_16x16x32_bf16 v[64:67], a[20:23], v[168:171], v[64:67]\n"
    "ds_read_b128 a[64:67], v1 offset:16384\n"
    "v_mfma_f32_16x16x32_bf16 v[68:71], a[20:23], v[172:175], v[68:71]\n"
    "ds_read_b128 a[68:71], v1 offset:17408\n"
    "s_waitcnt lgkmcnt(11)\n"
    "v_mfma_f32_16x16x32_bf16 v[72:75], a[24:27], v[168:171], v[72:75]\n"
    "v_mfma_f32_16x16x32_bf16 v[76:79], a[24:27], v[172:175], v[76:79]\n"
    "ds_read_b128 a[72:75], v1 offset:18432\n"
    "s_waitcnt lgkmcnt(11)\n"
    "v_mfma_f32_16x16x32_bf16 v[80:83], a[28:31], v[168:171], v[80:83]\n"
    "ds_read_b128 a[76:79], v1 offset:19456\n"
    "v_mfma_f32_16x16x32_bf16 v[84:87], a[28:31], v[172:175], v[84:87]\n"
    "s_waitcnt lgkmcnt(11)\n"
    "v_mfma_f32_16x16x32_bf16 v[88:91], a[32:35], v[168:171], v[88:91]\n"
    "v_mfma_f32_16x16x32_bf16 v[92:95], a[32:35], v[172:175], v[92:95]\n"
    "s_waitcnt lgkmcnt(10)\n"
    "v_mfma_f32_16x16x32_bf16 v[96:99], a[36:39], v[168:171], v[96:99]\n"
    "v_mfma_f32_16x16x32_bf16 v[100:103], a[36:39], v[172:175], v[100:103]\n"
    "s_waitcnt vmcnt(17) lgkmcnt(9)\n"
    "v_mfma_f32_16x16x32_bf16 v[24:27], a[40:43], v[136:139], v[24:27]\n"
    "s_waitcnt vmcnt(11)\n"
    "s_waitcnt lgkmcnt(0)\n"
    "s_barrier\n"
    "v_mfma_f32_16x16x32_bf16 v[28:31], a[40:43], v[140:143], v[28:31]\n"
    "ds_read_b128 a[0:3], v1 offset:20480\n"
    "v_mfma_f32_16x16x32_bf16 v[32:35], a[44:47], v[136:139], v[32:35]\n"
    "ds_read_b128 a[4:7], v1 offset:21504\n"
    "s_mov_b32 s66, 0x1d000\n"
    "buffer_load_dwordx4 v[168:171], v1, s[56:59], s66 offen offset:0 sc1\n"
    "v_mfma_f32_16x16x32_bf16 v[36:39], a[44:47], v[140:143], v[36:39]\n"
    "s_mov_b32 s65, 0x12c000\n"
    "s_add_u32 m0, s64, 0xf000\n"
    "s_nop 0\n"
    "buffer_load_dwordx4 v1, s[52:55], s65 offen offset:0 sc1 lds\n"
    "v_mfma_f32_16x16x32_bf16 v[40:43], a[48:51], v[136:139], v[40:43]\n"
    "ds_read_b128 a[8:11], v1 offset:22528\n"
    "v_mfma_f32_16x16x32_bf16 v[44:47], a[48:51], v[140:143], v[44:47]\n"
    "ds_read_b128 a[12:15], v1 offset:23552\n"
    "buffer_load_dwordx4 v1, s[52:55], s65 offen offset:1024 sc1 lds\n"
    "v_mfma_f32_16x16x32_bf16 v[48:51], a[52:55], v[136:139], v[48:51]\n"
    "v_mfma_f32_16x16x32_bf16 v[52:55], a[52:55], v[140:143], v[52:55]\n"
    "ds_read_b128 a[16:19], v1 offset:24576\n"
    "v_mfma_f32_16x16x32_bf16 v[56:59], a[56:59], v[136:139], v[56:59]\n"
    "ds_read_b128 a[20:23], v1 offset:25600\n"
    "buffer_load_dwordx4 v[172:175], v1, s[56:59], s66 offen offset:1024 sc1\n"
    "buffer_load_dwordx4 v1, s[52:55], s65 offen offset:2048 sc1 lds\n"
    "v_mfma_f32_16x16x32_bf16 v[60:63], a[56:59], v[140:143], v[60:63]\n"
    "buffer_load_dwordx4 v1, s[52:55], s65 offen offset:3072 sc1 lds\n"
    "v_mfma_f32_16x16x32_bf16 v[64:67], a[60:63], v[136:139], v[64:67]\n"
    "ds_read_b128 a[24:27], v1 offset:26624\n"
    "v_mfma_f32_16x16x32_bf16 v[68:71], a[60:63], v[140:143], v[68:71]\n"
    "s_add_u32 m0, s64, 0x10000\n"
    "ds_read_b128 a[28:31], v1 offset:27648\n"
    "buffer_load_dwordx4 v2, s[52:55], s65 offen offset:0 sc1 lds\n"
    "v_mfma_f32_16x16x32_bf16 v[72:75], a[64:67], v[136:139], v[72:75]\n"
    "v_mfma_f32_16x16x32_bf16 v[76:79], a[64:67], v[140:143], v[76:79]\n"
    "ds_read_b128 a[32:35], v1 offset:28672\n"
    "v_mfma_f32_16x16x32_bf16 v[80:83], a[68:71], v[136:139], v[80:83]\n"
    "ds_read_b128 a[36:39], v1 offset:29696\n"
    "v_mfma_f32_16x16x32_bf16 v[84:87], a[68:71], v[140:143], v[84:87]\n"
    "v_mfma_f32_16x16x32_bf16 v[88:91], a[72:75], v[136:139], v[88:91]\n"
    "v_mfma_f32_16x16x32_bf16 v[92:95], a[72:75], v[140:143], v[92:95]\n"
    "v_mfma_f32_16x16x32_bf16 v[96:99], a[76:79], v[136:139], v[96:99]\n"
    "v_mfma_f32_16x16x32_bf16 v[100:103], a[76:79], v[140:143], v[100:103]\n"
    "s_waitcnt vmcnt(17) lgkmcnt(9)\n"
    "v_mfma_f32_16x16x32_bf16 v[24:27], a[0:3], v[144:147], v[24:27]\n"
    "s_waitcnt vmcnt(16)\n"
    "v_mfma_f32_16x16x32_bf16 v[28:31], a[0:3], v[148:151], v[28:31]\n"
    "ds_read_b128 a[40:43], v1 offset:30720\n"
    "s_waitcnt lgkmcnt(9)\n"
    "v_mfma_f32_16x16x32_bf16 v[32:35], a[4:7], v[144:147], v[32:35]\n"
    "ds_read_b128 a[44:47], v1 offset:31744\n"
    "s_mov_b32 s66, 0x1e000\n"
    "buffer_load_dwordx4 v[136:139], v1, s[56:59], s66 offen offset:0 sc1\n"
    "v_mfma_f32_16x16x32_bf16 v[36:39], a[4:7], v[148:151], v[36:39]\n"
    "s_waitcnt lgkmcnt(9)\n"
    "v_mfma_f32_16x16x32_bf16 v[40:43], a[8:11], v[144:147], v[40:43]\n"
    "ds_read_b128 a[48:51], v1 offset:32768\n"
    "v_mfma_f32_16x16x32_bf16 v[44:47], a[8:11], v[148:151], v[44:47]\n"
    "ds_read_b128 a[52:55], v1 offset:33792\n"
    "s_waitcnt lgkmcnt(10)\n"
    "v_mfma_f32_16x16x32_bf16 v[48:51], a[12:15], v[144:147], v[48:51]\n"
    "v_mfma_f32_16x16x32_bf16 v[52:55], a[12:15], v[148:151], v[52:55]\n"
    "ds_read_b128 a[56:59], v1 offset:34816\n"
    "s_waitcnt lgkmcnt(10)\n"
    "v_mfma_f32_16x16x32_bf16 v[56:59], a[16:19], v[144:147], v[56:59]\n"
    "ds_read_b128 a[60:63], v1 offset:35840\n"
    "buffer_load_dwordx4 v[140:143], v1, s[56:59], s66 offen offset:1024 sc1\n"
    "v_mfma_f32_16x16x32_bf16 v[60:63], a[16:19], v[148:151], v[60:63]\n"
    "s_waitcnt lgkmcnt(10)\n"
    "v_mfma_f32_16x16x32_bf16 v[64:67], a[20:23], v[144:147], v[64:67]\n"
    "ds_read_b128 a[64:67], v1 offset:36864\n"
    "v_mfma_f32_16x16x32_bf16 v[68:71], a[20:23], v[148:151], v[68:71]\n"
    "ds_read_b128 a[68:71], v1 offset:37888\n"
    "s_waitcnt lgkmcnt(11)\n"
    "v_mfma_f32_16x16x32_bf16 v[72:75], a[24:27], v[144:147], v[72:75]\n"
    "v_mfma_f32_16x16x32_bf16 v[76:79], a[24:27], v[148:151], v[76:79]\n"
    "ds_read_b128 a[72:75], v1 offset:38912\n"
    "s_waitcnt lgkmcnt(11)\n"
    "v_mfma_f32_16x16x32_bf16 v[80:83], a[28:31], v[144:147], v[80:83]\n"
    "ds_read_b128 a[76:79], v1 offset:39936\n"
    "v_mfma_f32_16x16x32_bf16 v[84:87], a[28:31], v[148:151], v[84:87]\n"
    "s_waitcnt lgkmcnt(11)\n"
    "v_mfma_f32_16x16x32_bf16 v[88:91], a[32:35], v[144:147], v[88:91]\n"
    "v_mfma_f32_16x16x32_bf16 v[92:95], a[32:35], v[148:151], v[92:95]\n"
    "s_waitcnt lgkmcnt(10)\n"
    "v_mfma_f32_16x16x32_bf16 v[96:99], a[36:39], v[144:147], v[96:99]\n"
    "v_mfma_f32_16x16x32_bf16 v[100:103], a[36:39], v[148:151], v[100:103]\n"
    "s_waitcnt vmcnt(17) lgkmcnt(9)\n"
    "v_mfma_f32_16x16x32_bf16 v[24:27], a[40:43], v[152:155], v[24:27]\n"
    "s_waitcnt vmcnt(11)\n"
    "s_waitcnt lgkmcnt(0)\n"
    "s_barrier\n"
    "v_mfma_f32_16x16x32_bf16 v[28:31], a[40:43], v[156:159], v[28:31]\n"
    "ds_read_b128 a[0:3], v1 offset:40960\n"
    "v_mfma_f32_16x16x32_bf16 v[32:35], a[44:47], v[152:155], v[32:35]\n"
    "ds_read_b128 a[4:7], v1 offset:41984\n"
    "s_mov_b32 s66, 0x1f000\n"
    "buffer_load_dwordx4 v[144:147], v1, s[56:59], s66 offen offset:0 sc1\n"
    "v_mfma_f32_16x16x32_bf16 v[36:39], a[44:47], v[156:159], v[36:39]\n"
    "v_mfma_f32_16x16x32_bf16 v[40:43], a[48:51], v[152:155], v[40:43]\n"
    "ds_read_b128 a[8:11], v1 offset:43008\n"
    "v_mfma_f32_16x16x32_bf16 v[44:47], a[48:51], v[156:159], v[44:47]\n"
    "ds_read_b128 a[12:15], v1 offset:44032\n"
    "v_mfma_f32_16x16x32_bf16 v[48:51], a[52:55], v[152:155], v[48:51]\n"
    "v_mfma_f32_16x16x32_bf16 v[52:55], a[52:55], v[156:159], v[52:55]\n"
    "ds_read_b128 a[16:19], v1 offset:45056\n"
    "v_mfma_f32_16x16x32_bf16 v[56:59], a[56:59], v[152:155], v[56:59]\n"
    "ds_read_b128 a[20:23], v1 offset:46080\n"
    "buffer_load_dwordx4 v[148:151], v1, s[56:59], s66 offen offset:1024 sc1\n"
    "v_mfma_f32_16x16x32_bf16 v[60:63], a[56:59], v[156:159], v[60:63]\n"
    "v_mfma_f32_16x16x32_bf16 v[64:67], a[60:63], v[152:155], v[64:67]\n"
    "ds_read_b128 a[24:27], v1 offset:47104\n"
    "v_mfma_f32_16x16x32_bf16 v[68:71], a[60:63], v[156:159], v[68:71]\n"
    "ds_read_b128 a[28:31], v1 offset:48128\n"
    "v_mfma_f32_16x16x32_bf16 v[72:75], a[64:67], v[152:155], v[72:75]\n"
    "v_mfma_f32_16x16x32_bf16 v[76:79], a[64:67], v[156:159], v[76:79]\n"
    "ds_read_b128 a[32:35], v1 offset:49152\n"
    "v_mfma_f32_16x16x32_bf16 v[80:83], a[68:71], v[152:155], v[80:83]\n"
    "ds_read_b128 a[36:39], v1 offset:50176\n"
    "v_mfma_f32_16x16x32_bf16 v[84:87], a[68:71], v[156:159], v[84:87]\n"
    "v_mfma_f32_16x16x32_bf16 v[88:91], a[72:75], v[152:155], v[88:91]\n"
    "v_mfma_f32_16x16x32_bf16 v[92:95], a[72:75], v[156:159], v[92:95]\n"
    "v_mfma_f32_16x16x32_bf16 v[96:99], a[76:79], v[152:155], v[96:99]\n"
    "v_mfma_f32_16x16x32_bf16 v[100:103], a[76:79], v[156:159], v[100:103]\n"
    "s_waitcnt vmcnt(12) lgkmcnt(9)\n"
    "v_mfma_f32_16x16x32_bf16 v[24:27], a[0:3], v[160:163], v[24:27]\n"
    "s_waitcnt vmcnt(11)\n"
    "v_mfma_f32_16x16x32_bf16 v[28:31], a[0:3], v[164:167], v[28:31]\n"
    "ds_read_b128 a[40:43], v1 offset:51200\n"
    "s_waitcnt lgkmcnt(9)\n"
    "v_mfma_f32_16x16x32_bf16 v[32:35], a[4:7], v[160:163], v[32:35]\n"
    "ds_read_b128 a[44:47], v1 offset:52224\n"
    "v_mfma_f32_16x16x32_bf16 v[36:39], a[4:7], v[164:167], v[36:39]\n"
    "s_waitcnt lgkmcnt(9)\n"
    "v_mfma_f32_16x16x32_bf16 v[40:43], a[8:11], v[160:163], v[40:43]\n"
    "ds_read_b128 a[48:51], v1 offset:53248\n"
    "v_mfma_f32_16x16x32_bf16 v[44:47], a[8:11], v[164:167], v[44:47]\n"
    "ds_read_b128 a[52:55], v1 offset:54272\n"
    "s_waitcnt lgkmcnt(10)\n"
    "v_mfma_f32_16x16x32_bf16 v[48:51], a[12:15], v[160:163], v[48:51]\n"
    "v_mfma_f32_16x16x32_bf16 v[52:55], a[12:15], v[164:167], v[52:55]\n"
    "ds_read_b128 a[56:59], v1 offset:55296\n"
    "s_waitcnt lgkmcnt(10)\n"
    "v_mfma_f32_16x16x32_bf16 v[56:59], a[16:19], v[160:163], v[56:59]\n"
    "ds_read_b128 a[60:63], v1 offset:56320\n"
    "v_mfma_f32_16x16x32_bf16 v[60:63], a[16:19], v[164:167], v[60:63]\n"
    "s_waitcnt lgkmcnt(10)\n"
    "v_mfma_f32_16x16x32_bf16 v[64:67], a[20:23], v[160:163], v[64:67]\n"
    "ds_read_b128 a[64:67], v1 offset:57344\n"
    "v_mfma_f32_16x16x32_bf16 v[68:71], a[20:23], v[164:167], v[68:71]\n"
    "ds_read_b128 a[68:71], v1 offset:58368\n"
    "s_waitcnt lgkmcnt(11)\n"
    "v_mfma_f32_16x16x32_bf16 v[72:75], a[24:27], v[160:163], v[72:75]\n"
    "v_mfma_f32_16x16x32_bf16 v[76:79], a[24:27], v[164:167], v[76:79]\n"
    "ds_read_b128 a[72:75], v1 offset:59392\n"
    "s_waitcnt lgkmcnt(11)\n"
    "v_mfma_f32_16x16x32_bf16 v[80:83], a[28:31], v[160:163], v[80:83]\n"
    "ds_read_b128 a[76:79], v1 offset:60416\n"
    "v_mfma_f32_16x16x32_bf16 v[84:87], a[28:31], v[164:167], v[84:87]\n"
    "s_waitcnt lgkmcnt(11)\n"
    "v_mfma_f32_16x16x32_bf16 v[88:91], a[32:35], v[160:163], v[88:91]\n"
    "v_mfma_f32_16x16x32_bf16 v[92:95], a[32:35], v[164:167], v[92:95]\n"
    "s_waitcnt lgkmcnt(10)\n"
    "v_mfma_f32_16x16x32_bf16 v[96:99], a[36:39], v[160:163], v[96:99]\n"
    "v_mfma_f32_16x16x32_bf16 v[100:103], a[36:39], v[164:167], v[100:103]\n"
    "s_waitcnt vmcnt(10) lgkmcnt(9)\n"
    "v_mfma_f32_16x16x32_bf16 v[24:27], a[40:43], v[168:171], v[24:27]\n"
    "s_waitcnt vmcnt(4)\n"
    "s_waitcnt lgkmcnt(0)\n"
    "s_barrier\n"
    "v_mfma_f32_16x16x32_bf16 v[28:31], a[40:43], v[172:175], v[28:31]\n"
    "ds_read_b128 a[0:3], v1 offset:61440\n"
    "v_mfma_f32_16x16x32_bf16 v[32:35], a[44:47], v[168:171], v[32:35]\n"
    "ds_read_b128 a[4:7], v1 offset:62464\n"
    "v_mfma_f32_16x16x32_bf16 v[36:39], a[44:47], v[172:175], v[36:39]\n"
    "v_mfma_f32_16x16x32_bf16 v[40:43], a[48:51], v[168:171], v[40:43]\n"
    "ds_read_b128 a[8:11], v1 offset:63488\n"
    "v_mfma_f32_16x16x32_bf16 v[44:47], a[48:51], v[172:175], v[44:47]\n"
    "ds_read_b128 a[12:15], v1 offset:64512\n"
    "v_mfma_f32_16x16x32_bf16 v[48:51], a[52:55], v[168:171], v[48:51]\n"
    "v_mfma_f32_16x16x32_bf16 v[52:55], a[52:55], v[172:175], v[52:55]\n"
    "ds_read_b128 a[16:19], v3 offset:0\n"
    "v_mfma_f32_16x16x32_bf16 v[56:59], a[56:59], v[168:171], v[56:59]\n"
    "ds_read_b128 a[20:23], v3 offset:1024\n"
    "v_mfma_f32_16x16x32_bf16 v[60:63], a[56:59], v[172:175], v[60:63]\n"
    "v_mfma_f32_16x16x32_bf16 v[64:67], a[60:63], v[168:171], v[64:67]\n"
    "ds_read_b128 a[24:27], v3 offset:2048\n"
    "v_mfma_f32_16x16x32_bf16 v[68:71], a[60:63], v[172:175], v[68:71]\n"
    "ds_read_b128 a[28:31], v3 offset:3072\n"
    "v_mfma_f32_16x16x32_bf16 v[72:75], a[64:67], v[168:171], v[72:75]\n"
    "v_mfma_f32_16x16x32_bf16 v[76:79], a[64:67], v[172:175], v[76:79]\n"
    "ds_read_b128 a[32:35], v3 offset:4096\n"
    "v_mfma_f32_16x16x32_bf16 v[80:83], a[68:71], v[168:171], v[80:83]\n"
    "ds_read_b128 a[36:39], v3 offset:5120\n"
    "v_mfma_f32_16x16x32_bf16 v[84:87], a[68:71], v[172:175], v[84:87]\n"
    "v_mfma_f32_16x16x32_bf16 v[88:91], a[72:75], v[168:171], v[88:91]\n"
    "v_mfma_f32_16x16x32_bf16 v[92:95], a[72:75], v[172:175], v[92:95]\n"
    "v_mfma_f32_16x16x32_bf16 v[96:99], a[76:79], v[168:171], v[96:99]\n"
    "v_mfma_f32_16x16x32_bf16 v[100:103], a[76:79], v[172:175], v[100:103]\n"
    "s_waitcnt vmcnt(3) lgkmcnt(9)\n"
    "v_mfma_f32_16x16x32_bf16 v[24:27], a[0:3], v[136:139], v[24:27]\n"
    "s_waitcnt vmcnt(2)\n"
    "v_mfma_f32_16x16x32_bf16 v[28:31], a[0:3], v[140:143], v[28:31]\n"
    "ds_read_b128 a[40:43], v3 offset:6144\n"
    "s_waitcnt lgkmcnt(9)\n"
    "v_mfma_f32_16x16x32_bf16 v[32:35], a[4:7], v[136:139], v[32:35]\n"
    "ds_read_b128 a[44:47], v3 offset:7168\n"
    "v_mfma_f32_16x16x32_bf16 v[36:39], a[4:7], v[140:143], v[36:39]\n"
    "s_waitcnt lgkmcnt(9)\n"
    "v_mfma_f32_16x16x32_bf16 v[40:43], a[8:11], v[136:139], v[40:43]\n"
    "ds_read_b128 a[48:51], v3 offset:8192\n"
    "v_mfma_f32_16x16x32_bf16 v[44:47], a[8:11], v[140:143], v[44:47]\n"
    "ds_read_b128 a[52:55], v3 offset:9216\n"
    "s_waitcnt lgkmcnt(10)\n"
    "v_mfma_f32_16x16x32_bf16 v[48:51], a[12:15], v[136:139], v[48:51]\n"
    "v_mfma_f32_16x16x32_bf16 v[52:55], a[12:15], v[140:143], v[52:55]\n"
    "ds_read_b128 a[56:59], v3 offset:10240\n"
    "s_waitcnt lgkmcnt(10)\n"
    "v_mfma_f32_16x16x32_bf16 v[56:59], a[16:19], v[136:139], v[56:59]\n"
    "ds_read_b128 a[60:63], v3 offset:11264\n"
    "v_mfma_f32_16x16x32_bf16 v[60:63], a[16:19], v[140:143], v[60:63]\n"
    "s_waitcnt lgkmcnt(10)\n"
    "v_mfma_f32_16x16x32_bf16 v[64:67], a[20:23], v[136:139], v[64:67]\n"
    "ds_read_b128 a[64:67], v3 offset:12288\n"
    "v_mfma_f32_16x16x32_bf16 v[68:71], a[20:23], v[140:143], v[68:71]\n"
    "ds_read_b128 a[68:71], v3 offset:13312\n"
    "s_waitcnt lgkmcnt(11)\n"
    "v_mfma_f32_16x16x32_bf16 v[72:75], a[24:27], v[136:139], v[72:75]\n"
    "v_mfma_f32_16x16x32_bf16 v[76:79], a[24:27], v[140:143], v[76:79]\n"
    "ds_read_b128 a[72:75], v3 offset:14336\n"
    "s_waitcnt lgkmcnt(11)\n"
    "v_mfma_f32_16x16x32_bf16 v[80:83], a[28:31], v[136:139], v[80:83]\n"
    "ds_read_b128 a[76:79], v3 offset:15360\n"
    "v_mfma_f32_16x16x32_bf16 v[84:87], a[28:31], v[140:143], v[84:87]\n"
    "s_waitcnt lgkmcnt(11)\n"
    "v_mfma_f32_16x16x32_bf16 v[88:91], a[32:35], v[136:139], v[88:91]\n"
    "v_mfma_f32_16x16x32_bf16 v[92:95], a[32:35], v[140:143], v[92:95]\n"
    "s_waitcnt lgkmcnt(10)\n"
    "v_mfma_f32_16x16x32_bf16 v[96:99], a[36:39], v[136:139], v[96:99]\n"
    "v_mfma_f32_16x16x32_bf16 v[100:103], a[36:39], v[140:143], v[100:103]\n"
    "s_waitcnt vmcnt(1) lgkmcnt(9)\n"
    "v_mfma_f32_16x16x32_bf16 v[24:27], a[40:43], v[144:147], v[24:27]\n"
    "s_waitcnt vmcnt(0)\n"
    "v_mfma_f32_16x16x32_bf16 v[28:31], a[40:43], v[148:151], v[28:31]\n"
    "s_waitcnt lgkmcnt(8)\n"
    "v_mfma_f32_16x16x32_bf16 v[32:35], a[44:47], v[144:147], v[32:35]\n"
    "v_mfma_f32_16x16x32_bf16 v[36:39], a[44:47], v[148:151], v[36:39]\n"
    "s_waitcnt lgkmcnt(7)\n"
    "v_mfma_f32_16x16x32_bf16 v[40:43], a[48:51], v[144:147], v[40:43]\n"
    "v_mfma_f32_16x16x32_bf16 v[44:47], a[48:51], v[148:151], v[44:47]\n"
    "s_waitcnt lgkmcnt(6)\n"
    "v_mfma_f32_16x16x32_bf16 v[48:51], a[52:55], v[144:147], v[48:51]\n"
    "v_mfma_f32_16x16x32_bf16 v[52:55], a[52:55], v[148:151], v[52:55]\n"
    "s_waitcnt lgkmcnt(5)\n"
    "v_mfma_f32_16x16x32_bf16 v[56:59], a[56:59], v[144:147], v[56:59]\n"
    "v_mfma_f32_16x16x32_bf16 v[60:63], a[56:59], v[148:151], v[60:63]\n"
    "s_waitcnt lgkmcnt(4)\n"
    "v_mfma_f32_16x16x32_bf16 v[64:67], a[60:63], v[144:147], v[64:67]\n"
    "v_mfma_f32_16x16x32_bf16 v[68:71], a[60:63], v[148:151], v[68:71]\n"
    "s_waitcnt lgkmcnt(3)\n"
    "v_mfma_f32_16x16x32_bf16 v[72:75], a[64:67], v[144:147], v[72:75]\n"
    "v_mfma_f32_16x16x32_bf16 v[76:79], a[64:67], v[148:151], v[76:79]\n"
    "s_waitcnt lgkmcnt(2)\n"
    "v_mfma_f32_16x16x32_bf16 v[80:83], a[68:71], v[144:147], v[80:83]\n"
    "v_mfma_f32_16x16x32_bf16 v[84:87], a[68:71], v[148:151], v[84:87]\n"
    "s_waitcnt lgkmcnt(1)\n"
    "v_mfma_f32_16x16x32_bf16 v[88:91], a[72:75], v[144:147], v[88:91]\n"
    "v_mfma_f32_16x16x32_bf16 v[92:95], a[72:75], v[148:151], v[92:95]\n"
    "s_waitcnt lgkmcnt(0)\n"
    "v_mfma_f32_16x16x32_bf16 v[96:99], a[76:79], v[144:147], v[96:99]\n"
    "v_mfma_f32_16x16x32_bf16 v[100:103], a[76:79], v[148:151], v[100:103]\n"
    "v_cvt_pk_bf16_f32 v8, v24, v25\n"
    "v_cvt_pk_bf16_f32 v9, v26, v27\n"
    "v_cvt_pk_bf16_f32 v10, v32, v33\n"
    "v_cvt_pk_bf16_f32 v11, v34, v35\n"
    "s_mov_b32 s46, 0\n"
    "buffer_store_dwordx4 v[8:11], v1, s[60:63], s46 offen offset:0 sc1\n"
    "v_cvt_pk_bf16_f32 v12, v40, v41\n"
    "v_cvt_pk_bf16_f32 v13, v42, v43\n"
    "v_cvt_pk_bf16_f32 v14, v48, v49\n"
    "v_cvt_pk_bf16_f32 v15, v50, v51\n"
    "buffer_store_dwordx4 v[12:15], v1, s[60:63], s46 offen offset:1024 sc1\n"
    "v_cvt_pk_bf16_f32 v16, v56, v57\n"
    "v_cvt_pk_bf16_f32 v17, v58, v59\n"
    "v_cvt_pk_bf16_f32 v18, v64, v65\n"
    "v_cvt_pk_bf16_f32 v19, v66, v67\n"
    "buffer_store_dwordx4 v[16:19], v1, s[60:63], s46 offen offset:2048 sc1\n"
    "v_cvt_pk_bf16_f32 v8, v72, v73\n"
    "v_cvt_pk_bf16_f32 v9, v74, v75\n"
    "v_cvt_pk_bf16_f32 v10, v80, v81\n"
    "v_cvt_pk_bf16_f32 v11, v82, v83\n"
    "buffer_store_dwordx4 v[8:11], v1, s[60:63], s46 offen offset:3072 sc1\n"
    "v_cvt_pk_bf16_f32 v12, v88, v89\n"
    "v_cvt_pk_bf16_f32 v13, v90, v91\n"
    "v_cvt_pk_bf16_f32 v14, v96, v97\n"
    "v_cvt_pk_bf16_f32 v15, v98, v99\n"
    "s_mov_b32 s46, 0x1000\n"
    "buffer_store_dwordx4 v[12:15], v1, s[60:63], s46 offen offset:0 sc1\n"
    "v_cvt_pk_bf16_f32 v16, v28, v29\n"
    "v_cvt_pk_bf16_f32 v17, v30, v31\n"
    "v_cvt_pk_bf16_f32 v18, v36, v37\n"
    "v_cvt_pk_bf16_f32 v19, v38, v39\n"
    "s_mov_b32 s46, 0x1000\n"
    "buffer_store_dwordx4 v[16:19], v1, s[60:63], s46 offen offset:1024 sc1\n"
    "v_cvt_pk_bf16_f32 v8, v44, v45\n"
    "v_cvt_pk_bf16_f32 v9, v46, v47\n"
    "v_cvt_pk_bf16_f32 v10, v52, v53\n"
    "v_cvt_pk_bf16_f32 v11, v54, v55\n"
    "buffer_store_dwordx4 v[8:11], v1, s[60:63], s46 offen offset:2048 sc1\n"
    "v_cvt_pk_bf16_f32 v12, v60, v61\n"
    "v_cvt_pk_bf16_f32 v13, v62, v63\n"
    "v_cvt_pk_bf16_f32 v14, v68, v69\n"
    "v_cvt_pk_bf16_f32 v15, v70, v71\n"
    "buffer_store_dwordx4 v[12:15], v1, s[60:63], s46 offen offset:3072 sc1\n"
    "v_cvt_pk_bf16_f32 v16, v76, v77\n"
    "v_cvt_pk_bf16_f32 v17, v78, v79\n"
    "v_cvt_pk_bf16_f32 v18, v84, v85\n"
    "v_cvt_pk_bf16_f32 v19, v86, v87\n"
    "s_mov_b32 s46, 0x2000\n"
    "buffer_store_dwordx4 v[16:19], v1, s[60:63], s46 offen offset:0 sc1\n"
    "v_cvt_pk_bf16_f32 v8, v92, v93\n"
    "v_cvt_pk_bf16_f32 v9, v94, v95\n"
    "v_cvt_pk_bf16_f32 v10, v100, v101\n"
    "v_cvt_pk_bf16_f32 v11, v102, v103\n"
    "buffer_store_dwordx4 v[8:11], v1, s[60:63], s46 offen offset:1024 sc1\n"
    "s_barrier\n"
    "s_lshr_b32 s88, s38, 2\n"
    "s_lshr_b32 s89, s38, 1\n"
    "s_and_b32 s89, s89, 1\n"
    "s_and_b32 s90, s38, 1\n"
    "s_lshl_b32 s46, s36, 2\n"
    "s_add_u32 s46, s46, s34\n"
    "s_lshl_b32 s46, s46, 2\n"
    "s_add_u32 s46, s46, s88\n"
    "s_mul_i32 s46, s46, 0x2800\n"
    "s_mul_i32 s47, s89, 0x1400\n"
    "s_add_u32 s46, s46, s47\n"
    "s_lshl_b32 s47, s90, 7\n"
    "s_add_u32 s46, s46, s47\n"
    "s_sub_u32 s47, 0xa00000, s46\n"
    "s_add_u32 s46, s46, 0x1e70000\n"
    "s_add_u32 s68, s26, s46\n"
    "s_addc_u32 s69, s27, 0\n"
    "s_mov_b32 s70, s47\n"
    "s_mov_b32 s71, 0x20000\n"
    "s_lshl_b32 s46, s36, 4\n"
    "s_add_u32 s46, s46, s38\n"
    "s_mul_i32 s46, s46, 0x2800\n"
    "s_mul_i32 s47, s34, 0x140\n"
    "s_add_u32 s46, s46, s47\n"
    "s_sub_u32 s48, 0x2800, s47\n"
    "s_add_u32 s72, s8, s46\n"
    "s_addc_u32 s73, s9, 0\n"
    "s_mov_b32 s74, s48\n"
    "s_mov_b32 s75, 0x20000\n"
    "s_add_u32 s46, s46, 0x12b0000\n"
    "s_add_u32 s80, s26, s46\n"
    "s_addc_u32 s81, s27, 0\n"
    "s_mov_b32 s82, s48\n"
    "s_mov_b32 s83, 0x20000\n"
    "s_add_u32 s46, s47, 0x10000\n"
    "s_sub_u32 s48, 0x1e00, s47\n"
    "s_add_u32 s76, s26, s46\n"
    "s_addc_u32 s77, s27, 0\n"
    "s_mov_b32 s78, s48\n"
    "s_mov_b32 s79, 0x20000\n"
    "s_lshl_b32 s46, s36, 2\n"
    "s_add_u32 s46, s46, s88\n"
    "s_mul_i32 s46, s46, 0xa000\n"
    "s_mul_i32 s47, s34, 0x2800\n"
    "s_add_u32 s46, s46, s47\n"
    "s_lshl_b32 s47, s89, 10\n"
    "s_add_u32 s46, s46, s47\n"
    "s_lshl_b32 s47, s90, 7\n"
    "s_add_u32 s46, s46, s47\n"
    "s_add_u32 s46, s46, 0x1530000\n"
    "s_add_u32 s84, s26, s46\n"
    "s_addc_u32 s85, s27, 0\n"
    "s_mov_b32 s86, 0x2800\n"
    "s_mov_b32 s87, 0x20000\n"
    "v_lshrrev_b32 v124, 4, v1\n"
    "v_and_b32 v125, 7, v124\n"
    "v_bfe_u32 v126, v124, 3, 2\n"
    "v_lshrrev_b32 v127, 5, v124\n"
    "v_mul_u32_u24 v4, 0x500000, v127\n"
    "v_lshl_add_u32 v4, v126, 8, v4\n"
    "v_lshl_add_u32 v4, v125, 4, v4\n"
    "v_lshlrev_b32 v6, 4, v126\n"
    "v_lshl_add_u32 v6, v127, 3, v6\n"
    "v_mul_u32_u24 v5, 0x500, v125\n"
    "v_add_u32 v5, v5, v6\n"
    "v_lshlrev_b32 v7, 8, v126\n"
    "v_lshl_add_u32 v7, v125, 4, v7\n"
    "v_lshl_add_u32 v7, v127, 3, v7\n"
    "v_mul_u32_u24 v141, 0x500, v125\n"
    "v_lshl_add_u32 v141, v126, 4, v141\n"
    "v_lshl_add_u32 v141, v127, 6, v141\n"
    "v_lshlrev_b32 v142, 8, v126\n"
    "v_lshl_add_u32 v142, v125, 4, v142\n"
    "v_lshl_add_u32 v142, v127, 11, v142\n"
    "v_lshlrev_b32 v140, 3, v125\n"
    "v_add_u32 v140, 0x27800, v140\n"
    "buffer_load_dwordx2 v[64:65], v5, s[72:75], 0 offen offset:0\n"
    "s_mov_b32 s88, 0xa00\n"
    "buffer_load_dwordx2 v[74:75], v6, s[76:79], s88 offen offset:0 sc1\n"
    "s_mov_b32 s89, 0xf00\n"
    "buffer_load_dwordx2 v[84:85], v6, s[76:79], s89 offen offset:0 sc1\n"
    "s_mov_b32 s90, 0x1400\n"
    "buffer_load_dwordx2 v[94:95], v6, s[76:79], s90 offen offset:0 sc1\n"
    "buffer_load_dwordx2 v[66:67], v5, s[72:75], 0 offen offset:64\n"
    "s_mov_b32 s88, 0xa00\n"
    "buffer_load_dwordx2 v[76:77], v6, s[76:79], s88 offen offset:64 sc1\n"
    "s_mov_b32 s89, 0xf00\n"
    "buffer_load_dwordx2 v[86:87], v6, s[76:79], s89 offen offset:64 sc1\n"
    "s_mov_b32 s90, 0x1400\n"
    "buffer_load_dwordx2 v[96:97], v6, s[76:79], s90 offen offset:64 sc1\n"
    "buffer_load_dwordx2 v[68:69], v5, s[72:75], 0 offen offset:128\n"
    "s_mov_b32 s88, 0xa00\n"
    "buffer_load_dwordx2 v[78:79], v6, s[76:79], s88 offen offset:128 sc1\n"
    "s_mov_b32 s89, 0xf00\n"
    "buffer_load_dwordx2 v[88:89], v6, s[76:79], s89 offen offset:128 sc1\n"
    "s_mov_b32 s90, 0x1400\n"
    "buffer_load_dwordx2 v[98:99], v6, s[76:79], s90 offen offset:128 sc1\n"
    "buffer_load_dwordx2 v[70:71], v5, s[72:75], 0 offen offset:192\n"
    "s_mov_b32 s88, 0xa00\n"
    "buffer_load_dwordx2 v[80:81], v6, s[76:79], s88 offen offset:192 sc1\n"
    "s_mov_b32 s89, 0xf00\n"
    "buffer_load_dwordx2 v[90:91], v6, s[76:79], s89 offen offset:192 sc1\n"
    "s_mov_b32 s90, 0x1400\n"
    "buffer_load_dwordx2 v[100:101], v6, s[76:79], s90 offen offset:192 sc1\n"
    "buffer_load_dwordx2 v[72:73], v5, s[72:75], 0 offen offset:256\n"
    "s_mov_b32 s88, 0xa00\n"
    "buffer_load_dwordx2 v[82:83], v6, s[76:79], s88 offen offset:256 sc1\n"
    "s_mov_b32 s89, 0xf00\n"
    "buffer_load_dwordx2 v[92:93], v6, s[76:79], s89 offen offset:256 sc1\n"
    "s_mov_b32 s90, 0x1400\n"
    "buffer_load_dwordx2 v[102:103], v6, s[76:79], s90 offen offset:256 sc1\n"
    "; ==== P3b: x1 = x + ga * sum(partials), h2 = LN(x1) * (1 + cb) + sb for 8 tokens; the partials are their own hand-off flag\n"
    "s_mov_b32 s44, 0\n"
    "Ldf215_%=:\n"
    "s_mov_b32 s88, 0x1000\n"
    "buffer_load_dwordx4 v[56:59], v4, s[68:71], s88 offen offset:0 sc1\n"
    "s_mov_b32 s89, 0x281000\n"
    "buffer_load_dwordx4 v[60:63], v4, s[68:71], s89 offen offset:0 sc1\n"
    "s_waitcnt vmcnt(0)\n"
    "v_max3_u32 v139, v56, v57, v58\n"
    "v_max3_u32 v139, v139, v59, v60\n"
    "v_max3_u32 v139, v139, v61, v62\n"
    "v_max3_u32 v139, v139, v63, v63\n"
    "v_cmp_eq_u32 vcc, -1, v139\n"
    "s_nop 4\n"
    "s_cmp_eq_u64 vcc, 0\n"
    "s_cbranch_scc1 Ldf2ok16_%=\n"
    "s_add_u32 s44, s44, 1\n"
    "s_cmp_ge_u32 s44, 32768\n"
    "s_cbranch_scc1 Ldf2ok16_%=\n"
    "s_sleep 6\n"
    "s_branch Ldf215_%=\n"
    "Ldf2ok16_%=:\n"
    "s_mov_b32 s44, 0\n"
    "Ldf217_%=:\n"
    "s_mov_b32 s88, 0\n"
    "buffer_load_dwordx4 v[24:27], v4, s[68:71], s88 offen offset:0 sc1\n"
    "s_mov_b32 s89, 0x280000\n"
    "buffer_load_dwordx4 v[28:31], v4, s[68:71], s89 offen offset:0 sc1\n"
    "s_mov_b32 s88, 0\n"
    "buffer_load_dwordx4 v[32:35], v4, s[68:71], s88 offen offset:1024 sc1\n"
    "s_mov_b32 s89, 0x280000\n"
    "buffer_load_dwordx4 v[36:39], v4, s[68:71], s89 offen offset:1024 sc1\n"
    "s_mov_b32 s88, 0\n"
    "buffer_load_dwordx4 v[40:43], v4, s[68:71], s88 offen offset:2048 sc1\n"
    "s_mov_b32 s89, 0x280000\n"
    "buffer_load_dwordx4 v[44:47], v4, s[68:71], s89 offen offset:2048 sc1\n"
    "s_mov_b32 s88, 0\n"
    "buffer_load_dwordx4 v[48:51], v4, s[68:71], s88 offen offset:3072 sc1\n"
    "s_mov_b32 s89, 0x280000\n"
    "buffer_load_dwordx4 v[52:55], v4, s[68:71], s89 offen offset:3072 sc1\n"
    "s_mov_b32 s88, 0x1000\n"
    "buffer_load_dwordx4 v[56:59], v4, s[68:71], s88 offen offset:0 sc1\n"
    "s_mov_b32 s89, 0x281000\n"
    "buffer_load_dwordx4 v[60:63], v4, s[68:71], s89 offen offset:0 sc1\n"
    "s_waitcnt vmcnt(0)\n"
    "v_max3_u32 v139, v24, v25, v26\n"
    "v_max3_u32 v139, v139, v27, v28\n"
    "v_max3_u32 v139, v139, v29, v30\n"
    "v_max3_u32 v139, v139, v31, v32\n"
    "v_max3_u32 v139, v139, v33, v34\n"
    "v_max3_u32 v139, v139, v35, v36\n"
    "v_max3_u32 v139, v139, v37, v38\n"
    "v_max3_u32 v139, v139, v39, v40\n"
    "v_max3_u32 v139, v139, v41, v42\n"
    "v_max3_u32 v139, v139, v43, v44\n"
    "v_max3_u32 v139, v139, v45, v46\n"
    "v_max3_u32 v139, v139, v47, v48\n"
    "v_max3_u32 v139, v139, v49, v50\n"
    "v_max3_u32 v139, v139, v51, v52\n"
    "v_max3_u32 v139, v139, v53, v54\n"
    "v_max3_u32 v139, v139, v55, v56\n"
    "v_max3_u32 v139, v139, v57, v58\n"
    "v_max3_u32 v139, v139, v59, v60\n"
    "v_max3_u32 v139, v139, v61, v62\n"
    "v_max3_u32 v139, v139, v63, v63\n"
    "v_cmp_eq_u32 vcc, -1, v139\n"
    "s_nop 4\n"
    "s_cmp_eq_u64 vcc, 0\n"
    "s_cbranch_scc1 Ldf2ok18_%=\n"
    "s_add_u32 s44, s44, 1\n"
    "s_cmp_ge_u32 s44, 32768\n"
    "s_cbranch_scc1 Ldf2ok18_%=\n"
    "s_sleep 2\n"
    "s_branch Ldf217_%=\n"
    "Ldf2ok18_%=:\n"
    "v_permlane32_swap_b32 v24, v26\n"
    "v_permlane32_swap_b32 v25, v27\n"
    "v_permlane32_swap_b32 v28, v30\n"
    "v_permlane32_swap_b32 v29, v31\n"
    "v_lshlrev_b32 v104, 16, v24\n"
    "v_and_b32 v105, 0xffff0000, v24\n"
    "v_lshlrev_b32 v128, 16, v26\n"
    "v_and_b32 v129, 0xffff0000, v26\n"
    "v_add_f32 v104, v104, v128\n"
    "v_add_f32 v105, v105, v129\n"
    "v_lshlrev_b32 v128, 16, v28\n"
    "v_and_b32 v129, 0xffff0000, v28\n"
    "v_add_f32 v104, v104, v128\n"
    "v_add_f32 v105, v105, v129\n"
    "v_lshlrev_b32 v128, 16, v30\n"
    "v_and_b32 v129, 0xffff0000, v30\n"
    "v_add_f32 v104, v104, v128\n"
    "v_add_f32 v105, v105, v129\n"
    "v_lshlrev_b32 v106, 16, v25\n"
    "v_and_b32 v107, 0xffff0000, v25\n"
    "v_lshlrev_b32 v128, 16, v27\n"
    "v_and_b32 v129, 0xffff0000, v27\n"
    "v_add_f32 v106, v106, v128\n"
    "v_add_f32 v107, v107, v129\n"
    "v_lshlrev_b32 v128, 16, v29\n"
    "v_and_b32 v129, 0xffff0000, v29\n"
    "v_add_f32 v106, v106, v128\n"
    "v_add_f32 v107, v107, v129\n"
    "v_lshlrev_b32 v128, 16, v31\n"
    "v_and_b32 v129, 0xffff0000, v31\n"
    "v_add_f32 v106, v106, v128\n"
    "v_add_f32 v107, v107, v129\n"
    "v_lshlrev_b32 v128, 16, v64\n"
    "v_and_b32 v129, 0xffff0000, v64\n"
    "v_lshlrev_b32 v130, 16, v74\n"
    "v_and_b32 v131, 0xffff0000, v74\n"
    "v_fma_f32 v104, v130, v104, v128\n"
    "v_fma_f32 v105, v131, v105, v129\n"
    "v_lshlrev_b32 v128, 16, v65\n"
    "v_and_b32 v129, 0xffff0000, v65\n"
    "v_lshlrev_b32 v130, 16, v75\n"
    "v_and_b32 v131, 0xffff0000, v75\n"
    "v_fma_f32 v106, v130, v106, v128\n"
    "v_fma_f32 v107, v131, v107, v129\n"
    "v_cvt_pk_bf16_f32 v64, v104, v105\n"
    "v_cvt_pk_bf16_f32 v65, v106, v107\n"
    "v_lshlrev_b32 v104, 16, v64\n"
    "v_and_b32 v105, 0xffff0000, v64\n"
    "v_lshlrev_b32 v106, 16, v65\n"
    "v_and_b32 v107, 0xffff0000, v65\n"
    "v_add_f32 v124, v104, v105\n"
    "v_add_f32 v124, v124, v106\n"
    "v_add_f32 v124, v124, v107\n"
    "v_permlane32_swap_b32 v32, v34\n"
    "v_permlane32_swap_b32 v33, v35\n"
    "v_permlane32_swap_b32 v36, v38\n"
    "v_permlane32_swap_b32 v37, v39\n"
    "v_lshlrev_b32 v108, 16, v32\n"
    "v_and_b32 v109, 0xffff0000, v32\n"
    "v_lshlrev_b32 v128, 16, v34\n"
    "v_and_b32 v129, 0xffff0000, v34\n"
    "v_add_f32 v108, v108, v128\n"
    "v_add_f32 v109, v109, v129\n"
    "v_lshlrev_b32 v128, 16, v36\n"
    "v_and_b32 v129, 0xffff0000, v36\n"
    "v_add_f32 v108, v108, v128\n"
    "v_add_f32 v109, v109, v129\n"
    "v_lshlrev_b32 v128, 16, v38\n"
    "v_and_b32 v129, 0xffff0000, v38\n"
    "v_add_f32 v108, v108, v128\n"
    "v_add_f32 v109, v109, v129\n"
    "v_lshlrev_b32 v110, 16, v33\n"
    "v_and_b32 v111, 0xffff0000, v33\n"
    "v_lshlrev_b32 v128, 16, v35\n"
    "v_and_b32 v129, 0xffff0000, v35\n"
    "v_add_f32 v110, v110, v128\n"
    "v_add_f32 v111, v111, v129\n"
    "v_lshlrev_b32 v128, 16, v37\n"
    "v_and_b32 v129, 0xffff0000, v37\n"
    "v_add_f32 v110, v110, v128\n"
    "v_add_f32 v111, v111, v129\n"
    "v_lshlrev_b32 v128, 16, v39\n"
    "v_and_b32 v129, 0xffff0000, v39\n"
    "v_add_f32 v110, v110, v128\n"
    "v_add_f32 v111, v111, v129\n"
    "v_lshlrev_b32 v128, 16, v66\n"
    "v_and_b32 v129, 0xffff0000, v66\n"
    "v_lshlrev_b32 v130, 16, v76\n"
    "v_and_b32 v131, 0xffff0000, v76\n"
    "v_fma_f32 v108, v130, v108, v128\n"
    "v_fma_f32 v109, v131, v109, v129\n"
    "v_lshlrev_b32 v128, 16, v67\n"
    "v_and_b32 v129, 0xffff0000, v67\n"
    "v_lshlrev_b32 v130, 16, v77\n"
    "v_and_b32 v131, 0xffff0000, v77\n"
    "v_fma_f32 v110, v130, v110, v128\n"
    "v_fma_f32 v111, v131, v111, v129\n"
    "v_cvt_pk_bf16_f32 v66, v108, v109\n"
    "v_cvt_pk_bf16_f32 v67, v110, v111\n"
    "v_lshlrev_b32 v108, 16, v66\n"
    "v_and_b32 v109, 0xffff0000, v66\n"
    "v_lshlrev_b32 v110, 16, v67\n"
    "v_and_b32 v111, 0xffff0000, v67\n"
    "v_permlane32_swap_b32 v64, v66\n"
    "v_permlane32_swap_b32 v65, v67\n"
    "buffer_store_dwordx4 v[64:67], v141, s[80:83], 0 offen offset:0 sc1\n"
    "v_add_f32 v124, v124, v108\n"
    "v_add_f32 v124, v124, v109\n"
    "v_add_f32 v124, v124, v110\n"
    "v_add_f32 v124, v124, v111\n"
    "v_permlane32_swap_b32 v40, v42\n"
    "v_permlane32_swap_b32 v41, v43\n"
    "v_permlane32_swap_b32 v44, v46\n"
    "v_permlane32_swap_b32 v45, v47\n"
    "v_lshlrev_b32 v112, 16, v40\n"
    "v_and_b32 v113, 0xffff0000, v40\n"
    "v_lshlrev_b32 v128, 16, v42\n"
    "v_and_b32 v129, 0xffff0000, v42\n"
    "v_add_f32 v112, v112, v128\n"
    "v_add_f32 v113, v113, v129\n"
    "v_lshlrev_b32 v128, 16, v44\n"
    "v_and_b32 v129, 0xffff0000, v44\n"
    "v_add_f32 v112, v112, v128\n"
    "v_add_f32 v113, v113, v129\n"
    "v_lshlrev_b32 v128, 16, v46\n"
    "v_and_b32 v129, 0xffff0000, v46\n"
    "v_add_f32 v112, v112, v128\n"
    "v_add_f32 v113, v113, v129\n"
    "v_lshlrev_b32 v114, 16, v41\n"
    "v_and_b32 v115, 0xffff0000, v41\n"
    "v_lshlrev_b32 v128, 16, v43\n"
    "v_and_b32 v129, 0xffff0000, v43\n"
    "v_add_f32 v114, v114, v128\n"
    "v_add_f32 v115, v115, v129\n"
    "v_lshlrev_b32 v128, 16, v45\n"
    "v_and_b32 v129, 0xffff0000, v45\n"
    "v_add_f32 v114, v114, v128\n"
    "v_add_f32 v115, v115, v129\n"
    "v_lshlrev_b32 v128, 16, v47\n"
    "v_and_b32 v129, 0xffff0000, v47\n"
    "v_add_f32 v114, v114, v128\n"
    "v_add_f32 v115, v115, v129\n"
    "v_lshlrev_b32 v128, 16, v68\n"
    "v_and_b32 v129, 0xffff0000, v68\n"
    "v_lshlrev_b32 v130, 16, v78\n"
    "v_and_b32 v131, 0xffff0000, v78\n"
    "v_fma_f32 v112, v130, v112, v128\n"
    "v_fma_f32 v113, v131, v113, v129\n"
    "v_lshlrev_b32 v128, 16, v69\n"
    "v_and_b32 v129, 0xffff0000, v69\n"
    "v_lshlrev_b32 v130, 16, v79\n"
    "v_and_b32 v131, 0xffff0000, v79\n"
    "v_fma_f32 v114, v130, v114, v128\n"
    "v_fma_f32 v115, v131, v115, v129\n"
    "v_cvt_pk_bf16_f32 v68, v112, v113\n"
    "v_cvt_pk_bf16_f32 v69, v114, v115\n"
    "v_lshlrev_b32 v112, 16, v68\n"
    "v_and_b32 v113, 0xffff0000, v68\n"
    "v_lshlrev_b32 v114, 16, v69\n"
    "v_and_b32 v115, 0xffff0000, v69\n"
    "v_add_f32 v124, v124, v112\n"
    "v_add_f32 v124, v124, v113\n"
    "v_add_f32 v124, v124, v114\n"
    "v_add_f32 v124, v124, v115\n"
    "v_permlane32_swap_b32 v48, v50\n"
    "v_permlane32_swap_b32 v49, v51\n"
    "v_permlane32_swap_b32 v52, v54\n"
    "v_permlane32_swap_b32 v53, v55\n"
    "v_lshlrev_b32 v116, 16, v48\n"
    "v_and_b32 v117, 0xffff0000, v48\n"
    "v_lshlrev_b32 v128, 16, v50\n"
    "v_and_b32 v129, 0xffff0000, v50\n"
    "v_add_f32 v116, v116, v128\n"
    "v_add_f32 v117, v117, v129\n"
    "v_lshlrev_b32 v128, 16, v52\n"
    "v_and_b32 v129, 0xffff0000, v52\n"
    "v_add_f32 v116, v116, v128\n"
    "v_add_f32 v117, v117, v129\n"
    "v_lshlrev_b32 v128, 16, v54\n"
    "v_and_b32 v129, 0xffff0000, v54\n"
    "v_add_f32 v116, v116, v128\n"
    "v_add_f32 v117, v117, v129\n"
    "v_lshlrev_b32 v118, 16, v49\n"
    "v_and_b32 v119, 0xffff0000, v49\n"
    "v_lshlrev_b32 v128, 16, v51\n"
    "v_and_b32 v129, 0xffff0000, v51\n"
    "v_add_f32 v118, v118, v128\n"
    "v_add_f32 v119, v119, v129\n"
    "v_lshlrev_b32 v128, 16, v53\n"
    "v_and_b32 v129, 0xffff0000, v53\n"
    "v_add_f32 v118, v118, v128\n"
    "v_add_f32 v119, v119, v129\n"
    "v_lshlrev_b32 v128, 16, v55\n"
    "v_and_b32 v129, 0xffff0000, v55\n"
    "v_add_f32 v118, v118, v128\n"
    "v_add_f32 v119, v119, v129\n"
    "v_lshlrev_b32 v128, 16, v70\n"
    "v_and_b32 v129, 0xffff0000, v70\n"
    "v_lshlrev_b32 v130, 16, v80\n"
    "v_and_b32 v131, 0xffff0000, v80\n"
    "v_fma_f32 v116, v130, v116, v128\n"
    "v_fma_f32 v117, v131, v117, v129\n"
    "v_lshlrev_b32 v128, 16, v71\n"
    "v_and_b32 v129, 0xffff0000, v71\n"
    "v_lshlrev_b32 v130, 16, v81\n"
    "v_and_b32 v131, 0xffff0000, v81\n"
    "v_fma_f32 v118, v130, v118, v128\n"
    "v_fma_f32 v119, v131, v119, v129\n"
    "v_cvt_pk_bf16_f32 v70, v116, v117\n"
    "v_cvt_pk_bf16_f32 v71, v118, v119\n"
    "v_lshlrev_b32 v116, 16, v70\n"
    "v_and_b32 v117, 0xffff0000, v70\n"
    "v_lshlrev_b32 v118, 16, v71\n"
    "v_and_b32 v119, 0xffff0000, v71\n"
    "v_permlane32_swap_b32 v68, v70\n"
    "v_permlane32_swap_b32 v69, v71\n"
    "buffer_store_dwordx4 v[68:71], v141, s[80:83], 0 offen offset:128 sc1\n"
    "v_add_f32 v124, v124, v116\n"
    "v_add_f32 v124, v124, v117\n"
    "v_add_f32 v124, v124, v118\n"
    "v_add_f32 v124, v124, v119\n"
    "v_permlane32_swap_b32 v56, v58\n"
    "v_permlane32_swap_b32 v57, v59\n"
    "v_permlane32_swap_b32 v60, v62\n"
    "v_permlane32_swap_b32 v61, v63\n"
    "v_lshlrev_b32 v120, 16, v56\n"
    "v_and_b32 v121, 0xffff0000, v56\n"
    "v_lshlrev_b32 v128, 16, v58\n"
    "v_and_b32 v129, 0xffff0000, v58\n"
    "v_add_f32 v120, v120, v128\n"
    "v_add_f32 v121, v121, v129\n"
    "v_lshlrev_b32 v128, 16, v60\n"
    "v_and_b32 v129, 0xffff0000, v60\n"
    "v_add_f32 v120, v120, v128\n"
    "v_add_f32 v121, v121, v129\n"
    "v_lshlrev_b32 v128, 16, v62\n"
    "v_and_b32 v129, 0xffff0000, v62\n"
    "v_add_f32 v120, v120, v128\n"
    "v_add_f32 v121, v121, v129\n"
    "v_lshlrev_b32 v122, 16, v57\n"
    "v_and_b32 v123, 0xffff0000, v57\n"
    "v_lshlrev_b32 v128, 16, v59\n"
    "v_and_b32 v129, 0xffff0000, v59\n"
    "v_add_f32 v122, v122, v128\n"
    "v_add_f32 v123, v123, v129\n"
    "v_lshlrev_b32 v128, 16, v61\n"
    "v_and_b32 v129, 0xffff0000, v61\n"
    "v_add_f32 v122, v122, v128\n"
    "v_add_f32 v123, v123, v129\n"
    "v_lshlrev_b32 v128, 16, v63\n"
    "v_and_b32 v129, 0xffff0000, v63\n"
    "v_add_f32 v122, v122, v128\n"
    "v_add_f32 v123, v123, v129\n"
    "v_lshlrev_b32 v128, 16, v72\n"
    "v_and_b32 v129, 0xffff0000, v72\n"
    "v_lshlrev_b32 v130, 16, v82\n"
    "v_and_b32 v131, 0xffff0000, v82\n"
    "v_fma_f32 v120, v130, v120, v128\n"
    "v_fma_f32 v121, v131, v121, v129\n"
    "v_lshlrev_b32 v128, 16, v73\n"
    "v_and_b32 v129, 0xffff0000, v73\n"
    "v_lshlrev_b32 v130, 16, v83\n"
    "v_and_b32 v131, 0xffff0000, v83\n"
    "v_fma_f32 v122, v130, v122, v128\n"
    "v_fma_f32 v123, v131, v123, v129\n"
    "v_cvt_pk_bf16_f32 v72, v120, v121\n"
    "v_cvt_pk_bf16_f32 v73, v122, v123\n"
    "v_lshlrev_b32 v120, 16, v72\n"
    "v_and_b32 v121, 0xffff0000, v72\n"
    "v_lshlrev_b32 v122, 16, v73\n"
    "v_and_b32 v123, 0xffff0000, v73\n"
    "buffer_store_dwordx2 v[72:73], v5, s[80:83], 0 offen offset:256 sc1\n"
    "v_add_f32 v124, v124, v120\n"
    "v_add_f32 v124, v124, v121\n"
    "v_add_f32 v124, v124, v122\n"
    "v_add_f32 v124, v124, v123\n"
    "s_nop 1\n"
    "v_add_f32_dpp v124, v124, v124 row_ror:8 row_mask:0xf bank_mask:0xf\n"
    "v_mov_b32 v126, v124\n"
    "s_nop 1\n"
    "v_permlane16_swap_b32 v126, v124\n"
    "v_add_f32 v124, v124, v126\n"
    "v_mov_b32 v126, v124\n"
    "s_nop 1\n"
    "v_permlane32_swap_b32 v126, v124\n"
    "v_add_f32 v124, v124, v126\n"
    "v_mul_f32 v124, 0x3bcccccd, v124\n"
    "v_sub_f32 v126, v104, v124\n"
    "v_mul_f32 v125, v126, v126\n"
    "v_sub_f32 v126, v105, v124\n"
    "v_fmac_f32 v125, v126, v126\n"
    "v_sub_f32 v126, v106, v124\n"
    "v_fmac_f32 v125, v126, v126\n"
    "v_sub_f32 v126, v107, v124\n"
    "v_fmac_f32 v125, v126, v126\n"
    "v_sub_f32 v126, v108, v124\n"
    "v_fmac_f32 v125, v126, v126\n"
    "v_sub_f32 v126, v109, v124\n"
    "v_fmac_f32 v125, v126, v126\n"
    "v_sub_f32 v126, v110, v124\n"
    "v_fmac_f32 v125, v126, v126\n"
    "v_sub_f32 v126, v111, v124\n"
    "v_fmac_f32 v125, v126, v126\n"
    "v_sub_f32 v126, v112, v124\n"
    "v_fmac_f32 v125, v126, v126\n"
    "v_sub_f32 v126, v113, v124\n"
    "v_fmac_f32 v125, v126, v126\n"
    "v_sub_f32 v126, v114, v124\n"
    "v_fmac_f32 v125, v126, v126\n"
    "v_sub_f32 v126, v115, v124\n"
    "v_fmac_f32 v125, v126, v126\n"
    "v_sub_f32 v126, v116, v124\n"
    "v_fmac_f32 v125, v126, v126\n"
    "v_sub_f32 v126, v117, v124\n"
    "v_fmac_f32 v125, v126, v126\n"
    "v_sub_f32 v126, v118, v124\n"
    "v_fmac_f32 v125, v126, v126\n"
    "v_sub_f32 v126, v119, v124\n"
    "v_fmac_f32 v125, v126, v126\n"
    "v_sub_f32 v126, v120, v124\n"
    "v_fmac_f32 v125, v126, v126\n"
    "v_sub_f32 v126, v121, v124\n"
    "v_fmac_f32 v125, v126, v126\n"
    "v_sub_f32 v126, v122, v124\n"
    "v_fmac_f32 v125, v126, v126\n"
    "v_sub_f32 v126, v123, v124\n"
    "v_fmac_f32 v125, v126, v126\n"
    "s_nop 1\n"
    "v_add_f32_dpp v125, v125, v125 row_ror:8 row_mask:0xf bank_mask:0xf\n"
    "v_mov_b32 v126, v125\n"
    "s_nop 1\n"
    "v_permlane16_swap_b32 v126, v125\n"
    "v_add_f32 v125, v125, v126\n"
    "v_mov_b32 v126, v125\n"
    "s_nop 1\n"
    "v_permlane32_swap_b32 v126, v125\n"
    "v_add_f32 v125, v125, v126\n"
    "s_lshl_b32 s88, s34, 6\n"
    "v_add_u32 v127, s88, v140\n"
    "v_mov_b32 v128, v124\n"
    "v_mov_b32 v129, v125\n"
    "ds_write_b64 v127, v[128:129] offset:0\n"
    "s_waitcnt lgkmcnt(0)\n"
    "s_barrier\n"
    "ds_read_b64 v[130:131], v140 offset:0\n"
    "ds_read_b64 v[132:133], v140 offset:64\n"
    "ds_read_b64 v[134:135], v140 offset:128\n"
    "ds_read_b64 v[136:137], v140 offset:192\n"
    "s_waitcnt lgkmcnt(2)\n"
    "v_add_f32 v124, v130, v132\n"
    "s_waitcnt lgkmcnt(0)\n"
    "v_add_f32 v126, v134, v136\n"
    "v_add_f32 v124, v124, v126\n"
    "v_mul_f32 v124, 0x3e800000, v124\n"
    "v_add_f32 v125, v131, v133\n"
    "v_add_f32 v126, v135, v137\n"
    "v_add_f32 v125, v125, v126\n"
    "v_sub_f32 v126, v130, v124\n"
    "v_mul_f32 v126, v126, v126\n"
    "v_fmac_f32 v125, 0x43200000, v126\n"
    "v_sub_f32 v126, v132, v124\n"
    "v_mul_f32 v126, v126, v126\n"
    "v_fmac_f32 v125, 0x43200000, v126\n"
    "v_sub_f32 v126, v134, v124\n"
    "v_mul_f32 v126, v126, v126\n"
    "v_fmac_f32 v125, 0x43200000, v126\n"
    "v_sub_f32 v126, v136, v124\n"
    "v_mul_f32 v126, v126, v126\n"
    "v_fmac_f32 v125, 0x43200000, v126\n"
    "v_mul_f32 v125, 0x3acccccd, v125\n"
    "v_add_f32 v125, 0x3727c5ac, v125\n"
    "v_rsq_f32 v127, v125\n"
    "v_lshlrev_b32 v128, 16, v94\n"
    "v_and_b32 v129, 0xffff0000, v94\n"
    "v_lshlrev_b32 v130, 16, v84\n"
    "v_and_b32 v131, 0xffff0000, v84\n"
    "v_sub_f32 v104, v104, v124\n"
    "v_mul_f32 v104, v104, v127\n"
    "v_fma_f32 v104, v104, v128, v130\n"
    "v_sub_f32 v105, v105, v124\n"
    "v_mul_f32 v105, v105, v127\n"
    "v_fma_f32 v105, v105, v129, v131\n"
    "v_lshlrev_b32 v128, 16, v95\n"
    "v_and_b32 v129, 0xffff0000, v95\n"
    "v_lshlrev_b32 v130, 16, v85\n"
    "v_and_b32 v131, 0xffff0000, v85\n"
    "v_sub_f32 v106, v106, v124\n"
    "v_mul_f32 v106, v106, v127\n"
    "v_fma_f32 v106, v106, v128, v130\n"
    "v_sub_f32 v107, v107, v124\n"
    "v_mul_f32 v107, v107, v127\n"
    "v_fma_f32 v107, v107, v129, v131\n"
    "v_cvt_pk_bf16_f32 v74, v104, v105\n"
    "v_cvt_pk_bf16_f32 v75, v106, v107\n"
    "v_lshlrev_b32 v128, 16, v96\n"
    "v_and_b32 v129, 0xffff0000, v96\n"
    "v_lshlrev_b32 v130, 16, v86\n"
    "v_and_b32 v131, 0xffff0000, v86\n"
    "v_sub_f32 v108, v108, v124\n"
    "v_mul_f32 v108, v108, v127\n"
    "v_fma_f32 v108, v108, v128, v130\n"
    "v_sub_f32 v109, v109, v124\n"
    "v_mul_f32 v109, v109, v127\n"
    "v_fma_f32 v109, v109, v129, v131\n"
    "v_lshlrev_b32 v128, 16, v97\n"
    "v_and_b32 v129, 0xffff0000, v97\n"
    "v_lshlrev_b32 v130, 16, v87\n"
    "v_and_b32 v131, 0xffff0000, v87\n"
    "v_sub_f32 v110, v110, v124\n"
    "v_mul_f32 v110, v110, v127\n"
    "v_fma_f32 v110, v110, v128, v130\n"
    "v_sub_f32 v111, v111, v124\n"
    "v_mul_f32 v111, v111, v127\n"
    "v_fma_f32 v111, v111, v129, v131\n"
    "v_cvt_pk_bf16_f32 v76, v108, v109\n"
    "v_cvt_pk_bf16_f32 v77, v110, v111\n"
    "s_mov_b32 s88, 0\n"
    "v_permlane32_swap_b32 v74, v76\n"
    "v_permlane32_swap_b32 v75, v77\n"
    "buffer_store_dwordx4 v[74:77], v142, s[84:87], s88 offen offset:0 sc1\n"
    "v_lshlrev_b32 v128, 16, v98\n"
    "v_and_b32 v129, 0xffff0000, v98\n"
    "v_lshlrev_b32 v130, 16, v88\n"
    "v_and_b32 v131, 0xffff0000, v88\n"
    "v_sub_f32 v112, v112, v124\n"
    "v_mul_f32 v112, v112, v127\n"
    "v_fma_f32 v112, v112, v128, v130\n"
    "v_sub_f32 v113, v113, v124\n"
    "v_mul_f32 v113, v113, v127\n"
    "v_fma_f32 v113, v113, v129, v131\n"
    "v_lshlrev_b32 v128, 16, v99\n"
    "v_and_b32 v129, 0xffff0000, v99\n"
    "v_lshlrev_b32 v130, 16, v89\n"
    "v_and_b32 v131, 0xffff0000, v89\n"
    "v_sub_f32 v114, v114, v124\n"
    "v_mul_f32 v114, v114, v127\n"
    "v_fma_f32 v114, v114, v128, v130\n"
    "v_sub_f32 v115, v115, v124\n"
    "v_mul_f32 v115, v115, v127\n"
    "v_fma_f32 v115, v115, v129, v131\n"
    "v_cvt_pk_bf16_f32 v78, v112, v113\n"
    "v_cvt_pk_bf16_f32 v79, v114, v115\n"
    "v_lshlrev_b32 v128, 16, v100\n"
    "v_and_b32 v129, 0xffff0000, v100\n"
    "v_lshlrev_b32 v130, 16, v90\n"
    "v_and_b32 v131, 0xffff0000, v90\n"
    "v_sub_f32 v116, v116, v124\n"
    "v_mul_f32 v116, v116, v127\n"
    "v_fma_f32 v116, v116, v128, v130\n"
    "v_sub_f32 v117, v117, v124\n"
    "v_mul_f32 v117, v117, v127\n"
    "v_fma_f32 v117, v117, v129, v131\n"
    "v_lshlrev_b32 v128, 16, v101\n"
    "v_and_b32 v129, 0xffff0000, v101\n"
    "v_lshlrev_b32 v130, 16, v91\n"
    "v_and_b32 v131, 0xffff0000, v91\n"
    "v_sub_f32 v118, v118, v124\n"
    "v_mul_f32 v118, v118, v127\n"
    "v_fma_f32 v118, v118, v128, v130\n"
    "v_sub_f32 v119, v119, v124\n"
    "v_mul_f32 v119, v119, v127\n"
    "v_fma_f32 v119, v119, v129, v131\n"
    "v_cvt_pk_bf16_f32 v80, v116, v117\n"
    "v_cvt_pk_bf16_f32 v81, v118, v119\n"
    "s_mov_b32 s88, 0x1000\n"
    "v_permlane32_swap_b32 v78, v80\n"
    "v_permlane32_swap_b32 v79, v81\n"
    "buffer_store_dwordx4 v[78:81], v142, s[84:87], s88 offen offset:0 sc1\n"
    "v_lshlrev_b32 v128, 16, v102\n"
    "v_and_b32 v129, 0xffff0000, v102\n"
    "v_lshlrev_b32 v130, 16, v92\n"
    "v_and_b32 v131, 0xffff0000, v92\n"
    "v_sub_f32 v120, v120, v124\n"
    "v_mul_f32 v120, v120, v127\n"
    "v_fma_f32 v120, v120, v128, v130\n"
    "v_sub_f32 v121, v121, v124\n"
    "v_mul_f32 v121, v121, v127\n"
    "v_fma_f32 v121, v121, v129, v131\n"
    "v_lshlrev_b32 v128, 16, v103\n"
    "v_and_b32 v129, 0xffff0000, v103\n"
    "v_lshlrev_b32 v130, 16, v93\n"
    "v_and_b32 v131, 0xffff0000, v93\n"
    "v_sub_f32 v122, v122, v124\n"
    "v_mul_f32 v122, v122, v127\n"
    "v_fma_f32 v122, v122, v128, v130\n"
    "v_sub_f32 v123, v123, v124\n"
    "v_mul_f32 v123, v123, v127\n"
    "v_fma_f32 v123, v123, v129, v131\n"
    "v_cvt_pk_bf16_f32 v82, v120, v121\n"
    "v_cvt_pk_bf16_f32 v83, v122, v123\n"
    "s_mov_b32 s88, 0x2000\n"
    "buffer_store_dwordx2 v[82:83], v7, s[84:87], s88 offen offset:0 sc1\n"
    "s_lshl_b32 s46, s36, 8\n"
    "s_lshl_b32 s47, s38, 3\n"
    "s_add_u32 s46, s46, s47\n"
    "s_add_u32 s46, s46, 0x4000\n"
    "v_mov_b32 v8, s40\n"
    "v_mov_b32 v9, s41\n"
    "v_mov_b32 v10, s46\n"
    "s_waitcnt vmcnt(0)\n"
    "s_barrier\n"
    "s_mov_b64 exec, 1\n"
    "global_store_dwordx2 v10, v[8:9], s[26:27] offset:0 sc1\n"
    "s_mov_b64 exec, -1\n"
    "s_mul_i32 s67, s38, 7\n"
    "s_min_u32 s67, s67, 0x65\n"
    "s_lshl_b32 s46, s67, 10\n"
    "s_lshr_b32 s48, s34, 1\n"
    "s_mul_i32 s48, s48, 0x1b000\n"
    "s_add_u32 s46, s46, s48\n"
    "s_sub_u32 s47, 0x21c000, s46\n"
    "s_and_b32 s48, s34, 1\n"
    "s_mul_i32 s48, s48, 0x21c000\n"
    "s_add_u32 s46, s46, s48\n"
    "s_add_u32 s46, s46, 0x371c000\n"
    "s_add_u32 s52, s26, s46\n"
    "s_addc_u32 s53, s27, 0\n"
    "s_mov_b32 s54, s47\n"
    "s_mov_b32 s55, 0x20000\n"
    "s_mul_i32 s64, s34, 0x1c00\n"
    "v_add_u32 v2, 0x1000, v1\n"
    "s_mov_b32 s65, 0\n"
    "s_add_u32 m0, s64, 0\n"
    "s_nop 0\n"
    "buffer_load_dwordx4 v1, s[52:55], s65 offen offset:0 sc1 lds\n"
    "buffer_load_dwordx4 v1, s[52:55], s65 offen offset:1024 sc1 lds\n"
    "buffer_load_dwordx4 v1, s[52:55], s65 offen offset:2048 sc1 lds\n"
    "buffer_load_dwordx4 v1, s[52:55], s65 offen offset:3072 sc1 lds\n"
    "s_add_u32 m0, s64, 0x1000\n"
    "s_nop 0\n"
    "buffer_load_dwordx4 v2, s[52:55], s65 offen offset:0 sc1 lds\n"
    "buffer_load_dwordx4 v2, s[52:55], s65 offen offset:1024 sc1 lds\n"
    "buffer_load_dwordx4 v2, s[52:55], s65 offen offset:2048 sc1 lds\n"
    "s_mov_b32 s65, 0x36000\n"
    "s_add_u32 m0, s64, 0x7000\n"
    "s_nop 0\n"
    "buffer_load_dwordx4 v1, s[52:55], s65 offen offset:0 sc1 lds\n"
    "buffer_load_dwordx4 v1, s[52:55], s65 offen offset:1024 sc1 lds\n"
    "buffer_load_dwordx4 v1, s[52:55], s65 offen offset:2048 sc1 lds\n"
    "buffer_load_dwordx4 v1, s[52:55], s65 offen offset:3072 sc1 lds\n"
    "s_add_u32 m0, s64, 0x8000\n"
    "s_nop 0\n"
    "buffer_load_dwordx4 v2, s[52:55], s65 offen offset:0 sc1 lds\n"
    "buffer_load_dwordx4 v2, s[52:55], s65 offen offset:1024 sc1 lds\n"
    "buffer_load_dwordx4 v2, s[52:55], s65 offen offset:2048 sc1 lds\n"
    "s_mov_b32 s65, 0x6c000\n"
    "s_add_u32 m0, s64, 0xe000\n"
    "s_nop 0\n"
    "buffer_load_dwordx4 v1, s[52:55], s65 offen offset:0 sc1 lds\n"
    "buffer_load_dwordx4 v1, s[52:55], s65 offen offset:1024 sc1 lds\n"
    "buffer_load_dwordx4 v1, s[52:55], s65 offen offset:2048 sc1 lds\n"
    "buffer_load_dwordx4 v1, s[52:55], s65 offen offset:3072 sc1 lds\n"
    "s_add_u32 m0, s64, 0xf000\n"
    "s_nop 0\n"
    "buffer_load_dwordx4 v2, s[52:55], s65 offen offset:0 sc1 lds\n"
    "buffer_load_dwordx4 v2, s[52:55], s65 offen offset:1024 sc1 lds\n"
    "buffer_load_dwordx4 v2, s[52:55], s65 offen offset:2048 sc1 lds\n"
    "s_lshl_b32 s46, s36, 2\n"
    "s_add_u32 s46, s46, s34\n"
    "s_mul_i32 s46, s46, 0xa000\n"
    "s_add_u32 s46, s46, 0x1530000\n"
    "s_add_u32 s56, s26, s46\n"
    "s_addc_u32 s57, s27, 0\n"
    "s_mov_b32 s58, 0xa000\n"
    "s_mov_b32 s59, 0x20000\n"
    "s_lshl_b32 s46, s36, 1\n"
    "s_lshr_b32 s48, s34, 1\n"
    "s_add_u32 s46, s46, s48\n"
    "s_mul_i32 s46, s46, 0x36000\n"
    "s_and_b32 s48, s34, 1\n"
    "s_lshl_b32 s48, s48, 11\n"
    "s_add_u32 s46, s46, s48\n"
    "s_add_u32 s46, s46, 0x17b0000\n"
    "s_add_u32 s60, s26, s46\n"
    "s_addc_u32 s61, s27, 0\n"
    "s_mov_b32 s62, 0x36000\n"
    "s_mov_b32 s63, 0x20000\n"
    "v_add_u32 v3, 0x10000, v1\n"
    "v_add_u32 v200, 0x20000, v1\n"
    "s_lshl_b32 s48, s36, 8\n"
    "s_add_u32 s48, s48, 0x4000\n"
    "s_cmp_eq_u32 s34, 0\n"
    "s_cbranch_scc0 Lpollskip21_%=\n"
    "v_lshrrev_b32 v10, 4, v1\n"
    "v_min_u32 v10, 15, v10\n"
    "v_lshlrev_b32 v10, 3, v10\n"
    "v_add_u32 v10, s48, v10\n"
    "s_mov_b32 s44, 0\n"
    "Lpoll19_%=:\n"
    "global_load_dwordx2 v[8:9], v10, s[26:27] sc1\n"
    "s_waitcnt vmcnt(0)\n"
    "v_cmp_ne_u64 vcc, s[40:41], v[8:9]\n"
    "s_nop 4\n"
    "s_mov_b64 s[50:51], vcc\n"
    "s_cmp_eq_u64 s[50:51], 0\n"
    "s_cbranch_scc1 Lpollok20_%=\n"
    "s_add_u32 s44, s44, 1\n"
    "s_cmp_ge_u32 s44, 32768\n"
    "s_cbranch_scc1 Lpollok20_%=\n"
    "s_branch Lpoll19_%=\n"
    "Lpollok20_%=:\n"
    "Lpollskip21_%=:\n"
    "s_barrier\n"
    "; ==== P4: hh = (h2 Wg) * (h2 Wu), 128 tokens x 112 features\n"
    "s_mov_b32 s66, 0\n"
    "buffer_load_dwordx4 v[136:139], v1, s[56:59], s66 offen offset:0 sc1\n"
    "buffer_load_dwordx4 v[140:143], v1, s[56:59], s66 offen offset:1024 sc1\n"
    "s_mov_b32 s66, 0x800\n"
    "buffer_load_dwordx4 v[144:147], v1, s[56:59], s66 offen offset:0 sc1\n"
    "buffer_load_dwordx4 v[148:151], v1, s[56:59], s66 offen offset:1024 sc1\n"
    "s_mov_b32 s66, 0x1000\n"
    "buffer_load_dwordx4 v[152:155], v1, s[56:59], s66 offen offset:0 sc1\n"
    "buffer_load_dwordx4 v[156:159], v1, s[56:59], s66 offen offset:1024 sc1\n"
    "s_mov_b32 s66, 0x1800\n"
    "buffer_load_dwordx4 v[160:163], v1, s[56:59], s66 offen offset:0 sc1\n"
    "buffer_load_dwordx4 v[164:167], v1, s[56:59], s66 offen offset:1024 sc1\n"
    "s_waitcnt vmcnt(22)\n"
    "s_barrier\n"
    "ds_read_b128 a[0:3], v1 offset:0\n"
    "ds_read_b128 a[4:7], v1 offset:1024\n"
    "ds_read_b128 a[8:11], v1 offset:2048\n"
    "ds_read_b128 a[12:15], v1 offset:3072\n"
    "ds_read_b128 a[16:19], v1 offset:4096\n"
    "ds_read_b128 a[20:23], v1 offset:5120\n"
    "ds_read_b128 a[24:27], v1 offset:6144\n"
    "ds_read_b128 a[28:31], v1 offset:7168\n"
    "ds_read_b128 a[32:35], v1 offset:8192\n"
    "ds_read_b128 a[36:39], v1 offset:9216\n"
    "ds_read_b128 a[40:43], v1 offset:10240\n"
    "ds_read_b128 a[44:47], v1 offset:11264\n"
    "ds_read_b128 a[48:51], v1 offset:12288\n"
    "ds_read_b128 a[52:55], v1 offset:13312\n"
    "s_waitcnt vmcnt(7) lgkmcnt(13)\n"
    "v_mfma_f32_16x16x32_bf16 v[24:27], a[0:3], v[136:139], 0\n"
    "s_waitcnt vmcnt(6)\n"
    "v_mfma_f32_16x16x32_bf16 v[28:31], a[0:3], v[140:143], 0\n"
    "ds_read_b128 a[56:59], v1 offset:14336\n"
    "s_waitcnt lgkmcnt(13)\n"
    "v_mfma_f32_16x16x32_bf16 v[32:35], a[4:7], v[136:139], 0\n"
    "ds_read_b128 a[60:63], v1 offset:15360\n"
    "s_mov_b32 s66, 0x2000\n"
    "buffer_load_dwordx4 v[168:171], v1, s[56:59], s66 offen offset:0 sc1\n"
    "v_mfma_f32_16x16x32_bf16 v[36:39], a[4:7], v[140:143], 0\n"
    "s_waitcnt lgkmcnt(13)\n"
    "v_mfma_f32_16x16x32_bf16 v[40:43], a[8:11], v[136:139], 0\n"
    "ds_read_b128 a[64:67], v1 offset:16384\n"
    "v_mfma_f32_16x16x32_bf16 v[44:47], a[8:11], v[140:143], 0\n"
    "ds_read_b128 a[68:71], v1 offset:17408\n"
    "s_waitcnt lgkmcnt(14)\n"
    "v_mfma_f32_16x16x32_bf16 v[48:51], a[12:15], v[136:139], 0\n"
    "v_mfma_f32_16x16x32_bf16 v[52:55], a[12:15], v[140:143], 0\n"
    "ds_read_b128 a[72:75], v1 offset:18432\n"
    "s_waitcnt lgkmcnt(14)\n"
    "v_mfma_f32_16x16x32_bf16 v[56:59], a[16:19], v[136:139], 0\n"
    "ds_read_b128 a[76:79], v1 offset:19456\n"
    "buffer_load_dwordx4 v[172:175], v1, s[56:59], s66 offen offset:1024 sc1\n"
    "v_mfma_f32_16x16x32_bf16 v[60:63], a[16:19], v[140:143], 0\n"
    "s_waitcnt lgkmcnt(14)\n"
    "v_mfma_f32_16x16x32_bf16 v[64:67], a[20:23], v[136:139], 0\n"
    "ds_read_b128 a[80:83], v1 offset:20480\n"
    "v_mfma_f32_16x16x32_bf16 v[68:71], a[20:23], v[140:143], 0\n"
    "ds_read_b128 a[84:87], v1 offset:21504\n"
    "s_waitcnt lgkmcnt(15)\n"
    "v_mfma_f32_16x16x32_bf16 v[72:75], a[24:27], v[136:139], 0\n"
    "v_mfma_f32_16x16x32_bf16 v[76:79], a[24:27], v[140:143], 0\n"
    "ds_read_b128 a[88:91], v1 offset:22528\n"
    "s_waitcnt lgkmcnt(15)\n"
    "v_mfma_f32_16x16x32_bf16 v[80:83], a[28:31], v[136:139], 0\n"
    "ds_read_b128 a[92:95], v1 offset:23552\n"
    "v_mfma_f32_16x16x32_bf16 v[84:87], a[28:31], v[140:143], 0\n"
    "s_waitcnt lgkmcnt(15)\n"
    "v_mfma_f32_16x16x32_bf16 v[88:91], a[32:35], v[136:139], 0\n"
    "ds_read_b128 a[96:99], v1 offset:24576\n"
    "v_mfma_f32_16x16x32_bf16 v[92:95], a[32:35], v[140:143], 0\n"
    "ds_read_b128 a[100:103], v1 offset:25600\n"
    "s_waitcnt lgkmcnt(15)\n"
    "v_mfma_f32_16x16x32_bf16 v[96:99], a[36:39], v[136:139], 0\n"
    "v_mfma_f32_16x16x32_bf16 v[100:103], a[36:39], v[140:143], 0\n"
    "ds_read_b128 a[104:107], v1 offset:26624\n"
    "v_mfma_f32_16x16x32_bf16 v[104:107], a[40:43], v[136:139], 0\n"
    "ds_read_b128 a[108:111], v1 offset:27648\n"
    "v_mfma_f32_16x16x32_bf16 v[108:111], a[40:43], v[140:143], 0\n"
    "s_waitcnt lgkmcnt(15)\n"
    "v_mfma_f32_16x16x32_bf16 v[112:115], a[44:47], v[136:139], 0\n"
    "v_mfma_f32_16x16x32_bf16 v[116:119], a[44:47], v[140:143], 0\n"
    "v_mfma_f32_16x16x32_bf16 v[120:123], a[48:51], v[136:139], 0\n"
    "v_mfma_f32_16x16x32_bf16 v[124:127], a[48:51], v[140:143], 0\n"
    "s_waitcnt lgkmcnt(14)\n"
    "v_mfma_f32_16x16x32_bf16 v[128:131], a[52:55], v[136:139], 0\n"
    "v_mfma_f32_16x16x32_bf16 v[132:135], a[52:55], v[140:143], 0\n"
    "s_waitcnt vmcnt(7) lgkmcnt(13)\n"
    "v_mfma_f32_16x16x32_bf16 v[24:27], a[56:59], v[144:147], v[24:27]\n"
    "s_waitcnt lgkmcnt(0)\n"
    "s_barrier\n"
    "s_waitcnt vmcnt(6)\n"
    "v_mfma_f32_16x16x32_bf16 v[28:31], a[56:59], v[148:151], v[28:31]\n"
    "ds_read_b128 a[0:3], v1 offset:28672\n"
    "v_mfma_f32_16x16x32_bf16 v[32:35], a[60:63], v[144:147], v[32:35]\n"
    "ds_read_b128 a[4:7], v1 offset:29696\n"
    "s_mov_b32 s66, 0x2800\n"
    "buffer_load_dwordx4 v[136:139], v1, s[56:59], s66 offen offset:0 sc1\n"
    "v_mfma_f32_16x16x32_bf16 v[36:39], a[60:63], v[148:151], v[36:39]\n"
    "s_mov_b32 s65, 0xa2000\n"
    "s_add_u32 m0, s64, 0x15000\n"
    "s_nop 0\n"
    "buffer_load_dwordx4 v1, s[52:55], s65 offen offset:0 sc1 lds\n"
    "v_mfma_f32_16x16x32_bf16 v[40:43], a[64:67], v[144:147], v[40:43]\n"
    "ds_read_b128 a[8:11], v1 offset:30720\n"
    "v_mfma_f32_16x16x32_bf16 v[44:47], a[64:67], v[148:151], v[44:47]\n"
    "ds_read_b128 a[12:15], v1 offset:31744\n"
    "buffer_load_dwordx4 v1, s[52:55], s65 offen offset:1024 sc1 lds\n"
    "v_mfma_f32_16x16x32_bf16 v[48:51], a[68:71], v[144:147], v[48:51]\n"
    "v_mfma_f32_16x16x32_bf16 v[52:55], a[68:71], v[148:151], v[52:55]\n"
    "ds_read_b128 a[16:19], v1 offset:32768\n"
    "v_mfma_f32_16x16x32_bf16 v[56:59], a[72:75], v[144:147], v[56:59]\n"
    "ds_read_b128 a[20:23], v1 offset:33792\n"
    "buffer_load_dwordx4 v[140:143], v1, s[56:59], s66 offen offset:1024 sc1\n"
    "buffer_load_dwordx4 v1, s[52:55], s65 offen offset:2048 sc1 lds\n"
    "v_mfma_f32_16x16x32_bf16 v[60:63], a[72:75], v[148:151], v[60:63]\n"
    "buffer_load_dwordx4 v1, s[52:55], s65 offen offset:3072 sc1 lds\n"
    "v_mfma_f32_16x16x32_bf16 v[64:67], a[76:79], v[144:147], v[64:67]\n"
    "ds_read_b128 a[24:27], v1 offset:34816\n"
    "v_mfma_f32_16x16x32_bf16 v[68:71], a[76:79], v[148:151], v[68:71]\n"
    "s_add_u32 m0, s64, 0x16000\n"
    "ds_read_b128 a[28:31], v1 offset:35840\n"
    "buffer_load_dwordx4 v2, s[52:55], s65 offen offset:0 sc1 lds\n"
    "v_mfma_f32_16x16x32_bf16 v[72:75], a[80:83], v[144:147], v[72:75]\n"
    "v_mfma_f32_16x16x32_bf16 v[76:79], a[80:83], v[148:151], v[76:79]\n"
    "ds_read_b128 a[32:35], v1 offset:36864\n"
    "v_mfma_f32_16x16x32_bf16 v[80:83], a[84:87], v[144:147], v[80:83]\n"
    "ds_read_b128 a[36:39], v1 offset:37888\n"
    "buffer_load_dwordx4 v2, s[52:55], s65 offen offset:1024 sc1 lds\n"
    "v_mfma_f32_16x16x32_bf16 v[84:87], a[84:87], v[148:151], v[84:87]\n"
    "buffer_load_dwordx4 v2, s[52:55], s65 offen offset:2048 sc1 lds\n"
    "v_mfma_f32_16x16x32_bf16 v[88:91], a[88:91], v[144:147], v[88:91]\n"
    "ds_read_b128 a[40:43], v1 offset:38912\n"
    "v_mfma_f32_16x16x32_bf16 v[92:95], a[88:91], v[148:151], v[92:95]\n"
    "ds_read_b128 a[44:47], v1 offset:39936\n"
    "v_mfma_f32_16x16x32_bf16 v[96:99], a[92:95], v[144:147], v[96:99]\n"
    "v_mfma_f32_16x16x32_bf16 v[100:103], a[92:95], v[148:151], v[100:103]\n"
    "ds_read_b128 a[48:51], v1 offset:40960\n"
    "v_mfma_f32_16x16x32_bf16 v[104:107], a[96:99], v[144:147], v[104:107]\n"
    "ds_read_b128 a[52:55], v1 offset:41984\n"
    "v_mfma_f32_16x16x32_bf16 v[108:111], a[96:99], v[148:151], v[108:111]\n"
    "v_mfma_f32_16x16x32_bf16 v[112:115], a[100:103], v[144:147], v[112:115]\n"
    "v_mfma_f32_16x16x32_bf16 v[116:119], a[100:103], v[148:151], v[116:119]\n"
    "v_mfma_f32_16x16x32_bf16 v[120:123], a[104:107], v[144:147], v[120:123]\n"
    "v_mfma_f32_16x16x32_bf16 v[124:127], a[104:107], v[148:151], v[124:127]\n"
    "v_mfma_f32_16x16x32_bf16 v[128:131], a[108:111], v[144:147], v[128:131]\n"
    "v_mfma_f32_16x16x32_bf16 v[132:135], a[108:111], v[148:151], v[132:135]\n"
    "s_waitcnt vmcnt(14) lgkmcnt(13)\n"
    "v_mfma_f32_16x16x32_bf16 v[24:27], a[0:3], v[152:155], v[24:27]\n"
    "s_waitcnt vmcnt(13)\n"
    "v_mfma_f32_16x16x32_bf16 v[28:31], a[0:3], v[156:159], v[28:31]\n"
    "ds_read_b128 a[56:59], v1 offset:43008\n"
    "s_waitcnt lgkmcnt(13)\n"
    "v_mfma_f32_16x16x32_bf16 v[32:35], a[4:7], v[152:155], v[32:35]\n"
    "ds_read_b128 a[60:63], v1 offset:44032\n"
    "s_mov_b32 s66, 0x3000\n"
    "buffer_load_dwordx4 v[144:147], v1, s[56:59], s66 offen offset:0 sc1\n"
    "v_mfma_f32_16x16x32_bf16 v[36:39], a[4:7], v[156:159], v[36:39]\n"
    "s_waitcnt lgkmcnt(13)\n"
    "v_mfma_f32_16x16x32_bf16 v[40:43], a[8:11], v[152:155], v[40:43]\n"
    "ds_read_b128 a[64:67], v1 offset:45056\n"
    "v_mfma_f32_16x16x32_bf16 v[44:47], a[8:11], v[156:159], v[44:47]\n"
    "ds_read_b128 a[68:71], v1 offset:46080\n"
    "s_waitcnt lgkmcnt(14)\n"
    "v_mfma_f32_16x16x32_bf16 v[48:51], a[12:15], v[152:155], v[48:51]\n"
    "v_mfma_f32_16x16x32_bf16 v[52:55], a[12:15], v[156:159], v[52:55]\n"
    "ds_read_b128 a[72:75], v1 offset:47104\n"
    "s_waitcnt lgkmcnt(14)\n"
    "v_mfma_f32_16x16x32_bf16 v[56:59], a[16:19], v[152:155], v[56:59]\n"
    "ds_read_b128 a[76:79], v1 offset:48128\n"
    "buffer_load_dwordx4 v[148:151], v1, s[56:59], s66 offen offset:1024 sc1\n"
    "v_mfma_f32_16x16x32_bf16 v[60:63], a[16:19], v[156:159], v[60:63]\n"
    "s_waitcnt lgkmcnt(14)\n"
    "v_mfma_f32_16x16x32_bf16 v[64:67], a[20:23], v[152:155], v[64:67]\n"
    "ds_read_b128 a[80:83], v1 offset:49152\n"
    "v_mfma_f32_16x16x32_bf16 v[68:71], a[20:23], v[156:159], v[68:71]\n"
    "ds_read_b128 a[84:87], v1 offset:50176\n"
    "s_waitcnt lgkmcnt(15)\n"
    "v_mfma_f32_16x16x32_bf16 v[72:75], a[24:27], v[152:155], v[72:75]\n"
    "v_mfma_f32_16x16x32_bf16 v[76:79], a[24:27], v[156:159], v[76:79]\n"
    "ds_read_b128 a[88:91], v1 offset:51200\n"
    "s_waitcnt lgkmcnt(15)\n"
    "v_mfma_f32_16x16x32_bf16 v[80:83], a[28:31], v[152:155], v[80:83]\n"
    "ds_read_b128 a[92:95], v1 offset:52224\n"
    "v_mfma_f32_16x16x32_bf16 v[84:87], a[28:31], v[156:159], v[84:87]\n"
    "s_waitcnt lgkmcnt(15)\n"
    "v_mfma_f32_16x16x32_bf16 v[88:91], a[32:35], v[152:155], v[88:91]\n"
    "ds_read_b128 a[96:99], v1 offset:53248\n"
    "v_mfma_f32_16x16x32_bf16 v[92:95], a[32:35], v[156:159], v[92:95]\n"
    "ds_read_b128 a[100:103], v1 offset:54272\n"
    "s_waitcnt lgkmcnt(15)\n"
    "v_mfma_f32_16x16x32_bf16 v[96:99], a[36:39], v[152:155], v[96:99]\n"
    "v_mfma_f32_16x16x32_bf16 v[100:103], a[36:39], v[156:159], v[100:103]\n"
    "ds_read_b128 a[104:107], v1 offset:55296\n"
    "v_mfma_f32_16x16x32_bf16 v[104:107], a[40:43], v[152:155], v[104:107]\n"
    "ds_read_b128 a[108:111], v1 offset:56320\n"
    "v_mfma_f32_16x16x32_bf16 v[108:111], a[40:43], v[156:159], v[108:111]\n"
    "s_waitcnt lgkmcnt(15)\n"
    "v_mfma_f32_16x16x32_bf16 v[112:115], a[44:47], v[152:155], v[112:115]\n"
    "v_mfma_f32_16x16x32_bf16 v[116:119], a[44:47], v[156:159], v[116:119]\n"
    "v_mfma_f32_16x16x32_bf16 v[120:123], a[48:51], v[152:155], v[120:123]\n"
    "v_mfma_f32_16x16x32_bf16 v[124:127], a[48:51], v[156:159], v[124:127]\n"
    "s_waitcnt lgkmcnt(14)\n"
    "v_mfma_f32_16x16x32_bf16 v[128:131], a[52:55], v[152:155], v[128:131]\n"
    "v_mfma_f32_16x16x32_bf16 v[132:135], a[52:55], v[156:159], v[132:135]\n"
    "s_waitcnt vmcnt(14) lgkmcnt(13)\n"
    "v_mfma_f32_16x16x32_bf16 v[24:27], a[56:59], v[160:163], v[24:27]\n"
    "s_waitcnt lgkmcnt(0)\n"
    "s_barrier\n"
    "s_waitcnt vmcnt(13)\n"
    "v_mfma_f32_16x16x32_bf16 v[28:31], a[56:59], v[164:167], v[28:31]\n"
    "ds_read_b128 a[0:3], v1 offset:57344\n"
    "v_mfma_f32_16x16x32_bf16 v[32:35], a[60:63], v[160:163], v[32:35]\n"
    "ds_read_b128 a[4:7], v1 offset:58368\n"
    "s_mov_b32 s66, 0x3800\n"
    "buffer_load_dwordx4 v[152:155], v1, s[56:59], s66 offen offset:0 sc1\n"
    "v_mfma_f32_16x16x32_bf16 v[36:39], a[60:63], v[164:167], v[36:39]\n"
    "s_mov_b32 s65, 0xd8000\n"
    "s_add_u32 m0, s64, 0\n"
    "s_nop 0\n"
    "buffer_load_dwordx4 v1, s[52:55], s65 offen offset:0 sc1 lds\n"
    "v_mfma_f32_16x16x32_bf16 v[40:43], a[64:67], v[160:163], v[40:43]\n"
    "ds_read_b128 a[8:11], v1 offset:59392\n"
    "v_mfma_f32_16x16x32_bf16 v[44:47], a[64:67], v[164:167], v[44:47]\n"
    "ds_read_b128 a[12:15], v1 offset:60416\n"
    "buffer_load_dwordx4 v1, s[52:55], s65 offen offset:1024 sc1 lds\n"
    "v_mfma_f32_16x16x32_bf16 v[48:51], a[68:71], v[160:163], v[48:51]\n"
    "v_mfma_f32_16x16x32_bf16 v[52:55], a[68:71], v[164:167], v[52:55]\n"
    "ds_read_b128 a[16:19], v1 offset:61440\n"
    "v_mfma_f32_16x16x32_bf16 v[56:59], a[72:75], v[160:163], v[56:59]\n"
    "ds_read_b128 a[20:23], v1 offset:62464\n"
    "buffer_load_dwordx4 v[156:159], v1, s[56:59], s66 offen offset:1024 sc1\n"
    "buffer_load_dwordx4 v1, s[52:55], s65 offen offset:2048 sc1 lds\n"
    "v_mfma_f32_16x16x32_bf16 v[60:63], a[72:75], v[164:167], v[60:63]\n"
    "buffer_load_dwordx4 v1, s[52:55], s65 offen offset:3072 sc1 lds\n"
    "v_mfma_f32_16x16x32_bf16 v[64:67], a[76:79], v[160:163], v[64:67]\n"
    "ds_read_b128 a[24:27], v1 offset:63488\n"
    "v_mfma_f32_16x16x32_bf16 v[68:71], a[76:79], v[164:167], v[68:71]\n"
    "s_add_u32 m0, s64, 0x1000\n"
    "ds_read_b128 a[28:31], v1 offset:64512\n"
    "buffer_load_dwordx4 v2, s[52:55], s65 offen offset:0 sc1 lds\n"
    "v_mfma_f32_16x16x32_bf16 v[72:75], a[80:83], v[160:163], v[72:75]\n"
    "v_mfma_f32_16x16x32_bf16 v[76:79], a[80:83], v[164:167], v[76:79]\n"
    "ds_read_b128 a[32:35], v3 offset:0\n"
    "v_mfma_f32_16x16x32_bf16 v[80:83], a[84:87], v[160:163], v[80:83]\n"
    "ds_read_b128 a[36:39], v3 offset:1024\n"
    "buffer_load_dwordx4 v2, s[52:55], s65 offen offset:1024 sc1 lds\n"
    "v_mfma_f32_16x16x32_bf16 v[84:87], a[84:87], v[164:167], v[84:87]\n"
    "buffer_load_dwordx4 v2, s[52:55], s65 offen offset:2048 sc1 lds\n"
    "v_mfma_f32_16x16x32_bf16 v[88:91], a[88:91], v[160:163], v[88:91]\n"
    "ds_read_b128 a[40:43], v3 offset:2048\n"
    "v_mfma_f32_16x16x32_bf16 v[92:95], a[88:91], v[164:167], v[92:95]\n"
    "ds_read_b128 a[44:47], v3 offset:3072\n"
    "v_mfma_f32_16x16x32_bf16 v[96:99], a[92:95], v[160:163], v[96:99]\n"
    "v_mfma_f32_16x16x32_bf16 v[100:103], a[92:95], v[164:167], v[100:103]\n"
    "ds_read_b128 a[48:51], v3 offset:4096\n"
    "v_mfma_f32_16x16x32_bf16 v[104:107], a[96:99], v[160:163], v[104:107]\n"
    "ds_read_b128 a[52:55], v3 offset:5120\n"
    "v_mfma_f32_16x16x32_bf16 v[108:111], a[96:99], v[164:167], v[108:111]\n"
    "v_mfma_f32_16x16x32_bf16 v[112:115], a[100:103], v[160:163], v[112:115]\n"
    "v_mfma_f32_16x16x32_bf16 v[116:119], a[100:103], v[164:167], v[116:119]\n"
    "v_mfma_f32_16x16x32_bf16 v[120:123], a[104:107], v[160:163], v[120:123]\n"
    "v_mfma_f32_16x16x32_bf16 v[124:127], a[104:107], v[164:167], v[124:127]\n"
    "v_mfma_f32_16x16x32_bf16 v[128:131], a[108:111], v[160:163], v[128:131]\n"
    "v_mfma_f32_16x16x32_bf16 v[132:135], a[108:111], v[164:167], v[132:135]\n"
    "s_waitcnt vmcnt(21) lgkmcnt(13)\n"
    "v_mfma_f32_16x16x32_bf16 v[24:27], a[0:3], v[168:171], v[24:27]\n"
    "s_waitcnt vmcnt(20)\n"
    "v_mfma_f32_16x16x32_bf16 v[28:31], a[0:3], v[172:175], v[28:31]\n"
    "ds_read_b128 a[56:59], v3 offset:6144\n"
    "s_waitcnt lgkmcnt(13)\n"
    "v_mfma_f32_16x16x32_bf16 v[32:35], a[4:7], v[168:171], v[32:35]\n"
    "ds_read_b128 a[60:63], v3 offset:7168\n"
    "s_mov_b32 s66, 0x4000\n"
    "buffer_load_dwordx4 v[160:163], v1, s[56:59], s66 offen offset:0 sc1\n"
    "v_mfma_f32_16x16x32_bf16 v[36:39], a[4:7], v[172:175], v[36:39]\n"
    "s_waitcnt lgkmcnt(13)\n"
    "v_mfma_f32_16x16x32_bf16 v[40:43], a[8:11], v[168:171], v[40:43]\n"
    "ds_read_b128 a[64:67], v3 offset:8192\n"
    "v_mfma_f32_16x16x32_bf16 v[44:47], a[8:11], v[172:175], v[44:47]\n"
    "ds_read_b128 a[68:71], v3 offset:9216\n"
    "s_waitcnt lgkmcnt(14)\n"
    "v_mfma_f32_16x16x32_bf16 v[48:51], a[12:15], v[168:171], v[48:51]\n"
    "v_mfma_f32_16x16x32_bf16 v[52:55], a[12:15], v[172:175], v[52:55]\n"
    "ds_read_b128 a[72:75], v3 offset:10240\n"
    "s_waitcnt lgkmcnt(14)\n"
    "v_mfma_f32_16x16x32_bf16 v[56:59], a[16:19], v[168:171], v[56:59]\n"
    "ds_read_b128 a[76:79], v3 offset:11264\n"
    "buffer_load_dwordx4 v[164:167], v1, s[56:59], s66 offen offset:1024 sc1\n"
    "v_mfma_f32_16x16x32_bf16 v[60:63], a[16:19], v[172:175], v[60:63]\n"
    "s_waitcnt lgkmcnt(14)\n"
    "v_mfma_f32_16x16x32_bf16 v[64:67], a[20:23], v[168:171], v[64:67]\n"
    "ds_read_b128 a[80:83], v3 offset:12288\n"
    "v_mfma_f32_16x16x32_bf16 v[68:71], a[20:23], v[172:175], v[68:71]\n"
    "ds_read_b128 a[84:87], v3 offset:13312\n"
    "s_waitcnt lgkmcnt(15)\n"
    "v_mfma_f32_16x16x32_bf16 v[72:75], a[24:27], v[168:171], v[72:75]\n"
    "v_mfma_f32_16x16x32_bf16 v[76:79], a[24:27], v[172:175], v[76:79]\n"
    "ds_read_b128 a[88:91], v3 offset:14336\n"
    "s_waitcnt lgkmcnt(15)\n"
    "v_mfma_f32_16x16x32_bf16 v[80:83], a[28:31], v[168:171], v[80:83]\n"
    "ds_read_b128 a[92:95], v3 offset:15360\n"
    "v_mfma_f32_16x16x32_bf16 v[84:87], a[28:31], v[172:175], v[84:87]\n"
    "s_waitcnt lgkmcnt(15)\n"
    "v_mfma_f32_16x16x32_bf16 v[88:91], a[32:35], v[168:171], v[88:91]\n"
    "ds_read_b128 a[96:99], v3 offset:16384\n"
    "v_mfma_f32_16x16x32_bf16 v[92:95], a[32:35], v[172:175], v[92:95]\n"
    "ds_read_b128 a[100:103], v3 offset:17408\n"
    "s_waitcnt lgkmcnt(15)\n"
    "v_mfma_f32_16x16x32_bf16 v[96:99], a[36:39], v[168:171], v[96:99]\n"
    "v_mfma_f32_16x16x32_bf16 v[100:103], a[36:39], v[172:175], v[100:103]\n"
    "ds_read_b128 a[104:107], v3 offset:18432\n"
    "v_mfma_f32_16x16x32_bf16 v[104:107], a[40:43], v[168:171], v[104:107]\n"
    "ds_read_b128 a[108:111], v3 offset:19456\n"
    "v_mfma_f32_16x16x32_bf16 v[108:111], a[40:43], v[172:175], v[108:111]\n"
    "s_waitcnt lgkmcnt(15)\n"
    "v_mfma_f32_16x16x32_bf16 v[112:115], a[44:47], v[168:171], v[112:115]\n"
    "v_mfma_f32_16x16x32_bf16 v[116:119], a[44:47], v[172:175], v[116:119]\n"
    "v_mfma_f32_16x16x32_bf16 v[120:123], a[48:51], v[168:171], v[120:123]\n"
    "v_mfma_f32_16x16x32_bf16 v[124:127], a[48:51], v[172:175], v[124:127]\n"
    "s_waitcnt lgkmcnt(14)\n"
    "v_mfma_f32_16x16x32_bf16 v[128:131], a[52:55], v[168:171], v[128:131]\n"
    "v_mfma_f32_16x16x32_bf16 v[132:135], a[52:55], v[172:175], v[132:135]\n"
    "s_waitcnt vmcnt(21) lgkmcnt(13)\n"
    "v_mfma_f32_16x16x32_bf16 v[24:27], a[56:59], v[136:139], v[24:27]\n"
    "s_waitcnt vmcnt(13)\n"
    "s_waitcnt lgkmcnt(0)\n"
    "s_barrier\n"
    "v_mfma_f32_16x16x32_bf16 v[28:31], a[56:59], v[140:143], v[28:31]\n"
    "ds_read_b128 a[0:3], v3 offset:20480\n"
    "v_mfma_f32_16x16x32_bf16 v[32:35], a[60:63], v[136:139], v[32:35]\n"
    "ds_read_b128 a[4:7], v3 offset:21504\n"
    "s_mov_b32 s66, 0x4800\n"
    "buffer_load_dwordx4 v[168:171], v1, s[56:59], s66 offen offset:0 sc1\n"
    "v_mfma_f32_16x16x32_bf16 v[36:39], a[60:63], v[140:143], v[36:39]\n"
    "s_mov_b32 s65, 0x10e000\n"
    "s_add_u32 m0, s64, 0x7000\n"
    "s_nop 0\n"
    "buffer_load_dwordx4 v1, s[52:55], s65 offen offset:0 sc1 lds\n"
    "v_mfma_f32_16x16x32_bf16 v[40:43], a[64:67], v[136:139], v[40:43]\n"
    "ds_read_b128 a[8:11], v3 offset:22528\n"
    "v_mfma_f32_16x16x32_bf16 v[44:47], a[64:67], v[140:143], v[44:47]\n"
    "ds_read_b128 a[12:15], v3 offset:23552\n"
    "buffer_load_dwordx4 v1, s[52:55], s65 offen offset:1024 sc1 lds\n"
    "v_mfma_f32_16x16x32_bf16 v[48:51], a[68:71], v[136:139], v[48:51]\n"
    "v_mfma_f32_16x16x32_bf16 v[52:55], a[68:71], v[140:143], v[52:55]\n"
    "ds_read_b128 a[16:19], v3 offset:24576\n"
    "v_mfma_f32_16x16x32_bf16 v[56:59], a[72:75], v[136:139], v[56:59]\n"
    "ds_read_b128 a[20:23], v3 offset:25600\n"
    "buffer_load_dwordx4 v[172:175], v1, s[56:59], s66 offen offset:1024 sc1\n"
    "buffer_load_dwordx4 v1, s[52:55], s65 offen offset:2048 sc1 lds\n"
    "v_mfma_f32_16x16x32_bf16 v[60:63], a[72:75], v[140:143], v[60:63]\n"
    "buffer_load_dwordx4 v1, s[52:55], s65 offen offset:3072 sc1 lds\n"
    "v_mfma_f32_16x16x32_bf16 v[64:67], a[76:79], v[136:139], v[64:67]\n"
    "ds_read_b128 a[24:27], v3 offset:26624\n"
    "v_mfma_f32_16x16x32_bf16 v[68:71], a[76:79], v[140:143], v[68:71]\n"
    "s_add_u32 m0, s64, 0x8000\n"
    "ds_read_b128 a[28:31], v3 offset:27648\n"
    "buffer_load_dwordx4 v2, s[52:55], s65 offen offset:0 sc1 lds\n"
    "v_mfma_f32_16x16x32_bf16 v[72:75], a[80:83], v[136:139], v[72:75]\n"
    "v_mfma_f32_16x16x32_bf16 v[76:79], a[80:83], v[140:143], v[76:79]\n"
    "ds_read_b128 a[32:35], v3 offset:28672\n"
    "v_mfma_f32_16x16x32_bf16 v[80:83], a[84:87], v[136:139], v[80:83]\n"
    "ds_read_b128 a[36:39], v3 offset:29696\n"
    "buffer_load_dwordx4 v2, s[52:55], s65 offen offset:1024 sc1 lds\n"
    "v_mfma_f32_16x16x32_bf16 v[84:87], a[84:87], v[140:143], v[84:87]\n"
    "buffer_load_dwordx4 v2, s[52:55], s65 offen offset:2048 sc1 lds\n"
    "v_mfma_f32_16x16x32_bf16 v[88:91], a[88:91], v[136:139], v[88:91]\n"
    "ds_read_b128 a[40:43], v3 offset:30720\n"
    "v_mfma_f32_16x16x32_bf16 v[92:95], a[88:91], v[140:143], v[92:95]\n"
    "ds_read_b128 a[44:47], v3 offset:31744\n"
    "v_mfma_f32_16x16x32_bf16 v[96:99], a[92:95], v[136:139], v[96:99]\n"
    "v_mfma_f32_16x16x32_bf16 v[100:103], a[92:95], v[140:143], v[100:103]\n"
    "ds_read_b128 a[48:51], v3 offset:32768\n"
    "v_mfma_f32_16x16x32_bf16 v[104:107], a[96:99], v[136:139], v[104:107]\n"
    "ds_read_b128 a[52:55], v3 offset:33792\n"
    "v_mfma_f32_16x16x32_bf16 v[108:111], a[96:99], v[140:143], v[108:111]\n"
    "v_mfma_f32_16x16x32_bf16 v[112:115], a[100:103], v[136:139], v[112:115]\n"
    "v_mfma_f32_16x16x32_bf16 v[116:119], a[100:103], v[140:143], v[116:119]\n"
    "v_mfma_f32_16x16x32_bf16 v[120:123], a[104:107], v[136:139], v[120:123]\n"
    "v_mfma_f32_16x16x32_bf16 v[124:127], a[104:107], v[140:143], v[124:127]\n"
    "v_mfma_f32_16x16x32_bf16 v[128:131], a[108:111], v[136:139], v[128:131]\n"
    "v_mfma_f32_16x16x32_bf16 v[132:135], a[108:111], v[140:143], v[132:135]\n"
    "s_waitcnt vmcnt(21) lgkmcnt(13)\n"
    "v_mfma_f32_16x16x32_bf16 v[24:27], a[0:3], v[144:147], v[24:27]\n"
    "s_waitcnt vmcnt(20)\n"
    "v_mfma_f32_16x16x32_bf16 v[28:31], a[0:3], v[148:151], v[28:31]\n"
    "ds_read_b128 a[56:59], v3 offset:34816\n"
    "s_waitcnt lgkmcnt(13)\n"
    "v_mfma_f32_16x16x32_bf16 v[32:35], a[4:7], v[144:147], v[32:35]\n"
    "ds_read_b128 a[60:63], v3 offset:35840\n"
    "s_mov_b32 s66, 0x5000\n"
    "buffer_load_dwordx4 v[136:139], v1, s[56:59], s66 offen offset:0 sc1\n"
    "v_mfma_f32_16x16x32_bf16 v[36:39], a[4:7], v[148:151], v[36:39]\n"
    "s_waitcnt lgkmcnt(13)\n"
    "v_mfma_f32_16x16x32_bf16 v[40:43], a[8:11], v[144:147], v[40:43]\n"
    "ds_read_b128 a[64:67], v3 offset:36864\n"
    "v_mfma_f32_16x16x32_bf16 v[44:47], a[8:11], v[148:151], v[44:47]\n"
    "ds_read_b128 a[68:71], v3 offset:37888\n"
    "s_waitcnt lgkmcnt(14)\n"
    "v_mfma_f32_16x16x32_bf16 v[48:51], a[12:15], v[144:147], v[48:51]\n"
    "v_mfma_f32_16x16x32_bf16 v[52:55], a[12:15], v[148:151], v[52:55]\n"
    "ds_read_b128 a[72:75], v3 offset:38912\n"
    "s_waitcnt lgkmcnt(14)\n"
    "v_mfma_f32_16x16x32_bf16 v[56:59], a[16:19], v[144:147], v[56:59]\n"
    "ds_read_b128 a[76:79], v3 offset:39936\n"
    "buffer_load_dwordx4 v[140:143], v1, s[56:59], s66 offen offset:1024 sc1\n"
    "v_mfma_f32_16x16x32_bf16 v[60:63], a[16:19], v[148:151], v[60:63]\n"
    "s_waitcnt lgkmcnt(14)\n"
    "v_mfma_f32_16x16x32_bf16 v[64:67], a[20:23], v[144:147], v[64:67]\n"
    "ds_read_b128 a[80:83], v3 offset:40960\n"
    "v_mfma_f32_16x16x32_bf16 v[68:71], a[20:23], v[148:151], v[68:71]\n"
    "ds_read_b128 a[84:87], v3 offset:41984\n"
    "s_waitcnt lgkmcnt(15)\n"
    "v_mfma_f32_16x16x32_bf16 v[72:75], a[24:27], v[144:147], v[72:75]\n"
    "v_mfma_f32_16x16x32_bf16 v[76:79], a[24:27], v[148:151], v[76:79]\n"
    "ds_read_b128 a[88:91], v3 offset:43008\n"
    "s_waitcnt lgkmcnt(15)\n"
    "v_mfma_f32_16x16x32_bf16 v[80:83], a[28:31], v[144:147], v[80:83]\n"
    "ds_read_b128 a[92:95], v3 offset:44032\n"
    "v_mfma_f32_16x16x32_bf16 v[84:87], a[28:31], v[148:151], v[84:87]\n"
    "s_waitcnt lgkmcnt(15)\n"
    "v_mfma_f32_16x16x32_bf16 v[88:91], a[32:35], v[144:147], v[88:91]\n"
    "ds_read_b128 a[96:99], v3 offset:45056\n"
    "v_mfma_f32_16x16x32_bf16 v[92:95], a[32:35], v[148:151], v[92:95]\n"
    "ds_read_b128 a[100:103], v3 offset:46080\n"
    "s_waitcnt lgkmcnt(15)\n"
    "v_mfma_f32_16x16x32_bf16 v[96:99], a[36:39], v[144:147], v[96:99]\n"
    "v_mfma_f32_16x16x32_bf16 v[100:103], a[36:39], v[148:151], v[100:103]\n"
    "ds_read_b128 a[104:107], v3 offset:47104\n"
    "v_mfma_f32_16x16x32_bf16 v[104:107], a[40:43], v[144:147], v[104:107]\n"
    "ds_read_b128 a[108:111], v3 offset:48128\n"
    "v_mfma_f32_16x16x32_bf16 v[108:111], a[40:43], v[148:151], v[108:111]\n"
    "s_waitcnt lgkmcnt(15)\n"
    "v_mfma_f32_16x16x32_bf16 v[112:115], a[44:47], v[144:147], v[112:115]\n"
    "v_mfma_f32_16x16x32_bf16 v[116:119], a[44:47], v[148:151], v[116:119]\n"
    "v_mfma_f32_16x16x32_bf16 v[120:123], a[48:51], v[144:147], v[120:123]\n"
    "v_mfma_f32_16x16x32_bf16 v[124:127], a[48:51], v[148:151], v[124:127]\n"
    "s_waitcnt lgkmcnt(14)\n"
    "v_mfma_f32_16x16x32_bf16 v[128:131], a[52:55], v[144:147], v[128:131]\n"
    "v_mfma_f32_16x16x32_bf16 v[132:135], a[52:55], v[148:151], v[132:135]\n"
    "s_waitcnt vmcnt(21) lgkmcnt(13)\n"
    "v_mfma_f32_16x16x32_bf16 v[24:27], a[56:59], v[152:155], v[24:27]\n"
    "s_waitcnt vmcnt(13)\n"
    "s_waitcnt lgkmcnt(0)\n"
    "s_barrier\n"
    "v_mfma_f32_16x16x32_bf16 v[28:31], a[56:59], v[156:159], v[28:31]\n"
    "ds_read_b128 a[0:3], v1 offset:0\n"
    "v_mfma_f32_16x16x32_bf16 v[32:35], a[60:63], v[152:155], v[32:35]\n"
    "ds_read_b128 a[4:7], v1 offset:1024\n"
    "s_mov_b32 s66, 0x5800\n"
    "buffer_load_dwordx4 v[144:147], v1, s[56:59], s66 offen offset:0 sc1\n"
    "v_mfma_f32_16x16x32_bf16 v[36:39], a[60:63], v[156:159], v[36:39]\n"
    "s_mov_b32 s65, 0x144000\n"
    "s_add_u32 m0, s64, 0xe000\n"
    "s_nop 0\n"
    "buffer_load_dwordx4 v1, s[52:55], s65 offen offset:0 sc1 lds\n"
    "v_mfma_f32_16x16x32_bf16 v[40:43], a[64:67], v[152:155], v[40:43]\n"
    "ds_read_b128 a[8:11], v1 offset:2048\n"
    "v_mfma_f32_16x16x32_bf16 v[44:47], a[64:67], v[156:159], v[44:47]\n"
    "ds_read_b128 a[12:15], v1 offset:3072\n"
    "buffer_load_dwordx4 v1, s[52:55], s65 offen offset:1024 sc1 lds\n"
    "v_mfma_f32_16x16x32_bf16 v[48:51], a[68:71], v[152:155], v[48:51]\n"
    "v_mfma_f32_16x16x32_bf16 v[52:55], a[68:71], v[156:159], v[52:55]\n"
    "ds_read_b128 a[16:19], v1 offset:4096\n"
    "v_mfma_f32_16x16x32_bf16 v[56:59], a[72:75], v[152:155], v[56:59]\n"
    "ds_read_b128 a[20:23], v1 offset:5120\n"
    "buffer_load_dwordx4 v[148:151], v1, s[56:59], s66 offen offset:1024 sc1\n"
    "buffer_load_dwordx4 v1, s[52:55], s65 offen offset:2048 sc1 lds\n"
    "v_mfma_f32_16x16x32_bf16 v[60:63], a[72:75], v[156:159], v[60:63]\n"
    "buffer_load_dwordx4 v1, s[52:55], s65 offen offset:3072 sc1 lds\n"
    "v_mfma_f32_16x16x32_bf16 v[64:67], a[76:79], v[152:155], v[64:67]\n"
    "ds_read_b128 a[24:27], v1 offset:6144\n"
    "v_mfma_f32_16x16x32_bf16 v[68:71], a[76:79], v[156:159], v[68:71]\n"
    "s_add_u32 m0, s64, 0xf000\n"
    "ds_read_b128 a[28:31], v1 offset:7168\n"
    "buffer_load_dwordx4 v2, s[52:55], s65 offen offset:0 sc1 lds\n"
    "v_mfma_f32_16x16x32_bf16 v[72:75], a[80:83], v[152:155], v[72:75]\n"
    "v_mfma_f32_16x16x32_bf16 v[76:79], a[80:83], v[156:159], v[76:79]\n"
    "ds_read_b128 a[32:35], v1 offset:8192\n"
    "v_mfma_f32_16x16x32_bf16 v[80:83], a[84:87], v[152:155], v[80:83]\n"
    "ds_read_b128 a[36:39], v1 offset:9216\n"
    "buffer_load_dwordx4 v2, s[52:55], s65 offen offset:1024 sc1 lds\n"
    "v_mfma_f32_16x16x32_bf16 v[84:87], a[84:87], v[156:159], v[84:87]\n"
    "buffer_load_dwordx4 v2, s[52:55], s65 offen offset:2048 sc1 lds\n"
    "v_mfma_f32_16x16x32_bf16 v[88:91], a[88:91], v[152:155], v[88:91]\n"
    "ds_read_b128 a[40:43], v1 offset:10240\n"
    "v_mfma_f32_16x16x32_bf16 v[92:95], a[88:91], v[156:159], v[92:95]\n"
    "ds_read_b128 a[44:47], v1 offset:11264\n"
    "v_mfma_f32_16x16x32_bf16 v[96:99], a[92:95], v[152:155], v[96:99]\n"
    "v_mfma_f32_16x16x32_bf16 v[100:103], a[92:95], v[156:159], v[100:103]\n"
    "ds_read_b128 a[48:51], v1 offset:12288\n"
    "v_mfma_f32_16x16x32_bf16 v[104:107], a[96:99], v[152:155], v[104:107]\n"
    "ds_read_b128 a[52:55], v1 offset:13312\n"
    "v_mfma_f32_16x16x32_bf16 v[108:111], a[96:99], v[156:159], v[108:111]\n"
    "v_mfma_f32_16x16x32_bf16 v[112:115], a[100:103], v[152:155], v[112:115]\n"
    "v_mfma_f32_16x16x32_bf16 v[116:119], a[100:103], v[156:159], v[116:119]\n"
    "v_mfma_f32_16x16x32_bf16 v[120:123], a[104:107], v[152:155], v[120:123]\n"
    "v_mfma_f32_16x16x32_bf16 v[124:127], a[104:107], v[156:159], v[124:127]\n"
    "v_mfma_f32_16x16x32_bf16 v[128:131], a[108:111], v[152:155], v[128:131]\n"
    "v_mfma_f32_16x16x32_bf16 v[132:135], a[108:111], v[156:159], v[132:135]\n"
    "s_waitcnt vmcnt(21) lgkmcnt(13)\n"
    "v_mfma_f32_16x16x32_bf16 v[24:27], a[0:3], v[160:163], v[24:27]\n"
    "s_waitcnt vmcnt(20)\n"
    "v_mfma_f32_16x16x32_bf16 v[28:31], a[0:3], v[164:167], v[28:31]\n"
    "ds_read_b128 a[56:59], v1 offset:14336\n"
    "s_waitcnt lgkmcnt(13)\n"
    "v_mfma_f32_16x16x32_bf16 v[32:35], a[4:7], v[160:163], v[32:35]\n"
    "ds_read_b128 a[60:63], v1 offset:15360\n"
    "s_mov_b32 s66, 0x6000\n"
    "buffer_load_dwordx4 v[152:155], v1, s[56:59], s66 offen offset:0 sc1\n"
    "v_mfma_f32_16x16x32_bf16 v[36:39], a[4:7], v[164:167], v[36:39]\n"
    "s_waitcnt lgkmcnt(13)\n"
    "v_mfma_f32_16x16x32_bf16 v[40:43], a[8:11], v[160:163], v[40:43]\n"
    "ds_read_b128 a[64:67], v1 offset:16384\n"
    "v_mfma_f32_16x16x32_bf16 v[44:47], a[8:11], v[164:167], v[44:47]\n"
    "ds_read_b128 a[68:71], v1 offset:17408\n"
    "s_waitcnt lgkmcnt(14)\n"
    "v_mfma_f32_16x16x32_bf16 v[48:51], a[12:15], v[160:163], v[48:51]\n"
    "v_mfma_f32_16x16x32_bf16 v[52:55], a[12:15], v[164:167], v[52:55]\n"
    "ds_read_b128 a[72:75], v1 offset:18432\n"
    "s_waitcnt lgkmcnt(14)\n"
    "v_mfma_f32_16x16x32_bf16 v[56:59], a[16:19], v[160:163], v[56:59]\n"
    "ds_read_b128 a[76:79], v1 offset:19456\n"
    "buffer_load_dwordx4 v[156:159], v1, s[56:59], s66 offen offset:1024 sc1\n"
    "v_mfma_f32_16x16x32_bf16 v[60:63], a[16:19], v[164:167], v[60:63]\n"
    "s_waitcnt lgkmcnt(14)\n"
    "v_mfma_f32_16x16x32_bf16 v[64:67], a[20:23], v[160:163], v[64:67]\n"
    "ds_read_b128 a[80:83], v1 offset:20480\n"
    "v_mfma_f32_16x16x32_bf16 v[68:71], a[20:23], v[164:167], v[68:71]\n"
    "ds_read_b128 a[84:87], v1 offset:21504\n"
    "s_waitcnt lgkmcnt(15)\n"
    "v_mfma_f32_16x16x32_bf16 v[72:75], a[24:27], v[160:163], v[72:75]\n"
    "v_mfma_f32_16x16x32_bf16 v[76:79], a[24:27], v[164:167], v[76:79]\n"
    "ds_read_b128 a[88:91], v1 offset:22528\n"
    "s_waitcnt lgkmcnt(15)\n"
    "v_mfma_f32_16x16x32_bf16 v[80:83], a[28:31], v[160:163], v[80:83]\n"
    "ds_read_b128 a[92:95], v1 offset:23552\n"
    "v_mfma_f32_16x16x32_bf16 v[84:87], a[28:31], v[164:167], v[84:87]\n"
    "s_waitcnt lgkmcnt(15)\n"
    "v_mfma_f32_16x16x32_bf16 v[88:91], a[32:35], v[160:163], v[88:91]\n"
    "ds_read_b128 a[96:99], v1 offset:24576\n"
    "v_mfma_f32_16x16x32_bf16 v[92:95], a[32:35], v[164:167], v[92:95]\n"
    "ds_read_b128 a[100:103], v1 offset:25600\n"
    "s_waitcnt lgkmcnt(15)\n"
    "v_mfma_f32_16x16x32_bf16 v[96:99], a[36:39], v[160:163], v[96:99]\n"
    "v_mfma_f32_16x16x32_bf16 v[100:103], a[36:39], v[164:167], v[100:103]\n"
    "ds_read_b128 a[104:107], v1 offset:26624\n"
    "v_mfma_f32_16x16x32_bf16 v[104:107], a[40:43], v[160:163], v[104:107]\n"
    "ds_read_b128 a[108:111], v1 offset:27648\n"
    "v_mfma_f32_16x16x32_bf16 v[108:111], a[40:43], v[164:167], v[108:111]\n"
    "s_waitcnt lgkmcnt(15)\n"
    "v_mfma_f32_16x16x32_bf16 v[112:115], a[44:47], v[160:163], v[112:115]\n"
    "v_mfma_f32_16x16x32_bf16 v[116:119], a[44:47], v[164:167], v[116:119]\n"
    "v_mfma_f32_16x16x32_bf16 v[120:123], a[48:51], v[160:163], v[120:123]\n"
    "v_mfma_f32_16x16x32_bf16 v[124:127], a[48:51], v[164:167], v[124:127]\n"
    "s_waitcnt lgkmcnt(14)\n"
    "v_mfma_f32_16x16x32_bf16 v[128:131], a[52:55], v[160:163], v[128:131]\n"
    "v_mfma_f32_16x16x32_bf16 v[132:135], a[52:55], v[164:167], v[132:135]\n"
    "s_waitcnt vmcnt(21) lgkmcnt(13)\n"
    "v_mfma_f32_16x16x32_bf16 v[24:27], a[56:59], v[168:171], v[24:27]\n"
    "s_waitcnt vmcnt(13)\n"
    "s_waitcnt lgkmcnt(0)\n"
    "s_barrier\n"
    "v_mfma_f32_16x16x32_bf16 v[28:31], a[56:59], v[172:175], v[28:31]\n"
    "ds_read_b128 a[0:3], v1 offset:28672\n"
    "v_mfma_f32_16x16x32_bf16 v[32:35], a[60:63], v[168:171], v[32:35]\n"
    "ds_read_b128 a[4:7], v1 offset:29696\n"
    "s_mov_b32 s66, 0x6800\n"
    "buffer_load_dwordx4 v[160:163], v1, s[56:59], s66 offen offset:0 sc1\n"
    "v_mfma_f32_16x16x32_bf16 v[36:39], a[60:63], v[172:175], v[36:39]\n"
    "s_mov_b32 s65, 0x17a000\n"
    "s_add_u32 m0, s64, 0x15000\n"
    "s_nop 0\n"
    "buffer_load_dwordx4 v1, s[52:55], s65 offen offset:0 sc1 lds\n"
    "v_mfma_f32_16x16x32_bf16 v[40:43], a[64:67], v[168:171], v[40:43]\n"
    "ds_read_b128 a[8:11], v1 offset:30720\n"
    "v_mfma_f32_16x16x32_bf16 v[44:47], a[64:67], v[172:175], v[44:47]\n"
    "ds_read_b128 a[12:15], v1 offset:31744\n"
    "buffer_load_dwordx4 v1, s[52:55], s65 offen offset:1024 sc1 lds\n"
    "v_mfma_f32_16x16x32_bf16 v[48:51], a[68:71], v[168:171], v[48:51]\n"
    "v_mfma_f32_16x16x32_bf16 v[52:55], a[68:71], v[172:175], v[52:55]\n"
    "ds_read_b128 a[16:19], v1 offset:32768\n"
    "v_mfma_f32_16x16x32_bf16 v[56:59], a[72:75], v[168:171], v[56:59]\n"
    "ds_read_b128 a[20:23], v1 offset:33792\n"
    "buffer_load_dwordx4 v[164:167], v1, s[56:59], s66 offen offset:1024 sc1\n"
    "buffer_load_dwordx4 v1, s[52:55], s65 offen offset:2048 sc1 lds\n"
    "v_mfma_f32_16x16x32_bf16 v[60:63], a[72:75], v[172:175], v[60:63]\n"
    "buffer_load_dwordx4 v1, s[52:55], s65 offen offset:3072 sc1 lds\n"
    "v_mfma_f32_16x16x32_bf16 v[64:67], a[76:79], v[168:171], v[64:67]\n"
    "ds_read_b128 a[24:27], v1 offset:34816\n"
    "v_mfma_f32_16x16x32_bf16 v[68:71], a[76:79], v[172:175], v[68:71]\n"
    "s_add_u32 m0, s64, 0x16000\n"
    "ds_read_b128 a[28:31], v1 offset:35840\n"
    "buffer_load_dwordx4 v2, s[52:55], s65 offen offset:0 sc1 lds\n"
    "v_mfma_f32_16x16x32_bf16 v[72:75], a[80:83], v[168:171], v[72:75]\n"
    "v_mfma_f32_16x16x32_bf16 v[76:79], a[80:83], v[172:175], v[76:79]\n"
    "ds_read_b128 a[32:35], v1 offset:36864\n"
    "v_mfma_f32_16x16x32_bf16 v[80:83], a[84:87], v[168:171], v[80:83]\n"
    "ds_read_b128 a[36:39], v1 offset:37888\n"
    "buffer_load_dwordx4 v2, s[52:55], s65 offen offset:1024 sc1 lds\n"
    "v_mfma_f32_16x16x32_bf16 v[84:87], a[84:87], v[172:175], v[84:87]\n"
    "buffer_load_dwordx4 v2, s[52:55], s65 offen offset:2048 sc1 lds\n"
    "v_mfma_f32_16x16x32_bf16 v[88:91], a[88:91], v[168:171], v[88:91]\n"
    "ds_read_b128 a[40:43], v1 offset:38912\n"
    "v_mfma_f32_16x16x32_bf16 v[92:95], a[88:91], v[172:175], v[92:95]\n"
    "ds_read_b128 a[44:47], v1 offset:39936\n"
    "v_mfma_f32_16x16x32_bf16 v[96:99], a[92:95], v[168:171], v[96:99]\n"
    "v_mfma_f32_16x16x32_bf16 v[100:103], a[92:95], v[172:175], v[100:103]\n"
    "ds_read_b128 a[48:51], v1 offset:40960\n"
    "v_mfma_f32_16x16x32_bf16 v[104:107], a[96:99], v[168:171], v[104:107]\n"
    "ds_read_b128 a[52:55], v1 offset:41984\n"
    "v_mfma_f32_16x16x32_bf16 v[108:111], a[96:99], v[172:175], v[108:111]\n"
    "v_mfma_f32_16x16x32_bf16 v[112:115], a[100:103], v[168:171], v[112:115]\n"
    "v_mfma_f32_16x16x32_bf16 v[116:119], a[100:103], v[172:175], v[116:119]\n"
    "v_mfma_f32_16x16x32_bf16 v[120:123], a[104:107], v[168:171], v[120:123]\n"
    "v_mfma_f32_16x16x32_bf16 v[124:127], a[104:107], v[172:175], v[124:127]\n"
    "v_mfma_f32_16x16x32_bf16 v[128:131], a[108:111], v[168:171], v[128:131]\n"
    "v_mfma_f32_16x16x32_bf16 v[132:135], a[108:111], v[172:175], v[132:135]\n"
    "s_waitcnt vmcnt(21) lgkmcnt(13)\n"
    "v_mfma_f32_16x16x32_bf16 v[24:27], a[0:3], v[136:139], v[24:27]\n"
    "s_waitcnt vmcnt(20)\n"
    "v_mfma_f32_16x16x32_bf16 v[28:31], a[0:3], v[140:143], v[28:31]\n"
    "ds_read_b128 a[56:59], v1 offset:43008\n"
    "s_waitcnt lgkmcnt(13)\n"
    "v_mfma_f32_16x16x32_bf16 v[32:35], a[4:7], v[136:139], v[32:35]\n"
    "ds_read_b128 a[60:63], v1 offset:44032\n"
    "s_mov_b32 s66, 0x7000\n"
    "buffer_load_dwordx4 v[168:171], v1, s[56:59], s66 offen offset:0 sc1\n"
    "v_mfma_f32_16x16x32_bf16 v[36:39], a[4:7], v[140:143], v[36:39]\n"
    "s_waitcnt lgkmcnt(13)\n"
    "v_mfma_f32_16x16x32_bf16 v[40:43], a[8:11], v[136:139], v[40:43]\n"
    "ds_read_b128 a[64:67], v1 offset:45056\n"
    "v_mfma_f32_16x16x32_bf16 v[44:47], a[8:11], v[140:143], v[44:47]\n"
    "ds_read_b128 a[68:71], v1 offset:46080\n"
    "s_waitcnt lgkmcnt(14)\n"
    "v_mfma_f32_16x16x32_bf16 v[48:51], a[12:15], v[136:139], v[48:51]\n"
    "v_mfma_f32_16x16x32_bf16 v[52:55], a[12:15], v[140:143], v[52:55]\n"
    "ds_read_b128 a[72:75], v1 offset:47104\n"
    "s_waitcnt lgkmcnt(14)\n"
    "v_mfma_f32_16x16x32_bf16 v[56:59], a[16:19], v[136:139], v[56:59]\n"
    "ds_read_b128 a[76:79], v1 offset:48128\n"
    "buffer_load_dwordx4 v[172:175], v1, s[56:59], s66 offen offset:1024 sc1\n"
    "v_mfma_f32_16x16x32_bf16 v[60:63], a[16:19], v[140:143], v[60:63]\n"
    "s_waitcnt lgkmcnt(14)\n"
    "v_mfma_f32_16x16x32_bf16 v[64:67], a[20:23], v[136:139], v[64:67]\n"
    "ds_read_b128 a[80:83], v1 offset:49152\n"
    "v_mfma_f32_16x16x32_bf16 v[68:71], a[20:23], v[140:143], v[68:71]\n"
    "ds_read_b128 a[84:87], v1 offset:50176\n"
    "s_waitcnt lgkmcnt(15)\n"
    "v_mfma_f32_16x16x32_bf16 v[72:75], a[24:27], v[136:139], v[72:75]\n"
    "v_mfma_f32_16x16x32_bf16 v[76:79], a[24:27], v[140:143], v[76:79]\n"
    "ds_read_b128 a[88:91], v1 offset:51200\n"
    "s_waitcnt lgkmcnt(15)\n"
    "v_mfma_f32_16x16x32_bf16 v[80:83], a[28:31], v[136:139], v[80:83]\n"
    "ds_read_b128 a[92:95], v1 offset:52224\n"
    "v_mfma_f32_16x16x32_bf16 v[84:87], a[28:31], v[140:143], v[84:87]\n"
    "s_waitcnt lgkmcnt(15)\n"
    "v_mfma_f32_16x16x32_bf16 v[88:91], a[32:35], v[136:139], v[88:91]\n"
    "ds_read_b128 a[96:99], v1 offset:53248\n"
    "v_mfma_f32_16x16x32_bf16 v[92:95], a[32:35], v[140:143], v[92:95]\n"
    "ds_read_b128 a[100:103], v1 offset:54272\n"
    "s_waitcnt lgkmcnt(15)\n"
    "v_mfma_f32_16x16x32_bf16 v[96:99], a[36:39], v[136:139], v[96:99]\n"
    "v_mfma_f32_16x16x32_bf16 v[100:103], a[36:39], v[140:143], v[100:103]\n"
    "ds_read_b128 a[104:107], v1 offset:55296\n"
    "v_mfma_f32_16x16x32_bf16 v[104:107], a[40:43], v[136:139], v[104:107]\n"
    "ds_read_b128 a[108:111], v1 offset:56320\n"
    "v_mfma_f32_16x16x32_bf16 v[108:111], a[40:43], v[140:143], v[108:111]\n"
    "s_waitcnt lgkmcnt(15)\n"
    "v_mfma_f32_16x16x32_bf16 v[112:115], a[44:47], v[136:139], v[112:115]\n"
    "v_mfma_f32_16x16x32_bf16 v[116:119], a[44:47], v[140:143], v[116:119]\n"
    "v_mfma_f32_16x16x32_bf16 v[120:123], a[48:51], v[136:139], v[120:123]\n"
    "v_mfma_f32_16x16x32_bf16 v[124:127], a[48:51], v[140:143], v[124:127]\n"
    "s_waitcnt lgkmcnt(14)\n"
    "v_mfma_f32_16x16x32_bf16 v[128:131], a[52:55], v[136:139], v[128:131]\n"
    "v_mfma_f32_16x16x32_bf16 v[132:135], a[52:55], v[140:143], v[132:135]\n"
    "s_waitcnt vmcnt(21) lgkmcnt(13)\n"
    "v_mfma_f32_16x16x32_bf16 v[24:27], a[56:59], v[144:147], v[24:27]\n"
    "s_waitcnt vmcnt(13)\n"
    "s_waitcnt lgkmcnt(0)\n"
    "s_barrier\n"
    "v_mfma_f32_16x16x32_bf16 v[28:31], a[56:59], v[148:151], v[28:31]\n"
    "ds_read_b128 a[0:3], v1 offset:57344\n"
    "v_mfma_f32_16x16x32_bf16 v[32:35], a[60:63], v[144:147], v[32:35]\n"
    "ds_read_b128 a[4:7], v1 offset:58368\n"
    "s_mov_b32 s66, 0x7800\n"
    "buffer_load_dwordx4 v[136:139], v1, s[56:59], s66 offen offset:0 sc1\n"
    "v_mfma_f32_16x16x32_bf16 v[36:39], a[60:63], v[148:151], v[36:39]\n"
    "s_mov_b32 s65, 0x1b0000\n"
    "s_add_u32 m0, s64, 0\n"
    "s_nop 0\n"
    "buffer_load_dwordx4 v1, s[52:55], s65 offen offset:0 sc1 lds\n"
    "v_mfma_f32_16x16x32_bf16 v[40:43], a[64:67], v[144:147], v[40:43]\n"
    "ds_read_b128 a[8:11], v1 offset:59392\n"
    "v_mfma_f32_16x16x32_bf16 v[44:47], a[64:67], v[148:151], v[44:47]\n"
    "ds_read_b128 a[12:15], v1 offset:60416\n"
    "buffer_load_dwordx4 v1, s[52:55], s65 offen offset:1024 sc1 lds\n"
    "v_mfma_f32_16x16x32_bf16 v[48:51], a[68:71], v[144:147], v[48:51]\n"
    "v_mfma_f32_16x16x32_bf16 v[52:55], a[68:71], v[148:151], v[52:55]\n"
    "ds_read_b128 a[16:19], v1 offset:61440\n"
    "v_mfma_f32_16x16x32_bf16 v[56:59], a[72:75], v[144:147], v[56:59]\n"
    "ds_read_b128 a[20:23], v1 offset:62464\n"
    "buffer_load_dwordx4 v[140:143], v1, s[56:59], s66 offen offset:1024 sc1\n"
    "buffer_load_dwordx4 v1, s[52:55], s65 offen offset:2048 sc1 lds\n"
    "v_mfma_f32_16x16x32_bf16 v[60:63], a[72:75], v[148:151], v[60:63]\n"
    "buffer_load_dwordx4 v1, s[52:55], s65 offen offset:3072 sc1 lds\n"
    "v_mfma_f32_16x16x32_bf16 v[64:67], a[76:79], v[144:147], v[64:67]\n"
    "ds_read_b128 a[24:27], v1 offset:63488\n"
    "v_mfma_f32_16x16x32_bf16 v[68:71], a[76:79], v[148:151], v[68:71]\n"
    "s_add_u32 m0, s64, 0x1000\n"
    "ds_read_b128 a[28:31], v1 offset:64512\n"
    "buffer_load_dwordx4 v2, s[52:55], s65 offen offset:0 sc1 lds\n"
    "v_mfma_f32_16x16x32_bf16 v[72:75], a[80:83], v[144:147], v[72:75]\n"
    "v_mfma_f32_16x16x32_bf16 v[76:79], a[80:83], v[148:151], v[76:79]\n"
    "ds_read_b128 a[32:35], v3 offset:0\n"
    "v_mfma_f32_16x16x32_bf16 v[80:83], a[84:87], v[144:147], v[80:83]\n"
    "ds_read_b128 a[36:39], v3 offset:1024\n"
    "buffer_load_dwordx4 v2, s[52:55], s65 offen offset:1024 sc1 lds\n"
    "v_mfma_f32_16x16x32_bf16 v[84:87], a[84:87], v[148:151], v[84:87]\n"
    "buffer_load_dwordx4 v2, s[52:55], s65 offen offset:2048 sc1 lds\n"
    "v_mfma_f32_16x16x32_bf16 v[88:91], a[88:91], v[144:147], v[88:91]\n"
    "ds_read_b128 a[40:43], v3 offset:2048\n"
    "v_mfma_f32_16x16x32_bf16 v[92:95], a[88:91], v[148:151], v[92:95]\n"
    "ds_read_b128 a[44:47], v3 offset:3072\n"
    "v_mfma_f32_16x16x32_bf16 v[96:99], a[92:95], v[144:147], v[96:99]\n"
    "v_mfma_f32_16x16x32_bf16 v[100:103], a[92:95], v[148:151], v[100:103]\n"
    "ds_read_b128 a[48:51], v3 offset:4096\n"
    "v_mfma_f32_16x16x32_bf16 v[104:107], a[96:99], v[144:147], v[104:107]\n"
    "ds_read_b128 a[52:55], v3 offset:5120\n"
    "v_mfma_f32_16x16x32_bf16 v[108:111], a[96:99], v[148:151], v[108:111]\n"
    "v_mfma_f32_16x16x32_bf16 v[112:115], a[100:103], v[144:147], v[112:115]\n"
    "v_mfma_f32_16x16x32_bf16 v[116:119], a[100:103], v[148:151], v[116:119]\n"
    "v_mfma_f32_16x16x32_bf16 v[120:123], a[104:107], v[144:147], v[120:123]\n"
    "v_mfma_f32_16x16x32_bf16 v[124:127], a[104:107], v[148:151], v[124:127]\n"
    "v_mfma_f32_16x16x32_bf16 v[128:131], a[108:111], v[144:147], v[128:131]\n"
    "v_mfma_f32_16x16x32_bf16 v[132:135], a[108:111], v[148:151], v[132:135]\n"
    "s_waitcnt vmcnt(21) lgkmcnt(13)\n"
    "v_mfma_f32_16x16x32_bf16 v[24:27], a[0:3], v[152:155], v[24:27]\n"
    "s_waitcnt vmcnt(20)\n"
    "v_mfma_f32_16x16x32_bf16 v[28:31], a[0:3], v[156:159], v[28:31]\n"
    "ds_read_b128 a[56:59], v3 offset:6144\n"
    "s_waitcnt lgkmcnt(13)\n"
    "v_mfma_f32_16x16x32_bf16 v[32:35], a[4:7], v[152:155], v[32:35]\n"
    "ds_read_b128 a[60:63], v3 offset:7168\n"
    "s_mov_b32 s66, 0x8000\n"
    "buffer_load_dwordx4 v[144:147], v1, s[56:59], s66 offen offset:0 sc1\n"
    "v_mfma_f32_16x16x32_bf16 v[36:39], a[4:7], v[156:159], v[36:39]\n"
    "s_waitcnt lgkmcnt(13)\n"
    "v_mfma_f32_16x16x32_bf16 v[40:43], a[8:11], v[152:155], v[40:43]\n"
    "ds_read_b128 a[64:67], v3 offset:8192\n"
    "v_mfma_f32_16x16x32_bf16 v[44:47], a[8:11], v[156:159], v[44:47]\n"
    "ds_read_b128 a[68:71], v3 offset:9216\n"
    "s_waitcnt lgkmcnt(14)\n"
    "v_mfma_f32_16x16x32_bf16 v[48:51], a[12:15], v[152:155], v[48:51]\n"
    "v_mfma_f32_16x16x32_bf16 v[52:55], a[12:15], v[156:159], v[52:55]\n"
    "ds_read_b128 a[72:75], v3 offset:10240\n"
    "s_waitcnt lgkmcnt(14)\n"
    "v_mfma_f32_16x16x32_bf16 v[56:59], a[16:19], v[152:155], v[56:59]\n"
    "ds_read_b128 a[76:79], v3 offset:11264\n"
    "buffer_load_dwordx4 v[148:151], v1, s[56:59], s66 offen offset:1024 sc1\n"
    "v_mfma_f32_16x16x32_bf16 v[60:63], a[16:19], v[156:159], v[60:63]\n"
    "s_waitcnt lgkmcnt(14)\n"
    "v_mfma_f32_16x16x32_bf16 v[64:67], a[20:23], v[152:155], v[64:67]\n"
    "ds_read_b128 a[80:83], v3 offset:12288\n"
    "v_mfma_f32_16x16x32_bf16 v[68:71], a[20:23], v[156:159], v[68:71]\n"
    "ds_read_b128 a[84:87], v3 offset:13312\n"
    "s_waitcnt lgkmcnt(15)\n"
    "v_mfma_f32_16x16x32_bf16 v[72:75], a[24:27], v[152:155], v[72:75]\n"
    "v_mfma_f32_16x16x32_bf16 v[76:79], a[24:27], v[156:159], v[76:79]\n"
    "ds_read_b128 a[88:91], v3 offset:14336\n"
    "s_waitcnt lgkmcnt(15)\n"
    "v_mfma_f32_16x16x32_bf16 v[80:83], a[28:31], v[152:155], v[80:83]\n"
    "ds_read_b128 a[92:95], v3 offset:15360\n"
    "v_mfma_f32_16x16x32_bf16 v[84:87], a[28:31], v[156:159], v[84:87]\n"
    "s_waitcnt lgkmcnt(15)\n"
    "v_mfma_f32_16x16x32_bf16 v[88:91], a[32:35], v[152:155], v[88:91]\n"
    "ds_read_b128 a[96:99], v3 offset:16384\n"
    "v_mfma_f32_16x16x32_bf16 v[92:95], a[32:35], v[156:159], v[92:95]\n"
    "ds_read_b128 a[100:103], v3 offset:17408\n"
    "s_waitcnt lgkmcnt(15)\n"
    "v_mfma_f32_16x16x32_bf16 v[96:99], a[36:39], v[152:155], v[96:99]\n"
    "v_mfma_f32_16x16x32_bf16 v[100:103], a[36:39], v[156:159], v[100:103]\n"
    "ds_read_b128 a[104:107], v3 offset:18432\n"
    "v_mfma_f32_16x16x32_bf16 v[104:107], a[40:43], v[152:155], v[104:107]\n"
    "ds_read_b128 a[108:111], v3 offset:19456\n"
    "v_mfma_f32_16x16x32_bf16 v[108:111], a[40:43], v[156:159], v[108:111]\n"
    "s_waitcnt lgkmcnt(15)\n"
    "v_mfma_f32_16x16x32_bf16 v[112:115], a[44:47], v[152:155], v[112:115]\n"
    "v_mfma_f32_16x16x32_bf16 v[116:119], a[44:47], v[156:159], v[116:119]\n"
    "v_mfma_f32_16x16x32_bf16 v[120:123], a[48:51], v[152:155], v[120:123]\n"
    "v_mfma_f32_16x16x32_bf16 v[124:127], a[48:51], v[156:159], v[124:127]\n"
    "s_waitcnt lgkmcnt(14)\n"
    "v_mfma_f32_16x16x32_bf16 v[128:131], a[52:55], v[152:155], v[128:131]\n"
    "v_mfma_f32_16x16x32_bf16 v[132:135], a[52:55], v[156:159], v[132:135]\n"
    "s_waitcnt vmcnt(21) lgkmcnt(13)\n"
    "v_mfma_f32_16x16x32_bf16 v[24:27], a[56:59], v[160:163], v[24:27]\n"
    "s_waitcnt vmcnt(13)\n"
    "s_waitcnt lgkmcnt(0)\n"
    "s_barrier\n"
    "v_mfma_f32_16x16x32_bf16 v[28:31], a[56:59], v[164:167], v[28:31]\n"
    "ds_read_b128 a[0:3], v3 offset:20480\n"
    "v_mfma_f32_16x16x32_bf16 v[32:35], a[60:63], v[160:163], v[32:35]\n"
    "ds_read_b128 a[4:7], v3 offset:21504\n"
    "s_mov_b32 s66, 0x8800\n"
    "buffer_load_dwordx4 v[152:155], v1, s[56:59], s66 offen offset:0 sc1\n"
    "v_mfma_f32_16x16x32_bf16 v[36:39], a[60:63], v[164:167], v[36:39]\n"
    "s_mov_b32 s65, 0x1e6000\n"
    "s_add_u32 m0, s64, 0x7000\n"
    "s_nop 0\n"
    "buffer_load_dwordx4 v1, s[52:55], s65 offen offset:0 sc1 lds\n"
    "v_mfma_f32_16x16x32_bf16 v[40:43], a[64:67], v[160:163], v[40:43]\n"
    "ds_read_b128 a[8:11], v3 offset:22528\n"
    "v_mfma_f32_16x16x32_bf16 v[44:47], a[64:67], v[164:167], v[44:47]\n"
    "ds_read_b128 a[12:15], v3 offset:23552\n"
    "buffer_load_dwordx4 v1, s[52:55], s65 offen offset:1024 sc1 lds\n"
    "v_mfma_f32_16x16x32_bf16 v[48:51], a[68:71], v[160:163], v[48:51]\n"
    "v_mfma_f32_16x16x32_bf16 v[52:55], a[68:71], v[164:167], v[52:55]\n"
    "ds_read_b128 a[16:19], v3 offset:24576\n"
    "v_mfma_f32_16x16x32_bf16 v[56:59], a[72:75], v[160:163], v[56:59]\n"
    "ds_read_b128 a[20:23], v3 offset:25600\n"
    "buffer_load_dwordx4 v[156:159], v1, s[56:59], s66 offen offset:1024 sc1\n"
    "buffer_load_dwordx4 v1, s[52:55], s65 offen offset:2048 sc1 lds\n"
    "v_mfma_f32_16x16x32_bf16 v[60:63], a[72:75], v[164:167], v[60:63]\n"
    "buffer_load_dwordx4 v1, s[52:55], s65 offen offset:3072 sc1 lds\n"
    "v_mfma_f32_16x16x32_bf16 v[64:67], a[76:79], v[160:163], v[64:67]\n"
    "ds_read_b128 a[24:27], v3 offset:26624\n"
    "v_mfma_f32_16x16x32_bf16 v[68:71], a[76:79], v[164:167], v[68:71]\n"
    "s_add_u32 m0, s64, 0x8000\n"
    "ds_read_b128 a[28:31], v3 offset:27648\n"
    "buffer_load_dwordx4 v2, s[52:55], s65 offen offset:0 sc1 lds\n"
    "v_mfma_f32_16x16x32_bf16 v[72:75], a[80:83], v[160:163], v[72:75]\n"
    "v_mfma_f32_16x16x32_bf16 v[76:79], a[80:83], v[164:167], v[76:79]\n"
    "ds_read_b128 a[32:35], v3 offset:28672\n"
    "v_mfma_f32_16x16x32_bf16 v[80:83], a[84:87], v[160:163], v[80:83]\n"
    "ds_read_b128 a[36:39], v3 offset:29696\n"
    "buffer_load_dwordx4 v2, s[52:55], s65 offen offset:1024 sc1 lds\n"
    "v_mfma_f32_16x16x32_bf16 v[84:87], a[84:87], v[164:167], v[84:87]\n"
    "buffer_load_dwordx4 v2, s[52:55], s65 offen offset:2048 sc1 lds\n"
    "v_mfma_f32_16x16x32_bf16 v[88:91], a[88:91], v[160:163], v[88:91]\n"
    "ds_read_b128 a[40:43], v3 offset:30720\n"
    "v_mfma_f32_16x16x32_bf16 v[92:95], a[88:91], v[164:167], v[92:95]\n"
    "ds_read_b128 a[44:47], v3 offset:31744\n"
    "v_mfma_f32_16x16x32_bf16 v[96:99], a[92:95], v[160:163], v[96:99]\n"
    "v_mfma_f32_16x16x32_bf16 v[100:103], a[92:95], v[164:167], v[100:103]\n"
    "ds_read_b128 a[48:51], v3 offset:32768\n"
    "v_mfma_f32_16x16x32_bf16 v[104:107], a[96:99], v[160:163], v[104:107]\n"
    "ds_read_b128 a[52:55], v3 offset:33792\n"
    "v_mfma_f32_16x16x32_bf16 v[108:111], a[96:99], v[164:167], v[108:111]\n"
    "v_mfma_f32_16x16x32_bf16 v[112:115], a[100:103], v[160:163], v[112:115]\n"
    "v_mfma_f32_16x16x32_bf16 v[116:119], a[100:103], v[164:167], v[116:119]\n"
    "v_mfma_f32_16x16x32_bf16 v[120:123], a[104:107], v[160:163], v[120:123]\n"
    "v_mfma_f32_16x16x32_bf16 v[124:127], a[104:107], v[164:167], v[124:127]\n"
    "v_mfma_f32_16x16x32_bf16 v[128:131], a[108:111], v[160:163], v[128:131]\n"
    "v_mfma_f32_16x16x32_bf16 v[132:135], a[108:111], v[164:167], v[132:135]\n"
    "s_waitcnt vmcnt(21) lgkmcnt(13)\n"
    "v_mfma_f32_16x16x32_bf16 v[24:27], a[0:3], v[168:171], v[24:27]\n"
    "s_waitcnt vmcnt(20)\n"
    "v_mfma_f32_16x16x32_bf16 v[28:31], a[0:3], v[172:175], v[28:31]\n"
    "ds_read_b128 a[56:59], v3 offset:34816\n"
    "s_waitcnt lgkmcnt(13)\n"
    "v_mfma_f32_16x16x32_bf16 v[32:35], a[4:7], v[168:171], v[32:35]\n"
    "ds_read_b128 a[60:63], v3 offset:35840\n"
    "s_mov_b32 s66, 0x9000\n"
    "buffer_load_dwordx4 v[160:163], v1, s[56:59], s66 offen offset:0 sc1\n"
    "v_mfma_f32_16x16x32_bf16 v[36:39], a[4:7], v[172:175], v[36:39]\n"
    "s_waitcnt lgkmcnt(13)\n"
    "v_mfma_f32_16x16x32_bf16 v[40:43], a[8:11], v[168:171], v[40:43]\n"
    "ds_read_b128 a[64:67], v3 offset:36864\n"
    "v_mfma_f32_16x16x32_bf16 v[44:47], a[8:11], v[172:175], v[44:47]\n"
    "ds_read_b128 a[68:71], v3 offset:37888\n"
    "s_waitcnt lgkmcnt(14)\n"
    "v_mfma_f32_16x16x32_bf16 v[48:51], a[12:15], v[168:171], v[48:51]\n"
    "v_mfma_f32_16x16x32_bf16 v[52:55], a[12:15], v[172:175], v[52:55]\n"
    "ds_read_b128 a[72:75], v3 offset:38912\n"
    "s_waitcnt lgkmcnt(14)\n"
    "v_mfma_f32_16x16x32_bf16 v[56:59], a[16:19], v[168:171], v[56:59]\n"
    "ds_read_b128 a[76:79], v3 offset:39936\n"
    "buffer_load_dwordx4 v[164:167], v1, s[56:59], s66 offen offset:1024 sc1\n"
    "v_mfma_f32_16x16x32_bf16 v[60:63], a[16:19], v[172:175], v[60:63]\n"
    "s_waitcnt lgkmcnt(14)\n"
    "v_mfma_f32_16x16x32_bf16 v[64:67], a[20:23], v[168:171], v[64:67]\n"
    "ds_read_b128 a[80:83], v3 offset:40960\n"
    "v_mfma_f32_16x16x32_bf16 v[68:71], a[20:23], v[172:175], v[68:71]\n"
    "ds_read_b128 a[84:87], v3 offset:41984\n"
    "s_waitcnt lgkmcnt(15)\n"
    "v_mfma_f32_16x16x32_bf16 v[72:75], a[24:27], v[168:171], v[72:75]\n"
    "v_mfma_f32_16x16x32_bf16 v[76:79], a[24:27], v[172:175], v[76:79]\n"
    "ds_read_b128 a[88:91], v3 offset:43008\n"
    "s_waitcnt lgkmcnt(15)\n"
    "v_mfma_f32_16x16x32_bf16 v[80:83], a[28:31], v[168:171], v[80:83]\n"
    "ds_read_b128 a[92:95], v3 offset:44032\n"
    "v_mfma_f32_16x16x32_bf16 v[84:87], a[28:31], v[172:175], v[84:87]\n"
    "s_waitcnt lgkmcnt(15)\n"
    "v_mfma_f32_16x16x32_bf16 v[88:91], a[32:35], v[168:171], v[88:91]\n"
    "ds_read_b128 a[96:99], v3 offset:45056\n"
    "v_mfma_f32_16x16x32_bf16 v[92:95], a[32:35], v[172:175], v[92:95]\n"
    "ds_read_b128 a[100:103], v3 offset:46080\n"
    "s_waitcnt lgkmcnt(15)\n"
    "v_mfma_f32_16x16x32_bf16 v[96:99], a[36:39], v[168:171], v[96:99]\n"
    "v_mfma_f32_16x16x32_bf16 v[100:103], a[36:39], v[172:175], v[100:103]\n"
    "ds_read_b128 a[104:107], v3 offset:47104\n"
    "v_mfma_f32_16x16x32_bf16 v[104:107], a[40:43], v[168:171], v[104:107]\n"
    "ds_read_b128 a[108:111], v3 offset:48128\n"
    "v_mfma_f32_16x16x32_bf16 v[108:111], a[40:43], v[172:175], v[108:111]\n"
    "s_waitcnt lgkmcnt(15)\n"
    "v_mfma_f32_16x16x32_bf16 v[112:115], a[44:47], v[168:171], v[112:115]\n"
    "v_mfma_f32_16x16x32_bf16 v[116:119], a[44:47], v[172:175], v[116:119]\n"
    "v_mfma_f32_16x16x32_bf16 v[120:123], a[48:51], v[168:171], v[120:123]\n"
    "v_mfma_f32_16x16x32_bf16 v[124:127], a[48:51], v[172:175], v[124:127]\n"
    "s_waitcnt lgkmcnt(14)\n"
    "v_mfma_f32_16x16x32_bf16 v[128:131], a[52:55], v[168:171], v[128:131]\n"
    "v_mfma_f32_16x16x32_bf16 v[132:135], a[52:55], v[172:175], v[132:135]\n"
    "s_waitcnt vmcnt(21) lgkmcnt(13)\n"
    "v_mfma_f32_16x16x32_bf16 v[24:27], a[56:59], v[136:139], v[24:27]\n"
    "s_waitcnt vmcnt(13)\n"
    "s_waitcnt lgkmcnt(0)\n"
    "s_barrier\n"
    "v_mfma_f32_16x16x32_bf16 v[28:31], a[56:59], v[140:143], v[28:31]\n"
    "ds_read_b128 a[0:3], v1 offset:0\n"
    "v_mfma_f32_16x16x32_bf16 v[32:35], a[60:63], v[136:139], v[32:35]\n"
    "ds_read_b128 a[4:7], v1 offset:1024\n"
    "s_mov_b32 s66, 0x9800\n"
    "buffer_load_dwordx4 v[168:171], v1, s[56:59], s66 offen offset:0 sc1\n"
    "v_mfma_f32_16x16x32_bf16 v[36:39], a[60:63], v[140:143], v[36:39]\n"
    "v_mfma_f32_16x16x32_bf16 v[40:43], a[64:67], v[136:139], v[40:43]\n"
    "ds_read_b128 a[8:11], v1 offset:2048\n"
    "v_mfma_f32_16x16x32_bf16 v[44:47], a[64:67], v[140:143], v[44:47]\n"
    "ds_read_b128 a[12:15], v1 offset:3072\n"
    "v_mfma_f32_16x16x32_bf16 v[48:51], a[68:71], v[136:139], v[48:51]\n"
    "v_mfma_f32_16x16x32_bf16 v[52:55], a[68:71], v[140:143], v[52:55]\n"
    "ds_read_b128 a[16:19], v1 offset:4096\n"
    "v_mfma_f32_16x16x32_bf16 v[56:59], a[72:75], v[136:139], v[56:59]\n"
    "ds_read_b128 a[20:23], v1 offset:5120\n"
    "buffer_load_dwordx4 v[172:175], v1, s[56:59], s66 offen offset:1024 sc1\n"
    "v_mfma_f32_16x16x32_bf16 v[60:63], a[72:75], v[140:143], v[60:63]\n"
    "v_mfma_f32_16x16x32_bf16 v[64:67], a[76:79], v[136:139], v[64:67]\n"
    "ds_read_b128 a[24:27], v1 offset:6144\n"
    "v_mfma_f32_16x16x32_bf16 v[68:71], a[76:79], v[140:143], v[68:71]\n"
    "ds_read_b128 a[28:31], v1 offset:7168\n"
    "v_mfma_f32_16x16x32_bf16 v[72:75], a[80:83], v[136:139], v[72:75]\n"
    "v_mfma_f32_16x16x32_bf16 v[76:79], a[80:83], v[140:143], v[76:79]\n"
    "ds_read_b128 a[32:35], v1 offset:8192\n"
    "v_mfma_f32_16x16x32_bf16 v[80:83], a[84:87], v[136:139], v[80:83]\n"
    "ds_read_b128 a[36:39], v1 offset:9216\n"
    "v_mfma_f32_16x16x32_bf16 v[84:87], a[84:87], v[140:143], v[84:87]\n"
    "v_mfma_f32_16x16x32_bf16 v[88:91], a[88:91], v[136:139], v[88:91]\n"
    "ds_read_b128 a[40:43], v1 offset:10240\n"
    "v_mfma_f32_16x16x32_bf16 v[92:95], a[88:91], v[140:143], v[92:95]\n"
    "ds_read_b128 a[44:47], v1 offset:11264\n"
    "v_mfma_f32_16x16x32_bf16 v[96:99], a[92:95], v[136:139], v[96:99]\n"
    "v_mfma_f32_16x16x32_bf16 v[100:103], a[92:95], v[140:143], v[100:103]\n"
    "ds_read_b128 a[48:51], v1 offset:12288\n"
    "v_mfma_f32_16x16x32_bf16 v[104:107], a[96:99], v[136:139], v[104:107]\n"
    "ds_read_b128 a[52:55], v1 offset:13312\n"
    "v_mfma_f32_16x16x32_bf16 v[108:111], a[96:99], v[140:143], v[108:111]\n"
    "v_mfma_f32_16x16x32_bf16 v[112:115], a[100:103], v[136:139], v[112:115]\n"
    "v_mfma_f32_16x16x32_bf16 v[116:119], a[100:103], v[140:143], v[116:119]\n"
    "v_mfma_f32_16x16x32_bf16 v[120:123], a[104:107], v[136:139], v[120:123]\n"
    "v_mfma_f32_16x16x32_bf16 v[124:127], a[104:107], v[140:143], v[124:127]\n"
    "v_mfma_f32_16x16x32_bf16 v[128:131], a[108:111], v[136:139], v[128:131]\n"
    "v_mfma_f32_16x16x32_bf16 v[132:135], a[108:111], v[140:143], v[132:135]\n"
    "s_waitcnt vmcnt(14) lgkmcnt(13)\n"
    "v_mfma_f32_16x16x32_bf16 v[24:27], a[0:3], v[144:147], v[24:27]\n"
    "s_waitcnt vmcnt(13)\n"
    "v_mfma_f32_16x16x32_bf16 v[28:31], a[0:3], v[148:151], v[28:31]\n"
    "ds_read_b128 a[56:59], v1 offset:14336\n"
    "s_waitcnt lgkmcnt(13)\n"
    "v_mfma_f32_16x16x32_bf16 v[32:35], a[4:7], v[144:147], v[32:35]\n"
    "ds_read_b128 a[60:63], v1 offset:15360\n"
    "v_mfma_f32_16x16x32_bf16 v[36:39], a[4:7], v[148:151], v[36:39]\n"
    "s_waitcnt lgkmcnt(13)\n"
    "v_mfma_f32_16x16x32_bf16 v[40:43], a[8:11], v[144:147], v[40:43]\n"
    "ds_read_b128 a[64:67], v1 offset:16384\n"
    "v_mfma_f32_16x16x32_bf16 v[44:47], a[8:11], v[148:151], v[44:47]\n"
    "ds_read_b128 a[68:71], v1 offset:17408\n"
    "s_waitcnt lgkmcnt(14)\n"
    "v_mfma_f32_16x16x32_bf16 v[48:51], a[12:15], v[144:147], v[48:51]\n"
    "v_mfma_f32_16x16x32_bf16 v[52:55], a[12:15], v[148:151], v[52:55]\n"
    "ds_read_b128 a[72:75], v1 offset:18432\n"
    "s_waitcnt lgkmcnt(14)\n"
    "v_mfma_f32_16x16x32_bf16 v[56:59], a[16:19], v[144:147], v[56:59]\n"
    "ds_read_b128 a[76:79], v1 offset:19456\n"
    "v_mfma_f32_16x16x32_bf16 v[60:63], a[16:19], v[148:151], v[60:63]\n"
    "s_waitcnt lgkmcnt(14)\n"
    "v_mfma_f32_16x16x32_bf16 v[64:67], a[20:23], v[144:147], v[64:67]\n"
    "ds_read_b128 a[80:83], v1 offset:20480\n"
    "v_mfma_f32_16x16x32_bf16 v[68:71], a[20:23], v[148:151], v[68:71]\n"
    "ds_read_b128 a[84:87], v1 offset:21504\n"
    "s_waitcnt lgkmcnt(15)\n"
    "v_mfma_f32_16x16x32_bf16 v[72:75], a[24:27], v[144:147], v[72:75]\n"
    "v_mfma_f32_16x16x32_bf16 v[76:79], a[24:27], v[148:151], v[76:79]\n"
    "ds_read_b128 a[88:91], v1 offset:22528\n"
    "s_waitcnt lgkmcnt(15)\n"
    "v_mfma_f32_16x16x32_bf16 v[80:83], a[28:31], v[144:147], v[80:83]\n"
    "ds_read_b128 a[92:95], v1 offset:23552\n"
    "v_mfma_f32_16x16x32_bf16 v[84:87], a[28:31], v[148:151], v[84:87]\n"
    "s_waitcnt lgkmcnt(15)\n"
    "v_mfma_f32_16x16x32_bf16 v[88:91], a[32:35], v[144:147], v[88:91]\n"
    "ds_read_b128 a[96:99], v1 offset:24576\n"
    "v_mfma_f32_16x16x32_bf16 v[92:95], a[32:35], v[148:151], v[92:95]\n"
    "ds_read_b128 a[100:103], v1 offset:25600\n"
    "s_waitcnt lgkmcnt(15)\n"
    "v_mfma_f32_16x16x32_bf16 v[96:99], a[36:39], v[144:147], v[96:99]\n"
    "v_mfma_f32_16x16x32_bf16 v[100:103], a[36:39], v[148:151], v[100:103]\n"
    "ds_read_b128 a[104:107], v1 offset:26624\n"
    "v_mfma_f32_16x16x32_bf16 v[104:107], a[40:43], v[144:147], v[104:107]\n"
    "ds_read_b128 a[108:111], v1 offset:27648\n"
    "v_mfma_f32_16x16x32_bf16 v[108:111], a[40:43], v[148:151], v[108:111]\n"
    "s_waitcnt lgkmcnt(15)\n"
    "v_mfma_f32_16x16x32_bf16 v[112:115], a[44:47], v[144:147], v[112:115]\n"
    "v_mfma_f32_16x16x32_bf16 v[116:119], a[44:47], v[148:151], v[116:119]\n"
    "v_mfma_f32_16x16x32_bf16 v[120:123], a[48:51], v[144:147], v[120:123]\n"
    "v_mfma_f32_16x16x32_bf16 v[124:127], a[48:51], v[148:151], v[124:127]\n"
    "s_waitcnt lgkmcnt(14)\n"
    "v_mfma_f32_16x16x32_bf16 v[128:131], a[52:55], v[144:147], v[128:131]\n"
    "v_mfma_f32_16x16x32_bf16 v[132:135], a[52:55], v[148:151], v[132:135]\n"
    "s_waitcnt vmcnt(12) lgkmcnt(13)\n"
    "v_mfma_f32_16x16x32_bf16 v[24:27], a[56:59], v[152:155], v[24:27]\n"
    "s_waitcnt vmcnt(4)\n"
    "s_waitcnt lgkmcnt(0)\n"
    "s_barrier\n"
    "v_mfma_f32_16x16x32_bf16 v[28:31], a[56:59], v[156:159], v[28:31]\n"
    "ds_read_b128 a[0:3], v1 offset:28672\n"
    "v_mfma_f32_16x16x32_bf16 v[32:35], a[60:63], v[152:155], v[32:35]\n"
    "ds_read_b128 a[4:7], v1 offset:29696\n"
    "v_mfma_f32_16x16x32_bf16 v[36:39], a[60:63], v[156:159], v[36:39]\n"
    "v_mfma_f32_16x16x32_bf16 v[40:43], a[64:67], v[152:155], v[40:43]\n"
    "ds_read_b128 a[8:11], v1 offset:30720\n"
    "v_mfma_f32_16x16x32_bf16 v[44:47], a[64:67], v[156:159], v[44:47]\n"
    "ds_read_b128 a[12:15], v1 offset:31744\n"
    "v_mfma_f32_16x16x32_bf16 v[48:51], a[68:71], v[152:155], v[48:51]\n"
    "v_mfma_f32_16x16x32_bf16 v[52:55], a[68:71], v[156:159], v[52:55]\n"
    "ds_read_b128 a[16:19], v1 offset:32768\n"
    "v_mfma_f32_16x16x32_bf16 v[56:59], a[72:75], v[152:155], v[56:59]\n"
    "ds_read_b128 a[20:23], v1 offset:33792\n"
    "v_mfma_f32_16x16x32_bf16 v[60:63], a[72:75], v[156:159], v[60:63]\n"
    "v_mfma_f32_16x16x32_bf16 v[64:67], a[76:79], v[152:155], v[64:67]\n"
    "ds_read_b128 a[24:27], v1 offset:34816\n"
    "v_mfma_f32_16x16x32_bf16 v[68:71], a[76:79], v[156:159], v[68:71]\n"
    "ds_read_b128 a[28:31], v1 offset:35840\n"
    "v_mfma_f32_16x16x32_bf16 v[72:75], a[80:83], v[152:155], v[72:75]\n"
    "v_mfma_f32_16x16x32_bf16 v[76:79], a[80:83], v[156:159], v[76:79]\n"
    "ds_read_b128 a[32:35], v1 offset:36864\n"
    "v_mfma_f32_16x16x32_bf16 v[80:83], a[84:87], v[152:155], v[80:83]\n"
    "ds_read_b128 a[36:39], v1 offset:37888\n"
    "v_mfma_f32_16x16x32_bf16 v[84:87], a[84:87], v[156:159], v[84:87]\n"
    "v_mfma_f32_16x16x32_bf16 v[88:91], a[88:91], v[152:155], v[88:91]\n"
    "ds_read_b128 a[40:43], v1 offset:38912\n"
    "v_mfma_f32_16x16x32_bf16 v[92:95], a[88:91], v[156:159], v[92:95]\n"
    "ds_read_b128 a[44:47], v1 offset:39936\n"
    "v_mfma_f32_16x16x32_bf16 v[96:99], a[92:95], v[152:155], v[96:99]\n"
    "v_mfma_f32_16x16x32_bf16 v[100:103], a[92:95], v[156:159], v[100:103]\n"
    "ds_read_b128 a[48:51], v1 offset:40960\n"
    "v_mfma_f32_16x16x32_bf16 v[104:107], a[96:99], v[152:155], v[104:107]\n"
    "ds_read_b128 a[52:55], v1 offset:41984\n"
    "v_mfma_f32_16x16x32_bf16 v[108:111], a[96:99], v[156:159], v[108:111]\n"
    "v_mfma_f32_16x16x32_bf16 v[112:115], a[100:103], v[152:155], v[112:115]\n"
    "v_mfma_f32_16x16x32_bf16 v[116:119], a[100:103], v[156:159], v[116:119]\n"
    "v_mfma_f32_16x16x32_bf16 v[120:123], a[104:107], v[152:155], v[120:123]\n"
    "v_mfma_f32_16x16x32_bf16 v[124:127], a[104:107], v[156:159], v[124:127]\n"
    "v_mfma_f32_16x16x32_bf16 v[128:131], a[108:111], v[152:155], v[128:131]\n"
    "v_mfma_f32_16x16x32_bf16 v[132:135], a[108:111], v[156:159], v[132:135]\n"
    "s_waitcnt vmcnt(3) lgkmcnt(13)\n"
    "v_mfma_f32_16x16x32_bf16 v[24:27], a[0:3], v[160:163], v[24:27]\n"
    "s_waitcnt vmcnt(2)\n"
    "v_mfma_f32_16x16x32_bf16 v[28:31], a[0:3], v[164:167], v[28:31]\n"
    "ds_read_b128 a[56:59], v1 offset:43008\n"
    "s_waitcnt lgkmcnt(13)\n"
    "v_mfma_f32_16x16x32_bf16 v[32:35], a[4:7], v[160:163], v[32:35]\n"
    "ds_read_b128 a[60:63], v1 offset:44032\n"
    "v_mfma_f32_16x16x32_bf16 v[36:39], a[4:7], v[164:167], v[36:39]\n"
    "s_waitcnt lgkmcnt(13)\n"
    "v_mfma_f32_16x16x32_bf16 v[40:43], a[8:11], v[160:163], v[40:43]\n"
    "ds_read_b128 a[64:67], v1 offset:45056\n"
    "v_mfma_f32_16x16x32_bf16 v[44:47], a[8:11], v[164:167], v[44:47]\n"
    "ds_read_b128 a[68:71], v1 offset:46080\n"
    "s_waitcnt lgkmcnt(14)\n"
    "v_mfma_f32_16x16x32_bf16 v[48:51], a[12:15], v[160:163], v[48:51]\n"
    "v_mfma_f32_16x16x32_bf16 v[52:55], a[12:15], v[164:167], v[52:55]\n"
    "ds_read_b128 a[72:75], v1 offset:47104\n"
    "s_waitcnt lgkmcnt(14)\n"
    "v_mfma_f32_16x16x32_bf16 v[56:59], a[16:19], v[160:163], v[56:59]\n"
    "ds_read_b128 a[76:79], v1 offset:48128\n"
    "v_mfma_f32_16x16x32_bf16 v[60:63], a[16:19], v[164:167], v[60:63]\n"
    "s_waitcnt lgkmcnt(14)\n"
    "v_mfma_f32_16x16x32_bf16 v[64:67], a[20:23], v[160:163], v[64:67]\n"
    "ds_read_b128 a[80:83], v1 offset:49152\n"
    "v_mfma_f32_16x16x32_bf16 v[68:71], a[20:23], v[164:167], v[68:71]\n"
    "ds_read_b128 a[84:87], v1 offset:50176\n"
    "s_waitcnt lgkmcnt(15)\n"
    "v_mfma_f32_16x16x32_bf16 v[72:75], a[24:27], v[160:163], v[72:75]\n"
    "v_mfma_f32_16x16x32_bf16 v[76:79], a[24:27], v[164:167], v[76:79]\n"
    "ds_read_b128 a[88:91], v1 offset:51200\n"
    "s_waitcnt lgkmcnt(15)\n"
    "v_mfma_f32_16x16x32_bf16 v[80:83], a[28:31], v[160:163], v[80:83]\n"
    "ds_read_b128 a[92:95], v1 offset:52224\n"
    "v_mfma_f32_16x16x32_bf16 v[84:87], a[28:31], v[164:167], v[84:87]\n"
    "s_waitcnt lgkmcnt(15)\n"
    "v_mfma_f32_16x16x32_bf16 v[88:91], a[32:35], v[160:163], v[88:91]\n"
    "ds_read_b128 a[96:99], v1 offset:53248\n"
    "v_mfma_f32_16x16x32_bf16 v[92:95], a[32:35], v[164:167], v[92:95]\n"
    "ds_read_b128 a[100:103], v1 offset:54272\n"
    "s_waitcnt lgkmcnt(15)\n"
    "v_mfma_f32_16x16x32_bf16 v[96:99], a[36:39], v[160:163], v[96:99]\n"
    "v_mfma_f32_16x16x32_bf16 v[100:103], a[36:39], v[164:167], v[100:103]\n"
    "ds_read_b128 a[104:107], v1 offset:55296\n"
    "v_mfma_f32_16x16x32_bf16 v[104:107], a[40:43], v[160:163], v[104:107]\n"
    "ds_read_b128 a[108:111], v1 offset:56320\n"
    "v_mfma_f32_16x16x32_bf16 v[108:111], a[40:43], v[164:167], v[108:111]\n"
    "s_waitcnt lgkmcnt(15)\n"
    "v_mfma_f32_16x16x32_bf16 v[112:115], a[44:47], v[160:163], v[112:115]\n"
    "v_mfma_f32_16x16x32_bf16 v[116:119], a[44:47], v[164:167], v[116:119]\n"
    "v_mfma_f32_16x16x32_bf16 v[120:123], a[48:51], v[160:163], v[120:123]\n"
    "v_mfma_f32_16x16x32_bf16 v[124:127], a[48:51], v[164:167], v[124:127]\n"
    "s_waitcnt lgkmcnt(14)\n"
    "v_mfma_f32_16x16x32_bf16 v[128:131], a[52:55], v[160:163], v[128:131]\n"
    "v_mfma_f32_16x16x32_bf16 v[132:135], a[52:55], v[164:167], v[132:135]\n"
    "s_waitcnt vmcnt(1) lgkmcnt(13)\n"
    "v_mfma_f32_16x16x32_bf16 v[24:27], a[56:59], v[168:171], v[24:27]\n"
    "s_waitcnt vmcnt(0)\n"
    "v_mfma_f32_16x16x32_bf16 v[28:31], a[56:59], v[172:175], v[28:31]\n"
    "s_waitcnt lgkmcnt(12)\n"
    "v_mfma_f32_16x16x32_bf16 v[32:35], a[60:63], v[168:171], v[32:35]\n"
    "v_mfma_f32_16x16x32_bf16 v[36:39], a[60:63], v[172:175], v[36:39]\n"
    "s_waitcnt lgkmcnt(11)\n"
    "v_mfma_f32_16x16x32_bf16 v[40:43], a[64:67], v[168:171], v[40:43]\n"
    "v_mfma_f32_16x16x32_bf16 v[44:47], a[64:67], v[172:175], v[44:47]\n"
    "s_waitcnt lgkmcnt(10)\n"
    "v_mfma_f32_16x16x32_bf16 v[48:51], a[68:71], v[168:171], v[48:51]\n"
    "v_mfma_f32_16x16x32_bf16 v[52:55], a[68:71], v[172:175], v[52:55]\n"
    "s_waitcnt lgkmcnt(9)\n"
    "v_mfma_f32_16x16x32_bf16 v[56:59], a[72:75], v[168:171], v[56:59]\n"
    "v_mfma_f32_16x16x32_bf16 v[60:63], a[72:75], v[172:175], v[60:63]\n"
    "s_waitcnt lgkmcnt(8)\n"
    "v_mfma_f32_16x16x32_bf16 v[64:67], a[76:79], v[168:171], v[64:67]\n"
    "v_mfma_f32_16x16x32_bf16 v[68:71], a[76:79], v[172:175], v[68:71]\n"
    "s_waitcnt lgkmcnt(7)\n"
    "v_mfma_f32_16x16x32_bf16 v[72:75], a[80:83], v[168:171], v[72:75]\n"
    "v_mfma_f32_16x16x32_bf16 v[76:79], a[80:83], v[172:175], v[76:79]\n"
    "s_waitcnt lgkmcnt(6)\n"
    "v_mfma_f32_16x16x32_bf16 v[80:83], a[84:87], v[168:171], v[80:83]\n"
    "v_mfma_f32_16x16x32_bf16 v[84:87], a[84:87], v[172:175], v[84:87]\n"
    "s_waitcnt lgkmcnt(5)\n"
    "v_mfma_f32_16x16x32_bf16 v[88:91], a[88:91], v[168:171], v[88:91]\n"
    "v_mfma_f32_16x16x32_bf16 v[92:95], a[88:91], v[172:175], v[92:95]\n"
    "s_waitcnt lgkmcnt(4)\n"
    "v_mfma_f32_16x16x32_bf16 v[96:99], a[92:95], v[168:171], v[96:99]\n"
    "v_mfma_f32_16x16x32_bf16 v[100:103], a[92:95], v[172:175], v[100:103]\n"
    "s_waitcnt lgkmcnt(3)\n"
    "v_mfma_f32_16x16x32_bf16 v[104:107], a[96:99], v[168:171], v[104:107]\n"
    "v_mfma_f32_16x16x32_bf16 v[108:111], a[96:99], v[172:175], v[108:111]\n"
    "s_waitcnt lgkmcnt(2)\n"
    "v_mfma_f32_16x16x32_bf16 v[112:115], a[100:103], v[168:171], v[112:115]\n"
    "v_mfma_f32_16x16x32_bf16 v[116:119], a[100:103], v[172:175], v[116:119]\n"
    "s_waitcnt lgkmcnt(1)\n"
    "v_mfma_f32_16x16x32_bf16 v[120:123], a[104:107], v[168:171], v[120:123]\n"
    "v_mfma_f32_16x16x32_bf16 v[124:127], a[104:107], v[172:175], v[124:127]\n"
    "s_waitcnt lgkmcnt(0)\n"
    "v_mfma_f32_16x16x32_bf16 v[128:131], a[108:111], v[168:171], v[128:131]\n"
    "v_mfma_f32_16x16x32_bf16 v[132:135], a[108:111], v[172:175], v[132:135]\n"
    "v_mul_f32 v24, v24, v80\n"
    "v_mul_f32 v25, v25, v81\n"
    "v_mul_f32 v26, v26, v82\n"
    "v_mul_f32 v27, v27, v83\n"
    "v_mul_f32 v28, v28, v84\n"
    "v_mul_f32 v29, v29, v85\n"
    "v_mul_f32 v30, v30, v86\n"
    "v_mul_f32 v31, v31, v87\n"
    "v_mul_f32 v32, v32, v88\n"
    "v_mul_f32 v33, v33, v89\n"
    "v_mul_f32 v34, v34, v90\n"
    "v_mul_f32 v35, v35, v91\n"
    "v_mul_f32 v36, v36, v92\n"
    "v_mul_f32 v37, v37, v93\n"
    "v_mul_f32 v38, v38, v94\n"
    "v_mul_f32 v39, v39, v95\n"
    "v_mul_f32 v40, v40, v96\n"
    "v_mul_f32 v41, v41, v97\n"
    "v_mul_f32 v42, v42, v98\n"
    "v_mul_f32 v43, v43, v99\n"
    "v_mul_f32 v44, v44, v100\n"
    "v_mul_f32 v45, v45, v101\n"
    "v_mul_f32 v46, v46, v102\n"
    "v_mul_f32 v47, v47, v103\n"
    "v_mul_f32 v48, v48, v104\n"
    "v_mul_f32 v49, v49, v105\n"
    "v_mul_f32 v50, v50, v106\n"
    "v_mul_f32 v51, v51, v107\n"
    "v_mul_f32 v52, v52, v108\n"
    "v_mul_f32 v53, v53, v109\n"
    "v_mul_f32 v54, v54, v110\n"
    "v_mul_f32 v55, v55, v111\n"
    "v_mul_f32 v56, v56, v112\n"
    "v_mul_f32 v57, v57, v113\n"
    "v_mul_f32 v58, v58, v114\n"
    "v_mul_f32 v59, v59, v115\n"
    "v_mul_f32 v60, v60, v116\n"
    "v_mul_f32 v61, v61, v117\n"
    "v_mul_f32 v62, v62, v118\n"
    "v_mul_f32 v63, v63, v119\n"
    "v_mul_f32 v64, v64, v120\n"
    "v_mul_f32 v65, v65, v121\n"
    "v_mul_f32 v66, v66, v122\n"
    "v_mul_f32 v67, v67, v123\n"
    "v_mul_f32 v68, v68, v124\n"
    "v_mul_f32 v69, v69, v125\n"
    "v_mul_f32 v70, v70, v126\n"
    "v_mul_f32 v71, v71, v127\n"
    "v_mul_f32 v72, v72, v128\n"
    "v_mul_f32 v73, v73, v129\n"
    "v_mul_f32 v74, v74, v130\n"
    "v_mul_f32 v75, v75, v131\n"
    "v_mul_f32 v76, v76, v132\n"
    "v_mul_f32 v77, v77, v133\n"
    "v_mul_f32 v78, v78, v134\n"
    "v_mul_f32 v79, v79, v135\n"
    "s_lshr_b32 s48, s67, 1\n"
    "s_lshl_b32 s48, s48, 12\n"
    "v_add_u32 v4, 8, v1\n"
    "s_and_b32 s46, s67, 1\n"
    "s_cmp_eq_u32 s46, 1\n"
    "s_cbranch_scc1 Lodd22_%=\n"
    "s_add_u32 s47, s48, 0\n"
    "v_cvt_pk_bf16_f32 v8, v24, v25\n"
    "v_cvt_pk_bf16_f32 v9, v26, v27\n"
    "v_cvt_pk_bf16_f32 v10, v32, v33\n"
    "v_cvt_pk_bf16_f32 v11, v34, v35\n"
    "buffer_store_dwordx4 v[8:11], v1, s[60:63], s47 offen offset:0 sc1\n"
    "v_cvt_pk_bf16_f32 v12, v28, v29\n"
    "v_cvt_pk_bf16_f32 v13, v30, v31\n"
    "v_cvt_pk_bf16_f32 v14, v36, v37\n"
    "v_cvt_pk_bf16_f32 v15, v38, v39\n"
    "buffer_store_dwordx4 v[12:15], v1, s[60:63], s47 offen offset:1024 sc1\n"
    "s_add_u32 s47, s48, 0x1000\n"
    "v_cvt_pk_bf16_f32 v16, v40, v41\n"
    "v_cvt_pk_bf16_f32 v17, v42, v43\n"
    "v_cvt_pk_bf16_f32 v18, v48, v49\n"
    "v_cvt_pk_bf16_f32 v19, v50, v51\n"
    "buffer_store_dwordx4 v[16:19], v1, s[60:63], s47 offen offset:0 sc1\n"
    "v_cvt_pk_bf16_f32 v20, v44, v45\n"
    "v_cvt_pk_bf16_f32 v21, v46, v47\n"
    "v_cvt_pk_bf16_f32 v22, v52, v53\n"
    "v_cvt_pk_bf16_f32 v23, v54, v55\n"
    "buffer_store_dwordx4 v[20:23], v1, s[60:63], s47 offen offset:1024 sc1\n"
    "s_add_u32 s47, s48, 0x2000\n"
    "v_cvt_pk_bf16_f32 v8, v56, v57\n"
    "v_cvt_pk_bf16_f32 v9, v58, v59\n"
    "v_cvt_pk_bf16_f32 v10, v64, v65\n"
    "v_cvt_pk_bf16_f32 v11, v66, v67\n"
    "buffer_store_dwordx4 v[8:11], v1, s[60:63], s47 offen offset:0 sc1\n"
    "v_cvt_pk_bf16_f32 v12, v60, v61\n"
    "v_cvt_pk_bf16_f32 v13, v62, v63\n"
    "v_cvt_pk_bf16_f32 v14, v68, v69\n"
    "v_cvt_pk_bf16_f32 v15, v70, v71\n"
    "buffer_store_dwordx4 v[12:15], v1, s[60:63], s47 offen offset:1024 sc1\n"
    "s_add_u32 s47, s48, 0x3000\n"
    "v_cvt_pk_bf16_f32 v16, v72, v73\n"
    "v_cvt_pk_bf16_f32 v17, v74, v75\n"
    "buffer_store_dwordx2 v[16:17], v1, s[60:63], s47 offen offset:0 sc1\n"
    "v_cvt_pk_bf16_f32 v20, v76, v77\n"
    "v_cvt_pk_bf16_f32 v21, v78, v79\n"
    "buffer_store_dwordx2 v[20:21], v1, s[60:63], s47 offen offset:1024 sc1\n"
    "s_branch Lp4e23_%=\n"
    "Lodd22_%=:\n"
    "s_nop 1\n"
    "s_add_u32 s47, s48, 0\n"
    "v_cvt_pk_bf16_f32 v8, v24, v25\n"
    "v_cvt_pk_bf16_f32 v9, v26, v27\n"
    "buffer_store_dwordx2 v[8:9], v4, s[60:63], s47 offen offset:0 sc1\n"
    "v_cvt_pk_bf16_f32 v12, v28, v29\n"
    "v_cvt_pk_bf16_f32 v13, v30, v31\n"
    "buffer_store_dwordx2 v[12:13], v4, s[60:63], s47 offen offset:1024 sc1\n"
    "s_add_u32 s47, s48, 0x1000\n"
    "v_cvt_pk_bf16_f32 v16, v32, v33\n"
    "v_cvt_pk_bf16_f32 v17, v34, v35\n"
    "v_cvt_pk_bf16_f32 v18, v40, v41\n"
    "v_cvt_pk_bf16_f32 v19, v42, v43\n"
    "buffer_store_dwordx4 v[16:19], v1, s[60:63], s47 offen offset:0 sc1\n"
    "v_cvt_pk_bf16_f32 v20, v36, v37\n"
    "v_cvt_pk_bf16_f32 v21, v38, v39\n"
    "v_cvt_pk_bf16_f32 v22, v44, v45\n"
    "v_cvt_pk_bf16_f32 v23, v46, v47\n"
    "buffer_store_dwordx4 v[20:23], v1, s[60:63], s47 offen offset:1024 sc1\n"
    "s_add_u32 s47, s48, 0x2000\n"
    "v_cvt_pk_bf16_f32 v8, v48, v49\n"
    "v_cvt_pk_bf16_f32 v9, v50, v51\n"
    "v_cvt_pk_bf16_f32 v10, v56, v57\n"
    "v_cvt_pk_bf16_f32 v11, v58, v59\n"
    "buffer_store_dwordx4 v[8:11], v1, s[60:63], s47 offen offset:0 sc1\n"
    "v_cvt_pk_bf16_f32 v12, v52, v53\n"
    "v_cvt_pk_bf16_f32 v13, v54, v55\n"
    "v_cvt_pk_bf16_f32 v14, v60, v61\n"
    "v_cvt_pk_bf16_f32 v15, v62, v63\n"
    "buffer_store_dwordx4 v[12:15], v1, s[60:63], s47 offen offset:1024 sc1\n"
    "s_add_u32 s47, s48, 0x3000\n"
    "v_cvt_pk_bf16_f32 v16, v64, v65\n"
    "v_cvt_pk_bf16_f32 v17, v66, v67\n"
    "v_cvt_pk_bf16_f32 v18, v72, v73\n"
    "v_cvt_pk_bf16_f32 v19, v74, v75\n"
    "buffer_store_dwordx4 v[16:19], v1, s[60:63], s47 offen offset:0 sc1\n"
    "v_cvt_pk_bf16_f32 v20, v68, v69\n"
    "v_cvt_pk_bf16_f32 v21, v70, v71\n"
    "v_cvt_pk_bf16_f32 v22, v76, v77\n"
    "v_cvt_pk_bf16_f32 v23, v78, v79\n"
    "buffer_store_dwordx4 v[20:23], v1, s[60:63], s47 offen offset:1024 sc1\n"
    "Lp4e23_%=:\n"
    "s_lshl_b32 s46, s36, 8\n"
    "s_lshl_b32 s47, s38, 3\n"
    "s_add_u32 s46, s46, s47\n"
    "s_add_u32 s46, s46, 0x5000\n"
    "v_mov_b32 v8, s40\n"
    "v_mov_b32 v9, s41\n"
    "v_mov_b32 v10, s46\n"
    "s_waitcnt vmcnt(0)\n"
    "s_barrier\n"
    "s_mov_b64 exec, 1\n"
    "global_store_dwordx2 v10, v[8:9], s[26:27] offset:0 sc1\n"
    "s_mov_b64 exec, -1\n"
    "s_lshl_b32 s66, s36, 1\n"
    "s_lshr_b32 s46, s38, 3\n"
    "s_add_u32 s66, s66, s46\n"
    "s_and_b32 s68, s38, 7\n"
    "s_mul_i32 s46, s66, 0x36000\n"
    "s_add_u32 s46, s46, 0x17b0000\n"
    "s_add_u32 s52, s26, s46\n"
    "s_addc_u32 s53, s27, 0\n"
    "s_mov_b32 s54, 0x36000\n"
    "s_mov_b32 s55, 0x20000\n"
    "s_mul_i32 s46, s68, 0x1400\n"
    "s_sub_u32 s47, 0x21c000, s46\n"
    "s_add_u32 s46, s46, 0x3500000\n"
    "s_add_u32 s56, s26, s46\n"
    "s_addc_u32 s57, s27, 0\n"
    "s_mov_b32 s58, s47\n"
    "s_mov_b32 s59, 0x20000\n"
    "s_lshl_b32 s47, s66, 2\n"
    "s_add_u32 s47, s47, s34\n"
    "s_mul_i32 s47, s47, 0x5000\n"
    "s_add_u32 s96, s24, s47\n"
    "s_addc_u32 s97, s25, 0\n"
    "s_mov_b32 s98, 0x5000\n"
    "s_mov_b32 s99, 0x20000\n"
    "s_add_u32 s47, s47, 0x12b0000\n"
    "s_add_u32 s60, s26, s47\n"
    "s_addc_u32 s61, s27, 0\n"
    "s_mov_b32 s62, 0x5000\n"
    "s_mov_b32 s63, 0x20000\n"
    "s_add_u32 s92, s26, 0x11900\n"
    "s_addc_u32 s93, s27, 0\n"
    "s_mov_b32 s94, 0x500\n"
    "s_mov_b32 s95, 0x20000\n"
    "s_mul_i32 s46, s68, 5\n"
    "s_add_u32 s46, s46, 0\n"
    "s_lshr_b32 s47, s46, 1\n"
    "s_lshl_b32 s47, s47, 6\n"
    "s_and_b32 s46, s46, 1\n"
    "s_lshl_b32 s46, s46, 3\n"
    "s_add_u32 s70, s46, s47\n"
    "s_mul_i32 s46, s68, 5\n"
    "s_add_u32 s46, s46, 1\n"
    "s_lshr_b32 s47, s46, 1\n"
    "s_lshl_b32 s47, s47, 6\n"
    "s_and_b32 s46, s46, 1\n"
    "s_lshl_b32 s46, s46, 3\n"
    "s_add_u32 s71, s46, s47\n"
    "s_mul_i32 s46, s68, 5\n"
    "s_add_u32 s46, s46, 2\n"
    "s_lshr_b32 s47, s46, 1\n"
    "s_lshl_b32 s47, s47, 6\n"
    "s_and_b32 s46, s46, 1\n"
    "s_lshl_b32 s46, s46, 3\n"
    "s_add_u32 s72, s46, s47\n"
    "s_mul_i32 s46, s68, 5\n"
    "s_add_u32 s46, s46, 3\n"
    "s_lshr_b32 s47, s46, 1\n"
    "s_lshl_b32 s47, s47, 6\n"
    "s_and_b32 s46, s46, 1\n"
    "s_lshl_b32 s46, s46, 3\n"
    "s_add_u32 s73, s46, s47\n"
    "s_mul_i32 s46, s68, 5\n"
    "s_add_u32 s46, s46, 4\n"
    "s_lshr_b32 s47, s46, 1\n"
    "s_lshl_b32 s47, s47, 6\n"
    "s_and_b32 s46, s46, 1\n"
    "s_lshl_b32 s46, s46, 3\n"
    "s_add_u32 s74, s46, s47\n"
    "v_lshrrev_b32 v8, 4, v1\n"
    "v_add_u32 v2, 0x1000, v1\n"
    "v_lshrrev_b32 v9, 4, v8\n"
    "v_and_b32 v10, 15, v8\n"
    "v_mul_u32_u24 v10, 0x500, v10\n"
    "v_lshl_add_u32 v4, v9, 4, v10\n"
    "v_lshlrev_b32 v5, 4, v9\n"
    "s_mul_i32 s46, s34, 0x5000\n"
    "v_add_u32 v6, s46, v1\n"
    "s_mul_i32 s46, s34, 0x1400\n"
    "v_add_u32 v7, s46, v1\n"
    "s_mul_i32 s64, s34, 0xa000\n"
    "s_add_u32 s64, s64, 0\n"
    "buffer_load_dwordx4 a[0:3], v1, s[56:59], s64 offen offset:0 sc1\n"
    "buffer_load_dwordx4 a[4:7], v1, s[56:59], s64 offen offset:1024 sc1\n"
    "buffer_load_dwordx4 a[8:11], v1, s[56:59], s64 offen offset:2048 sc1\n"
    "buffer_load_dwordx4 a[12:15], v1, s[56:59], s64 offen offset:3072 sc1\n"
    "buffer_load_dwordx4 a[16:19], v2, s[56:59], s64 offen offset:0 sc1\n"
    "s_mul_i32 s64, s34, 0xa000\n"
    "s_add_u32 s64, s64, 0x28000\n"
    "buffer_load_dwordx4 a[20:23], v1, s[56:59], s64 offen offset:0 sc1\n"
    "buffer_load_dwordx4 a[24:27], v1, s[56:59], s64 offen offset:1024 sc1\n"
    "buffer_load_dwordx4 a[28:31], v1, s[56:59], s64 offen offset:2048 sc1\n"
    "buffer_load_dwordx4 a[32:35], v1, s[56:59], s64 offen offset:3072 sc1\n"
    "buffer_load_dwordx4 a[36:39], v2, s[56:59], s64 offen offset:0 sc1\n"
    "s_mul_i32 s64, s34, 0xa000\n"
    "s_add_u32 s64, s64, 0x50000\n"
    "buffer_load_dwordx4 a[40:43], v1, s[56:59], s64 offen offset:0 sc1\n"
    "buffer_load_dwordx4 a[44:47], v1, s[56:59], s64 offen offset:1024 sc1\n"
    "buffer_load_dwordx4 a[48:51], v1, s[56:59], s64 offen offset:2048 sc1\n"
    "buffer_load_dwordx4 a[52:55], v1, s[56:59], s64 offen offset:3072 sc1\n"
    "buffer_load_dwordx4 a[56:59], v2, s[56:59], s64 offen offset:0 sc1\n"
    "s_mul_i32 s64, s34, 0xa000\n"
    "s_add_u32 s64, s64, 0x78000\n"
    "buffer_load_dwordx4 a[60:63], v1, s[56:59], s64 offen offset:0 sc1\n"
    "buffer_load_dwordx4 a[64:67], v1, s[56:59], s64 offen offset:1024 sc1\n"
    "buffer_load_dwordx4 a[68:71], v1, s[56:59], s64 offen offset:2048 sc1\n"
    "buffer_load_dwordx4 a[72:75], v1, s[56:59], s64 offen offset:3072 sc1\n"
    "buffer_load_dwordx4 a[76:79], v2, s[56:59], s64 offen offset:0 sc1\n"
    "s_lshl_b32 s48, s36, 8\n"
    "s_add_u32 s48, s48, 0x5000\n"
    "s_cmp_eq_u32 s34, 0\n"
    "s_cbranch_scc0 Lpollskip26_%=\n"
    "v_lshrrev_b32 v10, 4, v1\n"
    "v_min_u32 v10, 15, v10\n"
    "v_lshlrev_b32 v10, 3, v10\n"
    "v_add_u32 v10, s48, v10\n"
    "s_mov_b32 s44, 0\n"
    "Lpoll24_%=:\n"
    "global_load_dwordx2 v[8:9], v10, s[26:27] sc1\n"
    "s_waitcnt vmcnt(0)\n"
    "v_cmp_ne_u64 vcc, s[40:41], v[8:9]\n"
    "s_nop 4\n"
    "s_mov_b64 s[50:51], vcc\n"
    "s_cmp_eq_u64 s[50:51], 0\n"
    "s_cbranch_scc1 Lpollok25_%=\n"
    "s_add_u32 s44, s44, 1\n"
    "s_cmp_ge_u32 s44, 32768\n"
    "s_cbranch_scc1 Lpollok25_%=\n"
    "s_branch Lpoll24_%=\n"
    "Lpollok25_%=:\n"
    "Lpollskip26_%=:\n"
    "s_barrier\n"
    "; ==== P5: out = x1 + gb * (hh @ w_down), 64 tokens x 80 features, K split over the waves\n"
    "s_lshl_b32 s65, s34, 12\n"
    "s_add_u32 s65, s65, 0\n"
    "buffer_load_dwordx4 v[104:107], v1, s[52:55], s65 offen offset:0 sc1\n"
    "buffer_load_dwordx4 v[108:111], v1, s[52:55], s65 offen offset:1024 sc1\n"
    "buffer_load_dwordx4 v[112:115], v1, s[52:55], s65 offen offset:2048 sc1\n"
    "buffer_load_dwordx4 v[116:119], v1, s[52:55], s65 offen offset:3072 sc1\n"
    "s_lshl_b32 s65, s34, 12\n"
    "s_add_u32 s65, s65, 0x4000\n"
    "buffer_load_dwordx4 v[120:123], v1, s[52:55], s65 offen offset:0 sc1\n"
    "buffer_load_dwordx4 v[124:127], v1, s[52:55], s65 offen offset:1024 sc1\n"
    "buffer_load_dwordx4 v[128:131], v1, s[52:55], s65 offen offset:2048 sc1\n"
    "buffer_load_dwordx4 v[132:135], v1, s[52:55], s65 offen offset:3072 sc1\n"
    "s_lshl_b32 s65, s34, 12\n"
    "s_add_u32 s65, s65, 0x8000\n"
    "buffer_load_dwordx4 v[136:139], v1, s[52:55], s65 offen offset:0 sc1\n"
    "buffer_load_dwordx4 v[140:143], v1, s[52:55], s65 offen offset:1024 sc1\n"
    "buffer_load_dwordx4 v[144:147], v1, s[52:55], s65 offen offset:2048 sc1\n"
    "buffer_load_dwordx4 v[148:151], v1, s[52:55], s65 offen offset:3072 sc1\n"
    "s_lshl_b32 s65, s34, 12\n"
    "s_add_u32 s65, s65, 0xc000\n"
    "buffer_load_dwordx4 v[152:155], v1, s[52:55], s65 offen offset:0 sc1\n"
    "buffer_load_dwordx4 v[156:159], v1, s[52:55], s65 offen offset:1024 sc1\n"
    "buffer_load_dwordx4 v[160:163], v1, s[52:55], s65 offen offset:2048 sc1\n"
    "buffer_load_dwordx4 v[164:167], v1, s[52:55], s65 offen offset:3072 sc1\n"
    "s_waitcnt vmcnt(15)\n"
    "v_mfma_f32_16x16x32_bf16 v[24:27], a[0:3], v[104:107], 0\n"
    "s_waitcnt vmcnt(14)\n"
    "v_mfma_f32_16x16x32_bf16 v[28:31], a[0:3], v[108:111], 0\n"
    "s_lshl_b32 s65, s34, 12\n"
    "s_add_u32 s65, s65, 0x10000\n"
    "buffer_load_dwordx4 v[168:171], v1, s[52:55], s65 offen offset:0 sc1\n"
    "s_waitcnt vmcnt(14)\n"
    "v_mfma_f32_16x16x32_bf16 v[32:35], a[0:3], v[112:115], 0\n"
    "s_waitcnt vmcnt(13)\n"
    "v_mfma_f32_16x16x32_bf16 v[36:39], a[0:3], v[116:119], 0\n"
    "buffer_load_dwordx4 v[172:175], v1, s[52:55], s65 offen offset:1024 sc1\n"
    "v_mfma_f32_16x16x32_bf16 v[40:43], a[4:7], v[104:107], 0\n"
    "v_mfma_f32_16x16x32_bf16 v[44:47], a[4:7], v[108:111], 0\n"
    "buffer_load_dwordx4 v[176:179], v1, s[52:55], s65 offen offset:2048 sc1\n"
    "v_mfma_f32_16x16x32_bf16 v[48:51], a[4:7], v[112:115], 0\n"
    "v_mfma_f32_16x16x32_bf16 v[52:55], a[4:7], v[116:119], 0\n"
    "buffer_load_dwordx4 v[180:183], v1, s[52:55], s65 offen offset:3072 sc1\n"
    "v_mfma_f32_16x16x32_bf16 v[56:59], a[8:11], v[104:107], 0\n"
    "v_mfma_f32_16x16x32_bf16 v[60:63], a[8:11], v[108:111], 0\n"
    "s_mul_i32 s64, s34, 0xa000\n"
    "s_add_u32 s64, s64, 0xa0000\n"
    "buffer_load_dwordx4 a[80:83], v1, s[56:59], s64 offen offset:0 sc1\n"
    "v_mfma_f32_16x16x32_bf16 v[64:67], a[8:11], v[112:115], 0\n"
    "v_mfma_f32_16x16x32_bf16 v[68:71], a[8:11], v[116:119], 0\n"
    "buffer_load_dwordx4 a[84:87], v1, s[56:59], s64 offen offset:1024 sc1\n"
    "v_mfma_f32_16x16x32_bf16 v[72:75], a[12:15], v[104:107], 0\n"
    "v_mfma_f32_16x16x32_bf16 v[76:79], a[12:15], v[108:111], 0\n"
    "buffer_load_dwordx4 a[88:91], v1, s[56:59], s64 offen offset:2048 sc1\n"
    "v_mfma_f32_16x16x32_bf16 v[80:83], a[12:15], v[112:115], 0\n"
    "v_mfma_f32_16x16x32_bf16 v[84:87], a[12:15], v[116:119], 0\n"
    "buffer_load_dwordx4 a[92:95], v1, s[56:59], s64 offen offset:3072 sc1\n"
    "v_mfma_f32_16x16x32_bf16 v[88:91], a[16:19], v[104:107], 0\n"
    "v_mfma_f32_16x16x32_bf16 v[92:95], a[16:19], v[108:111], 0\n"
    "buffer_load_dwordx4 a[96:99], v2, s[56:59], s64 offen offset:0 sc1\n"
    "v_mfma_f32_16x16x32_bf16 v[96:99], a[16:19], v[112:115], 0\n"
    "v_mfma_f32_16x16x32_bf16 v[100:103], a[16:19], v[116:119], 0\n"
    "s_waitcnt vmcnt(20)\n"
    "v_mfma_f32_16x16x32_bf16 v[24:27], a[20:23], v[120:123], v[24:27]\n"
    "s_waitcnt vmcnt(19)\n"
    "v_mfma_f32_16x16x32_bf16 v[28:31], a[20:23], v[124:127], v[28:31]\n"
    "s_lshl_b32 s65, s34, 12\n"
    "s_add_u32 s65, s65, 0x14000\n"
    "buffer_load_dwordx4 v[104:107], v1, s[52:55], s65 offen offset:0 sc1\n"
    "s_waitcnt vmcnt(19)\n"
    "v_mfma_f32_16x16x32_bf16 v[32:35], a[20:23], v[128:131], v[32:35]\n"
    "s_waitcnt vmcnt(18)\n"
    "v_mfma_f32_16x16x32_bf16 v[36:39], a[20:23], v[132:135], v[36:39]\n"
    "buffer_load_dwordx4 v[108:111], v1, s[52:55], s65 offen offset:1024 sc1\n"
    "v_mfma_f32_16x16x32_bf16 v[40:43], a[24:27], v[120:123], v[40:43]\n"
    "v_mfma_f32_16x16x32_bf16 v[44:47], a[24:27], v[124:127], v[44:47]\n"
    "buffer_load_dwordx4 v[112:115], v1, s[52:55], s65 offen offset:2048 sc1\n"
    "v_mfma_f32_16x16x32_bf16 v[48:51], a[24:27], v[128:131], v[48:51]\n"
    "v_mfma_f32_16x16x32_bf16 v[52:55], a[24:27], v[132:135], v[52:55]\n"
    "buffer_load_dwordx4 v[116:119], v1, s[52:55], s65 offen offset:3072 sc1\n"
    "v_mfma_f32_16x16x32_bf16 v[56:59], a[28:31], v[120:123], v[56:59]\n"
    "v_mfma_f32_16x16x32_bf16 v[60:63], a[28:31], v[124:127], v[60:63]\n"
    "s_mul_i32 s64, s34, 0xa000\n"
    "s_add_u32 s64, s64, 0xc8000\n"
    "buffer_load_dwordx4 a[100:103], v1, s[56:59], s64 offen offset:0 sc1\n"
    "v_mfma_f32_16x16x32_bf16 v[64:67], a[28:31], v[128:131], v[64:67]\n"
    "v_mfma_f32_16x16x32_bf16 v[68:71], a[28:31], v[132:135], v[68:71]\n"
    "buffer_load_dwordx4 a[104:107], v1, s[56:59], s64 offen offset:1024 sc1\n"
    "v_mfma_f32_16x16x32_bf16 v[72:75], a[32:35], v[120:123], v[72:75]\n"
    "v_mfma_f32_16x16x32_bf16 v[76:79], a[32:35], v[124:127], v[76:79]\n"
    "buffer_load_dwordx4 a[108:111], v1, s[56:59], s64 offen offset:2048 sc1\n"
    "v_mfma_f32_16x16x32_bf16 v[80:83], a[32:35], v[128:131], v[80:83]\n"
    "v_mfma_f32_16x16x32_bf16 v[84:87], a[32:35], v[132:135], v[84:87]\n"
    "buffer_load_dwordx4 a[112:115], v1, s[56:59], s64 offen offset:3072 sc1\n"
    "v_mfma_f32_16x16x32_bf16 v[88:91], a[36:39], v[120:123], v[88:91]\n"
    "v_mfma_f32_16x16x32_bf16 v[92:95], a[36:39], v[124:127], v[92:95]\n"
    "buffer_load_dwordx4 a[116:119], v2, s[56:59], s64 offen offset:0 sc1\n"
    "v_mfma_f32_16x16x32_bf16 v[96:99], a[36:39], v[128:131], v[96:99]\n"
    "v_mfma_f32_16x16x32_bf16 v[100:103], a[36:39], v[132:135], v[100:103]\n"
    "s_waitcnt vmcnt(25)\n"
    "v_mfma_f32_16x16x32_bf16 v[24:27], a[40:43], v[136:139], v[24:27]\n"
    "s_waitcnt vmcnt(24)\n"
    "v_mfma_f32_16x16x32_bf16 v[28:31], a[40:43], v[140:143], v[28:31]\n"
    "s_lshl_b32 s65, s34, 12\n"
    "s_add_u32 s65, s65, 0x18000\n"
    "buffer_load_dwordx4 v[120:123], v1, s[52:55], s65 offen offset:0 sc1\n"
    "s_waitcnt vmcnt(24)\n"
    "v_mfma_f32_16x16x32_bf16 v[32:35], a[40:43], v[144:147], v[32:35]\n"
    "s_waitcnt vmcnt(23)\n"
    "v_mfma_f32_16x16x32_bf16 v[36:39], a[40:43], v[148:151], v[36:39]\n"
    "buffer_load_dwordx4 v[124:127], v1, s[52:55], s65 offen offset:1024 sc1\n"
    "v_mfma_f32_16x16x32_bf16 v[40:43], a[44:47], v[136:139], v[40:43]\n"
    "v_mfma_f32_16x16x32_bf16 v[44:47], a[44:47], v[140:143], v[44:47]\n"
    "buffer_load_dwordx4 v[128:131], v1, s[52:55], s65 offen offset:2048 sc1\n"
    "v_mfma_f32_16x16x32_bf16 v[48:51], a[44:47], v[144:147], v[48:51]\n"
    "v_mfma_f32_16x16x32_bf16 v[52:55], a[44:47], v[148:151], v[52:55]\n"
    "buffer_load_dwordx4 v[132:135], v1, s[52:55], s65 offen offset:3072 sc1\n"
    "v_mfma_f32_16x16x32_bf16 v[56:59], a[48:51], v[136:139], v[56:59]\n"
    "v_mfma_f32_16x16x32_bf16 v[60:63], a[48:51], v[140:143], v[60:63]\n"
    "s_mul_i32 s64, s34, 0xa000\n"
    "s_add_u32 s64, s64, 0xf0000\n"
    "buffer_load_dwordx4 a[120:123], v1, s[56:59], s64 offen offset:0 sc1\n"
    "v_mfma_f32_16x16x32_bf16 v[64:67], a[48:51], v[144:147], v[64:67]\n"
    "v_mfma_f32_16x16x32_bf16 v[68:71], a[48:51], v[148:151], v[68:71]\n"
    "buffer_load_dwordx4 a[124:127], v1, s[56:59], s64 offen offset:1024 sc1\n"
    "v_mfma_f32_16x16x32_bf16 v[72:75], a[52:55], v[136:139], v[72:75]\n"
    "v_mfma_f32_16x16x32_bf16 v[76:79], a[52:55], v[140:143], v[76:79]\n"
    "buffer_load_dwordx4 a[128:131], v1, s[56:59], s64 offen offset:2048 sc1\n"
    "v_mfma_f32_16x16x32_bf16 v[80:83], a[52:55], v[144:147], v[80:83]\n"
    "v_mfma_f32_16x16x32_bf16 v[84:87], a[52:55], v[148:151], v[84:87]\n"
    "buffer_load_dwordx4 a[132:135], v1, s[56:59], s64 offen offset:3072 sc1\n"
    "v_mfma_f32_16x16x32_bf16 v[88:91], a[56:59], v[136:139], v[88:91]\n"
    "v_mfma_f32_16x16x32_bf16 v[92:95], a[56:59], v[140:143], v[92:95]\n"
    "buffer_load_dwordx4 a[136:139], v2, s[56:59], s64 offen offset:0 sc1\n"
    "v_mfma_f32_16x16x32_bf16 v[96:99], a[56:59], v[144:147], v[96:99]\n"
    "v_mfma_f32_16x16x32_bf16 v[100:103], a[56:59], v[148:151], v[100:103]\n"
    "s_waitcnt vmcnt(30)\n"
    "v_mfma_f32_16x16x32_bf16 v[24:27], a[60:63], v[152:155], v[24:27]\n"
    "s_waitcnt vmcnt(29)\n"
    "v_mfma_f32_16x16x32_bf16 v[28:31], a[60:63], v[156:159], v[28:31]\n"
    "s_lshl_b32 s65, s34, 12\n"
    "s_add_u32 s65, s65, 0x1c000\n"
    "buffer_load_dwordx4 v[136:139], v1, s[52:55], s65 offen offset:0 sc1\n"
    "s_waitcnt vmcnt(29)\n"
    "v_mfma_f32_16x16x32_bf16 v[32:35], a[60:63], v[160:163], v[32:35]\n"
    "s_waitcnt vmcnt(28)\n"
    "v_mfma_f32_16x16x32_bf16 v[36:39], a[60:63], v[164:167], v[36:39]\n"
    "buffer_load_dwordx4 v[140:143], v1, s[52:55], s65 offen offset:1024 sc1\n"
    "v_mfma_f32_16x16x32_bf16 v[40:43], a[64:67], v[152:155], v[40:43]\n"
    "v_mfma_f32_16x16x32_bf16 v[44:47], a[64:67], v[156:159], v[44:47]\n"
    "buffer_load_dwordx4 v[144:147], v1, s[52:55], s65 offen offset:2048 sc1\n"
    "v_mfma_f32_16x16x32_bf16 v[48:51], a[64:67], v[160:163], v[48:51]\n"
    "v_mfma_f32_16x16x32_bf16 v[52:55], a[64:67], v[164:167], v[52:55]\n"
    "buffer_load_dwordx4 v[148:151], v1, s[52:55], s65 offen offset:3072 sc1\n"
    "v_mfma_f32_16x16x32_bf16 v[56:59], a[68:71], v[152:155], v[56:59]\n"
    "v_mfma_f32_16x16x32_bf16 v[60:63], a[68:71], v[156:159], v[60:63]\n"
    "s_mul_i32 s64, s34, 0xa000\n"
    "s_add_u32 s64, s64, 0x118000\n"
    "buffer_load_dwordx4 a[140:143], v1, s[56:59], s64 offen offset:0 sc1\n"
    "v_mfma_f32_16x16x32_bf16 v[64:67], a[68:71], v[160:163], v[64:67]\n"
    "v_mfma_f32_16x16x32_bf16 v[68:71], a[68:71], v[164:167], v[68:71]\n"
    "buffer_load_dwordx4 a[144:147], v1, s[56:59], s64 offen offset:1024 sc1\n"
    "v_mfma_f32_16x16x32_bf16 v[72:75], a[72:75], v[152:155], v[72:75]\n"
    "v_mfma_f32_16x16x32_bf16 v[76:79], a[72:75], v[156:159], v[76:79]\n"
    "buffer_load_dwordx4 a[148:151], v1, s[56:59], s64 offen offset:2048 sc1\n"
    "v_mfma_f32_16x16x32_bf16 v[80:83], a[72:75], v[160:163], v[80:83]\n"
    "v_mfma_f32_16x16x32_bf16 v[84:87], a[72:75], v[164:167], v[84:87]\n"
    "buffer_load_dwordx4 a[152:155], v1, s[56:59], s64 offen offset:3072 sc1\n"
    "v_mfma_f32_16x16x32_bf16 v[88:91], a[76:79], v[152:155], v[88:91]\n"
    "v_mfma_f32_16x16x32_bf16 v[92:95], a[76:79], v[156:159], v[92:95]\n"
    "buffer_load_dwordx4 a[156:159], v2, s[56:59], s64 offen offset:0 sc1\n"
    "v_mfma_f32_16x16x32_bf16 v[96:99], a[76:79], v[160:163], v[96:99]\n"
    "v_mfma_f32_16x16x32_bf16 v[100:103], a[76:79], v[164:167], v[100:103]\n"
    "s_waitcnt vmcnt(31)\n"
    "v_mfma_f32_16x16x32_bf16 v[24:27], a[80:83], v[168:171], v[24:27]\n"
    "v_mfma_f32_16x16x32_bf16 v[28:31], a[80:83], v[172:175], v[28:31]\n"
    "s_lshl_b32 s65, s34, 12\n"
    "s_add_u32 s65, s65, 0x20000\n"
    "buffer_load_dwordx4 v[152:155], v1, s[52:55], s65 offen offset:0 sc1\n"
    "v_mfma_f32_16x16x32_bf16 v[32:35], a[80:83], v[176:179], v[32:35]\n"
    "v_mfma_f32_16x16x32_bf16 v[36:39], a[80:83], v[180:183], v[36:39]\n"
    "buffer_load_dwordx4 v[156:159], v1, s[52:55], s65 offen offset:1024 sc1\n"
    "s_waitcnt vmcnt(32)\n"
    "v_mfma_f32_16x16x32_bf16 v[40:43], a[84:87], v[168:171], v[40:43]\n"
    "v_mfma_f32_16x16x32_bf16 v[44:47], a[84:87], v[172:175], v[44:47]\n"
    "buffer_load_dwordx4 v[160:163], v1, s[52:55], s65 offen offset:2048 sc1\n"
    "v_mfma_f32_16x16x32_bf16 v[48:51], a[84:87], v[176:179], v[48:51]\n"
    "v_mfma_f32_16x16x32_bf16 v[52:55], a[84:87], v[180:183], v[52:55]\n"
    "buffer_load_dwordx4 v[164:167], v1, s[52:55], s65 offen offset:3072 sc1\n"
    "s_waitcnt vmcnt(33)\n"
    "v_mfma_f32_16x16x32_bf16 v[56:59], a[88:91], v[168:171], v[56:59]\n"
    "v_mfma_f32_16x16x32_bf16 v[60:63], a[88:91], v[172:175], v[60:63]\n"
    "s_mul_i32 s64, s34, 0xa000\n"
    "s_add_u32 s64, s64, 0x140000\n"
    "buffer_load_dwordx4 a[160:163], v1, s[56:59], s64 offen offset:0 sc1\n"
    "v_mfma_f32_16x16x32_bf16 v[64:67], a[88:91], v[176:179], v[64:67]\n"
    "v_mfma_f32_16x16x32_bf16 v[68:71], a[88:91], v[180:183], v[68:71]\n"
    "buffer_load_dwordx4 a[164:167], v1, s[56:59], s64 offen offset:1024 sc1\n"
    "s_waitcnt vmcnt(34)\n"
    "v_mfma_f32_16x16x32_bf16 v[72:75], a[92:95], v[168:171], v[72:75]\n"
    "v_mfma_f32_16x16x32_bf16 v[76:79], a[92:95], v[172:175], v[76:79]\n"
    "buffer_load_dwordx4 a[168:171], v1, s[56:59], s64 offen offset:2048 sc1\n"
    "v_mfma_f32_16x16x32_bf16 v[80:83], a[92:95], v[176:179], v[80:83]\n"
    "v_mfma_f32_16x16x32_bf16 v[84:87], a[92:95], v[180:183], v[84:87]\n"
    "buffer_load_dwordx4 a[172:175], v1, s[56:59], s64 offen offset:3072 sc1\n"
    "s_waitcnt vmcnt(35)\n"
    "v_mfma_f32_16x16x32_bf16 v[88:91], a[96:99], v[168:171], v[88:91]\n"
    "v_mfma_f32_16x16x32_bf16 v[92:95], a[96:99], v[172:175], v[92:95]\n"
    "buffer_load_dwordx4 a[176:179], v2, s[56:59], s64 offen offset:0 sc1\n"
    "v_mfma_f32_16x16x32_bf16 v[96:99], a[96:99], v[176:179], v[96:99]\n"
    "v_mfma_f32_16x16x32_bf16 v[100:103], a[96:99], v[180:183], v[100:103]\n"
    "s_waitcnt vmcnt(31)\n"
    "v_mfma_f32_16x16x32_bf16 v[24:27], a[100:103], v[104:107], v[24:27]\n"
    "v_mfma_f32_16x16x32_bf16 v[28:31], a[100:103], v[108:111], v[28:31]\n"
    "s_lshl_b32 s65, s34, 12\n"
    "s_add_u32 s65, s65, 0x24000\n"
    "buffer_load_dwordx4 v[168:171], v1, s[52:55], s65 offen offset:0 sc1\n"
    "v_mfma_f32_16x16x32_bf16 v[32:35], a[100:103], v[112:115], v[32:35]\n"
    "v_mfma_f32_16x16x32_bf16 v[36:39], a[100:103], v[116:119], v[36:39]\n"
    "buffer_load_dwordx4 v[172:175], v1, s[52:55], s65 offen offset:1024 sc1\n"
    "s_waitcnt vmcnt(32)\n"
    "v_mfma_f32_16x16x32_bf16 v[40:43], a[104:107], v[104:107], v[40:43]\n"
    "v_mfma_f32_16x16x32_bf16 v[44:47], a[104:107], v[108:111], v[44:47]\n"
    "buffer_load_dwordx4 v[176:179], v1, s[52:55], s65 offen offset:2048 sc1\n"
    "v_mfma_f32_16x16x32_bf16 v[48:51], a[104:107], v[112:115], v[48:51]\n"
    "v_mfma_f32_16x16x32_bf16 v[52:55], a[104:107], v[116:119], v[52:55]\n"
    "buffer_load_dwordx4 v[180:183], v1, s[52:55], s65 offen offset:3072 sc1\n"
    "s_waitcnt vmcnt(33)\n"
    "v_mfma_f32_16x16x32_bf16 v[56:59], a[108:111], v[104:107], v[56:59]\n"
    "v_mfma_f32_16x16x32_bf16 v[60:63], a[108:111], v[108:111], v[60:63]\n"
    "s_mul_i32 s64, s34, 0xa000\n"
    "s_add_u32 s64, s64, 0x168000\n"
    "buffer_load_dwordx4 a[180:183], v1, s[56:59], s64 offen offset:0 sc1\n"
    "v_mfma_f32_16x16x32_bf16 v[64:67], a[108:111], v[112:115], v[64:67]\n"
    "v_mfma_f32_16x16x32_bf16 v[68:71], a[108:111], v[116:119], v[68:71]\n"
    "buffer_load_dwordx4 a[184:187], v1, s[56:59], s64 offen offset:1024 sc1\n"
    "s_waitcnt vmcnt(34)\n"
    "v_mfma_f32_16x16x32_bf16 v[72:75], a[112:115], v[104:107], v[72:75]\n"
    "v_mfma_f32_16x16x32_bf16 v[76:79], a[112:115], v[108:111], v[76:79]\n"
    "buffer_load_dwordx4 a[188:191], v1, s[56:59], s64 offen offset:2048 sc1\n"
    "v_mfma_f32_16x16x32_bf16 v[80:83], a[112:115], v[112:115], v[80:83]\n"
    "v_mfma_f32_16x16x32_bf16 v[84:87], a[112:115], v[116:119], v[84:87]\n"
    "buffer_load_dwordx4 a[192:195], v1, s[56:59], s64 offen offset:3072 sc1\n"
    "s_waitcnt vmcnt(35)\n"
    "v_mfma_f32_16x16x32_bf16 v[88:91], a[116:119], v[104:107], v[88:91]\n"
    "v_mfma_f32_16x16x32_bf16 v[92:95], a[116:119], v[108:111], v[92:95]\n"
    "buffer_load_dwordx4 a[196:199], v2, s[56:59], s64 offen offset:0 sc1\n"
    "v_mfma_f32_16x16x32_bf16 v[96:99], a[116:119], v[112:115], v[96:99]\n"
    "v_mfma_f32_16x16x32_bf16 v[100:103], a[116:119], v[116:119], v[100:103]\n"
    "s_waitcnt vmcnt(31)\n"
    "v_mfma_f32_16x16x32_bf16 v[24:27], a[120:123], v[120:123], v[24:27]\n"
    "v_mfma_f32_16x16x32_bf16 v[28:31], a[120:123], v[124:127], v[28:31]\n"
    "s_lshl_b32 s65, s34, 12\n"
    "s_add_u32 s65, s65, 0x28000\n"
    "buffer_load_dwordx4 v[104:107], v1, s[52:55], s65 offen offset:0 sc1\n"
    "v_mfma_f32_16x16x32_bf16 v[32:35], a[120:123], v[128:131], v[32:35]\n"
    "v_mfma_f32_16x16x32_bf16 v[36:39], a[120:123], v[132:135], v[36:39]\n"
    "buffer_load_dwordx4 v[108:111], v1, s[52:55], s65 offen offset:1024 sc1\n"
    "s_waitcnt vmcnt(32)\n"
    "v_mfma_f32_16x16x32_bf16 v[40:43], a[124:127], v[120:123], v[40:43]\n"
    "v_mfma_f32_16x16x32_bf16 v[44:47], a[124:127], v[124:127], v[44:47]\n"
    "buffer_load_dwordx4 v[112:115], v1, s[52:55], s65 offen offset:2048 sc1\n"
    "v_mfma_f32_16x16x32_bf16 v[48:51], a[124:127], v[128:131], v[48:51]\n"
    "v_mfma_f32_16x16x32_bf16 v[52:55], a[124:127], v[132:135], v[52:55]\n"
    "buffer_load_dwordx4 v[116:119], v1, s[52:55], s65 offen offset:3072 sc1\n"
    "s_waitcnt vmcnt(33)\n"
    "v_mfma_f32_16x16x32_bf16 v[56:59], a[128:131], v[120:123], v[56:59]\n"
    "v_mfma_f32_16x16x32_bf16 v[60:63], a[128:131], v[124:127], v[60:63]\n"
    "s_mul_i32 s64, s34, 0xa000\n"
    "s_add_u32 s64, s64, 0x190000\n"
    "buffer_load_dwordx4 a[200:203], v1, s[56:59], s64 offen offset:0 sc1\n"
    "v_mfma_f32_16x16x32_bf16 v[64:67], a[128:131], v[128:131], v[64:67]\n"
    "v_mfma_f32_16x16x32_bf16 v[68:71], a[128:131], v[132:135], v[68:71]\n"
    "buffer_load_dwordx4 a[204:207], v1, s[56:59], s64 offen offset:1024 sc1\n"
    "s_waitcnt vmcnt(34)\n"
    "v_mfma_f32_16x16x32_bf16 v[72:75], a[132:135], v[120:123], v[72:75]\n"
    "v_mfma_f32_16x16x32_bf16 v[76:79], a[132:135], v[124:127], v[76:79]\n"
    "buffer_load_dwordx4 a[208:211], v1, s[56:59], s64 offen offset:2048 sc1\n"
    "v_mfma_f32_16x16x32_bf16 v[80:83], a[132:135], v[128:131], v[80:83]\n"
    "v_mfma_f32_16x16x32_bf16 v[84:87], a[132:135], v[132:135], v[84:87]\n"
    "buffer_load_dwordx4 a[212:215], v1, s[56:59], s64 offen offset:3072 sc1\n"
    "s_waitcnt vmcnt(35)\n"
    "v_mfma_f32_16x16x32_bf16 v[88:91], a[136:139], v[120:123], v[88:91]\n"
    "v_mfma_f32_16x16x32_bf16 v[92:95], a[136:139], v[124:127], v[92:95]\n"
    "buffer_load_dwordx4 a[216:219], v2, s[56:59], s64 offen offset:0 sc1\n"
    "v_mfma_f32_16x16x32_bf16 v[96:99], a[136:139], v[128:131], v[96:99]\n"
    "v_mfma_f32_16x16x32_bf16 v[100:103], a[136:139], v[132:135], v[100:103]\n"
    "s_waitcnt vmcnt(31)\n"
    "v_mfma_f32_16x16x32_bf16 v[24:27], a[140:143], v[136:139], v[24:27]\n"
    "v_mfma_f32_16x16x32_bf16 v[28:31], a[140:143], v[140:143], v[28:31]\n"
    "s_lshl_b32 s65, s34, 12\n"
    "s_add_u32 s65, s65, 0x2c000\n"
    "buffer_load_dwordx4 v[120:123], v1, s[52:55], s65 offen offset:0 sc1\n"
    "v_mfma_f32_16x16x32_bf16 v[32:35], a[140:143], v[144:147], v[32:35]\n"
    "v_mfma_f32_16x16x32_bf16 v[36:39], a[140:143], v[148:151], v[36:39]\n"
    "buffer_load_dwordx4 v[124:127], v1, s[52:55], s65 offen offset:1024 sc1\n"
    "s_waitcnt vmcnt(32)\n"
    "v_mfma_f32_16x16x32_bf16 v[40:43], a[144:147], v[136:139], v[40:43]\n"
    "v_mfma_f32_16x16x32_bf16 v[44:47], a[144:147], v[140:143], v[44:47]\n"
    "buffer_load_dwordx4 v[128:131], v1, s[52:55], s65 offen offset:2048 sc1\n"
    "v_mfma_f32_16x16x32_bf16 v[48:51], a[144:147], v[144:147], v[48:51]\n"
    "v_mfma_f32_16x16x32_bf16 v[52:55], a[144:147], v[148:151], v[52:55]\n"
    "buffer_load_dwordx4 v[132:135], v1, s[52:55], s65 offen offset:3072 sc1\n"
    "s_waitcnt vmcnt(33)\n"
    "v_mfma_f32_16x16x32_bf16 v[56:59], a[148:151], v[136:139], v[56:59]\n"
    "v_mfma_f32_16x16x32_bf16 v[60:63], a[148:151], v[140:143], v[60:63]\n"
    "s_mul_i32 s64, s34, 0xa000\n"
    "s_add_u32 s64, s64, 0x1b8000\n"
    "buffer_load_dwordx4 a[220:223], v1, s[56:59], s64 offen offset:0 sc1\n"
    "v_mfma_f32_16x16x32_bf16 v[64:67], a[148:151], v[144:147], v[64:67]\n"
    "v_mfma_f32_16x16x32_bf16 v[68:71], a[148:151], v[148:151], v[68:71]\n"
    "buffer_load_dwordx4 a[224:227], v1, s[56:59], s64 offen offset:1024 sc1\n"
    "s_waitcnt vmcnt(34)\n"
    "v_mfma_f32_16x16x32_bf16 v[72:75], a[152:155], v[136:139], v[72:75]\n"
    "v_mfma_f32_16x16x32_bf16 v[76:79], a[152:155], v[140:143], v[76:79]\n"
    "buffer_load_dwordx4 a[228:231], v1, s[56:59], s64 offen offset:2048 sc1\n"
    "v_mfma_f32_16x16x32_bf16 v[80:83], a[152:155], v[144:147], v[80:83]\n"
    "v_mfma_f32_16x16x32_bf16 v[84:87], a[152:155], v[148:151], v[84:87]\n"
    "buffer_load_dwordx4 a[232:235], v1, s[56:59], s64 offen offset:3072 sc1\n"
    "s_waitcnt vmcnt(35)\n"
    "v_mfma_f32_16x16x32_bf16 v[88:91], a[156:159], v[136:139], v[88:91]\n"
    "v_mfma_f32_16x16x32_bf16 v[92:95], a[156:159], v[140:143], v[92:95]\n"
    "buffer_load_dwordx4 a[236:239], v2, s[56:59], s64 offen offset:0 sc1\n"
    "v_mfma_f32_16x16x32_bf16 v[96:99], a[156:159], v[144:147], v[96:99]\n"
    "v_mfma_f32_16x16x32_bf16 v[100:103], a[156:159], v[148:151], v[100:103]\n"
    "s_waitcnt vmcnt(31)\n"
    "v_mfma_f32_16x16x32_bf16 v[24:27], a[160:163], v[152:155], v[24:27]\n"
    "v_mfma_f32_16x16x32_bf16 v[28:31], a[160:163], v[156:159], v[28:31]\n"
    "s_lshl_b32 s65, s34, 12\n"
    "s_add_u32 s65, s65, 0x30000\n"
    "buffer_load_dwordx4 v[136:139], v1, s[52:55], s65 offen offset:0 sc1\n"
    "v_mfma_f32_16x16x32_bf16 v[32:35], a[160:163], v[160:163], v[32:35]\n"
    "v_mfma_f32_16x16x32_bf16 v[36:39], a[160:163], v[164:167], v[36:39]\n"
    "buffer_load_dwordx4 v[140:143], v1, s[52:55], s65 offen offset:1024 sc1\n"
    "s_waitcnt vmcnt(32)\n"
    "v_mfma_f32_16x16x32_bf16 v[40:43], a[164:167], v[152:155], v[40:43]\n"
    "v_mfma_f32_16x16x32_bf16 v[44:47], a[164:167], v[156:159], v[44:47]\n"
    "buffer_load_dwordx4 v[144:147], v1, s[52:55], s65 offen offset:2048 sc1\n"
    "v_mfma_f32_16x16x32_bf16 v[48:51], a[164:167], v[160:163], v[48:51]\n"
    "v_mfma_f32_16x16x32_bf16 v[52:55], a[164:167], v[164:167], v[52:55]\n"
    "buffer_load_dwordx4 v[148:151], v1, s[52:55], s65 offen offset:3072 sc1\n"
    "s_waitcnt vmcnt(33)\n"
    "v_mfma_f32_16x16x32_bf16 v[56:59], a[168:171], v[152:155], v[56:59]\n"
    "v_mfma_f32_16x16x32_bf16 v[60:63], a[168:171], v[156:159], v[60:63]\n"
    "s_mul_i32 s64, s34, 0xa000\n"
    "s_add_u32 s64, s64, 0x1e0000\n"
    "buffer_load_dwordx4 a[0:3], v1, s[56:59], s64 offen offset:0 sc1\n"
    "v_mfma_f32_16x16x32_bf16 v[64:67], a[168:171], v[160:163], v[64:67]\n"
    "v_mfma_f32_16x16x32_bf16 v[68:71], a[168:171], v[164:167], v[68:71]\n"
    "buffer_load_dwordx4 a[4:7], v1, s[56:59], s64 offen offset:1024 sc1\n"
    "s_waitcnt vmcnt(34)\n"
    "v_mfma_f32_16x16x32_bf16 v[72:75], a[172:175], v[152:155], v[72:75]\n"
    "v_mfma_f32_16x16x32_bf16 v[76:79], a[172:175], v[156:159], v[76:79]\n"
    "buffer_load_dwordx4 a[8:11], v1, s[56:59], s64 offen offset:2048 sc1\n"
    "v_mfma_f32_16x16x32_bf16 v[80:83], a[172:175], v[160:163], v[80:83]\n"
    "v_mfma_f32_16x16x32_bf16 v[84:87], a[172:175], v[164:167], v[84:87]\n"
    "buffer_load_dwordx4 a[12:15], v1, s[56:59], s64 offen offset:3072 sc1\n"
    "s_waitcnt vmcnt(35)\n"
    "v_mfma_f32_16x16x32_bf16 v[88:91], a[176:179], v[152:155], v[88:91]\n"
    "v_mfma_f32_16x16x32_bf16 v[92:95], a[176:179], v[156:159], v[92:95]\n"
    "buffer_load_dwordx4 a[16:19], v2, s[56:59], s64 offen offset:0 sc1\n"
    "v_mfma_f32_16x16x32_bf16 v[96:99], a[176:179], v[160:163], v[96:99]\n"
    "v_mfma_f32_16x16x32_bf16 v[100:103], a[176:179], v[164:167], v[100:103]\n"
    "s_waitcnt vmcnt(31)\n"
    "v_mfma_f32_16x16x32_bf16 v[24:27], a[180:183], v[168:171], v[24:27]\n"
    "v_mfma_f32_16x16x32_bf16 v[28:31], a[180:183], v[172:175], v[28:31]\n"
    "s_lshl_b32 s65, s34, 12\n"
    "s_add_u32 s65, s65, 0x34000\n"
    "buffer_load_dwordx4 v[152:155], v1, s[52:55], s65 offen offset:0 sc1\n"
    "v_mfma_f32_16x16x32_bf16 v[32:35], a[180:183], v[176:179], v[32:35]\n"
    "v_mfma_f32_16x16x32_bf16 v[36:39], a[180:183], v[180:183], v[36:39]\n"
    "buffer_load_dwordx4 v[156:159], v1, s[52:55], s65 offen offset:1024 sc1\n"
    "s_waitcnt vmcnt(32)\n"
    "v_mfma_f32_16x16x32_bf16 v[40:43], a[184:187], v[168:171], v[40:43]\n"
    "v_mfma_f32_16x16x32_bf16 v[44:47], a[184:187], v[172:175], v[44:47]\n"
    "buffer_load_dwordx4 v[160:163], v1, s[52:55], s65 offen offset:2048 sc1\n"
    "v_mfma_f32_16x16x32_bf16 v[48:51], a[184:187], v[176:179], v[48:51]\n"
    "v_mfma_f32_16x16x32_bf16 v[52:55], a[184:187], v[180:183], v[52:55]\n"
    "buffer_load_dwordx4 v[164:167], v1, s[52:55], s65 offen offset:3072 sc1\n"
    "s_waitcnt vmcnt(33)\n"
    "v_mfma_f32_16x16x32_bf16 v[56:59], a[188:191], v[168:171], v[56:59]\n"
    "v_mfma_f32_16x16x32_bf16 v[60:63], a[188:191], v[172:175], v[60:63]\n"
    "s_mul_i32 s64, s34, 0xa000\n"
    "s_add_u32 s64, s64, 0x208000\n"
    "buffer_load_dwordx4 a[20:23], v1, s[56:59], s64 offen offset:0 sc1\n"
    "v_mfma_f32_16x16x32_bf16 v[64:67], a[188:191], v[176:179], v[64:67]\n"
    "v_mfma_f32_16x16x32_bf16 v[68:71], a[188:191], v[180:183], v[68:71]\n"
    "buffer_load_dwordx4 a[24:27], v1, s[56:59], s64 offen offset:1024 sc1\n"
    "s_waitcnt vmcnt(34)\n"
    "v_mfma_f32_16x16x32_bf16 v[72:75], a[192:195], v[168:171], v[72:75]\n"
    "v_mfma_f32_16x16x32_bf16 v[76:79], a[192:195], v[172:175], v[76:79]\n"
    "buffer_load_dwordx4 a[28:31], v1, s[56:59], s64 offen offset:2048 sc1\n"
    "v_mfma_f32_16x16x32_bf16 v[80:83], a[192:195], v[176:179], v[80:83]\n"
    "v_mfma_f32_16x16x32_bf16 v[84:87], a[192:195], v[180:183], v[84:87]\n"
    "buffer_load_dwordx4 a[32:35], v1, s[56:59], s64 offen offset:3072 sc1\n"
    "s_waitcnt vmcnt(35)\n"
    "v_mfma_f32_16x16x32_bf16 v[88:91], a[196:199], v[168:171], v[88:91]\n"
    "v_mfma_f32_16x16x32_bf16 v[92:95], a[196:199], v[172:175], v[92:95]\n"
    "buffer_load_dwordx4 a[36:39], v2, s[56:59], s64 offen offset:0 sc1\n"
    "v_mfma_f32_16x16x32_bf16 v[96:99], a[196:199], v[176:179], v[96:99]\n"
    "v_mfma_f32_16x16x32_bf16 v[100:103], a[196:199], v[180:183], v[100:103]\n"
    "s_waitcnt vmcnt(31)\n"
    "v_mfma_f32_16x16x32_bf16 v[24:27], a[200:203], v[104:107], v[24:27]\n"
    "v_mfma_f32_16x16x32_bf16 v[28:31], a[200:203], v[108:111], v[28:31]\n"
    "buffer_load_dwordx2 v[224:225], v4, s[60:63], s70 offen offset:0 sc1\n"
    "v_mfma_f32_16x16x32_bf16 v[32:35], a[200:203], v[112:115], v[32:35]\n"
    "buffer_load_dwordx2 v[234:235], v5, s[92:95], s70 offen offset:0 sc1\n"
    "v_mfma_f32_16x16x32_bf16 v[36:39], a[200:203], v[116:119], v[36:39]\n"
    "buffer_load_dwordx2 v[226:227], v4, s[60:63], s71 offen offset:0 sc1\n"
    "s_waitcnt vmcnt(33)\n"
    "v_mfma_f32_16x16x32_bf16 v[40:43], a[204:207], v[104:107], v[40:43]\n"
    "buffer_load_dwordx2 v[236:237], v5, s[92:95], s71 offen offset:0 sc1\n"
    "v_mfma_f32_16x16x32_bf16 v[44:47], a[204:207], v[108:111], v[44:47]\n"
    "buffer_load_dwordx2 v[228:229], v4, s[60:63], s72 offen offset:0 sc1\n"
    "v_mfma_f32_16x16x32_bf16 v[48:51], a[204:207], v[112:115], v[48:51]\n"
    "buffer_load_dwordx2 v[238:239], v5, s[92:95], s72 offen offset:0 sc1\n"
    "v_mfma_f32_16x16x32_bf16 v[52:55], a[204:207], v[116:119], v[52:55]\n"
    "buffer_load_dwordx2 v[230:231], v4, s[60:63], s73 offen offset:0 sc1\n"
    "s_waitcnt vmcnt(36)\n"
    "v_mfma_f32_16x16x32_bf16 v[56:59], a[208:211], v[104:107], v[56:59]\n"
    "buffer_load_dwordx2 v[240:241], v5, s[92:95], s73 offen offset:0 sc1\n"
    "v_mfma_f32_16x16x32_bf16 v[60:63], a[208:211], v[108:111], v[60:63]\n"
    "buffer_load_dwordx2 v[232:233], v4, s[60:63], s74 offen offset:0 sc1\n"
    "v_mfma_f32_16x16x32_bf16 v[64:67], a[208:211], v[112:115], v[64:67]\n"
    "buffer_load_dwordx2 v[242:243], v5, s[92:95], s74 offen offset:0 sc1\n"
    "v_mfma_f32_16x16x32_bf16 v[68:71], a[208:211], v[116:119], v[68:71]\n"
    "s_waitcnt vmcnt(38)\n"
    "v_mfma_f32_16x16x32_bf16 v[72:75], a[212:215], v[104:107], v[72:75]\n"
    "v_mfma_f32_16x16x32_bf16 v[76:79], a[212:215], v[108:111], v[76:79]\n"
    "v_mfma_f32_16x16x32_bf16 v[80:83], a[212:215], v[112:115], v[80:83]\n"
    "v_mfma_f32_16x16x32_bf16 v[84:87], a[212:215], v[116:119], v[84:87]\n"
    "s_waitcnt vmcnt(37)\n"
    "v_mfma_f32_16x16x32_bf16 v[88:91], a[216:219], v[104:107], v[88:91]\n"
    "v_mfma_f32_16x16x32_bf16 v[92:95], a[216:219], v[108:111], v[92:95]\n"
    "v_mfma_f32_16x16x32_bf16 v[96:99], a[216:219], v[112:115], v[96:99]\n"
    "v_mfma_f32_16x16x32_bf16 v[100:103], a[216:219], v[116:119], v[100:103]\n"
    "s_waitcnt vmcnt(32)\n"
    "v_mfma_f32_16x16x32_bf16 v[24:27], a[220:223], v[120:123], v[24:27]\n"
    "v_mfma_f32_16x16x32_bf16 v[28:31], a[220:223], v[124:127], v[28:31]\n"
    "v_mfma_f32_16x16x32_bf16 v[32:35], a[220:223], v[128:131], v[32:35]\n"
    "v_mfma_f32_16x16x32_bf16 v[36:39], a[220:223], v[132:135], v[36:39]\n"
    "s_waitcnt vmcnt(31)\n"
    "v_mfma_f32_16x16x32_bf16 v[40:43], a[224:227], v[120:123], v[40:43]\n"
    "v_mfma_f32_16x16x32_bf16 v[44:47], a[224:227], v[124:127], v[44:47]\n"
    "v_mfma_f32_16x16x32_bf16 v[48:51], a[224:227], v[128:131], v[48:51]\n"
    "v_mfma_f32_16x16x32_bf16 v[52:55], a[224:227], v[132:135], v[52:55]\n"
    "s_waitcnt vmcnt(30)\n"
    "v_mfma_f32_16x16x32_bf16 v[56:59], a[228:231], v[120:123], v[56:59]\n"
    "v_mfma_f32_16x16x32_bf16 v[60:63], a[228:231], v[124:127], v[60:63]\n"
    "v_mfma_f32_16x16x32_bf16 v[64:67], a[228:231], v[128:131], v[64:67]\n"
    "v_mfma_f32_16x16x32_bf16 v[68:71], a[228:231], v[132:135], v[68:71]\n"
    "s_waitcnt vmcnt(29)\n"
    "v_mfma_f32_16x16x32_bf16 v[72:75], a[232:235], v[120:123], v[72:75]\n"
    "v_mfma_f32_16x16x32_bf16 v[76:79], a[232:235], v[124:127], v[76:79]\n"
    "v_mfma_f32_16x16x32_bf16 v[80:83], a[232:235], v[128:131], v[80:83]\n"
    "v_mfma_f32_16x16x32_bf16 v[84:87], a[232:235], v[132:135], v[84:87]\n"
    "s_waitcnt vmcnt(28)\n"
    "v_mfma_f32_16x16x32_bf16 v[88:91], a[236:239], v[120:123], v[88:91]\n"
    "v_mfma_f32_16x16x32_bf16 v[92:95], a[236:239], v[124:127], v[92:95]\n"
    "v_mfma_f32_16x16x32_bf16 v[96:99], a[236:239], v[128:131], v[96:99]\n"
    "v_mfma_f32_16x16x32_bf16 v[100:103], a[236:239], v[132:135], v[100:103]\n"
    "s_waitcnt vmcnt(23)\n"
    "v_mfma_f32_16x16x32_bf16 v[24:27], a[0:3], v[136:139], v[24:27]\n"
    "v_mfma_f32_16x16x32_bf16 v[28:31], a[0:3], v[140:143], v[28:31]\n"
    "v_mfma_f32_16x16x32_bf16 v[32:35], a[0:3], v[144:147], v[32:35]\n"
    "v_mfma_f32_16x16x32_bf16 v[36:39], a[0:3], v[148:151], v[36:39]\n"
    "s_waitcnt vmcnt(22)\n"
    "v_mfma_f32_16x16x32_bf16 v[40:43], a[4:7], v[136:139], v[40:43]\n"
    "v_mfma_f32_16x16x32_bf16 v[44:47], a[4:7], v[140:143], v[44:47]\n"
    "v_mfma_f32_16x16x32_bf16 v[48:51], a[4:7], v[144:147], v[48:51]\n"
    "v_mfma_f32_16x16x32_bf16 v[52:55], a[4:7], v[148:151], v[52:55]\n"
    "s_waitcnt vmcnt(21)\n"
    "v_mfma_f32_16x16x32_bf16 v[56:59], a[8:11], v[136:139], v[56:59]\n"
    "v_mfma_f32_16x16x32_bf16 v[60:63], a[8:11], v[140:143], v[60:63]\n"
    "v_mfma_f32_16x16x32_bf16 v[64:67], a[8:11], v[144:147], v[64:67]\n"
    "v_mfma_f32_16x16x32_bf16 v[68:71], a[8:11], v[148:151], v[68:71]\n"
    "s_waitcnt vmcnt(20)\n"
    "v_mfma_f32_16x16x32_bf16 v[72:75], a[12:15], v[136:139], v[72:75]\n"
    "v_mfma_f32_16x16x32_bf16 v[76:79], a[12:15], v[140:143], v[76:79]\n"
    "v_mfma_f32_16x16x32_bf16 v[80:83], a[12:15], v[144:147], v[80:83]\n"
    "v_mfma_f32_16x16x32_bf16 v[84:87], a[12:15], v[148:151], v[84:87]\n"
    "s_waitcnt vmcnt(19)\n"
    "v_mfma_f32_16x16x32_bf16 v[88:91], a[16:19], v[136:139], v[88:91]\n"
    "v_mfma_f32_16x16x32_bf16 v[92:95], a[16:19], v[140:143], v[92:95]\n"
    "v_mfma_f32_16x16x32_bf16 v[96:99], a[16:19], v[144:147], v[96:99]\n"
    "v_mfma_f32_16x16x32_bf16 v[100:103], a[16:19], v[148:151], v[100:103]\n"
    "s_waitcnt vmcnt(14)\n"
    "v_mfma_f32_16x16x32_bf16 v[24:27], a[20:23], v[152:155], v[24:27]\n"
    "v_mfma_f32_16x16x32_bf16 v[28:31], a[20:23], v[156:159], v[28:31]\n"
    "v_mfma_f32_16x16x32_bf16 v[32:35], a[20:23], v[160:163], v[32:35]\n"
    "v_mfma_f32_16x16x32_bf16 v[36:39], a[20:23], v[164:167], v[36:39]\n"
    "s_waitcnt vmcnt(13)\n"
    "v_mfma_f32_16x16x32_bf16 v[40:43], a[24:27], v[152:155], v[40:43]\n"
    "v_mfma_f32_16x16x32_bf16 v[44:47], a[24:27], v[156:159], v[44:47]\n"
    "v_mfma_f32_16x16x32_bf16 v[48:51], a[24:27], v[160:163], v[48:51]\n"
    "v_mfma_f32_16x16x32_bf16 v[52:55], a[24:27], v[164:167], v[52:55]\n"
    "s_waitcnt vmcnt(12)\n"
    "v_mfma_f32_16x16x32_bf16 v[56:59], a[28:31], v[152:155], v[56:59]\n"
    "v_mfma_f32_16x16x32_bf16 v[60:63], a[28:31], v[156:159], v[60:63]\n"
    "v_mfma_f32_16x16x32_bf16 v[64:67], a[28:31], v[160:163], v[64:67]\n"
    "v_mfma_f32_16x16x32_bf16 v[68:71], a[28:31], v[164:167], v[68:71]\n"
    "s_waitcnt vmcnt(11)\n"
    "v_mfma_f32_16x16x32_bf16 v[72:75], a[32:35], v[152:155], v[72:75]\n"
    "v_mfma_f32_16x16x32_bf16 v[76:79], a[32:35], v[156:159], v[76:79]\n"
    "v_mfma_f32_16x16x32_bf16 v[80:83], a[32:35], v[160:163], v[80:83]\n"
    "v_mfma_f32_16x16x32_bf16 v[84:87], a[32:35], v[164:167], v[84:87]\n"
    "s_waitcnt vmcnt(10)\n"
    "v_mfma_f32_16x16x32_bf16 v[88:91], a[36:39], v[152:155], v[88:91]\n"
    "v_mfma_f32_16x16x32_bf16 v[92:95], a[36:39], v[156:159], v[92:95]\n"
    "v_mfma_f32_16x16x32_bf16 v[96:99], a[36:39], v[160:163], v[96:99]\n"
    "v_mfma_f32_16x16x32_bf16 v[100:103], a[36:39], v[164:167], v[100:103]\n"
    "ds_write_b128 v6, v[24:27] offset:0\n"
    "ds_write_b128 v6, v[40:43] offset:1024\n"
    "ds_write_b128 v6, v[56:59] offset:2048\n"
    "ds_write_b128 v6, v[72:75] offset:3072\n"
    "s_nop 0\n"
    "ds_write_b128 v6, v[88:91] offset:4096\n"
    "ds_write_b128 v6, v[28:31] offset:5120\n"
    "ds_write_b128 v6, v[44:47] offset:6144\n"
    "ds_write_b128 v6, v[60:63] offset:7168\n"
    "ds_write_b128 v6, v[76:79] offset:8192\n"
    "ds_write_b128 v6, v[92:95] offset:9216\n"
    "ds_write_b128 v6, v[32:35] offset:10240\n"
    "ds_write_b128 v6, v[48:51] offset:11264\n"
    "ds_write_b128 v6, v[64:67] offset:12288\n"
    "ds_write_b128 v6, v[80:83] offset:13312\n"
    "ds_write_b128 v6, v[96:99] offset:14336\n"
    "ds_write_b128 v6, v[36:39] offset:15360\n"
    "ds_write_b128 v6, v[52:55] offset:16384\n"
    "ds_write_b128 v6, v[68:71] offset:17408\n"
    "ds_write_b128 v6, v[84:87] offset:18432\n"
    "ds_write_b128 v6, v[100:103] offset:19456\n"
    "s_waitcnt lgkmcnt(0)\n"
    "s_barrier\n"
    "v_add_u32 v8, 0xa000, v7\n"
    "ds_read_b128 v[104:107], v7 offset:0\n"
    "ds_read_b128 v[108:111], v7 offset:1024\n"
    "ds_read_b128 v[112:115], v7 offset:2048\n"
    "ds_read_b128 v[116:119], v7 offset:3072\n"
    "ds_read_b128 v[120:123], v7 offset:4096\n"
    "ds_read_b128 v[124:127], v7 offset:20480\n"
    "ds_read_b128 v[128:131], v7 offset:21504\n"
    "ds_read_b128 v[132:135], v7 offset:22528\n"
    "ds_read_b128 v[136:139], v7 offset:23552\n"
    "ds_read_b128 v[140:143], v7 offset:24576\n"
    "ds_read_b128 v[144:147], v8 offset:0\n"
    "ds_read_b128 v[148:151], v8 offset:1024\n"
    "ds_read_b128 v[152:155], v8 offset:2048\n"
    "ds_read_b128 v[156:159], v8 offset:3072\n"
    "ds_read_b128 v[160:163], v8 offset:4096\n"
    "ds_read_b128 v[164:167], v8 offset:20480\n"
    "ds_read_b128 v[168:171], v8 offset:21504\n"
    "ds_read_b128 v[172:175], v8 offset:22528\n"
    "ds_read_b128 v[176:179], v8 offset:23552\n"
    "ds_read_b128 v[180:183], v8 offset:24576\n"
    "s_waitcnt lgkmcnt(14)\n"
    "v_add_f32 v104, v104, v124\n"
    "s_waitcnt lgkmcnt(4)\n"
    "v_add_f32 v144, v144, v164\n"
    "v_add_f32 v104, v104, v144\n"
    "v_add_f32 v105, v105, v125\n"
    "v_add_f32 v145, v145, v165\n"
    "v_add_f32 v105, v105, v145\n"
    "v_add_f32 v106, v106, v126\n"
    "v_add_f32 v146, v146, v166\n"
    "v_add_f32 v106, v106, v146\n"
    "v_add_f32 v107, v107, v127\n"
    "v_add_f32 v147, v147, v167\n"
    "v_add_f32 v107, v107, v147\n"
    "s_waitcnt vmcnt(8)\n"
    "v_lshlrev_b32 v24, 16, v234\n"
    "v_and_b32 v25, 0xffff0000, v234\n"
    "v_lshlrev_b32 v26, 16, v224\n"
    "v_and_b32 v27, 0xffff0000, v224\n"
    "v_fma_f32 v28, v24, v104, v26\n"
    "v_fma_f32 v29, v25, v105, v27\n"
    "v_lshlrev_b32 v24, 16, v235\n"
    "v_and_b32 v25, 0xffff0000, v235\n"
    "v_lshlrev_b32 v26, 16, v225\n"
    "v_and_b32 v27, 0xffff0000, v225\n"
    "v_fma_f32 v30, v24, v106, v26\n"
    "v_fma_f32 v31, v25, v107, v27\n"
    "v_cvt_pk_bf16_f32 v24, v28, v29\n"
    "v_cvt_pk_bf16_f32 v25, v30, v31\n"
    "buffer_store_dwordx2 v[24:25], v4, s[96:99], s70 offen offset:0\n"
    "v_add_f32 v108, v108, v128\n"
    "s_waitcnt lgkmcnt(3)\n"
    "v_add_f32 v148, v148, v168\n"
    "v_add_f32 v108, v108, v148\n"
    "v_add_f32 v109, v109, v129\n"
    "v_add_f32 v149, v149, v169\n"
    "v_add_f32 v109, v109, v149\n"
    "v_add_f32 v110, v110, v130\n"
    "v_add_f32 v150, v150, v170\n"
    "v_add_f32 v110, v110, v150\n"
    "v_add_f32 v111, v111, v131\n"
    "v_add_f32 v151, v151, v171\n"
    "v_add_f32 v111, v111, v151\n"
    "s_waitcnt vmcnt(7)\n"
    "v_lshlrev_b32 v32, 16, v236\n"
    "v_and_b32 v33, 0xffff0000, v236\n"
    "v_lshlrev_b32 v34, 16, v226\n"
    "v_and_b32 v35, 0xffff0000, v226\n"
    "v_fma_f32 v36, v32, v108, v34\n"
    "v_fma_f32 v37, v33, v109, v35\n"
    "v_lshlrev_b32 v32, 16, v237\n"
    "v_and_b32 v33, 0xffff0000, v237\n"
    "v_lshlrev_b32 v34, 16, v227\n"
    "v_and_b32 v35, 0xffff0000, v227\n"
    "v_fma_f32 v38, v32, v110, v34\n"
    "v_fma_f32 v39, v33, v111, v35\n"
    "v_cvt_pk_bf16_f32 v32, v36, v37\n"
    "v_cvt_pk_bf16_f32 v33, v38, v39\n"
    "buffer_store_dwordx2 v[32:33], v4, s[96:99], s71 offen offset:0\n"
    "v_add_f32 v112, v112, v132\n"
    "s_waitcnt lgkmcnt(2)\n"
    "v_add_f32 v152, v152, v172\n"
    "v_add_f32 v112, v112, v152\n"
    "v_add_f32 v113, v113, v133\n"
    "v_add_f32 v153, v153, v173\n"
    "v_add_f32 v113, v113, v153\n"
    "v_add_f32 v114, v114, v134\n"
    "v_add_f32 v154, v154, v174\n"
    "v_add_f32 v114, v114, v154\n"
    "v_add_f32 v115, v115, v135\n"
    "v_add_f32 v155, v155, v175\n"
    "v_add_f32 v115, v115, v155\n"
    "s_waitcnt vmcnt(6)\n"
    "v_lshlrev_b32 v40, 16, v238\n"
    "v_and_b32 v41, 0xffff0000, v238\n"
    "v_lshlrev_b32 v42, 16, v228\n"
    "v_and_b32 v43, 0xffff0000, v228\n"
    "v_fma_f32 v44, v40, v112, v42\n"
    "v_fma_f32 v45, v41, v113, v43\n"
    "v_lshlrev_b32 v40, 16, v239\n"
    "v_and_b32 v41, 0xffff0000, v239\n"
    "v_lshlrev_b32 v42, 16, v229\n"
    "v_and_b32 v43, 0xffff0000, v229\n"
    "v_fma_f32 v46, v40, v114, v42\n"
    "v_fma_f32 v47, v41, v115, v43\n"
    "v_cvt_pk_bf16_f32 v40, v44, v45\n"
    "v_cvt_pk_bf16_f32 v41, v46, v47\n"
    "buffer_store_dwordx2 v[40:41], v4, s[96:99], s72 offen offset:0\n"
    "v_add_f32 v116, v116, v136\n"
    "s_waitcnt lgkmcnt(1)\n"
    "v_add_f32 v156, v156, v176\n"
    "v_add_f32 v116, v116, v156\n"
    "v_add_f32 v117, v117, v137\n"
    "v_add_f32 v157, v157, v177\n"
    "v_add_f32 v117, v117, v157\n"
    "v_add_f32 v118, v118, v138\n"
    "v_add_f32 v158, v158, v178\n"
    "v_add_f32 v118, v118, v158\n"
    "v_add_f32 v119, v119, v139\n"
    "v_add_f32 v159, v159, v179\n"
    "v_add_f32 v119, v119, v159\n"
    "s_waitcnt vmcnt(5)\n"
    "v_lshlrev_b32 v48, 16, v240\n"
    "v_and_b32 v49, 0xffff0000, v240\n"
    "v_lshlrev_b32 v50, 16, v230\n"
    "v_and_b32 v51, 0xffff0000, v230\n"
    "v_fma_f32 v52, v48, v116, v50\n"
    "v_fma_f32 v53, v49, v117, v51\n"
    "v_lshlrev_b32 v48, 16, v241\n"
    "v_and_b32 v49, 0xffff0000, v241\n"
    "v_lshlrev_b32 v50, 16, v231\n"
    "v_and_b32 v51, 0xffff0000, v231\n"
    "v_fma_f32 v54, v48, v118, v50\n"
    "v_fma_f32 v55, v49, v119, v51\n"
    "v_cvt_pk_bf16_f32 v48, v52, v53\n"
    "v_cvt_pk_bf16_f32 v49, v54, v55\n"
    "buffer_store_dwordx2 v[48:49], v4, s[96:99], s73 offen offset:0\n"
    "v_add_f32 v120, v120, v140\n"
    "s_waitcnt lgkmcnt(0)\n"
    "v_add_f32 v160, v160, v180\n"
    "v_add_f32 v120, v120, v160\n"
    "v_add_f32 v121, v121, v141\n"
    "v_add_f32 v161, v161, v181\n"
    "v_add_f32 v121, v121, v161\n"
    "v_add_f32 v122, v122, v142\n"
    "v_add_f32 v162, v162, v182\n"
    "v_add_f32 v122, v122, v162\n"
    "v_add_f32 v123, v123, v143\n"
    "v_add_f32 v163, v163, v183\n"
    "v_add_f32 v123, v123, v163\n"
    "s_waitcnt vmcnt(4)\n"
    "v_lshlrev_b32 v56, 16, v242\n"
    "v_and_b32 v57, 0xffff0000, v242\n"
    "v_lshlrev_b32 v58, 16, v232\n"
    "v_and_b32 v59, 0xffff0000, v232\n"
    "v_fma_f32 v60, v56, v120, v58\n"
    "v_fma_f32 v61, v57, v121, v59\n"
    "v_lshlrev_b32 v56, 16, v243\n"
    "v_and_b32 v57, 0xffff0000, v243\n"
    "v_lshlrev_b32 v58, 16, v233\n"
    "v_and_b32 v59, 0xffff0000, v233\n"
    "v_fma_f32 v62, v56, v122, v58\n"
    "v_fma_f32 v63, v57, v123, v59\n"
    "v_cvt_pk_bf16_f32 v56, v60, v61\n"
    "v_cvt_pk_bf16_f32 v57, v62, v63\n"
    "buffer_store_dwordx2 v[56:57], v4, s[96:99], s74 offen offset:0\n"
    "s_lshl_b32 s46, %1, 2\n"
    "s_add_u32 s46, s46, s34\n"
    "s_cmp_lg_u32 s46, 0\n"
    "s_cbranch_scc1 Lend27_%=\n"
    "v_mov_b32 v8, s40\n"
    "v_mov_b32 v9, s41\n"
    "v_mov_b32 v10, 0\n"
    "s_mov_b64 exec, 1\n"
    "global_store_dwordx2 v10, v[8:9], s[26:27] offset:0 sc1\n"
    "s_mov_b64 exec, -1\n"
    "Lend27_%=:\n"
    "s_endpgm\n"
    "; ==== out of line: exact two-pass softmax fall-back\n"
    "Lfb8_%=:\n"
    "s_nop 15\n"
    "s_nop 15\n"
    "s_barrier\n"
    "s_mov_b32 s89, 0\n"
    "v_mov_b32 v27, 0xff800000\n"
    "v_mov_b32 v28, 0xff800000\n"
    "Lfb8_%=a:\n"
    "s_lshl_b32 s83, s89, 14\n"
    "s_add_u32 m0, s81, 0x8000\n"
    "s_nop 0\n"
    "buffer_load_dwordx4 v6, s[56:59], s83 offen lds\n"
    "s_add_u32 m0, s81, 0x8400\n"
    "s_nop 0\n"
    "buffer_load_dwordx4 v7, s[56:59], s83 offen lds\n"
    "s_add_u32 m0, s81, 0xa000\n"
    "s_nop 0\n"
    "buffer_load_dwordx4 v8, s[56:59], s83 offen lds\n"
    "s_add_u32 m0, s81, 0xa400\n"
    "s_nop 0\n"
    "buffer_load_dwordx4 v9, s[56:59], s83 offen lds\n"
    "s_waitcnt vmcnt(0)\n"
    "s_barrier\n"
    "ds_read_b128 a[128:131], v22 offset:32768\n"
    "ds_read_b128 a[132:135], v23 offset:32768\n"
    "ds_read_b128 a[136:139], v22 offset:33280\n"
    "ds_read_b128 a[140:143], v23 offset:33280\n"
    "ds_read_b128 a[144:147], v22 offset:33792\n"
    "ds_read_b128 a[148:151], v23 offset:33792\n"
    "ds_read_b128 a[152:155], v22 offset:34304\n"
    "ds_read_b128 a[156:159], v23 offset:34304\n"
    "ds_read_b128 a[192:195], v22 offset:40960\n"
    "ds_read_b128 a[196:199], v23 offset:40960\n"
    "ds_read_b128 a[200:203], v22 offset:41472\n"
    "ds_read_b128 a[204:207], v23 offset:41472\n"
    "ds_read_b128 a[208:211], v22 offset:41984\n"
    "ds_read_b128 a[212:215], v23 offset:41984\n"
    "ds_read_b128 a[216:219], v22 offset:42496\n"
    "ds_read_b128 a[220:223], v23 offset:42496\n"
    "s_waitcnt lgkmcnt(15)\n"
    "v_mfma_f32_32x32x16_bf16 v[104:119], a[128:131], v[40:43], 0\n"
    "s_waitcnt lgkmcnt(14)\n"
    "v_mfma_f32_32x32x16_bf16 v[104:119], a[132:135], v[44:47], v[104:119]\n"
    "s_waitcnt lgkmcnt(13)\n"
    "v_mfma_f32_32x32x16_bf16 v[104:119], a[136:139], v[48:51], v[104:119]\n"
    "s_waitcnt lgkmcnt(12)\n"
    "v_mfma_f32_32x32x16_bf16 v[104:119], a[140:143], v[52:55], v[104:119]\n"
    "s_waitcnt lgkmcnt(11)\n"
    "v_mfma_f32_32x32x16_bf16 v[104:119], a[144:147], v[56:59], v[104:119]\n"
    "s_waitcnt lgkmcnt(10)\n"
    "v_mfma_f32_32x32x16_bf16 v[104:119], a[148:151], v[60:63], v[104:119]\n"
    "s_waitcnt lgkmcnt(9)\n"
    "v_mfma_f32_32x32x16_bf16 v[104:119], a[152:155], v[64:67], v[104:119]\n"
    "s_waitcnt lgkmcnt(8)\n"
    "v_mfma_f32_32x32x16_bf16 v[104:119], a[156:159], v[68:71], v[104:119]\n"
    "v_mfma_f32_32x32x16_bf16 v[120:135], a[128:131], v[72:75], 0\n"
    "v_mfma_f32_32x32x16_bf16 v[120:135], a[132:135], v[76:79], v[120:135]\n"
    "v_mfma_f32_32x32x16_bf16 v[120:135], a[136:139], v[80:83], v[120:135]\n"
    "v_mfma_f32_32x32x16_bf16 v[120:135], a[140:143], v[84:87], v[120:135]\n"
    "v_mfma_f32_32x32x16_bf16 v[120:135], a[144:147], v[88:91], v[120:135]\n"
    "v_mfma_f32_32x32x16_bf16 v[120:135], a[148:151], v[92:95], v[120:135]\n"
    "v_mfma_f32_32x32x16_bf16 v[120:135], a[152:155], v[96:99], v[120:135]\n"
    "v_mfma_f32_32x32x16_bf16 v[120:135], a[156:159], v[100:103], v[120:135]\n"
    "s_waitcnt lgkmcnt(7)\n"
    "v_mfma_f32_32x32x16_bf16 v[136:151], a[192:195], v[40:43], 0\n"
    "s_waitcnt lgkmcnt(6)\n"
    "v_mfma_f32_32x32x16_bf16 v[136:151], a[196:199], v[44:47], v[136:151]\n"
    "s_waitcnt lgkmcnt(5)\n"
    "v_mfma_f32_32x32x16_bf16 v[136:151], a[200:203], v[48:51], v[136:151]\n"
    "s_waitcnt lgkmcnt(4)\n"
    "v_mfma_f32_32x32x16_bf16 v[136:151], a[204:207], v[52:55], v[136:151]\n"
    "s_waitcnt lgkmcnt(3)\n"
    "v_mfma_f32_32x32x16_bf16 v[136:151], a[208:211], v[56:59], v[136:151]\n"
    "s_waitcnt lgkmcnt(2)\n"
    "v_mfma_f32_32x32x16_bf16 v[136:151], a[212:215], v[60:63], v[136:151]\n"
    "s_waitcnt lgkmcnt(1)\n"
    "v_mfma_f32_32x32x16_bf16 v[136:151], a[216:219], v[64:67], v[136:151]\n"
    "s_waitcnt lgkmcnt(0)\n"
    "v_mfma_f32_32x32x16_bf16 v[136:151], a[220:223], v[68:71], v[136:151]\n"
    "v_mfma_f32_32x32x16_bf16 v[152:167], a[192:195], v[72:75], 0\n"
    "v_mfma_f32_32x32x16_bf16 v[152:167], a[196:199], v[76:79], v[152:167]\n"
    "v_mfma_f32_32x32x16_bf16 v[152:167], a[200:203], v[80:83], v[152:167]\n"
    "v_mfma_f32_32x32x16_bf16 v[152:167], a[204:207], v[84:87], v[152:167]\n"
    "v_mfma_f32_32x32x16_bf16 v[152:167], a[208:211], v[88:91], v[152:167]\n"
    "v_mfma_f32_32x32x16_bf16 v[152:167], a[212:215], v[92:95], v[152:167]\n"
    "v_mfma_f32_32x32x16_bf16 v[152:167], a[216:219], v[96:99], v[152:167]\n"
    "v_mfma_f32_32x32x16_bf16 v[152:167], a[220:223], v[100:103], v[152:167]\n"
    "v_max3_f32 v27, v27, v104, v105\n"
    "v_max3_f32 v27, v27, v106, v107\n"
    "v_max3_f32 v27, v27, v108, v109\n"
    "v_max3_f32 v27, v27, v110, v111\n"
    "v_max3_f32 v27, v27, v112, v113\n"
    "v_max3_f32 v27, v27, v114, v115\n"
    "v_max3_f32 v27, v27, v116, v117\n"
    "v_max3_f32 v27, v27, v118, v119\n"
    "v_max3_f32 v27, v27, v136, v137\n"
    "v_max3_f32 v27, v27, v138, v139\n"
    "v_max3_f32 v27, v27, v140, v141\n"
    "v_max3_f32 v27, v27, v142, v143\n"
    "v_max3_f32 v27, v27, v144, v145\n"
    "v_max3_f32 v27, v27, v146, v147\n"
    "v_max3_f32 v27, v27, v148, v149\n"
    "v_max3_f32 v27, v27, v150, v151\n"
    "v_max3_f32 v28, v28, v120, v121\n"
    "v_max3_f32 v28, v28, v122, v123\n"
    "v_max3_f32 v28, v28, v124, v125\n"
    "v_max3_f32 v28, v28, v126, v127\n"
    "v_max3_f32 v28, v28, v128, v129\n"
    "v_max3_f32 v28, v28, v130, v131\n"
    "v_max3_f32 v28, v28, v132, v133\n"
    "v_max3_f32 v28, v28, v134, v135\n"
    "v_max3_f32 v28, v28, v152, v153\n"
    "v_max3_f32 v28, v28, v154, v155\n"
    "v_max3_f32 v28, v28, v156, v157\n"
    "v_max3_f32 v28, v28, v158, v159\n"
    "v_max3_f32 v28, v28, v160, v161\n"
    "v_max3_f32 v28, v28, v162, v163\n"
    "v_max3_f32 v28, v28, v164, v165\n"
    "v_max3_f32 v28, v28, v166, v167\n"
    "s_barrier\n"
    "s_add_u32 s89, s89, 1\n"
    "s_cmp_lt_u32 s89, 8\n"
    "s_cbranch_scc1 Lfb8_%=a\n"
    "v_mov_b32 v30, v27\n"
    "s_nop 1\n"
    "v_permlane32_swap_b32 v30, v27\n"
    "v_max_f32 v27, v27, v30\n"
    "v_sub_f32 v31, 0, v27\n"
    "v_mov_b32 v200, v31\n"
    "v_mov_b32 v201, v31\n"
    "v_mov_b32 v202, v31\n"
    "v_mov_b32 v203, v31\n"
    "v_mov_b32 v204, v31\n"
    "v_mov_b32 v205, v31\n"
    "v_mov_b32 v206, v31\n"
    "v_mov_b32 v207, v31\n"
    "v_mov_b32 v208, v31\n"
    "v_mov_b32 v209, v31\n"
    "v_mov_b32 v210, v31\n"
    "v_mov_b32 v211, v31\n"
    "v_mov_b32 v212, v31\n"
    "v_mov_b32 v213, v31\n"
    "v_mov_b32 v214, v31\n"
    "v_mov_b32 v215, v31\n"
    "v_mov_b32 v25, 0\n"
    "v_mov_b32 v30, v28\n"
    "s_nop 1\n"
    "v_permlane32_swap_b32 v30, v28\n"
    "v_max_f32 v28, v28, v30\n"
    "v_sub_f32 v31, 0, v28\n"
    "v_mov_b32 v216, v31\n"
    "v_mov_b32 v217, v31\n"
    "v_mov_b32 v218, v31\n"
    "v_mov_b32 v219, v31\n"
    "v_mov_b32 v220, v31\n"
    "v_mov_b32 v221, v31\n"
    "v_mov_b32 v222, v31\n"
    "v_mov_b32 v223, v31\n"
    "v_mov_b32 v224, v31\n"
    "v_mov_b32 v225, v31\n"
    "v_mov_b32 v226, v31\n"
    "v_mov_b32 v227, v31\n"
    "v_mov_b32 v228, v31\n"
    "v_mov_b32 v229, v31\n"
    "v_mov_b32 v230, v31\n"
    "v_mov_b32 v231, v31\n"
    "v_mov_b32 v26, 0\n"
    "v_accvgpr_write_b32 a0, 0\n"
    "v_accvgpr_write_b32 a1, 0\n"
    "v_accvgpr_write_b32 a2, 0\n"
    "v_accvgpr_write_b32 a3, 0\n"
    "v_accvgpr_write_b32 a4, 0\n"
    "v_accvgpr_write_b32 a5, 0\n"
    "v_accvgpr_write_b32 a6, 0\n"
    "v_accvgpr_write_b32 a7, 0\n"
    "v_accvgpr_write_b32 a8, 0\n"
    "v_accvgpr_write_b32 a9, 0\n"
    "v_accvgpr_write_b32 a10, 0\n"
    "v_accvgpr_write_b32 a11, 0\n"
    "v_accvgpr_write_b32 a12, 0\n"
    "v_accvgpr_write_b32 a13, 0\n"
    "v_accvgpr_write_b32 a14, 0\n"
    "v_accvgpr_write_b32 a15, 0\n"
    "v_accvgpr_write_b32 a16, 0\n"
    "v_accvgpr_write_b32 a17, 0\n"
    "v_accvgpr_write_b32 a18, 0\n"
    "v_accvgpr_write_b32 a19, 0\n"
    "v_accvgpr_write_b32 a20, 0\n"
    "v_accvgpr_write_b32 a21, 0\n"
    "v_accvgpr_write_b32 a22, 0\n"
    "v_accvgpr_write_b32 a23, 0\n"
    "v_accvgpr_write_b32 a24, 0\n"
    "v_accvgpr_write_b32 a25, 0\n"
    "v_accvgpr_write_b32 a26, 0\n"
    "v_accvgpr_write_b32 a27, 0\n"
    "v_accvgpr_write_b32 a28, 0\n"
    "v_accvgpr_write_b32 a29, 0\n"
    "v_accvgpr_write_b32 a30, 0\n"
    "v_accvgpr_write_b32 a31, 0\n"
    "v_accvgpr_write_b32 a32, 0\n"
    "v_accvgpr_write_b32 a33, 0\n"
    "v_accvgpr_write_b32 a34, 0\n"
    "v_accvgpr_write_b32 a35, 0\n"
    "v_accvgpr_write_b32 a36, 0\n"
    "v_accvgpr_write_b32 a37, 0\n"
    "v_accvgpr_write_b32 a38, 0\n"
    "v_accvgpr_write_b32 a39, 0\n"
    "v_accvgpr_write_b32 a40, 0\n"
    "v_accvgpr_write_b32 a41, 0\n"
    "v_accvgpr_write_b32 a42, 0\n"
    "v_accvgpr_write_b32 a43, 0\n"
    "v_accvgpr_write_b32 a44, 0\n"
    "v_accvgpr_write_b32 a45, 0\n"
    "v_accvgpr_write_b32 a46, 0\n"
    "v_accvgpr_write_b32 a47, 0\n"
    "v_accvgpr_write_b32 a48, 0\n"
    "v_accvgpr_write_b32 a49, 0\n"
    "v_accvgpr_write_b32 a50, 0\n"
    "v_accvgpr_write_b32 a51, 0\n"
    "v_accvgpr_write_b32 a52, 0\n"
    "v_accvgpr_write_b32 a53, 0\n"
    "v_accvgpr_write_b32 a54, 0\n"
    "v_accvgpr_write_b32 a55, 0\n"
    "v_accvgpr_write_b32 a56, 0\n"
    "v_accvgpr_write_b32 a57, 0\n"
    "v_accvgpr_write_b32 a58, 0\n"
    "v_accvgpr_write_b32 a59, 0\n"
    "v_accvgpr_write_b32 a60, 0\n"
    "v_accvgpr_write_b32 a61, 0\n"
    "v_accvgpr_write_b32 a62, 0\n"
    "v_accvgpr_write_b32 a63, 0\n"
    "v_accvgpr_write_b32 a64, 0\n"
    "v_accvgpr_write_b32 a65, 0\n"
    "v_accvgpr_write_b32 a66, 0\n"
    "v_accvgpr_write_b32 a67, 0\n"
    "v_accvgpr_write_b32 a68, 0\n"
    "v_accvgpr_write_b32 a69, 0\n"
    "v_accvgpr_write_b32 a70, 0\n"
    "v_accvgpr_write_b32 a71, 0\n"
    "v_accvgpr_write_b32 a72, 0\n"
    "v_accvgpr_write_b32 a73, 0\n"
    "v_accvgpr_write_b32 a74, 0\n"
    "v_accvgpr_write_b32 a75, 0\n"
    "v_accvgpr_write_b32 a76, 0\n"
    "v_accvgpr_write_b32 a77, 0\n"
    "v_accvgpr_write_b32 a78, 0\n"
    "v_accvgpr_write_b32 a79, 0\n"
    "v_accvgpr_write_b32 a80, 0\n"
    "v_accvgpr_write_b32 a81, 0\n"
    "v_accvgpr_write_b32 a82, 0\n"
    "v_accvgpr_write_b32 a83, 0\n"
    "v_accvgpr_write_b32 a84, 0\n"
    "v_accvgpr_write_b32 a85, 0\n"
    "v_accvgpr_write_b32 a86, 0\n"
    "v_accvgpr_write_b32 a87, 0\n"
    "v_accvgpr_write_b32 a88, 0\n"
    "v_accvgpr_write_b32 a89, 0\n"
    "v_accvgpr_write_b32 a90, 0\n"
    "v_accvgpr_write_b32 a91, 0\n"
    "v_accvgpr_write_b32 a92, 0\n"
    "v_accvgpr_write_b32 a93, 0\n"
    "v_accvgpr_write_b32 a94, 0\n"
    "v_accvgpr_write_b32 a95, 0\n"
    "v_accvgpr_write_b32 a96, 0\n"
    "v_accvgpr_write_b32 a97, 0\n"
    "v_accvgpr_write_b32 a98, 0\n"
    "v_accvgpr_write_b32 a99, 0\n"
    "v_accvgpr_write_b32 a100, 0\n"
    "v_accvgpr_write_b32 a101, 0\n"
    "v_accvgpr_write_b32 a102, 0\n"
    "v_accvgpr_write_b32 a103, 0\n"
    "v_accvgpr_write_b32 a104, 0\n"
    "v_accvgpr_write_b32 a105, 0\n"
    "v_accvgpr_write_b32 a106, 0\n"
    "v_accvgpr_write_b32 a107, 0\n"
    "v_accvgpr_write_b32 a108, 0\n"
    "v_accvgpr_write_b32 a109, 0\n"
    "v_accvgpr_write_b32 a110, 0\n"
    "v_accvgpr_write_b32 a111, 0\n"
    "v_accvgpr_write_b32 a112, 0\n"
    "v_accvgpr_write_b32 a113, 0\n"
    "v_accvgpr_write_b32 a114, 0\n"
    "v_accvgpr_write_b32 a115, 0\n"
    "v_accvgpr_write_b32 a116, 0\n"
    "v_accvgpr_write_b32 a117, 0\n"
    "v_accvgpr_write_b32 a118, 0\n"
    "v_accvgpr_write_b32 a119, 0\n"
    "v_accvgpr_write_b32 a120, 0\n"
    "v_accvgpr_write_b32 a121, 0\n"
    "v_accvgpr_write_b32 a122, 0\n"
    "v_accvgpr_write_b32 a123, 0\n"
    "v_accvgpr_write_b32 a124, 0\n"
    "v_accvgpr_write_b32 a125, 0\n"
    "v_accvgpr_write_b32 a126, 0\n"
    "v_accvgpr_write_b32 a127, 0\n"
    "s_mov_b32 s89, 0\n"
    "s_nop 3\n"
    "Lfb8_%=b:\n"
    "s_lshl_b32 s83, s89, 14\n"
    "s_add_u32 m0, s81, 0x8000\n"
    "s_nop 0\n"
    "buffer_load_dwordx4 v6, s[56:59], s83 offen lds\n"
    "s_add_u32 m0, s81, 0x8400\n"
    "s_nop 0\n"
    "buffer_load_dwordx4 v7, s[56:59], s83 offen lds\n"
    "s_add_u32 m0, s81, 0xa000\n"
    "s_nop 0\n"
    "buffer_load_dwordx4 v8, s[56:59], s83 offen lds\n"
    "s_add_u32 m0, s81, 0xa400\n"
    "s_nop 0\n"
    "buffer_load_dwordx4 v9, s[56:59], s83 offen lds\n"
    "s_lshl_b32 s83, s89, 14\n"
    "s_add_u32 m0, s80, 0x10000\n"
    "s_nop 0\n"
    "buffer_load_dwordx4 v10, s[60:63], s83 offen lds\n"
    "s_add_u32 m0, s80, 0x10400\n"
    "s_nop 0\n"
    "buffer_load_dwordx4 v11, s[60:63], s83 offen lds\n"
    "s_add_u32 m0, s80, 0x10800\n"
    "s_nop 0\n"
    "buffer_load_dwordx4 v12, s[60:63], s83 offen lds\n"
    "s_add_u32 m0, s80, 0x10c00\n"
    "s_nop 0\n"
    "buffer_load_dwordx4 v13, s[60:63], s83 offen lds\n"
    "s_waitcnt vmcnt(0)\n"
    "s_barrier\n"
    "ds_read_b128 a[128:131], v22 offset:32768\n"
    "ds_read_b128 a[132:135], v23 offset:32768\n"
    "ds_read_b128 a[136:139], v22 offset:33280\n"
    "ds_read_b128 a[140:143], v23 offset:33280\n"
    "ds_read_b128 a[144:147], v22 offset:33792\n"
    "ds_read_b128 a[148:151], v23 offset:33792\n"
    "ds_read_b128 a[152:155], v22 offset:34304\n"
    "ds_read_b128 a[156:159], v23 offset:34304\n"
    "ds_read_b128 a[192:195], v22 offset:40960\n"
    "ds_read_b128 a[196:199], v23 offset:40960\n"
    "ds_read_b128 a[200:203], v22 offset:41472\n"
    "ds_read_b128 a[204:207], v23 offset:41472\n"
    "ds_read_b128 a[208:211], v22 offset:41984\n"
    "ds_read_b128 a[212:215], v23 offset:41984\n"
    "ds_read_b128 a[216:219], v22 offset:42496\n"
    "ds_read_b128 a[220:223], v23 offset:42496\n"
    "s_waitcnt lgkmcnt(15)\n"
    "v_mfma_f32_32x32x16_bf16 v[104:119], a[128:131], v[40:43], v[200:215]\n"
    "s_waitcnt lgkmcnt(14)\n"
    "v_mfma_f32_32x32x16_bf16 v[104:119], a[132:135], v[44:47], v[104:119]\n"
    "s_waitcnt lgkmcnt(13)\n"
    "v_mfma_f32_32x32x16_bf16 v[104:119], a[136:139], v[48:51], v[104:119]\n"
    "s_waitcnt lgkmcnt(12)\n"
    "v_mfma_f32_32x32x16_bf16 v[104:119], a[140:143], v[52:55], v[104:119]\n"
    "s_waitcnt lgkmcnt(11)\n"
    "v_mfma_f32_32x32x16_bf16 v[104:119], a[144:147], v[56:59], v[104:119]\n"
    "s_waitcnt lgkmcnt(10)\n"
    "v_mfma_f32_32x32x16_bf16 v[104:119], a[148:151], v[60:63], v[104:119]\n"
    "s_waitcnt lgkmcnt(9)\n"
    "v_mfma_f32_32x32x16_bf16 v[104:119], a[152:155], v[64:67], v[104:119]\n"
    "s_waitcnt lgkmcnt(8)\n"
    "v_mfma_f32_32x32x16_bf16 v[104:119], a[156:159], v[68:71], v[104:119]\n"
    "v_mfma_f32_32x32x16_bf16 v[120:135], a[128:131], v[72:75], v[216:231]\n"
    "v_mfma_f32_32x32x16_bf16 v[120:135], a[132:135], v[76:79], v[120:135]\n"
    "v_mfma_f32_32x32x16_bf16 v[120:135], a[136:139], v[80:83], v[120:135]\n"
    "v_mfma_f32_32x32x16_bf16 v[120:135], a[140:143], v[84:87], v[120:135]\n"
    "v_mfma_f32_32x32x16_bf16 v[120:135], a[144:147], v[88:91], v[120:135]\n"
    "v_mfma_f32_32x32x16_bf16 v[120:135], a[148:151], v[92:95], v[120:135]\n"
    "v_mfma_f32_32x32x16_bf16 v[120:135], a[152:155], v[96:99], v[120:135]\n"
    "v_mfma_f32_32x32x16_bf16 v[120:135], a[156:159], v[100:103], v[120:135]\n"
    "s_waitcnt lgkmcnt(7)\n"
    "v_mfma_f32_32x32x16_bf16 v[136:151], a[192:195], v[40:43], v[200:215]\n"
    "s_waitcnt lgkmcnt(6)\n"
    "v_mfma_f32_32x32x16_bf16 v[136:151], a[196:199], v[44:47], v[136:151]\n"
    "s_waitcnt lgkmcnt(5)\n"
    "v_mfma_f32_32x32x16_bf16 v[136:151], a[200:203], v[48:51], v[136:151]\n"
    "s_waitcnt lgkmcnt(4)\n"
    "v_mfma_f32_32x32x16_bf16 v[136:151], a[204:207], v[52:55], v[136:151]\n"
    "s_waitcnt lgkmcnt(3)\n"
    "v_mfma_f32_32x32x16_bf16 v[136:151], a[208:211], v[56:59], v[136:151]\n"
    "s_waitcnt lgkmcnt(2)\n"
    "v_mfma_f32_32x32x16_bf16 v[136:151], a[212:215], v[60:63], v[136:151]\n"
    "s_waitcnt lgkmcnt(1)\n"
    "v_mfma_f32_32x32x16_bf16 v[136:151], a[216:219], v[64:67], v[136:151]\n"
    "s_waitcnt lgkmcnt(0)\n"
    "v_mfma_f32_32x32x16_bf16 v[136:151], a[220:223], v[68:71], v[136:151]\n"
    "v_mfma_f32_32x32x16_bf16 v[152:167], a[192:195], v[72:75], v[216:231]\n"
    "v_mfma_f32_32x32x16_bf16 v[152:167], a[196:199], v[76:79], v[152:167]\n"
    "v_mfma_f32_32x32x16_bf16 v[152:167], a[200:203], v[80:83], v[152:167]\n"
    "v_mfma_f32_32x32x16_bf16 v[152:167], a[204:207], v[84:87], v[152:167]\n"
    "v_mfma_f32_32x32x16_bf16 v[152:167], a[208:211], v[88:91], v[152:167]\n"
    "v_mfma_f32_32x32x16_bf16 v[152:167], a[212:215], v[92:95], v[152:167]\n"
    "v_mfma_f32_32x32x16_bf16 v[152:167], a[216:219], v[96:99], v[152:167]\n"
    "v_mfma_f32_32x32x16_bf16 v[152:167], a[220:223], v[100:103], v[152:167]\n"
    "v_exp_f32 v104, v104\n"
    "v_exp_f32 v105, v105\n"
    "v_exp_f32 v106, v106\n"
    "v_exp_f32 v107, v107\n"
    "v_exp_f32 v108, v108\n"
    "v_exp_f32 v109, v109\n"
    "v_exp_f32 v110, v110\n"
    "v_exp_f32 v111, v111\n"
    "v_exp_f32 v112, v112\n"
    "v_exp_f32 v113, v113\n"
    "v_exp_f32 v114, v114\n"
    "v_exp_f32 v115, v115\n"
    "v_exp_f32 v116, v116\n"
    "v_exp_f32 v117, v117\n"
    "v_exp_f32 v118, v118\n"
    "v_exp_f32 v119, v119\n"
    "v_exp_f32 v120, v120\n"
    "v_exp_f32 v121, v121\n"
    "v_exp_f32 v122, v122\n"
    "v_exp_f32 v123, v123\n"
    "v_exp_f32 v124, v124\n"
    "v_exp_f32 v125, v125\n"
    "v_exp_f32 v126, v126\n"
    "v_exp_f32 v127, v127\n"
    "v_exp_f32 v128, v128\n"
    "v_exp_f32 v129, v129\n"
    "v_exp_f32 v130, v130\n"
    "v_exp_f32 v131, v131\n"
    "v_exp_f32 v132, v132\n"
    "v_exp_f32 v133, v133\n"
    "v_exp_f32 v134, v134\n"
    "v_exp_f32 v135, v135\n"
    "v_add_f32 v25, v25, v104\n"
    "v_add_f32 v25, v25, v105\n"
    "v_add_f32 v25, v25, v106\n"
    "v_add_f32 v25, v25, v107\n"
    "v_add_f32 v25, v25, v108\n"
    "v_add_f32 v25, v25, v109\n"
    "v_add_f32 v25, v25, v110\n"
    "v_add_f32 v25, v25, v111\n"
    "v_add_f32 v25, v25, v112\n"
    "v_add_f32 v25, v25, v113\n"
    "v_add_f32 v25, v25, v114\n"
    "v_add_f32 v25, v25, v115\n"
    "v_add_f32 v25, v25, v116\n"
    "v_add_f32 v25, v25, v117\n"
    "v_add_f32 v25, v25, v118\n"
    "v_add_f32 v25, v25, v119\n"
    "v_add_f32 v26, v26, v120\n"
    "v_add_f32 v26, v26, v121\n"
    "v_add_f32 v26, v26, v122\n"
    "v_add_f32 v26, v26, v123\n"
    "v_add_f32 v26, v26, v124\n"
    "v_add_f32 v26, v26, v125\n"
    "v_add_f32 v26, v26, v126\n"
    "v_add_f32 v26, v26, v127\n"
    "v_add_f32 v26, v26, v128\n"
    "v_add_f32 v26, v26, v129\n"
    "v_add_f32 v26, v26, v130\n"
    "v_add_f32 v26, v26, v131\n"
    "v_add_f32 v26, v26, v132\n"
    "v_add_f32 v26, v26, v133\n"
    "v_add_f32 v26, v26, v134\n"
    "v_add_f32 v26, v26, v135\n"
    "v_cvt_pk_bf16_f32 v168, v104, v105\n"
    "v_cvt_pk_bf16_f32 v169, v106, v107\n"
    "v_cvt_pk_bf16_f32 v170, v108, v109\n"
    "v_cvt_pk_bf16_f32 v171, v110, v111\n"
    "v_cvt_pk_bf16_f32 v172, v112, v113\n"
    "v_cvt_pk_bf16_f32 v173, v114, v115\n"
    "v_cvt_pk_bf16_f32 v174, v116, v117\n"
    "v_cvt_pk_bf16_f32 v175, v118, v119\n"
    "v_cvt_pk_bf16_f32 v176, v120, v121\n"
    "v_cvt_pk_bf16_f32 v177, v122, v123\n"
    "v_cvt_pk_bf16_f32 v178, v124, v125\n"
    "v_cvt_pk_bf16_f32 v179, v126, v127\n"
    "v_cvt_pk_bf16_f32 v180, v128, v129\n"
    "v_cvt_pk_bf16_f32 v181, v130, v131\n"
    "v_cvt_pk_bf16_f32 v182, v132, v133\n"
    "v_cvt_pk_bf16_f32 v183, v134, v135\n"
    "ds_read_b64_tr_b16 a[160:161], v18 offset:0\n"
    "ds_read_b64_tr_b16 a[162:163], v18 offset:1024\n"
    "ds_read_b64_tr_b16 a[164:165], v19 offset:0\n"
    "ds_read_b64_tr_b16 a[166:167], v19 offset:1024\n"
    "ds_read_b64_tr_b16 a[168:169], v20 offset:0\n"
    "ds_read_b64_tr_b16 a[170:171], v20 offset:1024\n"
    "ds_read_b64_tr_b16 a[172:173], v21 offset:0\n"
    "ds_read_b64_tr_b16 a[174:175], v21 offset:1024\n"
    "ds_read_b64_tr_b16 a[176:177], v18 offset:4096\n"
    "ds_read_b64_tr_b16 a[178:179], v18 offset:5120\n"
    "ds_read_b64_tr_b16 a[180:181], v19 offset:4096\n"
    "ds_read_b64_tr_b16 a[182:183], v19 offset:5120\n"
    "ds_read_b64_tr_b16 a[184:185], v20 offset:4096\n"
    "ds_read_b64_tr_b16 a[186:187], v20 offset:5120\n"
    "ds_read_b64_tr_b16 a[188:189], v21 offset:4096\n"
    "ds_read_b64_tr_b16 a[190:191], v21 offset:5120\n"
    "s_waitcnt lgkmcnt(14)\n"
    "v_mfma_f32_32x32x16_bf16 a[0:15], a[160:163], v[168:171], a[0:15]\n"
    "v_mfma_f32_32x32x16_bf16 a[16:31], a[160:163], v[176:179], a[16:31]\n"
    "s_waitcnt lgkmcnt(12)\n"
    "v_mfma_f32_32x32x16_bf16 a[32:47], a[164:167], v[168:171], a[32:47]\n"
    "v_mfma_f32_32x32x16_bf16 a[48:63], a[164:167], v[176:179], a[48:63]\n"
    "s_waitcnt lgkmcnt(10)\n"
    "v_mfma_f32_32x32x16_bf16 a[64:79], a[168:171], v[168:171], a[64:79]\n"
    "v_mfma_f32_32x32x16_bf16 a[80:95], a[168:171], v[176:179], a[80:95]\n"
    "s_waitcnt lgkmcnt(8)\n"
    "v_mfma_f32_32x32x16_bf16 a[96:111], a[172:175], v[168:171], a[96:111]\n"
    "v_mfma_f32_32x32x16_bf16 a[112:127], a[172:175], v[176:179], a[112:127]\n"
    "s_waitcnt lgkmcnt(6)\n"
    "v_mfma_f32_32x32x16_bf16 a[0:15], a[176:179], v[172:175], a[0:15]\n"
    "v_mfma_f32_32x32x16_bf16 a[16:31], a[176:179], v[180:183], a[16:31]\n"
    "s_waitcnt lgkmcnt(4)\n"
    "v_mfma_f32_32x32x16_bf16 a[32:47], a[180:183], v[172:175], a[32:47]\n"
    "v_mfma_f32_32x32x16_bf16 a[48:63], a[180:183], v[180:183], a[48:63]\n"
    "s_waitcnt lgkmcnt(2)\n"
    "v_mfma_f32_32x32x16_bf16 a[64:79], a[184:187], v[172:175], a[64:79]\n"
    "v_mfma_f32_32x32x16_bf16 a[80:95], a[184:187], v[180:183], a[80:95]\n"
    "s_waitcnt lgkmcnt(0)\n"
    "v_mfma_f32_32x32x16_bf16 a[96:111], a[188:191], v[172:175], a[96:111]\n"
    "v_mfma_f32_32x32x16_bf16 a[112:127], a[188:191], v[180:183], a[112:127]\n"
    "v_exp_f32 v136, v136\n"
    "v_exp_f32 v137, v137\n"
    "v_exp_f32 v138, v138\n"
    "v_exp_f32 v139, v139\n"
    "v_exp_f32 v140, v140\n"
    "v_exp_f32 v141, v141\n"
    "v_exp_f32 v142, v142\n"
    "v_exp_f32 v143, v143\n"
    "v_exp_f32 v144, v144\n"
    "v_exp_f32 v145, v145\n"
    "v_exp_f32 v146, v146\n"
    "v_exp_f32 v147, v147\n"
    "v_exp_f32 v148, v148\n"
    "v_exp_f32 v149, v149\n"
    "v_exp_f32 v150, v150\n"
    "v_exp_f32 v151, v151\n"
    "v_exp_f32 v152, v152\n"
    "v_exp_f32 v153, v153\n"
    "v_exp_f32 v154, v154\n"
    "v_exp_f32 v155, v155\n"
    "v_exp_f32 v156, v156\n"
    "v_exp_f32 v157, v157\n"
    "v_exp_f32 v158, v158\n"
    "v_exp_f32 v159, v159\n"
    "v_exp_f32 v160, v160\n"
    "v_exp_f32 v161, v161\n"
    "v_exp_f32 v162, v162\n"
    "v_exp_f32 v163, v163\n"
    "v_exp_f32 v164, v164\n"
    "v_exp_f32 v165, v165\n"
    "v_exp_f32 v166, v166\n"
    "v_exp_f32 v167, v167\n"
    "v_add_f32 v25, v25, v136\n"
    "v_add_f32 v25, v25, v137\n"
    "v_add_f32 v25, v25, v138\n"
    "v_add_f32 v25, v25, v139\n"
    "v_add_f32 v25, v25, v140\n"
    "v_add_f32 v25, v25, v141\n"
    "v_add_f32 v25, v25, v142\n"
    "v_add_f32 v25, v25, v143\n"
    "v_add_f32 v25, v25, v144\n"
    "v_add_f32 v25, v25, v145\n"
    "v_add_f32 v25, v25, v146\n"
    "v_add_f32 v25, v25, v147\n"
    "v_add_f32 v25, v25, v148\n"
    "v_add_f32 v25, v25, v149\n"
    "v_add_f32 v25, v25, v150\n"
    "v_add_f32 v25, v25, v151\n"
    "v_add_f32 v26, v26, v152\n"
    "v_add_f32 v26, v26, v153\n"
    "v_add_f32 v26, v26, v154\n"
    "v_add_f32 v26, v26, v155\n"
    "v_add_f32 v26, v26, v156\n"
    "v_add_f32 v26, v26, v157\n"
    "v_add_f32 v26, v26, v158\n"
    "v_add_f32 v26, v26, v159\n"
    "v_add_f32 v26, v26, v160\n"
    "v_add_f32 v26, v26, v161\n"
    "v_add_f32 v26, v26, v162\n"
    "v_add_f32 v26, v26, v163\n"
    "v_add_f32 v26, v26, v164\n"
    "v_add_f32 v26, v26, v165\n"
    "v_add_f32 v26, v26, v166\n"
    "v_add_f32 v26, v26, v167\n"
    "v_cvt_pk_bf16_f32 v184, v136, v137\n"
    "v_cvt_pk_bf16_f32 v185, v138, v139\n"
    "v_cvt_pk_bf16_f32 v186, v140, v141\n"
    "v_cvt_pk_bf16_f32 v187, v142, v143\n"
    "v_cvt_pk_bf16_f32 v188, v144, v145\n"
    "v_cvt_pk_bf16_f32 v189, v146, v147\n"
    "v_cvt_pk_bf16_f32 v190, v148, v149\n"
    "v_cvt_pk_bf16_f32 v191, v150, v151\n"
    "v_cvt_pk_bf16_f32 v192, v152, v153\n"
    "v_cvt_pk_bf16_f32 v193, v154, v155\n"
    "v_cvt_pk_bf16_f32 v194, v156, v157\n"
    "v_cvt_pk_bf16_f32 v195, v158, v159\n"
    "v_cvt_pk_bf16_f32 v196, v160, v161\n"
    "v_cvt_pk_bf16_f32 v197, v162, v163\n"
    "v_cvt_pk_bf16_f32 v198, v164, v165\n"
    "v_cvt_pk_bf16_f32 v199, v166, v167\n"
    "ds_read_b64_tr_b16 a[160:161], v18 offset:8192\n"
    "ds_read_b64_tr_b16 a[162:163], v18 offset:9216\n"
    "ds_read_b64_tr_b16 a[164:165], v19 offset:8192\n"
    "ds_read_b64_tr_b16 a[166:167], v19 offset:9216\n"
    "ds_read_b64_tr_b16 a[168:169], v20 offset:8192\n"
    "ds_read_b64_tr_b16 a[170:171], v20 offset:9216\n"
    "ds_read_b64_tr_b16 a[172:173], v21 offset:8192\n"
    "ds_read_b64_tr_b16 a[174:175], v21 offset:9216\n"
    "ds_read_b64_tr_b16 a[176:177], v18 offset:12288\n"
    "ds_read_b64_tr_b16 a[178:179], v18 offset:13312\n"
    "ds_read_b64_tr_b16 a[180:181], v19 offset:12288\n"
    "ds_read_b64_tr_b16 a[182:183], v19 offset:13312\n"
    "ds_read_b64_tr_b16 a[184:185], v20 offset:12288\n"
    "ds_read_b64_tr_b16 a[186:187], v20 offset:13312\n"
    "ds_read_b64_tr_b16 a[188:189], v21 offset:12288\n"
    "ds_read_b64_tr_b16 a[190:191], v21 offset:13312\n"
    "s_waitcnt lgkmcnt(14)\n"
    "v_mfma_f32_32x32x16_bf16 a[0:15], a[160:163], v[184:187], a[0:15]\n"
    "v_mfma_f32_32x32x16_bf16 a[16:31], a[160:163], v[192:195], a[16:31]\n"
    "s_waitcnt lgkmcnt(12)\n"
    "v_mfma_f32_32x32x16_bf16 a[32:47], a[164:167], v[184:187], a[32:47]\n"
    "v_mfma_f32_32x32x16_bf16 a[48:63], a[164:167], v[192:195], a[48:63]\n"
    "s_waitcnt lgkmcnt(10)\n"
    "v_mfma_f32_32x32x16_bf16 a[64:79], a[168:171], v[184:187], a[64:79]\n"
    "v_mfma_f32_32x32x16_bf16 a[80:95], a[168:171], v[192:195], a[80:95]\n"
    "s_waitcnt lgkmcnt(8)\n"
    "v_mfma_f32_32x32x16_bf16 a[96:111], a[172:175], v[184:187], a[96:111]\n"
    "v_mfma_f32_32x32x16_bf16 a[112:127], a[172:175], v[192:195], a[112:127]\n"
    "s_waitcnt lgkmcnt(6)\n"
    "v_mfma_f32_32x32x16_bf16 a[0:15], a[176:179], v[188:191], a[0:15]\n"
    "v_mfma_f32_32x32x16_bf16 a[16:31], a[176:179], v[196:199], a[16:31]\n"
    "s_waitcnt lgkmcnt(4)\n"
    "v_mfma_f32_32x32x16_bf16 a[32:47], a[180:183], v[188:191], a[32:47]\n"
    "v_mfma_f32_32x32x16_bf16 a[48:63], a[180:183], v[196:199], a[48:63]\n"
    "s_waitcnt lgkmcnt(2)\n"
    "v_mfma_f32_32x32x16_bf16 a[64:79], a[184:187], v[188:191], a[64:79]\n"
    "v_mfma_f32_32x32x16_bf16 a[80:95], a[184:187], v[196:199], a[80:95]\n"
    "s_waitcnt lgkmcnt(0)\n"
    "v_mfma_f32_32x32x16_bf16 a[96:111], a[188:191], v[188:191], a[96:111]\n"
    "v_mfma_f32_32x32x16_bf16 a[112:127], a[188:191], v[196:199], a[112:127]\n"
    "s_nop 15\n"
    "s_barrier\n"
    "s_add_u32 s89, s89, 1\n"
    "s_cmp_lt_u32 s89, 8\n"
    "s_cbranch_scc1 Lfb8_%=b\n"
    "s_nop 15\n"
    "s_mov_b32 s98, 1\n"
    "v_mov_b32 v37, v25\n"
    "s_nop 1\n"
    "v_permlane32_swap_b32 v37, v25\n"
    "v_add_f32 v25, v25, v37\n"
    "v_rcp_f32 v30, v25\n"
    "v_mov_b32 v37, v26\n"
    "s_nop 1\n"
    "v_permlane32_swap_b32 v37, v26\n"
    "v_add_f32 v26, v26, v37\n"
    "v_rcp_f32 v32, v26\n"
    "v_mov_b32 v35, 0x58800000\n"
    "v_cmp_nle_f32 vcc, v25, v35\n"
    "s_nop 3\n"
    "s_mov_b64 s[50:51], vcc\n"
    "v_cmp_nle_f32 vcc, v26, v35\n"
    "s_nop 3\n"
    "s_or_b64 s[50:51], s[50:51], vcc\n"
    "s_cmp_lg_u64 s[50:51], 0\n"
    "s_cselect_b32 s49, 1, 0\n"
    "s_cmp_eq_u32 s98, 0\n"
    "s_cselect_b32 s49, s49, 0\n"
    "s_lshl_b32 s89, s34, 2\n"
    "s_add_u32 s89, s89, 0x20040\n"
    "v_mov_b32 v36, s49\n"
    "v_mov_b32 v38, s89\n"
    "ds_write_b32 v38, v36 offset:0\n"
    "v_accvgpr_read_b32 v104, a0\n"
    "v_accvgpr_read_b32 v105, a1\n"
    "v_accvgpr_read_b32 v106, a2\n"
    "v_accvgpr_read_b32 v107, a3\n"
    "v_accvgpr_read_b32 v108, a4\n"
    "v_accvgpr_read_b32 v109, a5\n"
    "v_accvgpr_read_b32 v110, a6\n"
    "v_accvgpr_read_b32 v111, a7\n"
    "v_pk_mul_f32 v[104:105], v[104:105], v[30:31] op_sel_hi:[1,0]\n"
    "v_pk_mul_f32 v[106:107], v[106:107], v[30:31] op_sel_hi:[1,0]\n"
    "v_pk_mul_f32 v[108:109], v[108:109], v[30:31] op_sel_hi:[1,0]\n"
    "v_pk_mul_f32 v[110:111], v[110:111], v[30:31] op_sel_hi:[1,0]\n"
    "v_cvt_pk_bf16_f32 v168, v104, v105\n"
    "v_cvt_pk_bf16_f32 v169, v106, v107\n"
    "v_cvt_pk_bf16_f32 v170, v108, v109\n"
    "v_cvt_pk_bf16_f32 v171, v110, v111\n"
    "s_mov_b32 s47, 0\n"
    "buffer_store_dwordx4 v[168:171], v24, s[68:71], s47 offen offset:0 sc1\n"
    "v_accvgpr_read_b32 v112, a8\n"
    "v_accvgpr_read_b32 v113, a9\n"
    "v_accvgpr_read_b32 v114, a10\n"
    "v_accvgpr_read_b32 v115, a11\n"
    "v_accvgpr_read_b32 v116, a12\n"
    "v_accvgpr_read_b32 v117, a13\n"
    "v_accvgpr_read_b32 v118, a14\n"
    "v_accvgpr_read_b32 v119, a15\n"
    "v_pk_mul_f32 v[112:113], v[112:113], v[30:31] op_sel_hi:[1,0]\n"
    "v_pk_mul_f32 v[114:115], v[114:115], v[30:31] op_sel_hi:[1,0]\n"
    "v_pk_mul_f32 v[116:117], v[116:117], v[30:31] op_sel_hi:[1,0]\n"
    "v_pk_mul_f32 v[118:119], v[118:119], v[30:31] op_sel_hi:[1,0]\n"
    "v_cvt_pk_bf16_f32 v172, v112, v113\n"
    "v_cvt_pk_bf16_f32 v173, v114, v115\n"
    "v_cvt_pk_bf16_f32 v174, v116, v117\n"
    "v_cvt_pk_bf16_f32 v175, v118, v119\n"
    "s_mov_b32 s47, 0\n"
    "buffer_store_dwordx4 v[172:175], v24, s[68:71], s47 offen offset:512 sc1\n"
    "v_accvgpr_read_b32 v120, a16\n"
    "v_accvgpr_read_b32 v121, a17\n"
    "v_accvgpr_read_b32 v122, a18\n"
    "v_accvgpr_read_b32 v123, a19\n"
    "v_accvgpr_read_b32 v124, a20\n"
    "v_accvgpr_read_b32 v125, a21\n"
    "v_accvgpr_read_b32 v126, a22\n"
    "v_accvgpr_read_b32 v127, a23\n"
    "v_pk_mul_f32 v[120:121], v[120:121], v[32:33] op_sel_hi:[1,0]\n"
    "v_pk_mul_f32 v[122:123], v[122:123], v[32:33] op_sel_hi:[1,0]\n"
    "v_pk_mul_f32 v[124:125], v[124:125], v[32:33] op_sel_hi:[1,0]\n"
    "v_pk_mul_f32 v[126:127], v[126:127], v[32:33] op_sel_hi:[1,0]\n"
    "v_cvt_pk_bf16_f32 v176, v120, v121\n"
    "v_cvt_pk_bf16_f32 v177, v122, v123\n"
    "v_cvt_pk_bf16_f32 v178, v124, v125\n"
    "v_cvt_pk_bf16_f32 v179, v126, v127\n"
    "s_mov_b32 s47, 0\n"
    "buffer_store_dwordx4 v[176:179], v24, s[68:71], s47 offen offset:2048 sc1\n"
    "v_accvgpr_read_b32 v128, a24\n"
    "v_accvgpr_read_b32 v129, a25\n"
    "v_accvgpr_read_b32 v130, a26\n"
    "v_accvgpr_read_b32 v131, a27\n"
    "v_accvgpr_read_b32 v132, a28\n"
    "v_accvgpr_read_b32 v133, a29\n"
    "v_accvgpr_read_b32 v134, a30\n"
    "v_accvgpr_read_b32 v135, a31\n"
    "v_pk_mul_f32 v[128:129], v[128:129], v[32:33] op_sel_hi:[1,0]\n"
    "v_pk_mul_f32 v[130:131], v[130:131], v[32:33] op_sel_hi:[1,0]\n"
    "v_pk_mul_f32 v[132:133], v[132:133], v[32:33] op_sel_hi:[1,0]\n"
    "v_pk_mul_f32 v[134:135], v[134:135], v[32:33] op_sel_hi:[1,0]\n"
    "v_cvt_pk_bf16_f32 v180, v128, v129\n"
    "v_cvt_pk_bf16_f32 v181, v130, v131\n"
    "v_cvt_pk_bf16_f32 v182, v132, v133\n"
    "v_cvt_pk_bf16_f32 v183, v134, v135\n"
    "s_mov_b32 s47, 0\n"
    "buffer_store_dwordx4 v[180:183], v24, s[68:71], s47 offen offset:2560 sc1\n"
    "v_accvgpr_read_b32 v104, a32\n"
    "v_accvgpr_read_b32 v105, a33\n"
    "v_accvgpr_read_b32 v106, a34\n"
    "v_accvgpr_read_b32 v107, a35\n"
    "v_accvgpr_read_b32 v108, a36\n"
    "v_accvgpr_read_b32 v109, a37\n"
    "v_accvgpr_read_b32 v110, a38\n"
    "v_accvgpr_read_b32 v111, a39\n"
    "v_pk_mul_f32 v[104:105], v[104:105], v[30:31] op_sel_hi:[1,0]\n"
    "v_pk_mul_f32 v[106:107], v[106:107], v[30:31] op_sel_hi:[1,0]\n"
    "v_pk_mul_f32 v[108:109], v[108:109], v[30:31] op_sel_hi:[1,0]\n"
    "v_pk_mul_f32 v[110:111], v[110:111], v[30:31] op_sel_hi:[1,0]\n"
    "v_cvt_pk_bf16_f32 v168, v104, v105\n"
    "v_cvt_pk_bf16_f32 v169, v106, v107\n"
    "v_cvt_pk_bf16_f32 v170, v108, v109\n"
    "v_cvt_pk_bf16_f32 v171, v110, v111\n"
    "s_mov_b32 s47, 0x1000\n"
    "buffer_store_dwordx4 v[168:171], v24, s[68:71], s47 offen offset:0 sc1\n"
    "v_accvgpr_read_b32 v112, a40\n"
    "v_accvgpr_read_b32 v113, a41\n"
    "v_accvgpr_read_b32 v114, a42\n"
    "v_accvgpr_read_b32 v115, a43\n"
    "v_accvgpr_read_b32 v116, a44\n"
    "v_accvgpr_read_b32 v117, a45\n"
    "v_accvgpr_read_b32 v118, a46\n"
    "v_accvgpr_read_b32 v119, a47\n"
    "v_pk_mul_f32 v[112:113], v[112:113], v[30:31] op_sel_hi:[1,0]\n"
    "v_pk_mul_f32 v[114:115], v[114:115], v[30:31] op_sel_hi:[1,0]\n"
    "v_pk_mul_f32 v[116:117], v[116:117], v[30:31] op_sel_hi:[1,0]\n"
    "v_pk_mul_f32 v[118:119], v[118:119], v[30:31] op_sel_hi:[1,0]\n"
    "v_cvt_pk_bf16_f32 v172, v112, v113\n"
    "v_cvt_pk_bf16_f32 v173, v114, v115\n"
    "v_cvt_pk_bf16_f32 v174, v116, v117\n"
    "v_cvt_pk_bf16_f32 v175, v118, v119\n"
    "s_mov_b32 s47, 0x1000\n"
    "buffer_store_dwordx4 v[172:175], v24, s[68:71], s47 offen offset:512 sc1\n"
    "v_accvgpr_read_b32 v120, a48\n"
    "v_accvgpr_read_b32 v121, a49\n"
    "v_accvgpr_read_b32 v122, a50\n"
    "v_accvgpr_read_b32 v123, a51\n"
    "v_accvgpr_read_b32 v124, a52\n"
    "v_accvgpr_read_b32 v125, a53\n"
    "v_accvgpr_read_b32 v126, a54\n"
    "v_accvgpr_read_b32 v127, a55\n"
    "v_pk_mul_f32 v[120:121], v[120:121], v[32:33] op_sel_hi:[1,0]\n"
    "v_pk_mul_f32 v[122:123], v[122:123], v[32:33] op_sel_hi:[1,0]\n"
    "v_pk_mul_f32 v[124:125], v[124:125], v[32:33] op_sel_hi:[1,0]\n"
    "v_pk_mul_f32 v[126:127], v[126:127], v[32:33] op_sel_hi:[1,0]\n"
    "v_cvt_pk_bf16_f32 v176, v120, v121\n"
    "v_cvt_pk_bf16_f32 v177, v122, v123\n"
    "v_cvt_pk_bf16_f32 v178, v124, v125\n"
    "v_cvt_pk_bf16_f32 v179, v126, v127\n"
    "s_mov_b32 s47, 0x1000\n"
    "buffer_store_dwordx4 v[176:179], v24, s[68:71], s47 offen offset:2048 sc1\n"
    "v_accvgpr_read_b32 v128, a56\n"
    "v_accvgpr_read_b32 v129, a57\n"
    "v_accvgpr_read_b32 v130, a58\n"
    "v_accvgpr_read_b32 v131, a59\n"
    "v_accvgpr_read_b32 v132, a60\n"
    "v_accvgpr_read_b32 v133, a61\n"
    "v_accvgpr_read_b32 v134, a62\n"
    "v_accvgpr_read_b32 v135, a63\n"
    "v_pk_mul_f32 v[128:129], v[128:129], v[32:33] op_sel_hi:[1,0]\n"
    "v_pk_mul_f32 v[130:131], v[130:131], v[32:33] op_sel_hi:[1,0]\n"
    "v_pk_mul_f32 v[132:133], v[132:133], v[32:33] op_sel_hi:[1,0]\n"
    "v_pk_mul_f32 v[134:135], v[134:135], v[32:33] op_sel_hi:[1,0]\n"
    "v_cvt_pk_bf16_f32 v180, v128, v129\n"
    "v_cvt_pk_bf16_f32 v181, v130, v131\n"
    "v_cvt_pk_bf16_f32 v182, v132, v133\n"
    "v_cvt_pk_bf16_f32 v183, v134, v135\n"
    "s_mov_b32 s47, 0x1000\n"
    "buffer_store_dwordx4 v[180:183], v24, s[68:71], s47 offen offset:2560 sc1\n"
    "v_accvgpr_read_b32 v104, a64\n"
    "v_accvgpr_read_b32 v105, a65\n"
    "v_accvgpr_read_b32 v106, a66\n"
    "v_accvgpr_read_b32 v107, a67\n"
    "v_accvgpr_read_b32 v108, a68\n"
    "v_accvgpr_read_b32 v109, a69\n"
    "v_accvgpr_read_b32 v110, a70\n"
    "v_accvgpr_read_b32 v111, a71\n"
    "v_pk_mul_f32 v[104:105], v[104:105], v[30:31] op_sel_hi:[1,0]\n"
    "v_pk_mul_f32 v[106:107], v[106:107], v[30:31] op_sel_hi:[1,0]\n"
    "v_pk_mul_f32 v[108:109], v[108:109], v[30:31] op_sel_hi:[1,0]\n"
    "v_pk_mul_f32 v[110:111], v[110:111], v[30:31] op_sel_hi:[1,0]\n"
    "v_cvt_pk_bf16_f32 v168, v104, v105\n"
    "v_cvt_pk_bf16_f32 v169, v106, v107\n"
    "v_cvt_pk_bf16_f32 v170, v108, v109\n"
    "v_cvt_pk_bf16_f32 v171, v110, v111\n"
    "s_mov_b32 s47, 0x2000\n"
    "buffer_store_dwordx4 v[168:171], v24, s[68:71], s47 offen offset:0 sc1\n"
    "v_accvgpr_read_b32 v112, a72\n"
    "v_accvgpr_read_b32 v113, a73\n"
    "v_accvgpr_read_b32 v114, a74\n"
    "v_accvgpr_read_b32 v115, a75\n"
    "v_accvgpr_read_b32 v116, a76\n"
    "v_accvgpr_read_b32 v117, a77\n"
    "v_accvgpr_read_b32 v118, a78\n"
    "v_accvgpr_read_b32 v119, a79\n"
    "v_pk_mul_f32 v[112:113], v[112:113], v[30:31] op_sel_hi:[1,0]\n"
    "v_pk_mul_f32 v[114:115], v[114:115], v[30:31] op_sel_hi:[1,0]\n"
    "v_pk_mul_f32 v[116:117], v[116:117], v[30:31] op_sel_hi:[1,0]\n"
    "v_pk_mul_f32 v[118:119], v[118:119], v[30:31] op_sel_hi:[1,0]\n"
    "v_cvt_pk_bf16_f32 v172, v112, v113\n"
    "v_cvt_pk_bf16_f32 v173, v114, v115\n"
    "v_cvt_pk_bf16_f32 v174, v116, v117\n"
    "v_cvt_pk_bf16_f32 v175, v118, v119\n"
    "s_mov_b32 s47, 0x2000\n"
    "buffer_store_dwordx4 v[172:175], v24, s[68:71], s47 offen offset:512 sc1\n"
    "v_accvgpr_read_b32 v120, a80\n"
    "v_accvgpr_read_b32 v121, a81\n"
    "v_accvgpr_read_b32 v122, a82\n"
    "v_accvgpr_read_b32 v123, a83\n"
    "v_accvgpr_read_b32 v124, a84\n"
    "v_accvgpr_read_b32 v125, a85\n"
    "v_accvgpr_read_b32 v126, a86\n"
    "v_accvgpr_read_b32 v127, a87\n"
    "v_pk_mul_f32 v[120:121], v[120:121], v[32:33] op_sel_hi:[1,0]\n"
    "v_pk_mul_f32 v[122:123], v[122:123], v[32:33] op_sel_hi:[1,0]\n"
    "v_pk_mul_f32 v[124:125], v[124:125], v[32:33] op_sel_hi:[1,0]\n"
    "v_pk_mul_f32 v[126:127], v[126:127], v[32:33] op_sel_hi:[1,0]\n"
    "v_cvt_pk_bf16_f32 v176, v120, v121\n"
    "v_cvt_pk_bf16_f32 v177, v122, v123\n"
    "v_cvt_pk_bf16_f32 v178, v124, v125\n"
    "v_cvt_pk_bf16_f32 v179, v126, v127\n"
    "s_mov_b32 s47, 0x2000\n"
    "buffer_store_dwordx4 v[176:179], v24, s[68:71], s47 offen offset:2048 sc1\n"
    "v_accvgpr_read_b32 v128, a88\n"
    "v_accvgpr_read_b32 v129, a89\n"
    "v_accvgpr_read_b32 v130, a90\n"
    "v_accvgpr_read_b32 v131, a91\n"
    "v_accvgpr_read_b32 v132, a92\n"
    "v_accvgpr_read_b32 v133, a93\n"
    "v_accvgpr_read_b32 v134, a94\n"
    "v_accvgpr_read_b32 v135, a95\n"
    "v_pk_mul_f32 v[128:129], v[128:129], v[32:33] op_sel_hi:[1,0]\n"
    "v_pk_mul_f32 v[130:131], v[130:131], v[32:33] op_sel_hi:[1,0]\n"
    "v_pk_mul_f32 v[132:133], v[132:133], v[32:33] op_sel_hi:[1,0]\n"
    "v_pk_mul_f32 v[134:135], v[134:135], v[32:33] op_sel_hi:[1,0]\n"
    "v_cvt_pk_bf16_f32 v180, v128, v129\n"
    "v_cvt_pk_bf16_f32 v181, v130, v131\n"
    "v_cvt_pk_bf16_f32 v182, v132, v133\n"
    "v_cvt_pk_bf16_f32 v183, v134, v135\n"
    "s_mov_b32 s47, 0x2000\n"
    "buffer_store_dwordx4 v[180:183], v24, s[68:71], s47 offen offset:2560 sc1\n"
    "v_accvgpr_read_b32 v104, a96\n"
    "v_accvgpr_read_b32 v105, a97\n"
    "v_accvgpr_read_b32 v106, a98\n"
    "v_accvgpr_read_b32 v107, a99\n"
    "v_accvgpr_read_b32 v108, a100\n"
    "v_accvgpr_read_b32 v109, a101\n"
    "v_accvgpr_read_b32 v110, a102\n"
    "v_accvgpr_read_b32 v111, a103\n"
    "v_pk_mul_f32 v[104:105], v[104:105], v[30:31] op_sel_hi:[1,0]\n"
    "v_pk_mul_f32 v[106:107], v[106:107], v[30:31] op_sel_hi:[1,0]\n"
    "v_pk_mul_f32 v[108:109], v[108:109], v[30:31] op_sel_hi:[1,0]\n"
    "v_pk_mul_f32 v[110:111], v[110:111], v[30:31] op_sel_hi:[1,0]\n"
    "v_cvt_pk_bf16_f32 v168, v104, v105\n"
    "v_cvt_pk_bf16_f32 v169, v106, v107\n"
    "v_cvt_pk_bf16_f32 v170, v108, v109\n"
    "v_cvt_pk_bf16_f32 v171, v110, v111\n"
    "s_mov_b32 s47, 0x3000\n"
    "buffer_store_dwordx4 v[168:171], v24, s[68:71], s47 offen offset:0 sc1\n"
    "v_accvgpr_read_b32 v112, a104\n"
    "v_accvgpr_read_b32 v113, a105\n"
    "v_accvgpr_read_b32 v114, a106\n"
    "v_accvgpr_read_b32 v115, a107\n"
    "v_accvgpr_read_b32 v116, a108\n"
    "v_accvgpr_read_b32 v117, a109\n"
    "v_accvgpr_read_b32 v118, a110\n"
    "v_accvgpr_read_b32 v119, a111\n"
    "v_pk_mul_f32 v[112:113], v[112:113], v[30:31] op_sel_hi:[1,0]\n"
    "v_pk_mul_f32 v[114:115], v[114:115], v[30:31] op_sel_hi:[1,0]\n"
    "v_pk_mul_f32 v[116:117], v[116:117], v[30:31] op_sel_hi:[1,0]\n"
    "v_pk_mul_f32 v[118:119], v[118:119], v[30:31] op_sel_hi:[1,0]\n"
    "v_cvt_pk_bf16_f32 v172, v112, v113\n"
    "v_cvt_pk_bf16_f32 v173, v114, v115\n"
    "v_cvt_pk_bf16_f32 v174, v116, v117\n"
    "v_cvt_pk_bf16_f32 v175, v118, v119\n"
    "s_mov_b32 s47, 0x3000\n"
    "buffer_store_dwordx4 v[172:175], v24, s[68:71], s47 offen offset:512 sc1\n"
    "v_accvgpr_read_b32 v120, a112\n"
    "v_accvgpr_read_b32 v121, a113\n"
    "v_accvgpr_read_b32 v122, a114\n"
    "v_accvgpr_read_b32 v123, a115\n"
    "v_accvgpr_read_b32 v124, a116\n"
    "v_accvgpr_read_b32 v125, a117\n"
    "v_accvgpr_read_b32 v126, a118\n"
    "v_accvgpr_read_b32 v127, a119\n"
    "v_pk_mul_f32 v[120:121], v[120:121], v[32:33] op_sel_hi:[1,0]\n"
    "v_pk_mul_f32 v[122:123], v[122:123], v[32:33] op_sel_hi:[1,0]\n"
    "v_pk_mul_f32 v[124:125], v[124:125], v[32:33] op_sel_hi:[1,0]\n"
    "v_pk_mul_f32 v[126:127], v[126:127], v[32:33] op_sel_hi:[1,0]\n"
    "v_cvt_pk_bf16_f32 v176, v120, v121\n"
    "v_cvt_pk_bf16_f32 v177, v122, v123\n"
    "v_cvt_pk_bf16_f32 v178, v124, v125\n"
    "v_cvt_pk_bf16_f32 v179, v126, v127\n"
    "s_mov_b32 s47, 0x3000\n"
    "buffer_store_dwordx4 v[176:179], v24, s[68:71], s47 offen offset:2048 sc1\n"
    "v_accvgpr_read_b32 v128, a120\n"
    "v_accvgpr_read_b32 v129, a121\n"
    "v_accvgpr_read_b32 v130, a122\n"
    "v_accvgpr_read_b32 v131, a123\n"
    "v_accvgpr_read_b32 v132, a124\n"
    "v_accvgpr_read_b32 v133, a125\n"
    "v_accvgpr_read_b32 v134, a126\n"
    "v_accvgpr_read_b32 v135, a127\n"
    "v_pk_mul_f32 v[128:129], v[128:129], v[32:33] op_sel_hi:[1,0]\n"
    "v_pk_mul_f32 v[130:131], v[130:131], v[32:33] op_sel_hi:[1,0]\n"
    "v_pk_mul_f32 v[132:133], v[132:133], v[32:33] op_sel_hi:[1,0]\n"
    "v_pk_mul_f32 v[134:135], v[134:135], v[32:33] op_sel_hi:[1,0]\n"
    "v_cvt_pk_bf16_f32 v180, v128, v129\n"
    "v_cvt_pk_bf16_f32 v181, v130, v131\n"
    "v_cvt_pk_bf16_f32 v182, v132, v133\n"
    "v_cvt_pk_bf16_f32 v183, v134, v135\n"
    "s_mov_b32 s47, 0x3000\n"
    "buffer_store_dwordx4 v[180:183], v24, s[68:71], s47 offen offset:2560 sc1\n"
    "s_branch Lepi7_%=\n"
    "Lend%=:\n"
    : : "s"((unsigned long long)(size_t)__builtin_amdgcn_kernarg_segment_ptr()), "s"(blockIdx.x), "v"(threadIdx.x), "s"((unsigned)(size_t)lds)
    : "v1", "v2", "v3", "v4", "v5", "v6", "v7", "v8", "v9", "v10", "v11", "v12", "v13", "v14", "v15", "v16", "v17", "v18", "v19", "v20", "v21", "v22", "v23", "v24", "v25", "v26", "v27", "v28", "v29", "v30", "v31", "v32", "v33", "v34", "v35", "v36", "v37", "v38", "v39", "v40", "v41", "v42", "v43", "v44", "v45", "v46", "v47", "v48", "v49", "v50", "v51", "v52", "v53", "v54", "v55", "v56", "v57", "v58", "v59", "v60", "v61", "v62", "v63", "v64", "v65", "v66", "v67", "v68", "v69", "v70", "v71", "v72", "v73", "v74", "v75", "v76", "v77", "v78", "v79", "v80", "v81", "v82", "v83", "v84", "v85", "v86", "v87", "v88", "v89", "v90", "v91", "v92", "v93", "v94", "v95", "v96", "v97", "v98", "v99", "v100", "v101", "v102", "v103", "v104", "v105", "v106", "v107", "v108", "v109", "v110", "v111", "v112", "v113", "v114", "v115", "v116", "v117", "v118", "v119", "v120", "v121", "v122", "v123", "v124", "v125", "v126", "v127", "v128", "v129", "v130", "v131", "v132", "v133", "v134", "v135", "v136", "v137", "v138", "v139", "v140", "v141", "v142", "v143", "v144", "v145", "v146", "v147", "v148", "v149", "v150", "v151", "v152", "v153", "v154", "v155", "v156", "v157", "v158", "v159", "v160", "v161", "v162", "v163", "v164", "v165", "v166", "v167", "v168", "v169", "v170", "v171", "v172", "v173", "v174", "v175", "v176", "v177", "v178", "v179", "v180", "v181", "v182", "v183", "v184", "v185", "v186", "v187", "v188", "v189", "v190", "v191", "v192", "v193", "v194", "v195", "v196", "v197", "v198", "v199", "v200", "v201", "v202", "v203", "v204", "v205", "v206", "v207", "v208", "v209", "v210", "v211", "v212", "v213", "v214", "v215", "v216", "v217", "v218", "v219", "v220", "v221", "v222", "v223", "v224", "v225", "v226", "v227", "v228", "v229", "v230", "v231", "v232", "v233", "v234", "v235", "v236", "v237", "v238", "v239", "v240", "v241", "v242", "v243", "v244", "v245", "v246", "v247", "v248", "v249", "v250", "v251", "v252", "v253", "v254", "v255", "a0", "a1", "a2", "a3", "a4", "a5", "a6", "a7", "a8", "a9", "a10", "a11", "a12", "a13", "a14", "a15", "a16", "a17", "a18", "a19", "a20", "a21", "a22", "a23", "a24", "a25", "a26", "a27", "a28", "a29", "a30", "a31", "a32", "a33", "a34", "a35", "a36", "a37", "a38", "a39", "a40", "a41", "a42", "a43", "a44", "a45", "a46", "a47", "a48", "a49", "a50", "a51", "a52", "a53", "a54", "a55", "a56", "a57", "a58", "a59", "a60", "a61", "a62", "a63", "a64", "a65", "a66", "a67", "a68", "a69", "a70", "a71", "a72", "a73", "a74", "a75", "a76", "a77", "a78", "a79", "a80", "a81", "a82", "a83", "a84", "a85", "a86", "a87", "a88", "a89", "a90", "a91", "a92", "a93", "a94", "a95", "a96", "a97", "a98", "a99", "a100", "a101", "a102", "a103", "a104", "a105", "a106", "a107", "a108", "a109", "a110", "a111", "a112", "a113", "a114", "a115", "a116", "a117", "a118", "a119", "a120", "a121", "a122", "a123", "a124", "a125", "a126", "a127", "a128", "a129", "a130", "a131", "a132", "a133", "a134", "a135", "a136", "a137", "a138", "a139", "a140", "a141", "a142", "a143", "a144", "a145", "a146", "a147", "a148", "a149", "a150", "a151", "a152", "a153", "a154", "a155", "a156", "a157", "a158", "a159", "a160", "a161", "a162", "a163", "a164", "a165", "a166", "a167", "a168", "a169", "a170", "a171", "a172", "a173", "a174", "a175", "a176", "a177", "a178", "a179", "a180", "a181", "a182", "a183", "a184", "a185", "a186", "a187", "a188", "a189", "a190", "a191", "a192", "a193", "a194", "a195", "a196", "a197", "a198", "a199", "a200", "a201", "a202", "a203", "a204", "a205", "a206", "a207", "a208", "a209", "a210", "a211", "a212", "a213", "a214", "a215", "a216", "a217", "a218", "a219", "a220", "a221", "a222", "a223", "a224", "a225", "a226", "a227", "a228", "a229", "a230", "a231", "a232", "a233", "a234", "a235", "a236", "a237", "a238", "a239", "a240", "a241", "a242", "a243", "a244", "a245", "a246", "a247", "a248", "a249", "a250", "a251", "a252", "a253", "a254", "a255", "s8", "s9", "s10", "s11", "s12", "s13", "s14", "s15", "s16", "s17", "s18", "s19", "s20", "s21", "s22", "s23", "s24", "s25", "s26", "s27", "s28", "s29", "s30", "s31", "s34", "s35", "s36", "s37", "s38", "s39", "s40", "s41", "s42", "s43", "s44", "s45", "s46", "s47", "s48", "s49", "s50", "s51", "s52", "s53", "s54", "s55", "s56", "s57", "s58", "s59", "s60", "s61", "s62", "s63", "s64", "s65", "s66", "s67", "s68", "s69", "s70", "s71", "s72", "s73", "s74", "s75", "s76", "s77", "s78", "s79", "s80", "s81", "s82", "s83", "s84", "s85", "s86", "s87", "s88", "s89", "s90", "s91", "s92", "s93", "s94", "s95", "s96", "s97", "s98", "s99", "vcc", "memory");
}

static void launch_sel(void* const* d_in, void* d_out, void* d_ws, size_t ws_size, hipStream_t stream, unsigned sel) {
  const u16* x = (const u16*)d_in[0]; const u16* kc = (const u16*)d_in[1]; const u16* vc = (const u16*)d_in[2];
  const u16* wq = (const u16*)d_in[3]; const u16* wo = (const u16*)d_in[4];
  const u16* wg = (const u16*)d_in[6]; const u16* wu = (const u16*)d_in[7]; const u16* wd = (const u16*)d_in[8];
  const u16* c = (const u16*)d_in[9]; const u16* wmod = (const u16*)d_in[10];
  char* ws = (char*)d_ws + (ws_size > ((size_t)72 << 20) ? ((ws_size - ((size_t)72 << 20)) & ~(size_t)0xFFFFF) : 0);
  if (sel & 4) k2_mega<<<256, 256, 0, stream>>>(x, kc, vc, wq, wo, wd, wg, wu, (u16*)d_out, ws, c, wmod);
}
extern "C" void kernel_launch(void* const* d_in, const int* in_sizes, int n_in, void* d_out, int out_size, void* d_ws, size_t ws_size, hipStream_t stream) {
  launch_sel(d_in, d_out, d_ws, ws_size, stream, 0xffff);
}
```
